```python
import torch

def _softmax(x, axis=-1):
    return torch.softmax(x, dim=axis)
def _gelu(x):
    return torch.nn.functional.gelu(x, approximate="tanh")
def _silu(x):
    return torch.nn.functional.silu(x)
def _sigmoid(x):
    return torch.sigmoid(x)
def _layernorm(x, axis=-1, eps=1e-5):
    m = x.mean(dim=axis, keepdim=True)
    v = x.var(dim=axis, keepdim=True, unbiased=False)
    return (x - m) / torch.sqrt(v + eps)
def _scatter_add(tgt, idx, src, axis=0):
    return tgt.clone().index_add_(axis, idx, src)
def _segment_sum(src, seg, n):
    out = torch.zeros((n,) + src.shape[1:], dtype=src.dtype)
    return out.index_add_(0, seg, src)
def _pad(x, pw):
    flat = tuple(v for pair in reversed(pw) for v in pair)
    return torch.nn.functional.pad(x, flat)

kernel_name = 'recurrent_scan__ssm_conv_h3__dense_activation_gelu__prompt'

def setup_inputs():

    _w = (lambda a: (lambda t: t.to(torch.bfloat16) if t.is_floating_point() else t)(torch.as_tensor(a)))
    def _A_setup():
        B, S, D = 1, 2048, 640
        ks = 4
        return {
            "q": _w((lambda *s: torch.randn(*s, dtype=torch.bfloat16) * 0.05)(B, S, D)),
            "k": _w((lambda *s: torch.randn(*s, dtype=torch.bfloat16) * 0.8)(B, S, D)),
            "v": _w((lambda *s: torch.randn(*s, dtype=torch.bfloat16) * 0.8)(B, S, D)),
            "shift_k": _w((lambda *s: torch.randn(*s, dtype=torch.bfloat16) * 0.8)(D, ks) * 0.5),
            "ssm_k": _w((lambda *s: torch.randn(*s, dtype=torch.bfloat16) * 0.8)(D, S) * (1.0 / S ** 0.5)),
            "ssm_D": _w((lambda *s: torch.randn(*s, dtype=torch.bfloat16) * 0.8)(D)),
        }
    _A_in = _A_setup()

    def _B_setup():
        B, S, D, F = 1, 2048, 640, 1728
        return {
            "x": _w((lambda *s: torch.randn(*s, dtype=torch.bfloat16) * 0.05)(B, S, D)),
            "w1": _w((lambda *s: torch.randn(*s, dtype=torch.bfloat16) * 0.2)(D, F) * (D ** -0.5)),
            "b1": _w((lambda *s: torch.randn(*s, dtype=torch.bfloat16) * 0.2)(F) * 0.01),
            "w2": _w((lambda *s: torch.randn(*s, dtype=torch.bfloat16) * 0.2)(F, D) * (F ** -0.5)),
            "b2": _w((lambda *s: torch.randn(*s, dtype=torch.bfloat16) * 0.2)(D) * 0.01),
        }
    _B_in = _B_setup()

    return {
        "A_q": _A_in["q"],
        "A_k": _A_in["k"],
        "A_v": _A_in["v"],
        "A_shift_k": _A_in["shift_k"],
        "A_ssm_k": _A_in["ssm_k"],
        "A_ssm_D": _A_in["ssm_D"],
        "B_x": _B_in["x"],
        "B_w1": _B_in["w1"],
        "B_b1": _B_in["b1"],
        "B_w2": _B_in["w2"],
        "B_b2": _B_in["b2"],
    }

def reference(A_q, A_k, A_v, A_shift_k, A_ssm_k, A_ssm_D, B_x, B_w1, B_b1, B_w2, B_b2):
    def _A_ref(q, k, v, shift_k, ssm_k, ssm_D):
        B, S, D = q.shape
        ks = shift_k.shape[-1]
        k_pad = _pad(k, ((0, 0), (ks - 1, 0), (0, 0)))
        k_shift = torch.zeros_like(k)
        for t in range(ks):
            k_shift = k_shift + k_pad[:, t:t + S, :] * shift_k[:, ks - 1 - t][None, None, :]
        kv = k_shift * v
        L = 2 * S
        kv_pad = _pad(kv, ((0, 0), (0, S), (0, 0)))
        kf_pad = _pad(ssm_k, ((0, 0), (0, S)))
        kv_f = (lambda x, axes=(-2,-1): torch.fft.rfft2(x.float(), dim=axes))(kv_pad[:, None, :, :], axes=(1, 2))
        kf_f = (lambda x, axes=(-2,-1): torch.fft.rfft2(x.float(), dim=axes))(kf_pad[:, None, :], axes=(1, 2))
        kf_f = (lambda x, *d: x.permute(*d))(kf_f, 1, 2, 0)
        y_f = kv_f * kf_f[None, :, :, :]
        y = (lambda x, s, axes=(-2,-1): torch.fft.irfft2(x, s=s, dim=axes).to(torch.bfloat16))(y_f, (1, L), axes=(1, 2))[:, 0, :S, :]
        y = y + kv * ssm_D[None, None, :]
        return q * y

    def _B_ref(x, w1, b1, w2, b2):
        h = torch.einsum("bsd,df->bsf", x, w1) + b1
        h = _gelu(h)
        return torch.einsum("bsf,fd->bsd", h, w2) + b2

    x = A_q
    xn = _layernorm(x, axis=-1)
    s = _A_ref(xn, A_k, A_v, A_shift_k, A_ssm_k, A_ssm_D)
    g = _silu(_B_ref(xn, B_w1, B_b1, B_w2, B_b2))
    return x + s * g
```

```cpp
#include <hip/hip_runtime.h>
#include <stdint.h>
namespace {
constexpr unsigned WS_STATS = 8192, WS_KV = 32768, WS_Y2 = 2818048, WS_W2T = 5603328, WS_HT = 7897088, WS_EX = 15237120, WS_END = 18896896;
constexpr unsigned KVPI = 4224, Y2PI = 4224;
#define BUF const void*, unsigned, unsigned
__global__ __launch_bounds__(512, 2) void fused_kernel(BUF, BUF, BUF, BUF, BUF, BUF, BUF, BUF, BUF, BUF, BUF, BUF) {
#if defined(__HIP_DEVICE_COMPILE__)
  unsigned tid = threadIdx.x;
  asm volatile(
      "s_load_dwordx16 s[4:19], s[0:1], 0x0\n"
      "s_load_dwordx16 s[20:35], s[0:1], 0x40\n"
      "s_load_dwordx16 s[36:51], s[0:1], 0x80\n"
      "s_getpc_b64 s[64:65]\n"
      ".Lfz_pc:\n"
      "v_lshrrev_b32 v2, 6, v0\n"
      "v_and_b32 v1, 63, v0\n"
      "s_mov_b32 s60, s2\n"
      "v_readfirstlane_b32 s52, v2\n"
      "s_lshr_b32 s61, s2, 3\n"
      "s_mov_b32 s66, .Lfz_end-.Lfz_pc\n"
      "s_mov_b32 s67, 0x20000\n"
      "s_lshl3_add_u32 s62, s61, s52\n"
      "s_lshl_b32 s62, s62, 7\n"
      "v_and_b32 v2, 32, v0\n"
      "v_lshlrev_b32 v2, 1, v2\n"
      "buffer_load_dword v3, v2, s[64:67], s62 offen\n"
      "s_and_b32 s53, s52, 3\n"
      "s_and_b32 s54, s2, 1\n"
      "s_mul_i32 s62, s61, 37\n"
      "s_lshr_b32 s62, s62, 8\n"
      "s_mul_i32 s63, s62, 7\n"
      "s_sub_u32 s63, s61, s63\n"
      "s_cmp_lt_u32 s61, 28\n"
      "s_cselect_b32 s62, s62, s63\n"
      "s_cselect_b32 s55, s63, 7\n"
      "s_bfe_u32 s63, s2, 0x20001\n"
      "s_lshl2_add_u32 s56, s63, s62\n"
      "s_mul_i32 s63, s54, 7\n"
      "s_add_u32 s57, s63, s55\n"
      "s_mov_b32 s92, 0xffff0000\n"
      "s_and_b32 s62, s57, 1\n"
      "s_xor_b32 s63, s54, 1\n"
      "s_lshl1_add_u32 s99, s63, s62\n"
      "s_cmp_lt_u32 s52, 4\n"
      "s_cbranch_scc1 .LM\n"
      "s_cmp_lt_u32 s61, 28\n"
      "s_cbranch_scc0 .LSPARE\n"
      "v_lshrrev_b32 v72, 3, v1\n"
      "v_and_b32 v73, 7, v1\n"
      "v_mul_u32_u24 v74, 1280, v72\n"
      "v_lshl_add_u32 v24, v73, 4, v74\n"
      "v_lshrrev_b32 v75, 4, v1\n"
      "s_and_b32 s62, s53, 1\n"
      "s_lshl_b32 s62, s62, 2\n"
      "v_xor_b32 v73, v73, v75\n"
      "v_xor_b32 v73, s62, v73\n"
      "v_lshl_add_u32 v74, v73, 4, v74\n"
      "s_mul_i32 s62, s99, 40960\n"
      "v_add_u32 v20, s62, v74\n"
      "v_and_b32 v73, 15, v1\n"
      "v_lshlrev_b32 v76, 2, v75\n"
      "v_xor_b32 v73, v73, v76\n"
      "s_cmp_eq_u32 s57, 13\n"
      "s_cselect_b32 s63, 7, 15\n"
      "v_and_b32 v73, s63, v73\n"
      "v_mul_u32_u24 v75, 3456, v75\n"
      "v_lshl_add_u32 v16, v73, 4, v75\n"
      "v_add_u32 v17, 12800, v16\n"
      "v_add_u32 v18, 25600, v16\n"
      "v_add_u32 v19, 38400, v16\n"
      "s_mul_i32 s63, s56, 3\n"
      "s_add_u32 s63, s63, s57\n"
      "s_mul_i32 s64, s63, 205\n"
      "s_lshr_b32 s64, s64, 11\n"
      "s_mul_i32 s64, s64, 10\n"
      "s_sub_u32 s63, s63, s64\n"
      "s_lshl_b32 s64, s56, 4\n"
      "s_add_u32 s64, s64, s53\n"
      "s_mul_i32 s68, s64, 10240\n"
      "s_mul_i32 s69, s53, 55296\n"
      "s_lshl_b32 s64, s57, 8\n"
      "s_add_u32 s69, s69, s64\n"
      "s_lshl_b32 s64, s63, 7\n"
      "s_add_u32 s70, s68, s64\n"
      "s_mul_i32 s64, s63, 221184\n"
      "s_add_u32 s80, s69, s64\n"
      "s_add_u32 s64, s68, 1280\n"
      "s_and_b32 s91, s2, 7\n"
      "s_mul_i32 s91, s91, 28\n"
      "s_add_u32 s91, s91, s61\n"
      "s_mul_i32 s93, s91, 205\n"
      "s_lshr_b32 s90, s93, 10\n"
      "s_lshr_b32 s93, s93, 11\n"
      "s_mul_i32 s65, s93, 80640\n"
      "s_lshl_b32 s67, s91, 7\n"
      "s_add_u32 s65, s65, s67\n"
      "s_sub_u32 s66, s65, 10240\n"
      "s_cselect_b32 s66, 1073741824, s66\n"
      "s_mul_i32 s67, s53, 20480\n"
      "s_add_u32 s65, s65, s67\n"
      "s_lshr_b32 s67, s67, 1\n"
      "s_mul_i32 s90, s90, 39680\n"
      "s_add_u32 s90, s90, s67\n"
      "s_lshl_b32 s67, s91, 8\n"
      "s_add_u32 s90, s90, s67\n"
      "v_add_u32 v25, 9200, v24\n"
      "v_cmp_gt_u32 vcc, 40, v1\n"
      "v_mov_b32 v72, 1073741824\n"
      "s_mul_i32 s67, s53, 2080\n"
      "v_cndmask_b32 v26, v24, v72, vcc\n"
      "v_lshrrev_b32 v72, 4, v1\n"
      "v_mul_u32_u24 v72, 1280, v72\n"
      "v_and_b32 v73, 15, v1\n"
      "v_lshl_add_u32 v27, v73, 4, v72\n"
      "v_add_u32 v28, 4080, v27\n"
      "s_add_u32 s71, s70, 128\n"
      "s_add_u32 s81, s80, 221184\n"
      "s_cmp_eq_u32 s71, s64\n"
      "s_cselect_b32 s71, s68, s71\n"
      "s_cselect_b32 s81, s69, s81\n"
      "s_lshl_b32 s94, s53, 12\n"
      "s_lshl_b32 s95, s53, 10\n"
      "s_setprio 2\n"
      "s_waitcnt lgkmcnt(0)\n"
      "s_barrier\n"
      "s_add_u32 m0, s94, 0\n"
      "s_nop 0\n"
      "buffer_load_dwordx4 v16, s[8:11], s80 offen offset:0 lds\n"
      "buffer_load_dwordx4 v17, s[8:11], s80 offen offset:1024 lds\n"
      "buffer_load_dwordx4 v18, s[8:11], s80 offen offset:2048 lds\n"
      "buffer_load_dwordx4 v19, s[8:11], s80 offen offset:3072 lds\n"
      "s_add_u32 m0, s95, 16384\n"
      "s_nop 0\n"
      "buffer_load_dwordx4 v20, s[4:7], s70 offen lds\n"
      "s_mov_b32 m0, 139520\n"
      "s_nop 0\n"
      "buffer_load_dwordx4 v26, s[12:15], s66 offen nt lds\n"
      "s_add_u32 m0, s67, 140560\n"
      "s_nop 0\n"
      "buffer_load_dwordx4 v24, s[12:15], s65 offen nt lds\n"
      "buffer_load_dwordx4 v25, s[12:15], s65 offen offset:1040 nt lds\n"
      "s_add_u32 m0, s67, 148880\n"
      "s_nop 0\n"
      "buffer_load_dwordx4 v24, s[16:19], s65 offen nt lds\n"
      "buffer_load_dwordx4 v25, s[16:19], s65 offen offset:1040 nt lds\n"
      "s_add_u32 m0, s67, 122880\n"
      "s_nop 0\n"
      "buffer_load_dwordx4 v27, s[40:43], s90 offen nt lds\n"
      "buffer_load_dwordx4 v28, s[40:43], s90 offen offset:1040 nt lds\n"
      "s_add_u32 s72, s71, 128\n"
      "s_add_u32 s82, s81, 221184\n"
      "s_cmp_eq_u32 s72, s64\n"
      "s_cselect_b32 s72, s68, s72\n"
      "s_cselect_b32 s82, s69, s82\n"
      "s_add_u32 s73, s72, 128\n"
      "s_add_u32 s83, s82, 221184\n"
      "s_cmp_eq_u32 s73, s64\n"
      "s_cselect_b32 s73, s68, s73\n"
      "s_cselect_b32 s83, s69, s83\n"
      "s_add_u32 s74, s73, 128\n"
      "s_add_u32 s84, s83, 221184\n"
      "s_cmp_eq_u32 s74, s64\n"
      "s_cselect_b32 s74, s68, s74\n"
      "s_cselect_b32 s84, s69, s84\n"
      "s_add_u32 s75, s74, 128\n"
      "s_add_u32 s85, s84, 221184\n"
      "s_cmp_eq_u32 s75, s64\n"
      "s_cselect_b32 s75, s68, s75\n"
      "s_cselect_b32 s85, s69, s85\n"
      "s_add_u32 s76, s75, 128\n"
      "s_add_u32 s86, s85, 221184\n"
      "s_cmp_eq_u32 s76, s64\n"
      "s_cselect_b32 s76, s68, s76\n"
      "s_cselect_b32 s86, s69, s86\n"
      "s_add_u32 s77, s76, 128\n"
      "s_add_u32 s87, s86, 221184\n"
      "s_cmp_eq_u32 s77, s64\n"
      "s_cselect_b32 s77, s68, s77\n"
      "s_cselect_b32 s87, s69, s87\n"
      "s_add_u32 s78, s77, 128\n"
      "s_add_u32 s88, s87, 221184\n"
      "s_cmp_eq_u32 s78, s64\n"
      "s_cselect_b32 s78, s68, s78\n"
      "s_cselect_b32 s88, s69, s88\n"
      "s_add_u32 s79, s78, 128\n"
      "s_add_u32 s89, s88, 221184\n"
      "s_cmp_eq_u32 s79, s64\n"
      "s_cselect_b32 s79, s68, s79\n"
      "s_cselect_b32 s89, s69, s89\n"
      "s_xor_b32 s62, s99, 1\n"
      "s_mul_i32 s62, s62, 40960\n"
      "v_add_u32 v21, s62, v74\n"
      "s_xor_b32 s62, s99, 2\n"
      "s_mul_i32 s62, s62, 40960\n"
      "v_add_u32 v22, s62, v74\n"
      "s_xor_b32 s62, s99, 3\n"
      "s_mul_i32 s62, s62, 40960\n"
      "v_add_u32 v23, s62, v74\n"
      "s_mul_i32 s62, s93, 10\n"
      "s_sub_u32 s62, s91, s62\n"
      "s_lshl2_add_u32 s62, s62, s53\n"
      "s_lshl_b32 s62, s62, 5\n"
      "s_add_u32 s62, s62, s93\n"
      "s_lshl_b32 s62, s62, 7\n"
      "s_add_u32 s62, s62, 18644992\n"
      "v_mov_b32 v29, 1073741824\n"
      "v_writelane_b32 v29, s62, 0\n"
      "s_mul_i32 s93, s91, 205\n"
      "s_lshr_b32 s93, s93, 10\n"
      "s_mul_i32 s62, s93, 5\n"
      "s_sub_u32 s91, s91, s62\n"
      "s_lshr_b32 s30, s93, 2\n"
      "s_and_b32 s29, s93, 3\n"
      "s_mul_i32 s29, s29, 7\n"
      "s_add_u32 s29, s29, s30\n"
      "s_cmp_ge_u32 s30, 7\n"
      "s_cselect_b32 s30, 21, 0\n"
      "s_add_u32 s29, s29, s30\n"
      "s_lshl_b32 s29, s29, 10\n"
      "s_mul_i32 s30, s91, 458752\n"
      "s_add_u32 s29, s29, s30\n"
      "s_add_u32 s29, s29, 5603328\n"
      "s_mul_i32 s30, s53, 114688\n"
      "s_add_u32 s29, s29, s30\n"
      "s_add_u32 s30, s29, 57344\n"
      "s_lshl3_add_u32 s62, s93, s91\n"
      "s_lshl_b32 s62, s62, 7\n"
      "s_add_u32 s62, s62, 18808832\n"
      "s_lshl3_add_u32 s63, s60, 4096\n"
      "s_cmp_eq_u32 s53, 0\n"
      "s_cselect_b32 s62, s62, 1073741824\n"
      "s_cselect_b32 s63, s63, 1073741824\n"
      "v_writelane_b32 v29, s62, 1\n"
      "v_writelane_b32 v29, s63, 2\n"
      "v_lshrrev_b32 v44, 4, v1\n"
      "v_and_b32 v72, 1, v44\n"
      "v_lshrrev_b32 v44, 1, v44\n"
      "v_lshl_add_u32 v44, v72, 2, v44\n"
      "v_mul_u32_u24 v44, 1040, v44\n"
      "v_bfe_u32 v72, v1, 2, 2\n"
      "v_lshl_add_u32 v44, v72, 8, v44\n"
      "v_and_b32 v72, 3, v1\n"
      "v_lshl_add_u32 v44, v72, 3, v44\n"
      "s_lshl_b32 s64, s53, 6\n"
      "s_add_u32 s64, s64, 122880\n"
      "v_add_u32 v44, s64, v44\n"
      "v_mov_b64 v[36:37], 0\n"
      "v_mov_b64 v[38:39], 0\n"
      "s_lshl_b32 s63, s53, 10\n"
      "s_add_u32 s63, s63, 157696\n"
      "v_lshl_add_u32 v40, v1, 4, s63\n"
      "ds_write_b128 v40, v[36:39]\n"
      "v_lshlrev_b32 v40, 2, v1\n"
      "v_add_u32 v40, 161792, v40\n"
      "ds_write_b32 v40, v36\n"
      "v_lshlrev_b32 v5, 4, v1\n"
      "v_add_u32 v6, 4096, v5\n"
      "s_lshr_b32 s62, s60, 1\n"
      "s_mul_i32 s62, s62, 5\n"
      "s_mul_i32 s63, s54, 3\n"
      "s_add_u32 s63, s63, s62\n"
      "s_lshl_b32 s24, s63, 12\n"
      "s_add_u32 s24, s24, s95\n"
      "s_add_u32 s25, s24, 4096\n"
      "s_add_u32 s63, s62, 2\n"
      "s_lshl_b32 s26, s63, 12\n"
      "s_add_u32 s26, s26, s95\n"
      "v_mov_b32 v32, s26\n"
      "v_writelane_b32 v32, s24, 0\n"
      "v_writelane_b32 v32, s25, 1\n"
      "s_cmp_eq_u32 s53, 0\n"
      "s_cselect_b32 s63, 1073741824, -4\n"
      "v_add_u32 v32, s63, v32\n"
      "v_lshrrev_b32 v33, 11, v32\n"
      "v_xor_b32 v33, 524288, v33\n"
      "s_lshr_b32 s91, s62, 4\n"
      "s_add_u32 s63, s62, 4\n"
      "s_lshr_b32 s63, s63, 4\n"
      "s_sub_u32 s63, s63, s91\n"
      "s_lshl_b32 s63, s63, 9\n"
      "v_and_b32 v72, 8, v1\n"
      "v_mul_u32_u24 v72, s63, v72\n"
      "v_and_b32 v73, 7, v1\n"
      "v_lshl_add_u32 v30, v73, 7, v72\n"
      "s_lshl_b32 s91, s91, 12\n"
      "s_add_u32 s91, s91, s95\n"
      "s_add_u32 s91, s91, 18644992\n"
      "v_add_u32 v30, s91, v30\n"
      "s_mul_i32 s63, s53, 4608\n"
      "s_cmp_eq_u32 s53, 3\n"
      "s_cselect_b32 s63, 9728, s63\n"
      "s_add_u32 s63, s63, 139520\n"
      "v_lshl_add_u32 v34, v1, 3, s63\n"
      "v_lshrrev_b32 v72, 1, v1\n"
      "v_min_u32 v72, 6, v72\n"
      "s_mul_i32 s62, s54, 28\n"
      "s_add_u32 s62, s62, s53\n"
      "v_lshl_add_u32 v72, v72, 2, s62\n"
      "v_min_u32 v72, 53, v72\n"
      "s_lshl_b32 s63, s56, 14\n"
      "s_add_u32 s63, s63, 18382848\n"
      "v_lshl_add_u32 v13, v72, 8, s63\n"
      "s_mul_i32 s63, s55, 80\n"
      "s_add_u32 s91, s63, 79\n"
      "s_lshr_b32 s63, s63, 7\n"
      "s_lshr_b32 s91, s91, 7\n"
      "s_sub_u32 s91, s91, s63\n"
      "v_and_b32 v73, s91, v1\n"
      "v_lshl_add_u32 v72, v72, 3, v73\n"
      "s_lshl_b32 s63, s63, 7\n"
      "s_add_u32 s63, s63, 18808832\n"
      "v_lshl_add_u32 v31, v72, 7, s63\n"
      "v_lshrrev_b32 v72, 3, v1\n"
      "v_min_u32 v72, 5, v72\n"
      "v_mul_u32_u24 v73, 4160, v72\n"
      "v_lshrrev_b32 v72, 2, v72\n"
      "v_mul_u32_u24 v72, 57600, v72\n"
      "v_sub_u32 v73, v73, v72\n"
      "v_add_u32 v73, 122880, v73\n"
      "v_and_b32 v72, 7, v1\n"
      "v_lshl_add_u32 v35, v72, 3, v73\n"
      "s_add_u32 s62, s95, 122944\n"
      "v_add_u32 v40, s62, v5\n"
      "v_subrev_u32 v41, 40960, v40\n"
      "v_and_b32 v72, 31, v1\n"
      "v_lshlrev_b32 v42, 3, v72\n"
      "s_add_u32 s62, s95, 157696\n"
      "v_lshl_add_u32 v43, v72, 5, s62\n"
      "s_xor_b32 s31, s99, s53\n"
      "s_lshl2_add_u32 s31, s56, s31\n"
      "s_lshl_b32 s31, s31, 8\n"
      "s_add_u32 s31, s31, 8192\n"
      "s_lshl2_add_u32 s62, s54, s53\n"
      "s_cmp_ge_u32 s62, 6\n"
      "s_cselect_b32 s63, 1073741824, 6144\n"
      "v_add_u32 v7, s63, v5\n"
      "s_mul_i32 s62, s62, 7168\n"
      "s_mul_i32 s63, s55, 286720\n"
      "s_add_u32 s63, s63, s62\n"
      "s_add_u32 s27, s63, 5603328\n"
      "s_mul_i32 s63, s56, 458752\n"
      "s_add_u32 s63, s63, s62\n"
      "s_add_u32 s63, s63, 7897088\n"
      "s_mul_i32 s62, s54, 229376\n"
      "s_add_u32 s91, s63, s62\n"
      "s_sub_u32 s62, 229376, s62\n"
      "s_add_u32 s93, s63, s62\n"
      "s_sub_u32 s62, 4, s53\n"
      "s_and_b32 s62, s62, 3\n"
      "s_mul_i32 s62, s62, 57344\n"
      "s_add_u32 s44, s93, s62\n"
      "s_add_u32 s48, s91, s62\n"
      "s_sub_u32 s62, 5, s53\n"
      "s_and_b32 s62, s62, 3\n"
      "s_mul_i32 s62, s62, 57344\n"
      "s_add_u32 s45, s93, s62\n"
      "s_add_u32 s49, s91, s62\n"
      "s_sub_u32 s62, 6, s53\n"
      "s_and_b32 s62, s62, 3\n"
      "s_mul_i32 s62, s62, 57344\n"
      "s_add_u32 s46, s93, s62\n"
      "s_add_u32 s50, s91, s62\n"
      "s_sub_u32 s62, 7, s53\n"
      "s_and_b32 s62, s62, 3\n"
      "s_mul_i32 s62, s62, 57344\n"
      "s_add_u32 s47, s93, s62\n"
      "s_add_u32 s51, s91, s62\n"
      "s_add_u32 s91, s53, 3\n"
      "s_and_b32 s91, s91, 3\n"
      "s_lshl2_add_u32 s62, s91, 161888\n"
      "v_mov_b32 v15, s62\n"
      "s_mul_i32 s62, s53, 5120\n"
      "s_add_u32 s63, s62, 40960\n"
      "v_add_u32 v8, s63, v5\n"
      "s_mul_i32 s63, s91, 5120\n"
      "s_add_u32 s63, s63, 40960\n"
      "v_add_u32 v9, s63, v5\n"
      "s_xor_b32 s63, s60, 1\n"
      "s_lshl2_add_u32 s63, s63, s53\n"
      "s_mul_i32 s28, s63, 3072\n"
      "s_add_u32 s28, s28, 15237120\n"
      "v_cmp_eq_u32 vcc, s53, v1\n"
      "v_and_b32 v14, 48, v1\n"
      "v_add_u32 v14, 163072, v14\n"
      "v_cndmask_b32 v55, 0, 1, vcc\n"
      "v_mov_b32 v45, 161920\n"
      "buffer_load_dwordx4 v[56:59], v5, s[36:39], s24 offen nt\n"
      "buffer_load_dwordx4 v[60:63], v5, s[36:39], s25 offen nt\n"
      "buffer_load_dwordx4 v[64:67], v5, s[36:39], s26 offen nt\n"
      "buffer_load_dword v52, v32, s[36:39], 0 offen nt\n"
      "buffer_load_ushort v53, v33, s[32:35], 0 offen\n"
      "s_add_u32 m0, s94, 20480\n"
      "s_nop 0\n"
      "buffer_load_dwordx4 v16, s[8:11], s81 offen offset:0 lds\n"
      "buffer_load_dwordx4 v17, s[8:11], s81 offen offset:1024 lds\n"
      "buffer_load_dwordx4 v18, s[8:11], s81 offen offset:2048 lds\n"
      "buffer_load_dwordx4 v19, s[8:11], s81 offen offset:3072 lds\n"
      "s_add_u32 m0, s95, 36864\n"
      "s_nop 0\n"
      "buffer_load_dwordx4 v20, s[4:7], s71 offen lds\n"
      "s_waitcnt vmcnt(17) lgkmcnt(0)\n"
      "v_cmp_eq_u32 vcc, 0, v1\n"
      "v_lshlrev_b32 v2, 2, v1\n"
      "v_add_u32 v2, 157696, v2\n"
      "v_cndmask_b32 v3, 0, 1, vcc\n"
      "s_barrier\n"
      "s_add_u32 m0, s94, 40960\n"
      "s_nop 0\n"
      "buffer_load_dwordx4 v16, s[8:11], s82 offen offset:0 lds\n"
      "buffer_load_dwordx4 v17, s[8:11], s82 offen offset:1024 lds\n"
      "buffer_load_dwordx4 v18, s[8:11], s82 offen offset:2048 lds\n"
      "buffer_load_dwordx4 v19, s[8:11], s82 offen offset:3072 lds\n"
      "s_add_u32 m0, s95, 57344\n"
      "s_nop 0\n"
      "buffer_load_dwordx4 v20, s[4:7], s72 offen lds\n"
      "s_waitcnt vmcnt(15)\n"
      "s_barrier\n"
      "ds_read_b64_tr_b16 v[88:89], v44 offset:0\n"
      "ds_read_b64_tr_b16 v[90:91], v44 offset:2080\n"
      "ds_read_b64_tr_b16 v[92:93], v44 offset:32\n"
      "ds_read_b64_tr_b16 v[94:95], v44 offset:2112\n"
      "ds_read_b64 v[46:47], v45\n"
      "s_add_u32 m0, s94, 61440\n"
      "s_nop 0\n"
      "buffer_load_dwordx4 v16, s[8:11], s83 offen offset:0 lds\n"
      "buffer_load_dwordx4 v17, s[8:11], s83 offen offset:1024 lds\n"
      "buffer_load_dwordx4 v18, s[8:11], s83 offen offset:2048 lds\n"
      "buffer_load_dwordx4 v19, s[8:11], s83 offen offset:3072 lds\n"
      "s_add_u32 m0, s95, 77824\n"
      "s_nop 0\n"
      "buffer_load_dwordx4 v20, s[4:7], s73 offen lds\n"
      "s_waitcnt lgkmcnt(0)\n"
      "buffer_store_dwordx4 v[88:91], v5, s[20:23], s29 offen sc1\n"
      "buffer_store_dwordx4 v[92:95], v5, s[20:23], s30 offen sc1\n"
      "s_waitcnt lgkmcnt(0)\n"
      "v_readfirstlane_b32 s58, v46\n"
      "v_readfirstlane_b32 s59, v47\n"
      "v_mov_b32 v1, v55\n"
      "s_waitcnt vmcnt(12)\n"
      "s_barrier\n"
      "s_add_u32 m0, s94, 81920\n"
      "s_nop 0\n"
      "buffer_load_dwordx4 v16, s[8:11], s84 offen offset:0 lds\n"
      "buffer_load_dwordx4 v17, s[8:11], s84 offen offset:1024 lds\n"
      "buffer_load_dwordx4 v18, s[8:11], s84 offen offset:2048 lds\n"
      "buffer_load_dwordx4 v19, s[8:11], s84 offen offset:3072 lds\n"
      "s_add_u32 m0, s95, 98304\n"
      "s_nop 0\n"
      "buffer_load_dwordx4 v20, s[4:7], s74 offen lds\n"
      "s_cmp_eq_u32 s53, 0\n"
      "s_cbranch_scc0 .Ld_1\n"
      "v_readlane_b32 s62, v53, 0\n"
      "v_readlane_b32 s63, v53, 1\n"
      "v_readlane_b32 s91, v53, 2\n"
      "s_mov_b64 exec, 1\n"
      "v_lshlrev_b32 v72, 16, v56\n"
      "s_lshl_b32 s62, s62, 16\n"
      "v_and_b32 v73, s92, v56\n"
      "v_add_f32 v72, s62, v72\n"
      "v_cvt_pk_bf16_f32 v56, v72, v73\n"
      "v_lshlrev_b32 v72, 16, v60\n"
      "s_lshl_b32 s63, s63, 16\n"
      "v_and_b32 v73, s92, v60\n"
      "v_add_f32 v72, s63, v72\n"
      "v_cvt_pk_bf16_f32 v60, v72, v73\n"
      "v_lshlrev_b32 v72, 16, v64\n"
      "s_lshl_b32 s91, s91, 16\n"
      "v_and_b32 v73, s92, v64\n"
      "v_add_f32 v72, s91, v72\n"
      "v_cvt_pk_bf16_f32 v64, v72, v73\n"
      "s_mov_b64 exec, -1\n"
      ".Ld_1:\n"
      "v_readlane_b32 s62, v52, 2\n"
      "v_alignbyte_b32 v107, v67, v66, 2\n"
      "v_alignbyte_b32 v105, v65, v64, 2\n"
      "v_writelane_b32 v72, s62, 0\n"
      "v_alignbyte_b32 v106, v66, v65, 2\n"
      "s_nop 0\n"
      "v_mov_b32_dpp v72, v67 wave_shr:1 row_mask:0xf bank_mask:0xf\n"
      "v_alignbyte_b32 v104, v64, v72, 2\n"
      "v_readlane_b32 s62, v52, 1\n"
      "v_alignbyte_b32 v103, v63, v62, 2\n"
      "v_alignbyte_b32 v101, v61, v60, 2\n"
      "v_writelane_b32 v72, s62, 0\n"
      "v_alignbyte_b32 v102, v62, v61, 2\n"
      "s_nop 0\n"
      "v_mov_b32_dpp v72, v63 wave_shr:1 row_mask:0xf bank_mask:0xf\n"
      "v_alignbyte_b32 v100, v60, v72, 2\n"
      "v_alignbyte_b32 v99, v59, v58, 2\n"
      "v_alignbyte_b32 v97, v57, v56, 2\n"
      "v_alignbyte_b32 v98, v58, v57, 2\n"
      "v_mov_b32_dpp v52, v59 wave_shr:1 row_mask:0xf bank_mask:0xf\n"
      "v_alignbyte_b32 v96, v56, v52, 2\n"
      "s_waitcnt vmcnt(12)\n"
      "s_barrier\n"
      "s_add_u32 m0, s94, 102400\n"
      "s_nop 0\n"
      "buffer_load_dwordx4 v16, s[8:11], s85 offen offset:0 lds\n"
      "buffer_load_dwordx4 v17, s[8:11], s85 offen offset:1024 lds\n"
      "buffer_load_dwordx4 v18, s[8:11], s85 offen offset:2048 lds\n"
      "buffer_load_dwordx4 v19, s[8:11], s85 offen offset:3072 lds\n"
      "s_add_u32 m0, s95, 118784\n"
      "s_nop 0\n"
      "buffer_load_dwordx4 v20, s[4:7], s75 offen lds\n"
      "buffer_load_dwordx2 v[76:77], v42, s[20:23], s31 offen sc1\n"
      "ds_write_b128 v40, v[56:59] offset:0\n"
      "s_waitcnt vmcnt(13)\n"
      "s_barrier\n"
      "s_add_u32 m0, s94, 0\n"
      "s_nop 0\n"
      "buffer_load_dwordx4 v16, s[8:11], s86 offen offset:0 lds\n"
      "buffer_load_dwordx4 v17, s[8:11], s86 offen offset:1024 lds\n"
      "buffer_load_dwordx4 v18, s[8:11], s86 offen offset:2048 lds\n"
      "buffer_load_dwordx4 v19, s[8:11], s86 offen offset:3072 lds\n"
      "s_add_u32 m0, s95, 16384\n"
      "s_nop 0\n"
      "buffer_load_dwordx4 v20, s[4:7], s76 offen lds\n"
      "ds_write_b128 v40, v[96:99] offset:4160\n"
      "s_waitcnt vmcnt(11)\n"
      "s_barrier\n"
      "s_add_u32 m0, s94, 20480\n"
      "s_nop 0\n"
      "buffer_load_dwordx4 v16, s[8:11], s87 offen offset:0 lds\n"
      "buffer_load_dwordx4 v17, s[8:11], s87 offen offset:1024 lds\n"
      "buffer_load_dwordx4 v18, s[8:11], s87 offen offset:2048 lds\n"
      "buffer_load_dwordx4 v19, s[8:11], s87 offen offset:3072 lds\n"
      "s_add_u32 m0, s95, 36864\n"
      "s_nop 0\n"
      "buffer_load_dwordx4 v20, s[4:7], s77 offen lds\n"
      "ds_write_b128 v40, v[60:63] offset:8320\n"
      "s_waitcnt vmcnt(10)\n"
      "s_barrier\n"
      "buffer_store_dwordx2 v[46:47], v29, s[20:23], 0 offen sc1\n"
      "s_add_u32 m0, s94, 40960\n"
      "s_nop 0\n"
      "buffer_load_dwordx4 v16, s[8:11], s88 offen offset:0 lds\n"
      "buffer_load_dwordx4 v17, s[8:11], s88 offen offset:1024 lds\n"
      "buffer_load_dwordx4 v18, s[8:11], s88 offen offset:2048 lds\n"
      "buffer_load_dwordx4 v19, s[8:11], s88 offen offset:3072 lds\n"
      "s_add_u32 m0, s95, 57344\n"
      "s_nop 0\n"
      "buffer_load_dwordx4 v20, s[4:7], s78 offen lds\n"
      "ds_write_b128 v40, v[100:103] offset:12480\n"
      "v_cmp_ne_u32 vcc, s58, v77\n"
      "s_cbranch_vccnz .Lt_2\n"
      ".Lt_3:\n"
      "v_and_b32 v82, s92, v76\n"
      "v_rcp_f32 v77, v82\n"
      "v_xor_b32 v85, 0x8000, v76\n"
      "s_mov_b32 s62, 0x01000100\n"
      "v_mul_f32 v80, 0xc0135761, v77\n"
      "v_mul_f32 v81, v77, v77\n"
      "v_cmp_nle_f32 vcc, 0x3089705f, v77\n"
      "v_mul_f32 v81, 0x3d372713, v81\n"
      "v_perm_b32 v84, v85, v85, s62\n"
      "v_mul_f32 v81, v81, v80\n"
      "v_mov_b32 v83, 1.0\n"
      "ds_write_b64 v43, v[84:85] offset:16\n"
      "s_cbranch_vccnz .Lb_5\n"
      ".Lb_6:\n"
      "ds_write_b128 v43, v[80:83]\n"
      "s_waitcnt vmcnt(11)\n"
      "s_barrier\n"
      "s_add_u32 m0, s94, 61440\n"
      "s_nop 0\n"
      "buffer_load_dwordx4 v16, s[8:11], s89 offen offset:0 lds\n"
      "buffer_load_dwordx4 v17, s[8:11], s89 offen offset:1024 lds\n"
      "buffer_load_dwordx4 v18, s[8:11], s89 offen offset:2048 lds\n"
      "buffer_load_dwordx4 v19, s[8:11], s89 offen offset:3072 lds\n"
      "s_add_u32 m0, s95, 77824\n"
      "s_nop 0\n"
      "buffer_load_dwordx4 v20, s[4:7], s79 offen lds\n"
      "s_waitcnt vmcnt(11)\n"
      "s_barrier\n"
      "s_add_u32 m0, s95, 81920\n"
      "s_nop 0\n"
      "buffer_load_dwordx4 v21, s[4:7], s70 offen lds\n"
      "s_add_u32 m0, s95, 86016\n"
      "s_nop 0\n"
      "buffer_load_dwordx4 v21, s[4:7], s71 offen lds\n"
      "s_add_u32 m0, s95, 90112\n"
      "s_nop 0\n"
      "buffer_load_dwordx4 v21, s[4:7], s72 offen lds\n"
      "s_add_u32 m0, s95, 94208\n"
      "s_nop 0\n"
      "buffer_load_dwordx4 v21, s[4:7], s73 offen lds\n"
      "s_add_u32 m0, s95, 98304\n"
      "s_nop 0\n"
      "buffer_load_dwordx4 v21, s[4:7], s74 offen lds\n"
      "s_waitcnt vmcnt(10)\n"
      "s_barrier\n"
      "s_add_u32 m0, s95, 102400\n"
      "s_nop 0\n"
      "buffer_load_dwordx4 v21, s[4:7], s75 offen lds\n"
      "s_add_u32 m0, s95, 106496\n"
      "s_nop 0\n"
      "buffer_load_dwordx4 v21, s[4:7], s76 offen lds\n"
      "s_add_u32 m0, s95, 110592\n"
      "s_nop 0\n"
      "buffer_load_dwordx4 v21, s[4:7], s77 offen lds\n"
      "s_add_u32 m0, s95, 114688\n"
      "s_nop 0\n"
      "buffer_load_dwordx4 v21, s[4:7], s78 offen lds\n"
      "s_add_u32 m0, s95, 118784\n"
      "s_nop 0\n"
      "buffer_load_dwordx4 v21, s[4:7], s79 offen lds\n"
      "s_waitcnt vmcnt(10) lgkmcnt(0)\n"
      "s_barrier\n"
      "s_waitcnt vmcnt(5)\n"
      "s_barrier\n"
      "s_add_u32 m0, s95, 0\n"
      "s_waitcnt vmcnt(7)\n"
      "buffer_load_dwordx4 v22, s[4:7], s70 offen lds\n"
      "s_add_u32 m0, s95, 4096\n"
      "s_waitcnt vmcnt(7)\n"
      "buffer_load_dwordx4 v22, s[4:7], s71 offen lds\n"
      "s_add_u32 m0, s95, 8192\n"
      "s_waitcnt vmcnt(7)\n"
      "buffer_load_dwordx4 v22, s[4:7], s72 offen lds\n"
      "s_add_u32 m0, s95, 12288\n"
      "s_waitcnt vmcnt(7)\n"
      "buffer_load_dwordx4 v22, s[4:7], s73 offen lds\n"
      "s_add_u32 m0, s95, 16384\n"
      "s_waitcnt vmcnt(7)\n"
      "buffer_load_dwordx4 v22, s[4:7], s74 offen lds\n"
      "s_add_u32 m0, s95, 20480\n"
      "s_waitcnt vmcnt(7)\n"
      "buffer_load_dwordx4 v22, s[4:7], s75 offen lds\n"
      "s_add_u32 m0, s95, 24576\n"
      "s_waitcnt vmcnt(7)\n"
      "buffer_load_dwordx4 v22, s[4:7], s76 offen lds\n"
      "s_add_u32 m0, s95, 28672\n"
      "s_waitcnt vmcnt(7)\n"
      "ds_add_u32 v2, v3 offset:24\n"
      "buffer_load_dwordx4 v22, s[4:7], s77 offen lds\n"
      "s_add_u32 m0, s95, 32768\n"
      "s_waitcnt vmcnt(7)\n"
      "buffer_load_dwordx4 v22, s[4:7], s78 offen lds\n"
      "s_add_u32 m0, s95, 36864\n"
      "s_waitcnt vmcnt(7)\n"
      "buffer_load_dwordx4 v22, s[4:7], s79 offen lds\n"
      "s_add_u32 m0, s95, 40960\n"
      "s_waitcnt vmcnt(7)\n"
      "buffer_load_dwordx4 v23, s[4:7], s70 offen lds\n"
      "s_add_u32 m0, s95, 45056\n"
      "s_waitcnt vmcnt(7)\n"
      "buffer_load_dwordx4 v23, s[4:7], s71 offen lds\n"
      "s_add_u32 m0, s95, 49152\n"
      "s_waitcnt vmcnt(7)\n"
      "ds_add_u32 v2, v3 offset:1048\n"
      "buffer_load_dwordx4 v23, s[4:7], s72 offen lds\n"
      "s_add_u32 m0, s95, 53248\n"
      "s_waitcnt vmcnt(7)\n"
      "buffer_load_dwordx4 v23, s[4:7], s73 offen lds\n"
      "s_add_u32 m0, s95, 57344\n"
      "s_waitcnt vmcnt(7)\n"
      "buffer_load_dwordx4 v23, s[4:7], s74 offen lds\n"
      "s_add_u32 m0, s95, 61440\n"
      "s_waitcnt vmcnt(7)\n"
      "buffer_load_dwordx4 v23, s[4:7], s75 offen lds\n"
      "s_add_u32 m0, s95, 65536\n"
      "s_waitcnt vmcnt(7)\n"
      "buffer_load_dwordx4 v23, s[4:7], s76 offen lds\n"
      "s_add_u32 m0, s95, 69632\n"
      "s_waitcnt vmcnt(7)\n"
      "ds_add_u32 v2, v3 offset:2072\n"
      "buffer_load_dwordx4 v23, s[4:7], s77 offen lds\n"
      "s_add_u32 m0, s95, 73728\n"
      "s_waitcnt vmcnt(7)\n"
      "buffer_load_dwordx4 v23, s[4:7], s78 offen lds\n"
      "s_add_u32 m0, s95, 77824\n"
      "s_waitcnt vmcnt(7)\n"
      "buffer_load_dwordx4 v23, s[4:7], s79 offen lds\n"
      "ds_add_u32 v2, v3 offset:4152\n"
      "ds_read_b32 v4, v2 offset:4152\n"
      "s_waitcnt lgkmcnt(0)\n"
      "v_readfirstlane_b32 s2, v4\n"
      "s_cmp_lt_u32 s2, 4\n"
      "s_cbranch_scc1 .Lw_7\n"
      ".Lw_8:\n"
      ".LS2:\n"
      "buffer_load_dwordx2 v[50:51], v31, s[20:23], 0 offen sc1\n"
      "buffer_load_dwordx2 v[48:49], v30, s[20:23], 0 offen sc1\n"
      "ds_write_b64 v34, v[36:37]\n"
      "s_waitcnt vmcnt(2)\n"
      "ds_add_u32 v2, v3 offset:3096\n"
      "s_waitcnt vmcnt(1)\n"
      "v_cmp_ne_u64 vcc, s[58:59], v[50:51]\n"
      "s_cbranch_vccnz .Lf_10\n"
      ".Lf_11:\n"
      "s_add_u32 s66, s27, 57344\n"
      "s_add_u32 s67, s27, 114688\n"
      "s_add_u32 s68, s27, 172032\n"
      "s_add_u32 s69, s27, 229376\n"
      "s_waitcnt vmcnt(6)\n"
      "buffer_load_dwordx4 v[116:119], v5, s[20:23], s27 offen offset:0 sc1\n"
      "buffer_load_dwordx4 v[136:139], v5, s[20:23], s27 offen offset:1024 sc1\n"
      "s_waitcnt vmcnt(6)\n"
      "buffer_load_dwordx4 v[156:159], v5, s[20:23], s27 offen offset:2048 sc1\n"
      "buffer_load_dwordx4 v[176:179], v5, s[20:23], s27 offen offset:3072 sc1\n"
      "s_waitcnt vmcnt(4)\n"
      "v_cmp_ne_u64 vcc, s[58:59], v[48:49]\n"
      "s_cbranch_vccnz .Lf_13\n"
      ".Lf_14:\n"
      "s_add_u32 m0, s95, 140032\n"
      "s_lshr_b32 s62, s24, 12\n"
      "s_mul_i32 s62, s62, 128\n"
      "s_add_u32 s24, s24, s62\n"
      "s_add_u32 s24, s24, 32768\n"
      "buffer_load_dwordx4 v5, s[20:23], s24 offen sc1 lds\n"
      "s_add_u32 m0, s95, 144640\n"
      "s_lshr_b32 s62, s25, 12\n"
      "s_mul_i32 s62, s62, 128\n"
      "s_add_u32 s25, s25, s62\n"
      "s_add_u32 s25, s25, 32768\n"
      "buffer_load_dwordx4 v5, s[20:23], s25 offen sc1 lds\n"
      "s_add_u32 m0, s95, 149760\n"
      "s_lshr_b32 s62, s26, 12\n"
      "s_mul_i32 s62, s62, 128\n"
      "s_add_u32 s26, s26, s62\n"
      "s_add_u32 s26, s26, 32768\n"
      "buffer_load_dwordx4 v5, s[20:23], s26 offen sc1 lds\n"
      "s_waitcnt vmcnt(6)\n"
      "buffer_load_dwordx4 v[196:199], v6, s[20:23], s27 offen offset:0 sc1\n"
      "buffer_load_dwordx4 v[216:219], v6, s[20:23], s27 offen offset:1024 sc1\n"
      "s_waitcnt vmcnt(9)\n"
      "ds_read_b32 v4, v2 offset:1052\n"
      "s_waitcnt vmcnt(6)\n"
      "buffer_load_dwordx4 v[236:239], v7, s[20:23], s27 offen offset:0 sc1\n"
      "buffer_load_dwordx4 v[120:123], v5, s[20:23], s66 offen offset:0 sc1\n"
      "s_waitcnt lgkmcnt(0)\n"
      "v_readfirstlane_b32 s2, v4\n"
      "s_cmp_lt_u32 s2, 4\n"
      "s_cbranch_scc1 .Lw_16\n"
      ".Lw_17:\n"
      "ds_write_b64 v35, v[36:37]\n"
      "ds_write_b128 v41, v[64:67] offset:0\n"
      "ds_write_b128 v41, v[104:107] offset:4160\n"
      "s_waitcnt vmcnt(6)\n"
      "buffer_load_dwordx4 v[140:143], v5, s[20:23], s66 offen offset:1024 sc1\n"
      "buffer_load_dwordx4 v[160:163], v5, s[20:23], s66 offen offset:2048 sc1\n"
      "s_waitcnt vmcnt(6)\n"
      "buffer_load_dwordx4 v[180:183], v5, s[20:23], s66 offen offset:3072 sc1\n"
      "buffer_load_dwordx4 v[200:203], v6, s[20:23], s66 offen offset:0 sc1\n"
      "s_waitcnt vmcnt(8)\n"
      "ds_add_u32 v2, v3 offset:4128\n"
      "s_waitcnt vmcnt(6)\n"
      "buffer_load_dwordx4 v[220:223], v6, s[20:23], s66 offen offset:1024 sc1\n"
      "buffer_load_dwordx4 v[240:243], v7, s[20:23], s66 offen offset:0 sc1\n"
      "s_waitcnt vmcnt(6)\n"
      "buffer_load_dwordx4 v[124:127], v5, s[20:23], s67 offen offset:0 sc1\n"
      "buffer_load_dwordx4 v[144:147], v5, s[20:23], s67 offen offset:1024 sc1\n"
      "s_waitcnt vmcnt(6)\n"
      "buffer_load_dwordx4 v[164:167], v5, s[20:23], s67 offen offset:2048 sc1\n"
      "buffer_load_dwordx4 v[184:187], v5, s[20:23], s67 offen offset:3072 sc1\n"
      "s_waitcnt vmcnt(6)\n"
      "buffer_load_dwordx4 v[204:207], v6, s[20:23], s67 offen offset:0 sc1\n"
      "buffer_load_dwordx4 v[224:227], v6, s[20:23], s67 offen offset:1024 sc1\n"
      "s_waitcnt vmcnt(6)\n"
      "buffer_load_dwordx4 v[244:247], v7, s[20:23], s67 offen offset:0 sc1\n"
      "buffer_load_dwordx4 v[128:131], v5, s[20:23], s68 offen offset:0 sc1\n"
      "s_waitcnt vmcnt(6)\n"
      "buffer_load_dwordx4 v[148:151], v5, s[20:23], s68 offen offset:1024 sc1\n"
      "buffer_load_dwordx4 v[168:171], v5, s[20:23], s68 offen offset:2048 sc1\n"
      "s_waitcnt vmcnt(6)\n"
      "buffer_load_dwordx4 v[188:191], v5, s[20:23], s68 offen offset:3072 sc1\n"
      "buffer_load_dwordx4 v[208:211], v6, s[20:23], s68 offen offset:0 sc1\n"
      "s_waitcnt vmcnt(6)\n"
      "buffer_load_dwordx4 v[228:231], v6, s[20:23], s68 offen offset:1024 sc1\n"
      "buffer_load_dwordx4 v[248:251], v7, s[20:23], s68 offen offset:0 sc1\n"
      "s_waitcnt vmcnt(6)\n"
      "buffer_load_dwordx4 v[132:135], v5, s[20:23], s69 offen offset:0 sc1\n"
      "buffer_load_dwordx4 v[152:155], v5, s[20:23], s69 offen offset:1024 sc1\n"
      "s_waitcnt vmcnt(6)\n"
      "buffer_load_dwordx4 v[172:175], v5, s[20:23], s69 offen offset:2048 sc1\n"
      "buffer_load_dwordx4 v[192:195], v5, s[20:23], s69 offen offset:3072 sc1\n"
      "s_waitcnt vmcnt(6)\n"
      "buffer_load_dwordx4 v[212:215], v6, s[20:23], s69 offen offset:0 sc1\n"
      "buffer_load_dwordx4 v[232:235], v6, s[20:23], s69 offen offset:1024 sc1\n"
      "s_waitcnt vmcnt(6)\n"
      "buffer_load_dwordx4 v[252:255], v7, s[20:23], s69 offen offset:0 sc1\n"
      "buffer_load_dwordx2 v[10:11], v13, s[20:23], 0 offen sc1\n"
      "s_and_b32 s75, s58, 0x1fff\n"
      "s_or_b32 s75, s75, 0x4000\n"
      "s_lshl_b32 s75, s75, 16\n"
      "s_mov_b32 s74, 128\n"
      "s_mov_b32 s73, 0x100000\n"
      "s_mov_b64 vcc, 0\n"
      "s_branch .LWARM\n"
      ".LWARME:\n"
      "s_waitcnt vmcnt(0)\n"
      "v_cmp_ne_u64 vcc, s[58:59], v[10:11]\n"
      "s_cbranch_vccnz .Lf_19\n"
      ".Lf_20:\n"
      "s_waitcnt vmcnt(6)\n"
      "buffer_load_dwordx4 v[32:35], v5, s[20:23], s44 offen offset:0 sc1\n"
      "buffer_load_dwordx4 v[36:39], v5, s[20:23], s44 offen offset:1024 sc1\n"
      "s_waitcnt vmcnt(6)\n"
      "buffer_load_dwordx4 v[40:43], v5, s[20:23], s44 offen offset:2048 sc1\n"
      "buffer_load_dwordx4 v[44:47], v5, s[20:23], s44 offen offset:3072 sc1\n"
      "s_waitcnt vmcnt(6)\n"
      "buffer_load_dwordx4 v[48:51], v6, s[20:23], s44 offen offset:0 sc1\n"
      "buffer_load_dwordx4 v[52:55], v6, s[20:23], s44 offen offset:1024 sc1\n"
      "s_waitcnt vmcnt(6)\n"
      "buffer_load_dwordx4 v[56:59], v7, s[20:23], s44 offen offset:0 sc1\n"
      "buffer_load_dwordx4 v[60:63], v5, s[20:23], s45 offen offset:0 sc1\n"
      "s_waitcnt vmcnt(6)\n"
      "buffer_load_dwordx4 v[64:67], v5, s[20:23], s45 offen offset:1024 sc1\n"
      "buffer_load_dwordx4 v[68:71], v5, s[20:23], s45 offen offset:2048 sc1\n"
      "s_waitcnt vmcnt(6)\n"
      "buffer_load_dwordx4 v[72:75], v5, s[20:23], s45 offen offset:3072 sc1\n"
      "buffer_load_dwordx4 v[76:79], v6, s[20:23], s45 offen offset:0 sc1\n"
      "s_waitcnt vmcnt(6)\n"
      "buffer_load_dwordx4 v[80:83], v6, s[20:23], s45 offen offset:1024 sc1\n"
      "buffer_load_dwordx4 v[84:87], v7, s[20:23], s45 offen offset:0 sc1\n"
      "s_waitcnt vmcnt(6)\n"
      "buffer_load_dwordx4 v[88:91], v5, s[20:23], s46 offen offset:0 sc1\n"
      "buffer_load_dwordx4 v[92:95], v5, s[20:23], s46 offen offset:1024 sc1\n"
      "s_waitcnt vmcnt(6)\n"
      "buffer_load_dwordx4 v[96:99], v5, s[20:23], s46 offen offset:2048 sc1\n"
      "buffer_load_dwordx4 v[100:103], v5, s[20:23], s46 offen offset:3072 sc1\n"
      "s_waitcnt vmcnt(6)\n"
      "buffer_load_dwordx4 v[104:107], v6, s[20:23], s46 offen offset:0 sc1\n"
      "buffer_load_dwordx4 v[108:111], v6, s[20:23], s46 offen offset:1024 sc1\n"
      "s_waitcnt vmcnt(6)\n"
      "buffer_load_dwordx4 v[112:115], v7, s[20:23], s46 offen offset:0 sc1\n"
      "s_setprio 1\n"
      "v_mfma_f32_16x16x32_bf16 v[16:19], v[116:119], v[32:35], 0\n"
      "v_mfma_f32_16x16x32_bf16 v[16:19], v[136:139], v[36:39], v[16:19]\n"
      "v_mfma_f32_16x16x32_bf16 v[16:19], v[156:159], v[40:43], v[16:19]\n"
      "v_mfma_f32_16x16x32_bf16 v[16:19], v[176:179], v[44:47], v[16:19]\n"
      "ds_read_b32 v4, v2 offset:4160\n"
      "v_mfma_f32_16x16x32_bf16 v[16:19], v[196:199], v[48:51], v[16:19]\n"
      "v_mfma_f32_16x16x32_bf16 v[16:19], v[216:219], v[52:55], v[16:19]\n"
      "v_mfma_f32_16x16x32_bf16 v[16:19], v[236:239], v[56:59], v[16:19]\n"
      "v_mfma_f32_16x16x32_bf16 v[20:23], v[120:123], v[32:35], 0\n"
      "v_mfma_f32_16x16x32_bf16 v[20:23], v[140:143], v[36:39], v[20:23]\n"
      "s_waitcnt lgkmcnt(0)\n"
      "v_readfirstlane_b32 s2, v4\n"
      "s_cmp_lt_u32 s2, 4\n"
      "s_cbranch_scc1 .Lw_22\n"
      ".Lw_23:\n"
      "v_mfma_f32_16x16x32_bf16 v[20:23], v[160:163], v[40:43], v[20:23]\n"
      "v_mfma_f32_16x16x32_bf16 v[20:23], v[180:183], v[44:47], v[20:23]\n"
      "v_mfma_f32_16x16x32_bf16 v[20:23], v[200:203], v[48:51], v[20:23]\n"
      "ds_write_b128 v8, v[16:19] offset:0\n"
      "v_mfma_f32_16x16x32_bf16 v[20:23], v[220:223], v[52:55], v[20:23]\n"
      "v_mfma_f32_16x16x32_bf16 v[20:23], v[240:243], v[56:59], v[20:23]\n"
      "v_mfma_f32_16x16x32_bf16 v[24:27], v[124:127], v[32:35], 0\n"
      "v_mfma_f32_16x16x32_bf16 v[24:27], v[144:147], v[36:39], v[24:27]\n"
      "ds_add_u32 v2, v1 offset:4192\n"
      "v_mfma_f32_16x16x32_bf16 v[24:27], v[164:167], v[40:43], v[24:27]\n"
      "v_mfma_f32_16x16x32_bf16 v[24:27], v[184:187], v[44:47], v[24:27]\n"
      "v_mfma_f32_16x16x32_bf16 v[24:27], v[204:207], v[48:51], v[24:27]\n"
      "s_nop 1\n"
      "ds_write_b128 v8, v[20:23] offset:1024\n"
      "v_mfma_f32_16x16x32_bf16 v[24:27], v[224:227], v[52:55], v[24:27]\n"
      "v_mfma_f32_16x16x32_bf16 v[24:27], v[244:247], v[56:59], v[24:27]\n"
      "buffer_load_dwordx2 v[10:11], v13, s[20:23], s74 offen sc1\n"
      "v_mfma_f32_16x16x32_bf16 v[28:31], v[128:131], v[32:35], 0\n"
      "v_mfma_f32_16x16x32_bf16 v[28:31], v[148:151], v[36:39], v[28:31]\n"
      "ds_add_u32 v2, v1 offset:4192\n"
      "v_mfma_f32_16x16x32_bf16 v[28:31], v[168:171], v[40:43], v[28:31]\n"
      "v_mfma_f32_16x16x32_bf16 v[28:31], v[188:191], v[44:47], v[28:31]\n"
      "v_mfma_f32_16x16x32_bf16 v[28:31], v[208:211], v[48:51], v[28:31]\n"
      "s_nop 0\n"
      "ds_write_b128 v8, v[24:27] offset:2048\n"
      "v_mfma_f32_16x16x32_bf16 v[28:31], v[228:231], v[52:55], v[28:31]\n"
      "v_mfma_f32_16x16x32_bf16 v[28:31], v[248:251], v[56:59], v[28:31]\n"
      "v_mfma_f32_16x16x32_bf16 v[16:19], v[132:135], v[32:35], 0\n"
      "buffer_load_dwordx4 v[32:35], v5, s[20:23], s47 offen offset:0 sc1\n"
      "ds_read_b32 v4, v15\n"
      "ds_read_b128 v[20:23], v9 offset:0\n"
      "v_mfma_f32_16x16x32_bf16 v[16:19], v[152:155], v[36:39], v[16:19]\n"
      "ds_add_u32 v2, v1 offset:4192\n"
      "v_mfma_f32_16x16x32_bf16 v[16:19], v[172:175], v[40:43], v[16:19]\n"
      "v_mfma_f32_16x16x32_bf16 v[16:19], v[192:195], v[44:47], v[16:19]\n"
      "s_waitcnt lgkmcnt(2)\n"
      "v_cmp_gt_u32 vcc, 1, v4\n"
      "v_mfma_f32_16x16x32_bf16 v[16:19], v[212:215], v[48:51], v[16:19]\n"
      "buffer_load_dwordx4 v[36:39], v5, s[20:23], s47 offen offset:1024 sc1\n"
      "ds_write_b128 v8, v[28:31] offset:3072\n"
      "v_mfma_f32_16x16x32_bf16 v[16:19], v[232:235], v[52:55], v[16:19]\n"
      "s_cbranch_vccnz .Lp_25\n"
      ".Lp_26:\n"
      "v_mfma_f32_16x16x32_bf16 v[16:19], v[252:255], v[56:59], v[16:19]\n"
      "s_waitcnt lgkmcnt(2)\n"
      "v_mfma_f32_16x16x32_bf16 v[20:23], v[116:119], v[60:63], v[20:23]\n"
      "ds_read_b32 v4, v15\n"
      "ds_read_b128 v[24:27], v9 offset:1024\n"
      "v_mfma_f32_16x16x32_bf16 v[20:23], v[136:139], v[64:67], v[20:23]\n"
      "buffer_load_dwordx4 v[40:43], v5, s[20:23], s47 offen offset:2048 sc1\n"
      "ds_add_u32 v2, v1 offset:4192\n"
      "v_mfma_f32_16x16x32_bf16 v[20:23], v[156:159], v[68:71], v[20:23]\n"
      "v_mfma_f32_16x16x32_bf16 v[20:23], v[176:179], v[72:75], v[20:23]\n"
      "s_waitcnt lgkmcnt(2)\n"
      "v_cmp_gt_u32 vcc, 2, v4\n"
      "v_mfma_f32_16x16x32_bf16 v[20:23], v[196:199], v[76:79], v[20:23]\n"
      "ds_write_b128 v8, v[16:19] offset:4096\n"
      "v_mfma_f32_16x16x32_bf16 v[20:23], v[216:219], v[80:83], v[20:23]\n"
      "buffer_load_dwordx4 v[44:47], v5, s[20:23], s47 offen offset:3072 sc1\n"
      "s_cbranch_vccnz .Lp_27\n"
      ".Lp_28:\n"
      "v_mfma_f32_16x16x32_bf16 v[20:23], v[236:239], v[84:87], v[20:23]\n"
      "s_waitcnt lgkmcnt(2)\n"
      "v_mfma_f32_16x16x32_bf16 v[24:27], v[120:123], v[60:63], v[24:27]\n"
      "ds_read_b32 v4, v15\n"
      "ds_read_b128 v[28:31], v9 offset:2048\n"
      "v_mfma_f32_16x16x32_bf16 v[24:27], v[140:143], v[64:67], v[24:27]\n"
      "ds_add_u32 v2, v1 offset:4192\n"
      "v_mfma_f32_16x16x32_bf16 v[24:27], v[160:163], v[68:71], v[24:27]\n"
      "buffer_load_dwordx4 v[48:51], v6, s[20:23], s47 offen offset:0 sc1\n"
      "v_mfma_f32_16x16x32_bf16 v[24:27], v[180:183], v[72:75], v[24:27]\n"
      "s_waitcnt lgkmcnt(2)\n"
      "v_cmp_gt_u32 vcc, 3, v4\n"
      "v_mfma_f32_16x16x32_bf16 v[24:27], v[200:203], v[76:79], v[24:27]\n"
      "ds_write_b128 v8, v[20:23] offset:20480\n"
      "v_mfma_f32_16x16x32_bf16 v[24:27], v[220:223], v[80:83], v[24:27]\n"
      "s_cbranch_vccnz .Lp_29\n"
      ".Lp_30:\n"
      "v_mfma_f32_16x16x32_bf16 v[24:27], v[240:243], v[84:87], v[24:27]\n"
      "buffer_load_dwordx4 v[52:55], v6, s[20:23], s47 offen offset:1024 sc1\n"
      "s_waitcnt lgkmcnt(2)\n"
      "v_mfma_f32_16x16x32_bf16 v[28:31], v[124:127], v[60:63], v[28:31]\n"
      "ds_read_b32 v4, v15\n"
      "ds_read_b128 v[16:19], v9 offset:3072\n"
      "v_mfma_f32_16x16x32_bf16 v[28:31], v[144:147], v[64:67], v[28:31]\n"
      "ds_add_u32 v2, v1 offset:4192\n"
      "v_mfma_f32_16x16x32_bf16 v[28:31], v[164:167], v[68:71], v[28:31]\n"
      "v_mfma_f32_16x16x32_bf16 v[28:31], v[184:187], v[72:75], v[28:31]\n"
      "buffer_load_dwordx4 v[56:59], v7, s[20:23], s47 offen offset:0 sc1\n"
      "s_waitcnt lgkmcnt(2)\n"
      "v_cmp_gt_u32 vcc, 4, v4\n"
      "v_mfma_f32_16x16x32_bf16 v[28:31], v[204:207], v[76:79], v[28:31]\n"
      "ds_write_b128 v8, v[24:27] offset:21504\n"
      "v_mfma_f32_16x16x32_bf16 v[28:31], v[224:227], v[80:83], v[28:31]\n"
      "s_cbranch_vccnz .Lp_31\n"
      ".Lp_32:\n"
      "v_mfma_f32_16x16x32_bf16 v[28:31], v[244:247], v[84:87], v[28:31]\n"
      "s_waitcnt lgkmcnt(2)\n"
      "v_mfma_f32_16x16x32_bf16 v[16:19], v[128:131], v[60:63], v[16:19]\n"
      "ds_read_b32 v4, v15\n"
      "ds_read_b128 v[20:23], v9 offset:4096\n"
      "v_mfma_f32_16x16x32_bf16 v[16:19], v[148:151], v[64:67], v[16:19]\n"
      "ds_add_u32 v2, v1 offset:4192\n"
      "v_mfma_f32_16x16x32_bf16 v[16:19], v[168:171], v[68:71], v[16:19]\n"
      "v_mfma_f32_16x16x32_bf16 v[16:19], v[188:191], v[72:75], v[16:19]\n"
      "s_waitcnt lgkmcnt(2)\n"
      "v_cmp_gt_u32 vcc, 5, v4\n"
      "v_mfma_f32_16x16x32_bf16 v[16:19], v[208:211], v[76:79], v[16:19]\n"
      "ds_write_b128 v8, v[28:31] offset:22528\n"
      "v_mfma_f32_16x16x32_bf16 v[16:19], v[228:231], v[80:83], v[16:19]\n"
      "s_cbranch_vccnz .Lp_33\n"
      ".Lp_34:\n"
      "v_mfma_f32_16x16x32_bf16 v[16:19], v[248:251], v[84:87], v[16:19]\n"
      "s_waitcnt vmcnt(7)\n"
      "v_cmp_ne_u64 vcc, s[58:59], v[10:11]\n"
      "s_cbranch_vccnz .Lf_35\n"
      ".Lf_36:\n"
      "s_waitcnt lgkmcnt(2)\n"
      "v_mfma_f32_16x16x32_bf16 v[20:23], v[132:135], v[60:63], v[20:23]\n"
      "buffer_load_dwordx4 v[60:63], v5, s[20:23], s48 offen offset:0 sc1\n"
      "ds_read_b32 v4, v15\n"
      "ds_read_b128 v[24:27], v9 offset:20480\n"
      "v_mfma_f32_16x16x32_bf16 v[20:23], v[152:155], v[64:67], v[20:23]\n"
      "ds_add_u32 v2, v1 offset:4192\n"
      "v_mfma_f32_16x16x32_bf16 v[20:23], v[172:175], v[68:71], v[20:23]\n"
      "v_mfma_f32_16x16x32_bf16 v[20:23], v[192:195], v[72:75], v[20:23]\n"
      "s_waitcnt lgkmcnt(2)\n"
      "v_cmp_gt_u32 vcc, 6, v4\n"
      "v_mfma_f32_16x16x32_bf16 v[20:23], v[212:215], v[76:79], v[20:23]\n"
      "buffer_load_dwordx4 v[64:67], v5, s[20:23], s48 offen offset:1024 sc1\n"
      "ds_write_b128 v8, v[16:19] offset:23552\n"
      "v_mfma_f32_16x16x32_bf16 v[20:23], v[232:235], v[80:83], v[20:23]\n"
      "s_cbranch_vccnz .Lp_38\n"
      ".Lp_39:\n"
      "v_mfma_f32_16x16x32_bf16 v[20:23], v[252:255], v[84:87], v[20:23]\n"
      "s_waitcnt lgkmcnt(2)\n"
      "v_mfma_f32_16x16x32_bf16 v[24:27], v[116:119], v[88:91], v[24:27]\n"
      "ds_read_b32 v4, v15\n"
      "ds_read_b128 v[28:31], v9 offset:21504\n"
      "v_mfma_f32_16x16x32_bf16 v[24:27], v[136:139], v[92:95], v[24:27]\n"
      "buffer_load_dwordx4 v[68:71], v5, s[20:23], s48 offen offset:2048 sc1\n"
      "ds_add_u32 v2, v1 offset:4192\n"
      "v_mfma_f32_16x16x32_bf16 v[24:27], v[156:159], v[96:99], v[24:27]\n"
      "v_mfma_f32_16x16x32_bf16 v[24:27], v[176:179], v[100:103], v[24:27]\n"
      "s_waitcnt lgkmcnt(2)\n"
      "v_cmp_gt_u32 vcc, 7, v4\n"
      "v_mfma_f32_16x16x32_bf16 v[24:27], v[196:199], v[104:107], v[24:27]\n"
      "ds_write_b128 v8, v[20:23] offset:24576\n"
      "v_mfma_f32_16x16x32_bf16 v[24:27], v[216:219], v[108:111], v[24:27]\n"
      "buffer_load_dwordx4 v[72:75], v5, s[20:23], s48 offen offset:3072 sc1\n"
      "s_cbranch_vccnz .Lp_40\n"
      ".Lp_41:\n"
      "v_mfma_f32_16x16x32_bf16 v[24:27], v[236:239], v[112:115], v[24:27]\n"
      "s_waitcnt lgkmcnt(2)\n"
      "v_mfma_f32_16x16x32_bf16 v[28:31], v[120:123], v[88:91], v[28:31]\n"
      "ds_read_b32 v4, v15\n"
      "ds_read_b128 v[16:19], v9 offset:22528\n"
      "v_mfma_f32_16x16x32_bf16 v[28:31], v[140:143], v[92:95], v[28:31]\n"
      "ds_add_u32 v2, v1 offset:4192\n"
      "v_mfma_f32_16x16x32_bf16 v[28:31], v[160:163], v[96:99], v[28:31]\n"
      "buffer_load_dwordx4 v[76:79], v6, s[20:23], s48 offen offset:0 sc1\n"
      "v_mfma_f32_16x16x32_bf16 v[28:31], v[180:183], v[100:103], v[28:31]\n"
      "s_waitcnt lgkmcnt(2)\n"
      "v_cmp_gt_u32 vcc, 8, v4\n"
      "v_mfma_f32_16x16x32_bf16 v[28:31], v[200:203], v[104:107], v[28:31]\n"
      "ds_write_b128 v8, v[24:27] offset:57344\n"
      "v_mfma_f32_16x16x32_bf16 v[28:31], v[220:223], v[108:111], v[28:31]\n"
      "s_cbranch_vccnz .Lp_42\n"
      ".Lp_43:\n"
      "v_mfma_f32_16x16x32_bf16 v[28:31], v[240:243], v[112:115], v[28:31]\n"
      "buffer_load_dwordx4 v[80:83], v6, s[20:23], s48 offen offset:1024 sc1\n"
      "s_waitcnt lgkmcnt(2)\n"
      "v_mfma_f32_16x16x32_bf16 v[16:19], v[124:127], v[88:91], v[16:19]\n"
      "ds_read_b32 v4, v15\n"
      "ds_read_b128 v[20:23], v9 offset:23552\n"
      "v_mfma_f32_16x16x32_bf16 v[16:19], v[144:147], v[92:95], v[16:19]\n"
      "ds_add_u32 v2, v1 offset:4192\n"
      "v_mfma_f32_16x16x32_bf16 v[16:19], v[164:167], v[96:99], v[16:19]\n"
      "v_mfma_f32_16x16x32_bf16 v[16:19], v[184:187], v[100:103], v[16:19]\n"
      "buffer_load_dwordx4 v[84:87], v7, s[20:23], s48 offen offset:0 sc1\n"
      "s_waitcnt lgkmcnt(2)\n"
      "v_cmp_gt_u32 vcc, 9, v4\n"
      "v_mfma_f32_16x16x32_bf16 v[16:19], v[204:207], v[104:107], v[16:19]\n"
      "ds_write_b128 v8, v[28:31] offset:58368\n"
      "v_mfma_f32_16x16x32_bf16 v[16:19], v[224:227], v[108:111], v[16:19]\n"
      "s_cbranch_vccnz .Lp_44\n"
      ".Lp_45:\n"
      "v_mfma_f32_16x16x32_bf16 v[16:19], v[244:247], v[112:115], v[16:19]\n"
      "s_waitcnt lgkmcnt(2)\n"
      "v_mfma_f32_16x16x32_bf16 v[20:23], v[128:131], v[88:91], v[20:23]\n"
      "ds_read_b32 v4, v15\n"
      "ds_read_b128 v[24:27], v9 offset:24576\n"
      "v_mfma_f32_16x16x32_bf16 v[20:23], v[148:151], v[92:95], v[20:23]\n"
      "ds_add_u32 v2, v1 offset:4192\n"
      "v_mfma_f32_16x16x32_bf16 v[20:23], v[168:171], v[96:99], v[20:23]\n"
      "v_mfma_f32_16x16x32_bf16 v[20:23], v[188:191], v[100:103], v[20:23]\n"
      "s_waitcnt lgkmcnt(2)\n"
      "v_cmp_gt_u32 vcc, 10, v4\n"
      "v_mfma_f32_16x16x32_bf16 v[20:23], v[208:211], v[104:107], v[20:23]\n"
      "ds_write_b128 v8, v[16:19] offset:59392\n"
      "v_mfma_f32_16x16x32_bf16 v[20:23], v[228:231], v[108:111], v[20:23]\n"
      "s_cbranch_vccnz .Lp_46\n"
      ".Lp_47:\n"
      "v_mfma_f32_16x16x32_bf16 v[20:23], v[248:251], v[112:115], v[20:23]\n"
      "s_waitcnt lgkmcnt(2)\n"
      "v_mfma_f32_16x16x32_bf16 v[24:27], v[132:135], v[88:91], v[24:27]\n"
      "buffer_load_dwordx4 v[88:91], v5, s[20:23], s49 offen offset:0 sc1\n"
      "ds_read_b32 v4, v15\n"
      "ds_read_b128 v[28:31], v9 offset:57344\n"
      "v_mfma_f32_16x16x32_bf16 v[24:27], v[152:155], v[92:95], v[24:27]\n"
      "ds_add_u32 v2, v1 offset:4192\n"
      "v_mfma_f32_16x16x32_bf16 v[24:27], v[172:175], v[96:99], v[24:27]\n"
      "v_mfma_f32_16x16x32_bf16 v[24:27], v[192:195], v[100:103], v[24:27]\n"
      "s_waitcnt lgkmcnt(2)\n"
      "v_cmp_gt_u32 vcc, 11, v4\n"
      "v_mfma_f32_16x16x32_bf16 v[24:27], v[212:215], v[104:107], v[24:27]\n"
      "buffer_load_dwordx4 v[92:95], v5, s[20:23], s49 offen offset:1024 sc1\n"
      "ds_write_b128 v8, v[20:23] offset:60416\n"
      "v_mfma_f32_16x16x32_bf16 v[24:27], v[232:235], v[108:111], v[24:27]\n"
      "s_cbranch_vccnz .Lp_48\n"
      ".Lp_49:\n"
      "v_mfma_f32_16x16x32_bf16 v[24:27], v[252:255], v[112:115], v[24:27]\n"
      "s_waitcnt vmcnt(13)\n"
      "s_waitcnt lgkmcnt(2)\n"
      "v_mfma_f32_16x16x32_bf16 v[28:31], v[116:119], v[32:35], v[28:31]\n"
      "ds_read_b32 v4, v15\n"
      "ds_read_b128 v[16:19], v9 offset:58368\n"
      "v_mfma_f32_16x16x32_bf16 v[28:31], v[136:139], v[36:39], v[28:31]\n"
      "buffer_load_dwordx4 v[96:99], v5, s[20:23], s49 offen offset:2048 sc1\n"
      "ds_add_u32 v2, v1 offset:4192\n"
      "v_mfma_f32_16x16x32_bf16 v[28:31], v[156:159], v[40:43], v[28:31]\n"
      "s_waitcnt vmcnt(10)\n"
      "v_mfma_f32_16x16x32_bf16 v[28:31], v[176:179], v[44:47], v[28:31]\n"
      "s_waitcnt lgkmcnt(2)\n"
      "v_cmp_gt_u32 vcc, 12, v4\n"
      "v_mfma_f32_16x16x32_bf16 v[28:31], v[196:199], v[48:51], v[28:31]\n"
      "ds_write_b128 v8, v[24:27] offset:61440\n"
      "v_mfma_f32_16x16x32_bf16 v[28:31], v[216:219], v[52:55], v[28:31]\n"
      "buffer_load_dwordx4 v[100:103], v5, s[20:23], s49 offen offset:3072 sc1\n"
      "s_cbranch_vccnz .Lp_50\n"
      ".Lp_51:\n"
      "v_mfma_f32_16x16x32_bf16 v[28:31], v[236:239], v[56:59], v[28:31]\n"
      "s_waitcnt lgkmcnt(2)\n"
      "v_mfma_f32_16x16x32_bf16 v[16:19], v[120:123], v[32:35], v[16:19]\n"
      "ds_read_b32 v4, v15\n"
      "ds_read_b128 v[20:23], v9 offset:59392\n"
      "v_mfma_f32_16x16x32_bf16 v[16:19], v[140:143], v[36:39], v[16:19]\n"
      "ds_add_u32 v2, v1 offset:4192\n"
      "v_mfma_f32_16x16x32_bf16 v[16:19], v[160:163], v[40:43], v[16:19]\n"
      "buffer_load_dwordx4 v[104:107], v6, s[20:23], s49 offen offset:0 sc1\n"
      "v_mfma_f32_16x16x32_bf16 v[16:19], v[180:183], v[44:47], v[16:19]\n"
      "s_waitcnt lgkmcnt(2)\n"
      "v_cmp_gt_u32 vcc, 13, v4\n"
      "v_mfma_f32_16x16x32_bf16 v[16:19], v[200:203], v[48:51], v[16:19]\n"
      "v_cvt_pk_bf16_f32 v10, v28, v29\n"
      "v_cvt_pk_bf16_f32 v11, v30, v31\n"
      "v_mfma_f32_16x16x32_bf16 v[16:19], v[220:223], v[52:55], v[16:19]\n"
      "s_cbranch_vccnz .Lp_52\n"
      ".Lp_53:\n"
      "v_mfma_f32_16x16x32_bf16 v[16:19], v[240:243], v[56:59], v[16:19]\n"
      "buffer_load_dwordx4 v[108:111], v6, s[20:23], s49 offen offset:1024 sc1\n"
      "s_waitcnt lgkmcnt(1)\n"
      "v_mfma_f32_16x16x32_bf16 v[20:23], v[124:127], v[32:35], v[20:23]\n"
      "ds_read_b32 v4, v15\n"
      "ds_read_b128 v[24:27], v9 offset:60416\n"
      "v_mfma_f32_16x16x32_bf16 v[20:23], v[144:147], v[36:39], v[20:23]\n"
      "v_mfma_f32_16x16x32_bf16 v[20:23], v[164:167], v[40:43], v[20:23]\n"
      "v_mfma_f32_16x16x32_bf16 v[20:23], v[184:187], v[44:47], v[20:23]\n"
      "buffer_load_dwordx4 v[112:115], v7, s[20:23], s49 offen offset:0 sc1\n"
      "s_waitcnt lgkmcnt(1)\n"
      "v_cmp_gt_u32 vcc, 14, v4\n"
      "v_mfma_f32_16x16x32_bf16 v[20:23], v[204:207], v[48:51], v[20:23]\n"
      "v_cvt_pk_bf16_f32 v12, v16, v17\n"
      "v_cvt_pk_bf16_f32 v13, v18, s75\n"
      "v_mfma_f32_16x16x32_bf16 v[20:23], v[224:227], v[52:55], v[20:23]\n"
      "s_cbranch_vccnz .Lp_54\n"
      ".Lp_55:\n"
      "v_mfma_f32_16x16x32_bf16 v[20:23], v[244:247], v[56:59], v[20:23]\n"
      "buffer_store_dwordx4 v[10:13], v5, s[20:23], s28 offen offset:0 sc1\n"
      "s_waitcnt lgkmcnt(0)\n"
      "v_mfma_f32_16x16x32_bf16 v[24:27], v[128:131], v[32:35], v[24:27]\n"
      "ds_read_b32 v4, v15\n"
      "ds_read_b128 v[28:31], v9 offset:61440\n"
      "v_mfma_f32_16x16x32_bf16 v[24:27], v[148:151], v[36:39], v[24:27]\n"
      "v_mfma_f32_16x16x32_bf16 v[24:27], v[168:171], v[40:43], v[24:27]\n"
      "v_mfma_f32_16x16x32_bf16 v[24:27], v[188:191], v[44:47], v[24:27]\n"
      "s_waitcnt lgkmcnt(1)\n"
      "v_cmp_gt_u32 vcc, 15, v4\n"
      "v_mfma_f32_16x16x32_bf16 v[24:27], v[208:211], v[48:51], v[24:27]\n"
      "v_cvt_pk_bf16_f32 v10, v19, v20\n"
      "v_cvt_pk_bf16_f32 v11, v21, v22\n"
      "v_mfma_f32_16x16x32_bf16 v[24:27], v[228:231], v[52:55], v[24:27]\n"
      "s_cbranch_vccnz .Lp_56\n"
      ".Lp_57:\n"
      "v_mfma_f32_16x16x32_bf16 v[24:27], v[248:251], v[56:59], v[24:27]\n"
      "s_waitcnt lgkmcnt(0)\n"
      "v_mfma_f32_16x16x32_bf16 v[28:31], v[132:135], v[32:35], v[28:31]\n"
      "buffer_load_dwordx4 v[32:35], v5, s[20:23], s50 offen offset:0 sc1\n"
      "ds_read_b128 v[16:19], v14 offset:0\n"
      "v_mfma_f32_16x16x32_bf16 v[28:31], v[152:155], v[36:39], v[28:31]\n"
      "v_mfma_f32_16x16x32_bf16 v[28:31], v[172:175], v[40:43], v[28:31]\n"
      "ds_read_b32 v4, v2 offset:4136\n"
      "v_mfma_f32_16x16x32_bf16 v[28:31], v[192:195], v[44:47], v[28:31]\n"
      "v_mfma_f32_16x16x32_bf16 v[28:31], v[212:215], v[48:51], v[28:31]\n"
      "buffer_load_dwordx4 v[36:39], v5, s[20:23], s50 offen offset:1024 sc1\n"
      "v_cvt_pk_bf16_f32 v12, v23, v24\n"
      "v_cvt_pk_bf16_f32 v13, v25, s75\n"
      "v_mfma_f32_16x16x32_bf16 v[28:31], v[232:235], v[52:55], v[28:31]\n"
      "v_mfma_f32_16x16x32_bf16 v[28:31], v[252:255], v[56:59], v[28:31]\n"
      "buffer_store_dwordx4 v[10:13], v5, s[20:23], s28 offen offset:1024 sc1\n"
      "s_waitcnt vmcnt(15)\n"
      "s_waitcnt lgkmcnt(1)\n"
      "v_mfma_f32_16x16x32_bf16 v[16:19], v[116:119], v[60:63], v[16:19]\n"
      "ds_read_b128 v[20:23], v14 offset:64\n"
      "v_mfma_f32_16x16x32_bf16 v[16:19], v[136:139], v[64:67], v[16:19]\n"
      "buffer_load_dwordx4 v[40:43], v5, s[20:23], s50 offen offset:2048 sc1\n"
      "v_mfma_f32_16x16x32_bf16 v[16:19], v[156:159], v[68:71], v[16:19]\n"
      "s_waitcnt vmcnt(12)\n"
      "v_mfma_f32_16x16x32_bf16 v[16:19], v[176:179], v[72:75], v[16:19]\n"
      "s_waitcnt lgkmcnt(1)\n"
      "v_readfirstlane_b32 s2, v4\n"
      "s_cmp_lt_u32 s2, 4\n"
      "s_cbranch_scc1 .Lw_58\n"
      ".Lw_59:\n"
      "v_mfma_f32_16x16x32_bf16 v[16:19], v[196:199], v[76:79], v[16:19]\n"
      "v_cvt_pk_bf16_f32 v10, v26, v27\n"
      "v_cvt_pk_bf16_f32 v11, v28, v29\n"
      "v_mfma_f32_16x16x32_bf16 v[16:19], v[216:219], v[80:83], v[16:19]\n"
      "buffer_load_dwordx4 v[44:47], v5, s[20:23], s50 offen offset:3072 sc1\n"
      "v_cvt_pk_bf16_f32 v12, v30, v31\n"
      "v_cvt_pk_bf16_f32 v13, v31, s75\n"
      "v_mfma_f32_16x16x32_bf16 v[16:19], v[236:239], v[84:87], v[16:19]\n"
      "buffer_store_dwordx4 v[10:13], v5, s[20:23], s28 offen offset:2048 sc1\n"
      "s_waitcnt lgkmcnt(0)\n"
      "v_mfma_f32_16x16x32_bf16 v[20:23], v[120:123], v[60:63], v[20:23]\n"
      "ds_read_b128 v[24:27], v14 offset:128\n"
      "v_mfma_f32_16x16x32_bf16 v[20:23], v[140:143], v[64:67], v[20:23]\n"
      "v_mfma_f32_16x16x32_bf16 v[20:23], v[160:163], v[68:71], v[20:23]\n"
      "buffer_load_dwordx4 v[48:51], v6, s[20:23], s50 offen offset:0 sc1\n"
      "v_mfma_f32_16x16x32_bf16 v[20:23], v[180:183], v[72:75], v[20:23]\n"
      "v_mfma_f32_16x16x32_bf16 v[20:23], v[200:203], v[76:79], v[20:23]\n"
      "v_add_u32 v8, 4294926336, v8\n"
      "ds_write_b128 v8, v[16:19] offset:0\n"
      "v_mfma_f32_16x16x32_bf16 v[20:23], v[220:223], v[80:83], v[20:23]\n"
      "v_mfma_f32_16x16x32_bf16 v[20:23], v[240:243], v[84:87], v[20:23]\n"
      "buffer_load_dwordx4 v[52:55], v6, s[20:23], s50 offen offset:1024 sc1\n"
      "s_waitcnt lgkmcnt(1)\n"
      "v_mfma_f32_16x16x32_bf16 v[24:27], v[124:127], v[60:63], v[24:27]\n"
      "ds_read_b128 v[28:31], v14 offset:192\n"
      "v_mfma_f32_16x16x32_bf16 v[24:27], v[144:147], v[64:67], v[24:27]\n"
      "ds_add_u32 v2, v1 offset:4192\n"
      "v_mfma_f32_16x16x32_bf16 v[24:27], v[164:167], v[68:71], v[24:27]\n"
      "v_mfma_f32_16x16x32_bf16 v[24:27], v[184:187], v[72:75], v[24:27]\n"
      "buffer_load_dwordx4 v[56:59], v7, s[20:23], s50 offen offset:0 sc1\n"
      "v_mfma_f32_16x16x32_bf16 v[24:27], v[204:207], v[76:79], v[24:27]\n"
      "ds_write_b128 v8, v[20:23] offset:1024\n"
      "v_mfma_f32_16x16x32_bf16 v[24:27], v[224:227], v[80:83], v[24:27]\n"
      "v_mfma_f32_16x16x32_bf16 v[24:27], v[244:247], v[84:87], v[24:27]\n"
      "s_waitcnt lgkmcnt(2)\n"
      "v_mfma_f32_16x16x32_bf16 v[28:31], v[128:131], v[60:63], v[28:31]\n"
      "ds_read_b128 v[16:19], v14 offset:256\n"
      "v_mfma_f32_16x16x32_bf16 v[28:31], v[148:151], v[64:67], v[28:31]\n"
      "ds_add_u32 v2, v1 offset:4192\n"
      "v_mfma_f32_16x16x32_bf16 v[28:31], v[168:171], v[68:71], v[28:31]\n"
      "v_mfma_f32_16x16x32_bf16 v[28:31], v[188:191], v[72:75], v[28:31]\n"
      "v_mfma_f32_16x16x32_bf16 v[28:31], v[208:211], v[76:79], v[28:31]\n"
      "ds_write_b128 v8, v[24:27] offset:2048\n"
      "v_mfma_f32_16x16x32_bf16 v[28:31], v[228:231], v[80:83], v[28:31]\n"
      "v_mfma_f32_16x16x32_bf16 v[28:31], v[248:251], v[84:87], v[28:31]\n"
      "s_waitcnt lgkmcnt(2)\n"
      "v_mfma_f32_16x16x32_bf16 v[16:19], v[132:135], v[60:63], v[16:19]\n"
      "buffer_load_dwordx4 v[60:63], v5, s[20:23], s51 offen offset:0 sc1\n"
      "v_add_u32 v9, 4294926336, v9\n"
      "ds_read_b32 v4, v15\n"
      "ds_read_b128 v[20:23], v9 offset:0\n"
      "v_mfma_f32_16x16x32_bf16 v[16:19], v[152:155], v[64:67], v[16:19]\n"
      "ds_add_u32 v2, v1 offset:4192\n"
      "v_mfma_f32_16x16x32_bf16 v[16:19], v[172:175], v[68:71], v[16:19]\n"
      "v_mfma_f32_16x16x32_bf16 v[16:19], v[192:195], v[72:75], v[16:19]\n"
      "s_waitcnt lgkmcnt(2)\n"
      "v_cmp_gt_u32 vcc, 16, v4\n"
      "v_mfma_f32_16x16x32_bf16 v[16:19], v[212:215], v[76:79], v[16:19]\n"
      "buffer_load_dwordx4 v[64:67], v5, s[20:23], s51 offen offset:1024 sc1\n"
      "ds_write_b128 v8, v[28:31] offset:3072\n"
      "v_mfma_f32_16x16x32_bf16 v[16:19], v[232:235], v[80:83], v[16:19]\n"
      "s_cbranch_vccnz .Lp_61\n"
      ".Lp_62:\n"
      "v_mfma_f32_16x16x32_bf16 v[16:19], v[252:255], v[84:87], v[16:19]\n"
      "s_waitcnt vmcnt(16)\n"
      "s_waitcnt lgkmcnt(2)\n"
      "v_mfma_f32_16x16x32_bf16 v[20:23], v[116:119], v[88:91], v[20:23]\n"
      "ds_read_b32 v4, v15\n"
      "ds_read_b128 v[24:27], v9 offset:1024\n"
      "v_mfma_f32_16x16x32_bf16 v[20:23], v[136:139], v[92:95], v[20:23]\n"
      "buffer_load_dwordx4 v[68:71], v5, s[20:23], s51 offen offset:2048 sc1\n"
      "ds_add_u32 v2, v1 offset:4192\n"
      "v_mfma_f32_16x16x32_bf16 v[20:23], v[156:159], v[96:99], v[20:23]\n"
      "s_waitcnt vmcnt(13)\n"
      "v_mfma_f32_16x16x32_bf16 v[20:23], v[176:179], v[100:103], v[20:23]\n"
      "s_waitcnt lgkmcnt(2)\n"
      "v_cmp_gt_u32 vcc, 17, v4\n"
      "v_mfma_f32_16x16x32_bf16 v[20:23], v[196:199], v[104:107], v[20:23]\n"
      "ds_write_b128 v8, v[16:19] offset:4096\n"
      "v_mfma_f32_16x16x32_bf16 v[20:23], v[216:219], v[108:111], v[20:23]\n"
      "buffer_load_dwordx4 v[72:75], v5, s[20:23], s51 offen offset:3072 sc1\n"
      "s_cbranch_vccnz .Lp_63\n"
      ".Lp_64:\n"
      "v_mfma_f32_16x16x32_bf16 v[20:23], v[236:239], v[112:115], v[20:23]\n"
      "s_waitcnt lgkmcnt(2)\n"
      "v_mfma_f32_16x16x32_bf16 v[24:27], v[120:123], v[88:91], v[24:27]\n"
      "ds_read_b32 v4, v15\n"
      "ds_read_b128 v[28:31], v9 offset:2048\n"
      "v_mfma_f32_16x16x32_bf16 v[24:27], v[140:143], v[92:95], v[24:27]\n"
      "ds_add_u32 v2, v1 offset:4192\n"
      "v_mfma_f32_16x16x32_bf16 v[24:27], v[160:163], v[96:99], v[24:27]\n"
      "buffer_load_dwordx4 v[76:79], v6, s[20:23], s51 offen offset:0 sc1\n"
      "v_mfma_f32_16x16x32_bf16 v[24:27], v[180:183], v[100:103], v[24:27]\n"
      "s_waitcnt lgkmcnt(2)\n"
      "v_cmp_gt_u32 vcc, 18, v4\n"
      "v_mfma_f32_16x16x32_bf16 v[24:27], v[200:203], v[104:107], v[24:27]\n"
      "ds_write_b128 v8, v[20:23] offset:20480\n"
      "v_mfma_f32_16x16x32_bf16 v[24:27], v[220:223], v[108:111], v[24:27]\n"
      "s_cbranch_vccnz .Lp_65\n"
      ".Lp_66:\n"
      "v_mfma_f32_16x16x32_bf16 v[24:27], v[240:243], v[112:115], v[24:27]\n"
      "buffer_load_dwordx4 v[80:83], v6, s[20:23], s51 offen offset:1024 sc1\n"
      "s_waitcnt lgkmcnt(2)\n"
      "v_mfma_f32_16x16x32_bf16 v[28:31], v[124:127], v[88:91], v[28:31]\n"
      "ds_read_b32 v4, v15\n"
      "ds_read_b128 v[16:19], v9 offset:3072\n"
      "v_mfma_f32_16x16x32_bf16 v[28:31], v[144:147], v[92:95], v[28:31]\n"
      "ds_add_u32 v2, v1 offset:4192\n"
      "v_mfma_f32_16x16x32_bf16 v[28:31], v[164:167], v[96:99], v[28:31]\n"
      "v_mfma_f32_16x16x32_bf16 v[28:31], v[184:187], v[100:103], v[28:31]\n"
      "buffer_load_dwordx4 v[84:87], v7, s[20:23], s51 offen offset:0 sc1\n"
      "s_waitcnt lgkmcnt(2)\n"
      "v_cmp_gt_u32 vcc, 19, v4\n"
      "v_mfma_f32_16x16x32_bf16 v[28:31], v[204:207], v[104:107], v[28:31]\n"
      "ds_write_b128 v8, v[24:27] offset:21504\n"
      "v_mfma_f32_16x16x32_bf16 v[28:31], v[224:227], v[108:111], v[28:31]\n"
      "s_cbranch_vccnz .Lp_67\n"
      ".Lp_68:\n"
      "v_mfma_f32_16x16x32_bf16 v[28:31], v[244:247], v[112:115], v[28:31]\n"
      "s_waitcnt lgkmcnt(2)\n"
      "v_mfma_f32_16x16x32_bf16 v[16:19], v[128:131], v[88:91], v[16:19]\n"
      "ds_read_b32 v4, v15\n"
      "ds_read_b128 v[20:23], v9 offset:4096\n"
      "v_mfma_f32_16x16x32_bf16 v[16:19], v[148:151], v[92:95], v[16:19]\n"
      "ds_add_u32 v2, v1 offset:4192\n"
      "v_mfma_f32_16x16x32_bf16 v[16:19], v[168:171], v[96:99], v[16:19]\n"
      "v_mfma_f32_16x16x32_bf16 v[16:19], v[188:191], v[100:103], v[16:19]\n"
      "s_waitcnt lgkmcnt(2)\n"
      "v_cmp_gt_u32 vcc, 20, v4\n"
      "v_mfma_f32_16x16x32_bf16 v[16:19], v[208:211], v[104:107], v[16:19]\n"
      "ds_write_b128 v8, v[28:31] offset:22528\n"
      "v_mfma_f32_16x16x32_bf16 v[16:19], v[228:231], v[108:111], v[16:19]\n"
      "s_cbranch_vccnz .Lp_69\n"
      ".Lp_70:\n"
      "v_mfma_f32_16x16x32_bf16 v[16:19], v[248:251], v[112:115], v[16:19]\n"
      "s_waitcnt lgkmcnt(2)\n"
      "v_mfma_f32_16x16x32_bf16 v[20:23], v[132:135], v[88:91], v[20:23]\n"
      "ds_read_b32 v4, v15\n"
      "ds_read_b128 v[24:27], v9 offset:20480\n"
      "v_mfma_f32_16x16x32_bf16 v[20:23], v[152:155], v[92:95], v[20:23]\n"
      "ds_add_u32 v2, v1 offset:4192\n"
      "v_mfma_f32_16x16x32_bf16 v[20:23], v[172:175], v[96:99], v[20:23]\n"
      "v_mfma_f32_16x16x32_bf16 v[20:23], v[192:195], v[100:103], v[20:23]\n"
      "s_waitcnt lgkmcnt(2)\n"
      "v_cmp_gt_u32 vcc, 21, v4\n"
      "v_mfma_f32_16x16x32_bf16 v[20:23], v[212:215], v[104:107], v[20:23]\n"
      "ds_write_b128 v8, v[16:19] offset:23552\n"
      "v_mfma_f32_16x16x32_bf16 v[20:23], v[232:235], v[108:111], v[20:23]\n"
      "s_cbranch_vccnz .Lp_71\n"
      ".Lp_72:\n"
      "v_mfma_f32_16x16x32_bf16 v[20:23], v[252:255], v[112:115], v[20:23]\n"
      "s_waitcnt vmcnt(12)\n"
      "s_waitcnt lgkmcnt(2)\n"
      "v_mfma_f32_16x16x32_bf16 v[24:27], v[116:119], v[32:35], v[24:27]\n"
      "ds_read_b32 v4, v15\n"
      "ds_read_b128 v[28:31], v9 offset:21504\n"
      "v_mfma_f32_16x16x32_bf16 v[24:27], v[136:139], v[36:39], v[24:27]\n"
      "ds_add_u32 v2, v1 offset:4192\n"
      "v_mfma_f32_16x16x32_bf16 v[24:27], v[156:159], v[40:43], v[24:27]\n"
      "s_waitcnt vmcnt(7)\n"
      "v_mfma_f32_16x16x32_bf16 v[24:27], v[176:179], v[44:47], v[24:27]\n"
      "s_waitcnt lgkmcnt(2)\n"
      "v_cmp_gt_u32 vcc, 22, v4\n"
      "v_mfma_f32_16x16x32_bf16 v[24:27], v[196:199], v[48:51], v[24:27]\n"
      "ds_write_b128 v8, v[20:23] offset:24576\n"
      "v_mfma_f32_16x16x32_bf16 v[24:27], v[216:219], v[52:55], v[24:27]\n"
      "s_cbranch_vccnz .Lp_73\n"
      ".Lp_74:\n"
      "v_mfma_f32_16x16x32_bf16 v[24:27], v[236:239], v[56:59], v[24:27]\n"
      "s_waitcnt lgkmcnt(2)\n"
      "v_mfma_f32_16x16x32_bf16 v[28:31], v[120:123], v[32:35], v[28:31]\n"
      "ds_read_b32 v4, v15\n"
      "ds_read_b128 v[16:19], v9 offset:22528\n"
      "v_mfma_f32_16x16x32_bf16 v[28:31], v[140:143], v[36:39], v[28:31]\n"
      "ds_add_u32 v2, v1 offset:4192\n"
      "v_mfma_f32_16x16x32_bf16 v[28:31], v[160:163], v[40:43], v[28:31]\n"
      "v_mfma_f32_16x16x32_bf16 v[28:31], v[180:183], v[44:47], v[28:31]\n"
      "s_waitcnt lgkmcnt(2)\n"
      "v_cmp_gt_u32 vcc, 23, v4\n"
      "v_mfma_f32_16x16x32_bf16 v[28:31], v[200:203], v[48:51], v[28:31]\n"
      "ds_write_b128 v8, v[24:27] offset:40960\n"
      "v_mfma_f32_16x16x32_bf16 v[28:31], v[220:223], v[52:55], v[28:31]\n"
      "s_cbranch_vccnz .Lp_75\n"
      ".Lp_76:\n"
      "v_mfma_f32_16x16x32_bf16 v[28:31], v[240:243], v[56:59], v[28:31]\n"
      "s_waitcnt lgkmcnt(2)\n"
      "v_mfma_f32_16x16x32_bf16 v[16:19], v[124:127], v[32:35], v[16:19]\n"
      "ds_read_b32 v4, v15\n"
      "ds_read_b128 v[20:23], v9 offset:23552\n"
      "v_mfma_f32_16x16x32_bf16 v[16:19], v[144:147], v[36:39], v[16:19]\n"
      "ds_add_u32 v2, v1 offset:4192\n"
      "v_mfma_f32_16x16x32_bf16 v[16:19], v[164:167], v[40:43], v[16:19]\n"
      "v_mfma_f32_16x16x32_bf16 v[16:19], v[184:187], v[44:47], v[16:19]\n"
      "s_waitcnt lgkmcnt(2)\n"
      "v_cmp_gt_u32 vcc, 24, v4\n"
      "v_mfma_f32_16x16x32_bf16 v[16:19], v[204:207], v[48:51], v[16:19]\n"
      "ds_write_b128 v8, v[28:31] offset:41984\n"
      "v_mfma_f32_16x16x32_bf16 v[16:19], v[224:227], v[52:55], v[16:19]\n"
      "s_cbranch_vccnz .Lp_77\n"
      ".Lp_78:\n"
      "v_mfma_f32_16x16x32_bf16 v[16:19], v[244:247], v[56:59], v[16:19]\n"
      "s_waitcnt lgkmcnt(2)\n"
      "v_mfma_f32_16x16x32_bf16 v[20:23], v[128:131], v[32:35], v[20:23]\n"
      "ds_read_b32 v4, v15\n"
      "ds_read_b128 v[24:27], v9 offset:24576\n"
      "v_mfma_f32_16x16x32_bf16 v[20:23], v[148:151], v[36:39], v[20:23]\n"
      "ds_add_u32 v2, v1 offset:4192\n"
      "v_mfma_f32_16x16x32_bf16 v[20:23], v[168:171], v[40:43], v[20:23]\n"
      "v_mfma_f32_16x16x32_bf16 v[20:23], v[188:191], v[44:47], v[20:23]\n"
      "s_waitcnt lgkmcnt(2)\n"
      "v_cmp_gt_u32 vcc, 25, v4\n"
      "v_mfma_f32_16x16x32_bf16 v[20:23], v[208:211], v[48:51], v[20:23]\n"
      "ds_write_b128 v8, v[16:19] offset:43008\n"
      "v_mfma_f32_16x16x32_bf16 v[20:23], v[228:231], v[52:55], v[20:23]\n"
      "s_cbranch_vccnz .Lp_79\n"
      ".Lp_80:\n"
      "v_mfma_f32_16x16x32_bf16 v[20:23], v[248:251], v[56:59], v[20:23]\n"
      "s_waitcnt lgkmcnt(2)\n"
      "v_mfma_f32_16x16x32_bf16 v[24:27], v[132:135], v[32:35], v[24:27]\n"
      "ds_read_b32 v4, v15\n"
      "ds_read_b128 v[28:31], v9 offset:40960\n"
      "v_mfma_f32_16x16x32_bf16 v[24:27], v[152:155], v[36:39], v[24:27]\n"
      "ds_add_u32 v2, v1 offset:4192\n"
      "v_mfma_f32_16x16x32_bf16 v[24:27], v[172:175], v[40:43], v[24:27]\n"
      "v_mfma_f32_16x16x32_bf16 v[24:27], v[192:195], v[44:47], v[24:27]\n"
      "s_waitcnt lgkmcnt(2)\n"
      "v_cmp_gt_u32 vcc, 26, v4\n"
      "v_mfma_f32_16x16x32_bf16 v[24:27], v[212:215], v[48:51], v[24:27]\n"
      "ds_write_b128 v8, v[20:23] offset:44032\n"
      "v_mfma_f32_16x16x32_bf16 v[24:27], v[232:235], v[52:55], v[24:27]\n"
      "s_cbranch_vccnz .Lp_81\n"
      ".Lp_82:\n"
      "v_mfma_f32_16x16x32_bf16 v[24:27], v[252:255], v[56:59], v[24:27]\n"
      "s_waitcnt vmcnt(4)\n"
      "s_waitcnt lgkmcnt(2)\n"
      "v_mfma_f32_16x16x32_bf16 v[28:31], v[116:119], v[60:63], v[28:31]\n"
      "ds_read_b32 v4, v15\n"
      "ds_read_b128 v[16:19], v9 offset:41984\n"
      "v_mfma_f32_16x16x32_bf16 v[28:31], v[136:139], v[64:67], v[28:31]\n"
      "ds_add_u32 v2, v1 offset:4192\n"
      "v_mfma_f32_16x16x32_bf16 v[28:31], v[156:159], v[68:71], v[28:31]\n"
      "s_waitcnt vmcnt(0)\n"
      "v_mfma_f32_16x16x32_bf16 v[28:31], v[176:179], v[72:75], v[28:31]\n"
      "s_waitcnt lgkmcnt(2)\n"
      "v_cmp_gt_u32 vcc, 27, v4\n"
      "v_mfma_f32_16x16x32_bf16 v[28:31], v[196:199], v[76:79], v[28:31]\n"
      "ds_write_b128 v8, v[24:27] offset:45056\n"
      "v_mfma_f32_16x16x32_bf16 v[28:31], v[216:219], v[80:83], v[28:31]\n"
      "s_cbranch_vccnz .Lp_83\n"
      ".Lp_84:\n"
      "v_mfma_f32_16x16x32_bf16 v[28:31], v[236:239], v[84:87], v[28:31]\n"
      "s_waitcnt lgkmcnt(2)\n"
      "v_mfma_f32_16x16x32_bf16 v[16:19], v[120:123], v[60:63], v[16:19]\n"
      "ds_read_b32 v4, v15\n"
      "ds_read_b128 v[20:23], v9 offset:43008\n"
      "v_mfma_f32_16x16x32_bf16 v[16:19], v[140:143], v[64:67], v[16:19]\n"
      "ds_add_u32 v2, v1 offset:4192\n"
      "v_mfma_f32_16x16x32_bf16 v[16:19], v[160:163], v[68:71], v[16:19]\n"
      "v_mfma_f32_16x16x32_bf16 v[16:19], v[180:183], v[72:75], v[16:19]\n"
      "s_waitcnt lgkmcnt(2)\n"
      "v_cmp_gt_u32 vcc, 28, v4\n"
      "v_mfma_f32_16x16x32_bf16 v[16:19], v[200:203], v[76:79], v[16:19]\n"
      "v_add_u32 v8, 61440, v8\n"
      "ds_write_b128 v8, v[28:31] offset:0\n"
      "v_mfma_f32_16x16x32_bf16 v[16:19], v[220:223], v[80:83], v[16:19]\n"
      "s_cbranch_vccnz .Lp_85\n"
      ".Lp_86:\n"
      "v_mfma_f32_16x16x32_bf16 v[16:19], v[240:243], v[84:87], v[16:19]\n"
      "s_waitcnt lgkmcnt(2)\n"
      "v_mfma_f32_16x16x32_bf16 v[20:23], v[124:127], v[60:63], v[20:23]\n"
      "ds_read_b32 v4, v15\n"
      "ds_read_b128 v[24:27], v9 offset:44032\n"
      "v_mfma_f32_16x16x32_bf16 v[20:23], v[144:147], v[64:67], v[20:23]\n"
      "ds_add_u32 v2, v1 offset:4192\n"
      "v_mfma_f32_16x16x32_bf16 v[20:23], v[164:167], v[68:71], v[20:23]\n"
      "v_mfma_f32_16x16x32_bf16 v[20:23], v[184:187], v[72:75], v[20:23]\n"
      "s_waitcnt lgkmcnt(2)\n"
      "v_cmp_gt_u32 vcc, 29, v4\n"
      "v_mfma_f32_16x16x32_bf16 v[20:23], v[204:207], v[76:79], v[20:23]\n"
      "ds_write_b128 v8, v[16:19] offset:1024\n"
      "v_mfma_f32_16x16x32_bf16 v[20:23], v[224:227], v[80:83], v[20:23]\n"
      "s_cbranch_vccnz .Lp_87\n"
      ".Lp_88:\n"
      "v_mfma_f32_16x16x32_bf16 v[20:23], v[244:247], v[84:87], v[20:23]\n"
      "s_waitcnt lgkmcnt(2)\n"
      "v_mfma_f32_16x16x32_bf16 v[24:27], v[128:131], v[60:63], v[24:27]\n"
      "ds_read_b32 v4, v15\n"
      "ds_read_b128 v[28:31], v9 offset:45056\n"
      "v_mfma_f32_16x16x32_bf16 v[24:27], v[148:151], v[64:67], v[24:27]\n"
      "ds_add_u32 v2, v1 offset:4192\n"
      "v_mfma_f32_16x16x32_bf16 v[24:27], v[168:171], v[68:71], v[24:27]\n"
      "v_mfma_f32_16x16x32_bf16 v[24:27], v[188:191], v[72:75], v[24:27]\n"
      "s_waitcnt lgkmcnt(2)\n"
      "v_cmp_gt_u32 vcc, 30, v4\n"
      "v_mfma_f32_16x16x32_bf16 v[24:27], v[208:211], v[76:79], v[24:27]\n"
      "ds_write_b128 v8, v[20:23] offset:2048\n"
      "v_mfma_f32_16x16x32_bf16 v[24:27], v[228:231], v[80:83], v[24:27]\n"
      "s_cbranch_vccnz .Lp_89\n"
      ".Lp_90:\n"
      "v_mfma_f32_16x16x32_bf16 v[24:27], v[248:251], v[84:87], v[24:27]\n"
      "s_waitcnt lgkmcnt(2)\n"
      "v_mfma_f32_16x16x32_bf16 v[28:31], v[132:135], v[60:63], v[28:31]\n"
      "v_mfma_f32_16x16x32_bf16 v[28:31], v[152:155], v[64:67], v[28:31]\n"
      "ds_add_u32 v2, v1 offset:4192\n"
      "v_mfma_f32_16x16x32_bf16 v[28:31], v[172:175], v[68:71], v[28:31]\n"
      "v_mfma_f32_16x16x32_bf16 v[28:31], v[192:195], v[72:75], v[28:31]\n"
      "v_mfma_f32_16x16x32_bf16 v[28:31], v[212:215], v[76:79], v[28:31]\n"
      "s_nop 0\n"
      "ds_write_b128 v8, v[24:27] offset:3072\n"
      "v_mfma_f32_16x16x32_bf16 v[28:31], v[232:235], v[80:83], v[28:31]\n"
      "v_mfma_f32_16x16x32_bf16 v[28:31], v[252:255], v[84:87], v[28:31]\n"
      "ds_add_u32 v2, v1 offset:4192\n"
      "s_nop 6\n"
      "ds_write_b128 v8, v[28:31] offset:4096\n"
      "ds_add_u32 v2, v1 offset:4192\n"
      "s_endpgm\n"
      ".LWARM:\n"
      ".Lp_25:\n"
      "s_cbranch_vccz .Lp_27\n"
      ".Lp_91:\n"
      "ds_read_b32 v4, v15\n"
      "ds_read_b128 v[20:23], v9 offset:0\n"
      "s_sub_u32 s73, s73, 1\n"
      "s_cselect_b32 s73, 0, s73\n"
      "s_waitcnt lgkmcnt(0)\n"
      "v_cmp_gt_u32 vcc, 1, v4\n"
      "s_cselect_b64 vcc, 0, vcc\n"
      "s_cbranch_vccnz .Lp_91\n"
      "s_branch .Lp_26\n"
      ".Lp_27:\n"
      "s_cbranch_vccz .Lp_29\n"
      ".Lp_92:\n"
      "ds_read_b32 v4, v15\n"
      "ds_read_b128 v[24:27], v9 offset:1024\n"
      "s_sub_u32 s73, s73, 1\n"
      "s_cselect_b32 s73, 0, s73\n"
      "s_waitcnt lgkmcnt(0)\n"
      "v_cmp_gt_u32 vcc, 2, v4\n"
      "s_cselect_b64 vcc, 0, vcc\n"
      "s_cbranch_vccnz .Lp_92\n"
      "s_branch .Lp_28\n"
      ".Lp_29:\n"
      "s_cbranch_vccz .Lp_31\n"
      ".Lp_93:\n"
      "ds_read_b32 v4, v15\n"
      "ds_read_b128 v[28:31], v9 offset:2048\n"
      "s_sub_u32 s73, s73, 1\n"
      "s_cselect_b32 s73, 0, s73\n"
      "s_waitcnt lgkmcnt(0)\n"
      "v_cmp_gt_u32 vcc, 3, v4\n"
      "s_cselect_b64 vcc, 0, vcc\n"
      "s_cbranch_vccnz .Lp_93\n"
      "s_branch .Lp_30\n"
      ".Lp_31:\n"
      "s_cbranch_vccz .Lp_33\n"
      ".Lp_94:\n"
      "ds_read_b32 v4, v15\n"
      "ds_read_b128 v[16:19], v9 offset:3072\n"
      "s_sub_u32 s73, s73, 1\n"
      "s_cselect_b32 s73, 0, s73\n"
      "s_waitcnt lgkmcnt(0)\n"
      "v_cmp_gt_u32 vcc, 4, v4\n"
      "s_cselect_b64 vcc, 0, vcc\n"
      "s_cbranch_vccnz .Lp_94\n"
      "s_branch .Lp_32\n"
      ".Lp_33:\n"
      "s_cbranch_vccz .Lp_38\n"
      ".Lp_95:\n"
      "ds_read_b32 v4, v15\n"
      "ds_read_b128 v[20:23], v9 offset:4096\n"
      "s_sub_u32 s73, s73, 1\n"
      "s_cselect_b32 s73, 0, s73\n"
      "s_waitcnt lgkmcnt(0)\n"
      "v_cmp_gt_u32 vcc, 5, v4\n"
      "s_cselect_b64 vcc, 0, vcc\n"
      "s_cbranch_vccnz .Lp_95\n"
      "s_branch .Lp_34\n"
      ".Lp_38:\n"
      "s_cbranch_vccz .Lp_40\n"
      ".Lp_96:\n"
      "ds_read_b32 v4, v15\n"
      "ds_read_b128 v[24:27], v9 offset:20480\n"
      "s_sub_u32 s73, s73, 1\n"
      "s_cselect_b32 s73, 0, s73\n"
      "s_waitcnt lgkmcnt(0)\n"
      "v_cmp_gt_u32 vcc, 6, v4\n"
      "s_cselect_b64 vcc, 0, vcc\n"
      "s_cbranch_vccnz .Lp_96\n"
      "s_branch .Lp_39\n"
      ".Lp_40:\n"
      "s_cbranch_vccz .Lp_42\n"
      ".Lp_97:\n"
      "ds_read_b32 v4, v15\n"
      "ds_read_b128 v[28:31], v9 offset:21504\n"
      "s_sub_u32 s73, s73, 1\n"
      "s_cselect_b32 s73, 0, s73\n"
      "s_waitcnt lgkmcnt(0)\n"
      "v_cmp_gt_u32 vcc, 7, v4\n"
      "s_cselect_b64 vcc, 0, vcc\n"
      "s_cbranch_vccnz .Lp_97\n"
      "s_branch .Lp_41\n"
      ".Lp_42:\n"
      "s_cbranch_vccz .Lp_44\n"
      ".Lp_98:\n"
      "ds_read_b32 v4, v15\n"
      "ds_read_b128 v[16:19], v9 offset:22528\n"
      "s_sub_u32 s73, s73, 1\n"
      "s_cselect_b32 s73, 0, s73\n"
      "s_waitcnt lgkmcnt(0)\n"
      "v_cmp_gt_u32 vcc, 8, v4\n"
      "s_cselect_b64 vcc, 0, vcc\n"
      "s_cbranch_vccnz .Lp_98\n"
      "s_branch .Lp_43\n"
      ".Lp_44:\n"
      "s_cbranch_vccz .Lp_46\n"
      ".Lp_99:\n"
      "ds_read_b32 v4, v15\n"
      "ds_read_b128 v[20:23], v9 offset:23552\n"
      "s_sub_u32 s73, s73, 1\n"
      "s_cselect_b32 s73, 0, s73\n"
      "s_waitcnt lgkmcnt(0)\n"
      "v_cmp_gt_u32 vcc, 9, v4\n"
      "s_cselect_b64 vcc, 0, vcc\n"
      "s_cbranch_vccnz .Lp_99\n"
      "s_branch .Lp_45\n"
      ".Lp_46:\n"
      "s_cbranch_vccz .Lp_48\n"
      ".Lp_100:\n"
      "ds_read_b32 v4, v15\n"
      "ds_read_b128 v[24:27], v9 offset:24576\n"
      "s_sub_u32 s73, s73, 1\n"
      "s_cselect_b32 s73, 0, s73\n"
      "s_waitcnt lgkmcnt(0)\n"
      "v_cmp_gt_u32 vcc, 10, v4\n"
      "s_cselect_b64 vcc, 0, vcc\n"
      "s_cbranch_vccnz .Lp_100\n"
      "s_branch .Lp_47\n"
      ".Lp_48:\n"
      "s_cbranch_vccz .Lp_50\n"
      ".Lp_101:\n"
      "ds_read_b32 v4, v15\n"
      "ds_read_b128 v[28:31], v9 offset:57344\n"
      "s_sub_u32 s73, s73, 1\n"
      "s_cselect_b32 s73, 0, s73\n"
      "s_waitcnt lgkmcnt(0)\n"
      "v_cmp_gt_u32 vcc, 11, v4\n"
      "s_cselect_b64 vcc, 0, vcc\n"
      "s_cbranch_vccnz .Lp_101\n"
      "s_branch .Lp_49\n"
      ".Lp_50:\n"
      "s_cbranch_vccz .Lp_52\n"
      ".Lp_102:\n"
      "ds_read_b32 v4, v15\n"
      "ds_read_b128 v[16:19], v9 offset:58368\n"
      "s_sub_u32 s73, s73, 1\n"
      "s_cselect_b32 s73, 0, s73\n"
      "s_waitcnt lgkmcnt(0)\n"
      "v_cmp_gt_u32 vcc, 12, v4\n"
      "s_cselect_b64 vcc, 0, vcc\n"
      "s_cbranch_vccnz .Lp_102\n"
      "s_branch .Lp_51\n"
      ".Lp_52:\n"
      "s_cbranch_vccz .Lp_54\n"
      ".Lp_103:\n"
      "ds_read_b32 v4, v15\n"
      "ds_read_b128 v[20:23], v9 offset:59392\n"
      "s_sub_u32 s73, s73, 1\n"
      "s_cselect_b32 s73, 0, s73\n"
      "s_waitcnt lgkmcnt(0)\n"
      "v_cmp_gt_u32 vcc, 13, v4\n"
      "s_cselect_b64 vcc, 0, vcc\n"
      "s_cbranch_vccnz .Lp_103\n"
      "s_branch .Lp_53\n"
      ".Lp_54:\n"
      "s_cbranch_vccz .Lp_56\n"
      ".Lp_104:\n"
      "ds_read_b32 v4, v15\n"
      "ds_read_b128 v[24:27], v9 offset:60416\n"
      "s_sub_u32 s73, s73, 1\n"
      "s_cselect_b32 s73, 0, s73\n"
      "s_waitcnt lgkmcnt(0)\n"
      "v_cmp_gt_u32 vcc, 14, v4\n"
      "s_cselect_b64 vcc, 0, vcc\n"
      "s_cbranch_vccnz .Lp_104\n"
      "s_branch .Lp_55\n"
      ".Lp_56:\n"
      "s_cbranch_vccz .Lp_61\n"
      ".Lp_105:\n"
      "ds_read_b32 v4, v15\n"
      "ds_read_b128 v[28:31], v9 offset:61440\n"
      "s_sub_u32 s73, s73, 1\n"
      "s_cselect_b32 s73, 0, s73\n"
      "s_waitcnt lgkmcnt(0)\n"
      "v_cmp_gt_u32 vcc, 15, v4\n"
      "s_cselect_b64 vcc, 0, vcc\n"
      "s_cbranch_vccnz .Lp_105\n"
      "s_branch .Lp_57\n"
      ".Lp_61:\n"
      "s_cbranch_vccz .Lp_63\n"
      ".Lp_106:\n"
      "ds_read_b32 v4, v15\n"
      "ds_read_b128 v[20:23], v9 offset:0\n"
      "s_sub_u32 s73, s73, 1\n"
      "s_cselect_b32 s73, 0, s73\n"
      "s_waitcnt lgkmcnt(0)\n"
      "v_cmp_gt_u32 vcc, 16, v4\n"
      "s_cselect_b64 vcc, 0, vcc\n"
      "s_cbranch_vccnz .Lp_106\n"
      "s_branch .Lp_62\n"
      ".Lp_63:\n"
      "s_cbranch_vccz .Lp_65\n"
      ".Lp_107:\n"
      "ds_read_b32 v4, v15\n"
      "ds_read_b128 v[24:27], v9 offset:1024\n"
      "s_sub_u32 s73, s73, 1\n"
      "s_cselect_b32 s73, 0, s73\n"
      "s_waitcnt lgkmcnt(0)\n"
      "v_cmp_gt_u32 vcc, 17, v4\n"
      "s_cselect_b64 vcc, 0, vcc\n"
      "s_cbranch_vccnz .Lp_107\n"
      "s_branch .Lp_64\n"
      ".Lp_65:\n"
      "s_cbranch_vccz .Lp_67\n"
      ".Lp_108:\n"
      "ds_read_b32 v4, v15\n"
      "ds_read_b128 v[28:31], v9 offset:2048\n"
      "s_sub_u32 s73, s73, 1\n"
      "s_cselect_b32 s73, 0, s73\n"
      "s_waitcnt lgkmcnt(0)\n"
      "v_cmp_gt_u32 vcc, 18, v4\n"
      "s_cselect_b64 vcc, 0, vcc\n"
      "s_cbranch_vccnz .Lp_108\n"
      "s_branch .Lp_66\n"
      ".Lp_67:\n"
      "s_cbranch_vccz .Lp_69\n"
      ".Lp_109:\n"
      "ds_read_b32 v4, v15\n"
      "ds_read_b128 v[16:19], v9 offset:3072\n"
      "s_sub_u32 s73, s73, 1\n"
      "s_cselect_b32 s73, 0, s73\n"
      "s_waitcnt lgkmcnt(0)\n"
      "v_cmp_gt_u32 vcc, 19, v4\n"
      "s_cselect_b64 vcc, 0, vcc\n"
      "s_cbranch_vccnz .Lp_109\n"
      "s_branch .Lp_68\n"
      ".Lp_69:\n"
      "s_cbranch_vccz .Lp_71\n"
      ".Lp_110:\n"
      "ds_read_b32 v4, v15\n"
      "ds_read_b128 v[20:23], v9 offset:4096\n"
      "s_sub_u32 s73, s73, 1\n"
      "s_cselect_b32 s73, 0, s73\n"
      "s_waitcnt lgkmcnt(0)\n"
      "v_cmp_gt_u32 vcc, 20, v4\n"
      "s_cselect_b64 vcc, 0, vcc\n"
      "s_cbranch_vccnz .Lp_110\n"
      "s_branch .Lp_70\n"
      ".Lp_71:\n"
      "s_cbranch_vccz .Lp_73\n"
      ".Lp_111:\n"
      "ds_read_b32 v4, v15\n"
      "ds_read_b128 v[24:27], v9 offset:20480\n"
      "s_sub_u32 s73, s73, 1\n"
      "s_cselect_b32 s73, 0, s73\n"
      "s_waitcnt lgkmcnt(0)\n"
      "v_cmp_gt_u32 vcc, 21, v4\n"
      "s_cselect_b64 vcc, 0, vcc\n"
      "s_cbranch_vccnz .Lp_111\n"
      "s_branch .Lp_72\n"
      ".Lp_73:\n"
      "s_cbranch_vccz .Lp_75\n"
      ".Lp_112:\n"
      "ds_read_b32 v4, v15\n"
      "ds_read_b128 v[28:31], v9 offset:21504\n"
      "s_sub_u32 s73, s73, 1\n"
      "s_cselect_b32 s73, 0, s73\n"
      "s_waitcnt lgkmcnt(0)\n"
      "v_cmp_gt_u32 vcc, 22, v4\n"
      "s_cselect_b64 vcc, 0, vcc\n"
      "s_cbranch_vccnz .Lp_112\n"
      "s_branch .Lp_74\n"
      ".Lp_75:\n"
      "s_cbranch_vccz .Lp_77\n"
      ".Lp_113:\n"
      "ds_read_b32 v4, v15\n"
      "ds_read_b128 v[16:19], v9 offset:22528\n"
      "s_sub_u32 s73, s73, 1\n"
      "s_cselect_b32 s73, 0, s73\n"
      "s_waitcnt lgkmcnt(0)\n"
      "v_cmp_gt_u32 vcc, 23, v4\n"
      "s_cselect_b64 vcc, 0, vcc\n"
      "s_cbranch_vccnz .Lp_113\n"
      "s_branch .Lp_76\n"
      ".Lp_77:\n"
      "s_cbranch_vccz .Lp_79\n"
      ".Lp_114:\n"
      "ds_read_b32 v4, v15\n"
      "ds_read_b128 v[20:23], v9 offset:23552\n"
      "s_sub_u32 s73, s73, 1\n"
      "s_cselect_b32 s73, 0, s73\n"
      "s_waitcnt lgkmcnt(0)\n"
      "v_cmp_gt_u32 vcc, 24, v4\n"
      "s_cselect_b64 vcc, 0, vcc\n"
      "s_cbranch_vccnz .Lp_114\n"
      "s_branch .Lp_78\n"
      ".Lp_79:\n"
      "s_cbranch_vccz .Lp_81\n"
      ".Lp_115:\n"
      "ds_read_b32 v4, v15\n"
      "ds_read_b128 v[24:27], v9 offset:24576\n"
      "s_sub_u32 s73, s73, 1\n"
      "s_cselect_b32 s73, 0, s73\n"
      "s_waitcnt lgkmcnt(0)\n"
      "v_cmp_gt_u32 vcc, 25, v4\n"
      "s_cselect_b64 vcc, 0, vcc\n"
      "s_cbranch_vccnz .Lp_115\n"
      "s_branch .Lp_80\n"
      ".Lp_81:\n"
      "s_cbranch_vccz .Lp_83\n"
      ".Lp_116:\n"
      "ds_read_b32 v4, v15\n"
      "ds_read_b128 v[28:31], v9 offset:40960\n"
      "s_sub_u32 s73, s73, 1\n"
      "s_cselect_b32 s73, 0, s73\n"
      "s_waitcnt lgkmcnt(0)\n"
      "v_cmp_gt_u32 vcc, 26, v4\n"
      "s_cselect_b64 vcc, 0, vcc\n"
      "s_cbranch_vccnz .Lp_116\n"
      "s_branch .Lp_82\n"
      ".Lp_83:\n"
      "s_cbranch_vccz .Lp_85\n"
      ".Lp_117:\n"
      "ds_read_b32 v4, v15\n"
      "ds_read_b128 v[16:19], v9 offset:41984\n"
      "s_sub_u32 s73, s73, 1\n"
      "s_cselect_b32 s73, 0, s73\n"
      "s_waitcnt lgkmcnt(0)\n"
      "v_cmp_gt_u32 vcc, 27, v4\n"
      "s_cselect_b64 vcc, 0, vcc\n"
      "s_cbranch_vccnz .Lp_117\n"
      "s_branch .Lp_84\n"
      ".Lp_85:\n"
      "s_cbranch_vccz .Lp_87\n"
      ".Lp_118:\n"
      "ds_read_b32 v4, v15\n"
      "ds_read_b128 v[20:23], v9 offset:43008\n"
      "s_sub_u32 s73, s73, 1\n"
      "s_cselect_b32 s73, 0, s73\n"
      "s_waitcnt lgkmcnt(0)\n"
      "v_cmp_gt_u32 vcc, 28, v4\n"
      "s_cselect_b64 vcc, 0, vcc\n"
      "s_cbranch_vccnz .Lp_118\n"
      "s_branch .Lp_86\n"
      ".Lp_87:\n"
      "s_cbranch_vccz .Lp_89\n"
      ".Lp_119:\n"
      "ds_read_b32 v4, v15\n"
      "ds_read_b128 v[24:27], v9 offset:44032\n"
      "s_sub_u32 s73, s73, 1\n"
      "s_cselect_b32 s73, 0, s73\n"
      "s_waitcnt lgkmcnt(0)\n"
      "v_cmp_gt_u32 vcc, 29, v4\n"
      "s_cselect_b64 vcc, 0, vcc\n"
      "s_cbranch_vccnz .Lp_119\n"
      "s_branch .Lp_88\n"
      ".Lp_89:\n"
      "s_cbranch_vccz .LWARME\n"
      ".Lp_120:\n"
      "ds_read_b32 v4, v15\n"
      "ds_read_b128 v[28:31], v9 offset:45056\n"
      "s_sub_u32 s73, s73, 1\n"
      "s_cselect_b32 s73, 0, s73\n"
      "s_waitcnt lgkmcnt(0)\n"
      "v_cmp_gt_u32 vcc, 30, v4\n"
      "s_cselect_b64 vcc, 0, vcc\n"
      "s_cbranch_vccnz .Lp_120\n"
      "s_branch .Lp_90\n"
      ".LM:\n"
      "s_cmp_lt_u32 s61, 28\n"
      "s_cselect_b32 s62, s57, 14\n"
      "s_cselect_b32 s63, 0, s54\n"
      "s_add_u32 s62, s62, s63\n"
      "s_lshl4_add_u32 s62, s56, s62\n"
      "s_lshl2_add_u32 s62, s62, s53\n"
      "s_mul_i32 s63, s62, 2560\n"
      "s_lshl_b32 s69, s62, 4\n"
      "v_lshrrev_b32 v5, 5, v1\n"
      "v_and_b32 v6, 31, v1\n"
      "v_mul_u32_u24 v7, 1280, v5\n"
      "v_lshl_add_u32 v18, v6, 4, v7\n"
      "v_lshl_add_u32 v19, v6, 3, v7\n"
      "v_lshlrev_b32 v11, 2, v1\n"
      "s_mul_i32 s64, s55, 160\n"
      "s_lshl2_add_u32 s65, s57, s53\n"
      "s_lshl_b32 s65, s65, 6\n"
      "v_lshlrev_b32 v12, 1, v6\n"
      "s_and_b32 s66, s2, 7\n"
      "s_mul_i32 s66, s66, 28\n"
      "s_add_u32 s66, s66, s61\n"
      "s_mul_i32 s67, s66, 205\n"
      "s_lshr_b32 s67, s67, 11\n"
      "s_mul_i32 s68, s67, 10\n"
      "s_sub_u32 s68, s66, s68\n"
      "s_lshl2_add_u32 s66, s68, s53\n"
      "s_lshl_b32 s70, s66, 7\n"
      "v_lshrrev_b32 v13, 3, v1\n"
      "v_lshlrev_b32 v14, 4, v13\n"
      "s_mov_b32 m0, 162816\n"
      "s_waitcnt lgkmcnt(0)\n"
      "buffer_load_dwordx4 v[96:99], v18, s[4:7], s63 offen\n"
      "buffer_load_dwordx4 v[100:103], v18, s[4:7], s63 offen offset:512\n"
      "buffer_load_dwordx2 v[104:105], v19, s[4:7], s63 offen offset:1024\n"
      "buffer_load_dwordx2 v[42:43], off, s[20:23], 0 sc1\n"
      "buffer_load_dword v11, s[44:47], s64 offen lds\n"
      "buffer_load_dwordx4 v[44:47], v14, s[24:27], s70 offen\n"
      "buffer_load_ushort v9, v12, s[28:31], s65 offen\n"
      "s_barrier\n"
      "s_setprio 3\n"
      "s_lshl_b32 s62, s53, 5\n"
      "s_add_u32 s62, s62, 140160\n"
      "v_and_b32 v53, 7, v1\n"
      "v_lshrrev_b32 v54, 3, v1\n"
      "v_mul_u32_u24 v78, 1040, v53\n"
      "v_lshl_add_u32 v78, v54, 2, v78\n"
      "v_add_u32 v78, s62, v78\n"
      "v_add_u32 v79, 8720, v78\n"
      "v_mul_u32_u24 v8, 8448, v54\n"
      "v_lshl_add_u32 v8, v53, 4, v8\n"
      "s_mul_i32 s84, s66, 67584\n"
      "s_lshl_b32 s63, s67, 7\n"
      "s_add_u32 s84, s84, s63\n"
      "s_add_u32 s84, s84, 32768\n"
      "s_add_u32 s85, s84, 4224\n"
      "v_lshrrev_b32 v14, 5, v1\n"
      "v_bfe_u32 v15, v1, 2, 2\n"
      "v_lshl_add_u32 v16, v14, 3, v15\n"
      "v_lshlrev_b32 v16, 8, v16\n"
      "v_bfe_u32 v17, v1, 4, 1\n"
      "v_mul_u32_u24 v29, 57344, v17\n"
      "s_lshl_b32 s64, s53, 1\n"
      "v_or_b32 v17, s64, v17\n"
      "v_lshlrev_b32 v15, 1, v15\n"
      "v_xor_b32 v17, v17, v15\n"
      "v_lshl_add_u32 v16, v17, 5, v16\n"
      "v_and_b32 v17, 3, v1\n"
      "v_lshl_add_u32 v64, v17, 3, v16\n"
      "v_add_u32 v65, 61440, v64\n"
      "v_and_b32 v15, 0x2f, v1\n"
      "v_lshl_add_u32 v29, v15, 4, v29\n"
      "v_and_b32 v15, 31, v1\n"
      "v_bfe_u32 v16, v1, 1, 3\n"
      "v_xor_b32 v16, v16, v14\n"
      "v_lshlrev_b32 v16, 4, v16\n"
      "v_lshl_add_u32 v4, v15, 7, v16\n"
      "v_xor_b32 v5, 32, v4\n"
      "v_xor_b32 v6, 64, v4\n"
      "v_xor_b32 v7, 96, v4\n"
      "v_add_u32 v66, 61440, v4\n"
      "v_add_u32 v67, 61440, v5\n"
      "v_add_u32 v68, 61440, v6\n"
      "v_add_u32 v69, 61440, v7\n"
      "v_lshlrev_b32 v30, 5, v15\n"
      "v_add_u32 v30, 157696, v30\n"
      "v_lshrrev_b32 v16, 4, v1\n"
      "v_xor_b32 v16, v16, v1\n"
      "v_and_b32 v16, 1, v16\n"
      "v_cmp_eq_u32 vcc, 0, v16\n"
      "v_mov_b32 v17, 0x3f803f80\n"
      "v_mov_b64 v[10:11], 0\n"
      "v_cndmask_b32 v16, 0, v17, vcc\n"
      "v_mov_b32 v17, v16\n"
      "v_mov_b32 v18, v16\n"
      "v_mov_b32 v19, v16\n"
      "v_mov_b64 v[14:15], 0\n"
      "s_lshl2_add_u32 s64, s54, s53\n"
      "s_mul_i32 s64, s64, 7\n"
      "s_add_u32 s64, s64, s55\n"
      "s_lshl_b32 s65, s64, 10\n"
      "s_mul_i32 s78, s56, 458752\n"
      "s_add_u32 s78, s78, s65\n"
      "s_add_u32 s78, s78, 7897088\n"
      "s_lshl2_add_u32 s65, s57, s53\n"
      "s_cmp_ge_u32 s65, 54\n"
      "s_cselect_b32 s78, 1073741824, s78\n"
      "s_lshl_b32 s64, s56, 6\n"
      "s_add_u32 s64, s64, s65\n"
      "s_lshl_b32 s64, s64, 8\n"
      "s_add_u32 s79, s64, 18382848\n"
      "s_xor_b32 s74, s99, 0\n"
      "s_mul_i32 s74, s74, 114688\n"
      "s_add_u32 s74, s74, s78\n"
      "s_xor_b32 s75, s99, 1\n"
      "s_mul_i32 s75, s75, 114688\n"
      "s_add_u32 s75, s75, s78\n"
      "s_xor_b32 s76, s99, 2\n"
      "s_mul_i32 s76, s76, 114688\n"
      "s_add_u32 s76, s76, s78\n"
      "s_xor_b32 s77, s99, 3\n"
      "s_mul_i32 s77, s77, 114688\n"
      "s_add_u32 s77, s77, s78\n"
      "s_mul_i32 s63, s56, 3\n"
      "s_add_u32 s63, s63, s57\n"
      "s_mul_i32 s64, s63, 205\n"
      "s_lshr_b32 s64, s64, 11\n"
      "s_mul_i32 s64, s64, 10\n"
      "s_sub_u32 s63, s63, s64\n"
      "s_sub_u32 s64, 3, s53\n"
      "s_lshr_b32 s65, s64, 1\n"
      "s_xor_b32 s65, s65, s99\n"
      "s_and_b32 s65, s65, 1\n"
      "s_mul_i32 s65, s65, 40960\n"
      "s_and_b32 s64, s64, 1\n"
      "s_lshl_b32 s64, s64, 11\n"
      "s_add_u32 s65, s65, s64\n"
      "s_mul_i32 s64, s55, 5\n"
      "s_lshr_b32 s71, s64, 2\n"
      "s_sub_u32 s71, s71, s63\n"
      "s_cselect_b32 s72, 10, 0\n"
      "s_add_u32 s71, s71, s72\n"
      "s_lshl_b32 s71, s71, 12\n"
      "s_add_u32 s71, s71, s65\n"
      "s_and_b32 s72, s64, 3\n"
      "s_lshl_b32 s72, s72, 5\n"
      "s_or_b32 s86, s71, s72\n"
      "s_add_u32 s64, s64, 1\n"
      "s_lshr_b32 s71, s64, 2\n"
      "s_sub_u32 s71, s71, s63\n"
      "s_cselect_b32 s72, 10, 0\n"
      "s_add_u32 s71, s71, s72\n"
      "s_lshl_b32 s71, s71, 12\n"
      "s_add_u32 s71, s71, s65\n"
      "s_and_b32 s72, s64, 3\n"
      "s_lshl_b32 s72, s72, 5\n"
      "s_or_b32 s87, s71, s72\n"
      "s_add_u32 s64, s64, 1\n"
      "s_lshr_b32 s71, s64, 2\n"
      "s_sub_u32 s71, s71, s63\n"
      "s_cselect_b32 s72, 10, 0\n"
      "s_add_u32 s71, s71, s72\n"
      "s_lshl_b32 s71, s71, 12\n"
      "s_add_u32 s71, s71, s65\n"
      "s_and_b32 s72, s64, 3\n"
      "s_lshl_b32 s72, s72, 5\n"
      "s_or_b32 s88, s71, s72\n"
      "s_add_u32 s64, s64, 1\n"
      "s_lshr_b32 s71, s64, 2\n"
      "s_sub_u32 s71, s71, s63\n"
      "s_cselect_b32 s72, 10, 0\n"
      "s_add_u32 s71, s71, s72\n"
      "s_lshl_b32 s71, s71, 12\n"
      "s_add_u32 s71, s71, s65\n"
      "s_and_b32 s72, s64, 3\n"
      "s_lshl_b32 s72, s72, 5\n"
      "s_or_b32 s89, s71, s72\n"
      "s_add_u32 s64, s64, 1\n"
      "s_lshr_b32 s71, s64, 2\n"
      "s_sub_u32 s71, s71, s63\n"
      "s_cselect_b32 s72, 10, 0\n"
      "s_add_u32 s71, s71, s72\n"
      "s_lshl_b32 s71, s71, 12\n"
      "s_add_u32 s71, s71, s65\n"
      "s_and_b32 s72, s64, 3\n"
      "s_lshl_b32 s72, s72, 5\n"
      "s_or_b32 s90, s71, s72\n"
      "s_add_u32 s64, s64, 1\n"
      "v_and_b32 v50, 15, v1\n"
      "v_bfe_u32 v51, v1, 1, 3\n"
      "v_lshrrev_b32 v52, 5, v1\n"
      "v_xor_b32 v51, v51, v52\n"
      "v_lshlrev_b32 v51, 4, v51\n"
      "v_lshl_add_u32 v51, v50, 7, v51\n"
      "v_bfe_u32 v52, v1, 4, 1\n"
      "v_lshl_add_u32 v28, v52, 3, v51\n"
      "s_mov_b32 s80, s92\n"
      "s_mov_b32 s81, s92\n"
      "s_mov_b32 s82, -1\n"
      "s_mov_b32 s83, 0\n"
      "s_waitcnt vmcnt(3)\n"
      "ds_swizzle_b32 v106, v96 offset:0\n"
      "v_lshlrev_b32 v116, 16, v96\n"
      "v_and_b32 v117, s92, v96\n"
      "v_lshlrev_b32 v118, 16, v97\n"
      "v_and_b32 v119, s92, v97\n"
      "v_lshlrev_b32 v120, 16, v98\n"
      "v_and_b32 v121, s92, v98\n"
      "v_lshlrev_b32 v122, 16, v99\n"
      "v_and_b32 v123, s92, v99\n"
      "v_lshlrev_b32 v124, 16, v100\n"
      "v_and_b32 v125, s92, v100\n"
      "v_lshlrev_b32 v126, 16, v101\n"
      "v_and_b32 v127, s92, v101\n"
      "v_lshlrev_b32 v128, 16, v102\n"
      "v_and_b32 v129, s92, v102\n"
      "v_lshlrev_b32 v130, 16, v103\n"
      "v_and_b32 v131, s92, v103\n"
      "v_lshlrev_b32 v132, 16, v104\n"
      "v_and_b32 v133, s92, v104\n"
      "v_lshlrev_b32 v134, 16, v105\n"
      "v_and_b32 v135, s92, v105\n"
      "v_readfirstlane_b32 s58, v42\n"
      "v_readfirstlane_b32 s59, v43\n"
      "s_waitcnt lgkmcnt(0)\n"
      "v_lshlrev_b32 v106, 16, v106\n"
      "v_mov_b32 v107, v106\n"
      "s_add_u32 s58, s58, 1\n"
      "s_addc_u32 s59, s59, 0\n"
      "v_pk_add_f32 v[116:117], v[116:117], v[106:107] neg_lo:[0,1] neg_hi:[0,1]\n"
      "v_pk_add_f32 v[118:119], v[118:119], v[106:107] neg_lo:[0,1] neg_hi:[0,1]\n"
      "v_pk_add_f32 v[120:121], v[120:121], v[106:107] neg_lo:[0,1] neg_hi:[0,1]\n"
      "v_pk_add_f32 v[122:123], v[122:123], v[106:107] neg_lo:[0,1] neg_hi:[0,1]\n"
      "v_pk_add_f32 v[124:125], v[124:125], v[106:107] neg_lo:[0,1] neg_hi:[0,1]\n"
      "v_pk_add_f32 v[126:127], v[126:127], v[106:107] neg_lo:[0,1] neg_hi:[0,1]\n"
      "v_pk_add_f32 v[128:129], v[128:129], v[106:107] neg_lo:[0,1] neg_hi:[0,1]\n"
      "v_pk_add_f32 v[130:131], v[130:131], v[106:107] neg_lo:[0,1] neg_hi:[0,1]\n"
      "v_pk_add_f32 v[132:133], v[132:133], v[106:107] neg_lo:[0,1] neg_hi:[0,1]\n"
      "v_pk_add_f32 v[134:135], v[134:135], v[106:107] neg_lo:[0,1] neg_hi:[0,1]\n"
      "v_pk_mul_f32 v[108:109], v[116:117], v[116:117]\n"
      "v_pk_mul_f32 v[110:111], v[118:119], v[118:119]\n"
      "v_pk_fma_f32 v[108:109], v[120:121], v[120:121], v[108:109]\n"
      "v_pk_add_f32 v[116:117], v[116:117], v[120:121]\n"
      "v_pk_fma_f32 v[110:111], v[122:123], v[122:123], v[110:111]\n"
      "v_pk_add_f32 v[118:119], v[118:119], v[122:123]\n"
      "v_pk_fma_f32 v[108:109], v[124:125], v[124:125], v[108:109]\n"
      "v_pk_add_f32 v[116:117], v[116:117], v[124:125]\n"
      "v_pk_fma_f32 v[110:111], v[126:127], v[126:127], v[110:111]\n"
      "v_pk_add_f32 v[118:119], v[118:119], v[126:127]\n"
      "v_pk_fma_f32 v[108:109], v[128:129], v[128:129], v[108:109]\n"
      "v_pk_add_f32 v[116:117], v[116:117], v[128:129]\n"
      "v_pk_fma_f32 v[110:111], v[130:131], v[130:131], v[110:111]\n"
      "v_pk_add_f32 v[118:119], v[118:119], v[130:131]\n"
      "v_pk_fma_f32 v[108:109], v[132:133], v[132:133], v[108:109]\n"
      "v_pk_add_f32 v[116:117], v[116:117], v[132:133]\n"
      "v_pk_fma_f32 v[110:111], v[134:135], v[134:135], v[110:111]\n"
      "v_pk_add_f32 v[118:119], v[118:119], v[134:135]\n"
      "v_pk_add_f32 v[108:109], v[108:109], v[110:111]\n"
      "v_pk_add_f32 v[116:117], v[116:117], v[118:119]\n"
      "v_add_f32 v108, v108, v109\n"
      "v_add_f32 v109, v116, v117\n"
      "s_and_b32 s93, s58, 0x1fff\n"
      "v_add_f32_dpp v108, v108, v108 quad_perm:[1,0,3,2] row_mask:0xf bank_mask:0xf\n"
      "v_add_f32_dpp v109, v109, v109 quad_perm:[1,0,3,2] row_mask:0xf bank_mask:0xf\n"
      "s_or_b32 s93, s93, 0x4000\n"
      "v_add_f32_dpp v108, v108, v108 quad_perm:[2,3,0,1] row_mask:0xf bank_mask:0xf\n"
      "v_add_f32_dpp v109, v109, v109 quad_perm:[2,3,0,1] row_mask:0xf bank_mask:0xf\n"
      "v_mov_b32 v114, 1073741824\n"
      "v_add_f32_dpp v108, v108, v108 row_half_mirror row_mask:0xf bank_mask:0xf\n"
      "v_add_f32_dpp v109, v109, v109 row_half_mirror row_mask:0xf bank_mask:0xf\n"
      "v_writelane_b32 v114, 0, 16\n"
      "v_add_f32_dpp v108, v108, v108 row_mirror row_mask:0xf bank_mask:0xf\n"
      "v_add_f32_dpp v109, v109, v109 row_mirror row_mask:0xf bank_mask:0xf\n"
      "v_writelane_b32 v114, 8, 48\n"
      "v_add_f32_dpp v108, v108, v108 row_bcast:15 row_mask:0xa bank_mask:0xf\n"
      "v_add_f32_dpp v109, v109, v109 row_bcast:15 row_mask:0xa bank_mask:0xf\n"
      "v_mul_f32 v109, 0x3acccccd, v109\n"
      "v_mul_f32 v108, 0x3acccccd, v108\n"
      "v_fma_f32 v108, -v109, v109, v108\n"
      "v_max_f32 v108, 0, v108\n"
      "v_cvt_pk_bf16_f32 v108, 0, v108\n"
      "v_add_f32 v108, 0x3727c5ac, v108\n"
      "v_cvt_pk_bf16_f32 v108, 0, v108\n"
      "v_sqrt_f32 v108, v108\n"
      "v_add_f32 v109, v106, v109\n"
      "s_add_u32 s69, s69, 8192\n"
      "v_cvt_pk_bf16_f32 v112, v109, v108\n"
      "v_mov_b32 v113, s58\n"
      "buffer_store_dwordx2 v[112:113], v114, s[20:23], s69 offen sc1\n"
      "s_cmp_lt_u32 s61, 28\n"
      "s_cbranch_scc0 .LSPARE\n"
      "v_mov_b64 v[12:13], s[58:59]\n"
      "v_cmp_eq_u32 vcc, 0, v1\n"
      "v_lshlrev_b32 v2, 2, v1\n"
      "v_add_u32 v2, 157696, v2\n"
      "v_cndmask_b32 v3, 0, 1, vcc\n"
      "s_waitcnt vmcnt(1)\n"
      "v_lshlrev_b32 v70, 16, v44\n"
      "v_lshlrev_b32 v71, 16, v46\n"
      "v_and_b32 v72, s92, v44\n"
      "v_and_b32 v73, s92, v46\n"
      "v_lshlrev_b32 v74, 16, v45\n"
      "v_lshlrev_b32 v75, 16, v47\n"
      "v_and_b32 v76, s92, v45\n"
      "v_and_b32 v77, s92, v47\n"
      "v_lshlrev_b32 v9, 16, v9\n"
      "s_barrier\n"
      "v_mov_b32 v31, 161920\n"
      "ds_write_b64 v31, v[12:13]\n"
      "ds_read_b32 v26, v2 offset:5120\n"
      "v_lshlrev_b32 v31, 3, v1\n"
      "v_add_u32 v31, 163072, v31\n"
      "s_waitcnt lgkmcnt(0)\n"
      "v_and_b32 v27, s92, v26\n"
      "v_lshlrev_b32 v26, 16, v26\n"
      "ds_write_b64 v31, v[26:27]\n"
      "ds_read_b64_tr_b16 v[96:97], v64 offset:0\n"
      "ds_read_b64_tr_b16 v[98:99], v64 offset:1024\n"
      "ds_read_b128 v[32:35], v4 offset:16384\n"
      "ds_read_b64_tr_b16 v[100:101], v64 offset:4096\n"
      "ds_read_b64_tr_b16 v[102:103], v64 offset:5120\n"
      "ds_read_b128 v[36:39], v5 offset:16384\n"
      "ds_read_b64_tr_b16 v[104:105], v64 offset:8192\n"
      "ds_read_b64_tr_b16 v[106:107], v64 offset:9216\n"
      "ds_read_b128 v[40:43], v6 offset:16384\n"
      "ds_read_b64_tr_b16 v[108:109], v64 offset:12288\n"
      "ds_read_b64_tr_b16 v[110:111], v64 offset:13312\n"
      "ds_read_b128 v[44:47], v7 offset:16384\n"
      "s_waitcnt lgkmcnt(9)\n"
      "v_mfma_f32_32x32x16_bf16 v[80:95], v[96:99], v[32:35], 0\n"
      "v_mfma_f32_16x16x32_bf16 v[20:23], v[16:19], v[96:99], 0\n"
      "s_waitcnt lgkmcnt(6)\n"
      "v_mfma_f32_32x32x16_bf16 v[80:95], v[100:103], v[36:39], v[80:95]\n"
      "v_mfma_f32_16x16x32_bf16 v[20:23], v[16:19], v[100:103], v[20:23]\n"
      "s_barrier\n"
      "ds_read2_b32 v[196:197], v78 offset0:0 offset1:32\n"
      "ds_read2_b32 v[198:199], v78 offset0:64 offset1:100\n"
      "ds_read2_b32 v[200:201], v78 offset0:132 offset1:164\n"
      "ds_read2_b32 v[202:203], v78 offset0:196 offset1:228\n"
      "ds_read_b32 v204, v78 offset:1040\n"
      "ds_read_b32 v205, v78 offset:1168\n"
      "ds_read_b32 v206, v78 offset:1296\n"
      "ds_read2_b32 v[208:209], v79 offset0:0 offset1:32\n"
      "ds_read2_b32 v[210:211], v79 offset0:64 offset1:96\n"
      "ds_read2_b32 v[212:213], v79 offset0:128 offset1:160\n"
      "ds_read2_b32 v[214:215], v79 offset0:192 offset1:224\n"
      "s_waitcnt lgkmcnt(11)\n"
      "v_mfma_f32_32x32x16_bf16 v[80:95], v[104:107], v[40:43], v[80:95]\n"
      "s_waitcnt lgkmcnt(9)\n"
      "v_lshlrev_b32 v216, 16, v196\n"
      "v_and_b32 v217, s92, v196\n"
      "v_lshlrev_b32 v218, 16, v197\n"
      "v_and_b32 v219, s92, v197\n"
      "v_mfma_f32_16x16x32_bf16 v[20:23], v[16:19], v[104:107], v[20:23]\n"
      "v_lshlrev_b32 v220, 16, v198\n"
      "v_and_b32 v221, s92, v198\n"
      "v_lshlrev_b32 v222, 16, v199\n"
      "v_and_b32 v223, s92, v199\n"
      "v_mfma_f32_32x32x16_bf16 v[80:95], v[108:111], v[44:47], v[80:95]\n"
      "s_waitcnt lgkmcnt(7)\n"
      "v_lshlrev_b32 v224, 16, v200\n"
      "v_and_b32 v225, s92, v200\n"
      "v_lshlrev_b32 v226, 16, v201\n"
      "v_and_b32 v227, s92, v201\n"
      "v_mfma_f32_16x16x32_bf16 v[20:23], v[16:19], v[108:111], v[20:23]\n"
      "v_lshlrev_b32 v228, 16, v202\n"
      "v_and_b32 v229, s92, v202\n"
      "v_lshlrev_b32 v230, 16, v203\n"
      "v_and_b32 v231, s92, v203\n"
      "s_waitcnt lgkmcnt(4)\n"
      "v_lshlrev_b32 v232, 16, v204\n"
      "v_and_b32 v233, s92, v204\n"
      "v_lshlrev_b32 v234, 16, v205\n"
      "v_and_b32 v235, s92, v205\n"
      "v_lshlrev_b32 v236, 16, v206\n"
      "v_and_b32 v237, s92, v206\n"
      "v_pk_mul_f32 v[240:241], v[216:217], v[76:77]\n"
      "v_pk_mul_f32 v[242:243], v[218:219], v[76:77]\n"
      "v_pk_mul_f32 v[244:245], v[220:221], v[76:77]\n"
      "v_pk_mul_f32 v[246:247], v[222:223], v[76:77]\n"
      "v_pk_mul_f32 v[248:249], v[224:225], v[76:77]\n"
      "v_pk_mul_f32 v[250:251], v[226:227], v[76:77]\n"
      "v_pk_mul_f32 v[252:253], v[228:229], v[76:77]\n"
      "v_pk_mul_f32 v[254:255], v[230:231], v[76:77]\n"
      "v_pk_fma_f32 v[240:241], v[218:219], v[74:75], v[240:241]\n"
      "v_pk_fma_f32 v[242:243], v[220:221], v[74:75], v[242:243]\n"
      "v_pk_fma_f32 v[244:245], v[222:223], v[74:75], v[244:245]\n"
      "v_pk_fma_f32 v[246:247], v[224:225], v[74:75], v[246:247]\n"
      "v_pk_fma_f32 v[248:249], v[226:227], v[74:75], v[248:249]\n"
      "v_pk_fma_f32 v[250:251], v[228:229], v[74:75], v[250:251]\n"
      "v_pk_fma_f32 v[252:253], v[230:231], v[74:75], v[252:253]\n"
      "v_pk_fma_f32 v[254:255], v[232:233], v[74:75], v[254:255]\n"
      "v_pk_fma_f32 v[240:241], v[220:221], v[72:73], v[240:241]\n"
      "v_pk_fma_f32 v[242:243], v[222:223], v[72:73], v[242:243]\n"
      "v_pk_fma_f32 v[244:245], v[224:225], v[72:73], v[244:245]\n"
      "v_pk_fma_f32 v[246:247], v[226:227], v[72:73], v[246:247]\n"
      "v_pk_fma_f32 v[248:249], v[228:229], v[72:73], v[248:249]\n"
      "v_pk_fma_f32 v[250:251], v[230:231], v[72:73], v[250:251]\n"
      "v_pk_fma_f32 v[252:253], v[232:233], v[72:73], v[252:253]\n"
      "v_pk_fma_f32 v[254:255], v[234:235], v[72:73], v[254:255]\n"
      "v_pk_fma_f32 v[240:241], v[222:223], v[70:71], v[240:241]\n"
      "v_pk_fma_f32 v[242:243], v[224:225], v[70:71], v[242:243]\n"
      "v_pk_fma_f32 v[244:245], v[226:227], v[70:71], v[244:245]\n"
      "v_pk_fma_f32 v[246:247], v[228:229], v[70:71], v[246:247]\n"
      "v_pk_fma_f32 v[248:249], v[230:231], v[70:71], v[248:249]\n"
      "v_pk_fma_f32 v[250:251], v[232:233], v[70:71], v[250:251]\n"
      "v_pk_fma_f32 v[252:253], v[234:235], v[70:71], v[252:253]\n"
      "v_pk_fma_f32 v[254:255], v[236:237], v[70:71], v[254:255]\n"
      "s_waitcnt lgkmcnt(0)\n"
      "v_lshlrev_b32 v216, 16, v208\n"
      "v_and_b32 v217, s92, v208\n"
      "v_lshlrev_b32 v218, 16, v209\n"
      "v_and_b32 v219, s92, v209\n"
      "v_lshlrev_b32 v220, 16, v210\n"
      "v_and_b32 v221, s92, v210\n"
      "v_lshlrev_b32 v222, 16, v211\n"
      "v_and_b32 v223, s92, v211\n"
      "v_lshlrev_b32 v224, 16, v212\n"
      "v_and_b32 v225, s92, v212\n"
      "v_lshlrev_b32 v226, 16, v213\n"
      "v_and_b32 v227, s92, v213\n"
      "v_lshlrev_b32 v228, 16, v214\n"
      "v_and_b32 v229, s92, v214\n"
      "v_lshlrev_b32 v230, 16, v215\n"
      "v_and_b32 v231, s92, v215\n"
      "v_pk_mul_f32 v[240:241], v[240:241], v[216:217]\n"
      "v_pk_mul_f32 v[242:243], v[242:243], v[218:219]\n"
      "v_pk_mul_f32 v[244:245], v[244:245], v[220:221]\n"
      "v_pk_mul_f32 v[246:247], v[246:247], v[222:223]\n"
      "v_pk_mul_f32 v[248:249], v[248:249], v[224:225]\n"
      "v_pk_mul_f32 v[250:251], v[250:251], v[226:227]\n"
      "v_pk_mul_f32 v[252:253], v[252:253], v[228:229]\n"
      "v_pk_mul_f32 v[254:255], v[254:255], v[230:231]\n"
      "v_cvt_pk_bf16_f32 v196, v242, v240\n"
      "v_cvt_pk_bf16_f32 v197, v246, v244\n"
      "v_cvt_pk_bf16_f32 v198, v250, v248\n"
      "v_cvt_pk_bf16_f32 v199, v254, v252\n"
      "buffer_store_dwordx4 v[196:199], v8, s[20:23], s84 offen sc1\n"
      "v_cvt_pk_bf16_f32 v200, v243, v241\n"
      "v_cvt_pk_bf16_f32 v201, v247, v245\n"
      "v_cvt_pk_bf16_f32 v202, v251, v249\n"
      "v_cvt_pk_bf16_f32 v203, v255, v253\n"
      "buffer_store_dwordx4 v[200:203], v8, s[20:23], s85 offen sc1\n"
      "s_barrier\n"
      "ds_read_b64_tr_b16 v[112:113], v64 offset:20480\n"
      "ds_read_b64_tr_b16 v[114:115], v64 offset:21504\n"
      "ds_read_b128 v[48:51], v4 offset:36864\n"
      "ds_read_b64_tr_b16 v[116:117], v64 offset:24576\n"
      "ds_read_b64_tr_b16 v[118:119], v64 offset:25600\n"
      "ds_read_b128 v[52:55], v5 offset:36864\n"
      "ds_read_b64_tr_b16 v[120:121], v64 offset:28672\n"
      "ds_read_b64_tr_b16 v[122:123], v64 offset:29696\n"
      "ds_read_b128 v[56:59], v6 offset:36864\n"
      "ds_read_b64_tr_b16 v[124:125], v64 offset:32768\n"
      "ds_read_b64_tr_b16 v[126:127], v64 offset:33792\n"
      "ds_read_b128 v[60:63], v7 offset:36864\n"
      "s_waitcnt lgkmcnt(9)\n"
      "v_mfma_f32_32x32x16_bf16 v[80:95], v[112:115], v[48:51], v[80:95]\n"
      "v_mfma_f32_16x16x32_bf16 v[20:23], v[16:19], v[112:115], v[20:23]\n"
      "s_waitcnt lgkmcnt(6)\n"
      "v_mfma_f32_32x32x16_bf16 v[80:95], v[116:119], v[52:55], v[80:95]\n"
      "v_mfma_f32_16x16x32_bf16 v[20:23], v[16:19], v[116:119], v[20:23]\n"
      "s_barrier\n"
      "ds_read_b64_tr_b16 v[128:129], v64 offset:40960\n"
      "ds_read_b64_tr_b16 v[130:131], v64 offset:41984\n"
      "ds_read_b128 v[32:35], v4 offset:57344\n"
      "s_waitcnt lgkmcnt(6)\n"
      "v_mfma_f32_32x32x16_bf16 v[80:95], v[120:123], v[56:59], v[80:95]\n"
      "ds_read_b64_tr_b16 v[132:133], v64 offset:45056\n"
      "ds_read_b64_tr_b16 v[134:135], v64 offset:46080\n"
      "ds_read_b128 v[36:39], v5 offset:57344\n"
      "v_mfma_f32_16x16x32_bf16 v[20:23], v[16:19], v[120:123], v[20:23]\n"
      "ds_read_b64_tr_b16 v[136:137], v64 offset:49152\n"
      "ds_read_b64_tr_b16 v[138:139], v64 offset:50176\n"
      "ds_read_b128 v[40:43], v6 offset:57344\n"
      "s_waitcnt lgkmcnt(9)\n"
      "v_mfma_f32_32x32x16_bf16 v[80:95], v[124:127], v[60:63], v[80:95]\n"
      "ds_read_b64_tr_b16 v[140:141], v64 offset:53248\n"
      "ds_read_b64_tr_b16 v[142:143], v64 offset:54272\n"
      "ds_read_b128 v[44:47], v7 offset:57344\n"
      "v_mfma_f32_16x16x32_bf16 v[20:23], v[16:19], v[124:127], v[20:23]\n"
      "s_waitcnt lgkmcnt(9)\n"
      "v_mfma_f32_32x32x16_bf16 v[80:95], v[128:131], v[32:35], v[80:95]\n"
      "v_mfma_f32_16x16x32_bf16 v[20:23], v[16:19], v[128:131], v[20:23]\n"
      "s_waitcnt lgkmcnt(6)\n"
      "v_mfma_f32_32x32x16_bf16 v[80:95], v[132:135], v[36:39], v[80:95]\n"
      "v_mfma_f32_16x16x32_bf16 v[20:23], v[16:19], v[132:135], v[20:23]\n"
      "s_barrier\n"
      "ds_read_b64_tr_b16 v[144:145], v65 offset:0\n"
      "ds_read_b64_tr_b16 v[146:147], v65 offset:1024\n"
      "ds_read_b128 v[48:51], v66 offset:16384\n"
      "s_waitcnt lgkmcnt(6)\n"
      "v_mfma_f32_32x32x16_bf16 v[80:95], v[136:139], v[40:43], v[80:95]\n"
      "ds_read_b64_tr_b16 v[148:149], v65 offset:4096\n"
      "ds_read_b64_tr_b16 v[150:151], v65 offset:5120\n"
      "ds_read_b128 v[52:55], v67 offset:16384\n"
      "v_mfma_f32_16x16x32_bf16 v[20:23], v[16:19], v[136:139], v[20:23]\n"
      "ds_read_b64_tr_b16 v[152:153], v65 offset:8192\n"
      "ds_read_b64_tr_b16 v[154:155], v65 offset:9216\n"
      "ds_read_b128 v[56:59], v68 offset:16384\n"
      "s_waitcnt lgkmcnt(9)\n"
      "v_mfma_f32_32x32x16_bf16 v[80:95], v[140:143], v[44:47], v[80:95]\n"
      "ds_read_b64_tr_b16 v[156:157], v65 offset:12288\n"
      "ds_read_b64_tr_b16 v[158:159], v65 offset:13312\n"
      "ds_read_b128 v[60:63], v69 offset:16384\n"
      "v_mfma_f32_16x16x32_bf16 v[20:23], v[16:19], v[140:143], v[20:23]\n"
      "s_waitcnt lgkmcnt(9)\n"
      "v_mfma_f32_32x32x16_bf16 v[80:95], v[144:147], v[48:51], v[80:95]\n"
      "v_mfma_f32_16x16x32_bf16 v[20:23], v[16:19], v[144:147], v[20:23]\n"
      "s_waitcnt lgkmcnt(6)\n"
      "v_mfma_f32_32x32x16_bf16 v[80:95], v[148:151], v[52:55], v[80:95]\n"
      "v_mfma_f32_16x16x32_bf16 v[20:23], v[16:19], v[148:151], v[20:23]\n"
      "s_barrier\n"
      "ds_read_b64_tr_b16 v[160:161], v65 offset:20480\n"
      "ds_read_b64_tr_b16 v[162:163], v65 offset:21504\n"
      "ds_read_b128 v[32:35], v66 offset:36864\n"
      "s_waitcnt lgkmcnt(6)\n"
      "v_mfma_f32_32x32x16_bf16 v[80:95], v[152:155], v[56:59], v[80:95]\n"
      "ds_read_b64_tr_b16 v[164:165], v65 offset:24576\n"
      "ds_read_b64_tr_b16 v[166:167], v65 offset:25600\n"
      "ds_read_b128 v[36:39], v67 offset:36864\n"
      "v_mfma_f32_16x16x32_bf16 v[20:23], v[16:19], v[152:155], v[20:23]\n"
      "ds_read_b64_tr_b16 v[168:169], v65 offset:28672\n"
      "ds_read_b64_tr_b16 v[170:171], v65 offset:29696\n"
      "ds_read_b128 v[40:43], v68 offset:36864\n"
      "s_waitcnt lgkmcnt(9)\n"
      "v_mfma_f32_32x32x16_bf16 v[80:95], v[156:159], v[60:63], v[80:95]\n"
      "ds_read_b64_tr_b16 v[172:173], v65 offset:32768\n"
      "ds_read_b64_tr_b16 v[174:175], v65 offset:33792\n"
      "ds_read_b128 v[44:47], v69 offset:36864\n"
      "v_mfma_f32_16x16x32_bf16 v[20:23], v[16:19], v[156:159], v[20:23]\n"
      "s_waitcnt lgkmcnt(9)\n"
      "v_mfma_f32_32x32x16_bf16 v[80:95], v[160:163], v[32:35], v[80:95]\n"
      "v_mfma_f32_16x16x32_bf16 v[20:23], v[16:19], v[160:163], v[20:23]\n"
      "s_waitcnt lgkmcnt(6)\n"
      "v_mfma_f32_32x32x16_bf16 v[80:95], v[164:167], v[36:39], v[80:95]\n"
      "v_mfma_f32_16x16x32_bf16 v[20:23], v[16:19], v[164:167], v[20:23]\n"
      "s_waitcnt vmcnt(0)\n"
      "s_barrier\n"
      "ds_read_b64_tr_b16 v[176:177], v65 offset:40960\n"
      "ds_read_b64_tr_b16 v[178:179], v65 offset:41984\n"
      "ds_read_b128 v[48:51], v66 offset:57344\n"
      "s_waitcnt lgkmcnt(6)\n"
      "v_mfma_f32_32x32x16_bf16 v[80:95], v[168:171], v[40:43], v[80:95]\n"
      "ds_read_b64_tr_b16 v[180:181], v65 offset:45056\n"
      "ds_read_b64_tr_b16 v[182:183], v65 offset:46080\n"
      "ds_read_b128 v[52:55], v67 offset:57344\n"
      "v_mfma_f32_16x16x32_bf16 v[20:23], v[16:19], v[168:171], v[20:23]\n"
      "ds_read_b64_tr_b16 v[184:185], v65 offset:49152\n"
      "ds_read_b64_tr_b16 v[186:187], v65 offset:50176\n"
      "ds_read_b128 v[56:59], v68 offset:57344\n"
      "s_waitcnt lgkmcnt(9)\n"
      "v_mfma_f32_32x32x16_bf16 v[80:95], v[172:175], v[44:47], v[80:95]\n"
      "ds_read_b64_tr_b16 v[188:189], v65 offset:53248\n"
      "ds_read_b64_tr_b16 v[190:191], v65 offset:54272\n"
      "ds_read_b128 v[60:63], v69 offset:57344\n"
      "v_mfma_f32_16x16x32_bf16 v[20:23], v[16:19], v[172:175], v[20:23]\n"
      "s_waitcnt lgkmcnt(9)\n"
      "v_mfma_f32_32x32x16_bf16 v[80:95], v[176:179], v[48:51], v[80:95]\n"
      "v_mfma_f32_16x16x32_bf16 v[20:23], v[16:19], v[176:179], v[20:23]\n"
      "s_waitcnt lgkmcnt(6)\n"
      "v_mfma_f32_32x32x16_bf16 v[80:95], v[180:183], v[52:55], v[80:95]\n"
      "v_mfma_f32_16x16x32_bf16 v[20:23], v[16:19], v[180:183], v[20:23]\n"
      "s_barrier\n"
      "ds_read_b64_tr_b16 v[192:193], v64 offset:0\n"
      "ds_read_b64_tr_b16 v[194:195], v64 offset:1024\n"
      "ds_read_b128 v[32:35], v4 offset:16384\n"
      "s_waitcnt lgkmcnt(6)\n"
      "v_mfma_f32_32x32x16_bf16 v[80:95], v[184:187], v[56:59], v[80:95]\n"
      "ds_read_b64_tr_b16 v[196:197], v64 offset:4096\n"
      "ds_read_b64_tr_b16 v[198:199], v64 offset:5120\n"
      "ds_read_b128 v[36:39], v5 offset:16384\n"
      "v_mfma_f32_16x16x32_bf16 v[20:23], v[16:19], v[184:187], v[20:23]\n"
      "ds_read_b64_tr_b16 v[200:201], v64 offset:8192\n"
      "ds_read_b64_tr_b16 v[202:203], v64 offset:9216\n"
      "ds_read_b128 v[40:43], v6 offset:16384\n"
      "s_waitcnt lgkmcnt(9)\n"
      "v_mfma_f32_32x32x16_bf16 v[80:95], v[188:191], v[60:63], v[80:95]\n"
      "ds_read_b64_tr_b16 v[204:205], v64 offset:12288\n"
      "ds_read_b64_tr_b16 v[206:207], v64 offset:13312\n"
      "ds_read_b128 v[44:47], v7 offset:16384\n"
      "v_mfma_f32_16x16x32_bf16 v[20:23], v[16:19], v[188:191], v[20:23]\n"
      "s_waitcnt lgkmcnt(9)\n"
      "v_mfma_f32_32x32x16_bf16 v[80:95], v[192:195], v[32:35], v[80:95]\n"
      "v_mfma_f32_16x16x32_bf16 v[20:23], v[16:19], v[192:195], v[20:23]\n"
      "s_waitcnt lgkmcnt(6)\n"
      "v_mfma_f32_32x32x16_bf16 v[80:95], v[196:199], v[36:39], v[80:95]\n"
      "v_mfma_f32_16x16x32_bf16 v[20:23], v[16:19], v[196:199], v[20:23]\n"
      "s_barrier\n"
      "ds_read_b64_tr_b16 v[208:209], v64 offset:20480\n"
      "ds_read_b64_tr_b16 v[210:211], v64 offset:21504\n"
      "ds_read_b128 v[48:51], v4 offset:36864\n"
      "s_waitcnt lgkmcnt(6)\n"
      "v_mfma_f32_32x32x16_bf16 v[80:95], v[200:203], v[40:43], v[80:95]\n"
      "ds_read_b64_tr_b16 v[212:213], v64 offset:24576\n"
      "ds_read_b64_tr_b16 v[214:215], v64 offset:25600\n"
      "ds_read_b128 v[52:55], v5 offset:36864\n"
      "v_mfma_f32_16x16x32_bf16 v[20:23], v[16:19], v[200:203], v[20:23]\n"
      "ds_read_b64_tr_b16 v[216:217], v64 offset:28672\n"
      "ds_read_b64_tr_b16 v[218:219], v64 offset:29696\n"
      "ds_read_b128 v[56:59], v6 offset:36864\n"
      "s_waitcnt lgkmcnt(9)\n"
      "v_mfma_f32_32x32x16_bf16 v[80:95], v[204:207], v[44:47], v[80:95]\n"
      "ds_read_b64_tr_b16 v[220:221], v64 offset:32768\n"
      "ds_read_b64_tr_b16 v[222:223], v64 offset:33792\n"
      "ds_read_b128 v[60:63], v7 offset:36864\n"
      "v_mfma_f32_16x16x32_bf16 v[20:23], v[16:19], v[204:207], v[20:23]\n"
      "s_waitcnt lgkmcnt(9)\n"
      "v_mfma_f32_32x32x16_bf16 v[80:95], v[208:211], v[48:51], v[80:95]\n"
      "v_mfma_f32_16x16x32_bf16 v[20:23], v[16:19], v[208:211], v[20:23]\n"
      "s_waitcnt lgkmcnt(6)\n"
      "v_mfma_f32_32x32x16_bf16 v[80:95], v[212:215], v[52:55], v[80:95]\n"
      "v_mfma_f32_16x16x32_bf16 v[20:23], v[16:19], v[212:215], v[20:23]\n"
      "s_barrier\n"
      "ds_read_b64_tr_b16 v[224:225], v64 offset:40960\n"
      "ds_read_b64_tr_b16 v[226:227], v64 offset:41984\n"
      "ds_read_b128 v[32:35], v4 offset:57344\n"
      "s_waitcnt lgkmcnt(6)\n"
      "v_mfma_f32_32x32x16_bf16 v[80:95], v[216:219], v[56:59], v[80:95]\n"
      "ds_read_b64_tr_b16 v[228:229], v64 offset:45056\n"
      "ds_read_b64_tr_b16 v[230:231], v64 offset:46080\n"
      "ds_read_b128 v[36:39], v5 offset:57344\n"
      "v_mfma_f32_16x16x32_bf16 v[20:23], v[16:19], v[216:219], v[20:23]\n"
      "ds_read_b64_tr_b16 v[232:233], v64 offset:49152\n"
      "ds_read_b64_tr_b16 v[234:235], v64 offset:50176\n"
      "ds_read_b128 v[40:43], v6 offset:57344\n"
      "s_waitcnt lgkmcnt(9)\n"
      "v_mfma_f32_32x32x16_bf16 v[80:95], v[220:223], v[60:63], v[80:95]\n"
      "ds_read_b64_tr_b16 v[236:237], v64 offset:53248\n"
      "ds_read_b64_tr_b16 v[238:239], v64 offset:54272\n"
      "ds_read_b128 v[44:47], v7 offset:57344\n"
      "v_mfma_f32_16x16x32_bf16 v[20:23], v[16:19], v[220:223], v[20:23]\n"
      "s_waitcnt lgkmcnt(9)\n"
      "v_mfma_f32_32x32x16_bf16 v[80:95], v[224:227], v[32:35], v[80:95]\n"
      "v_mfma_f32_16x16x32_bf16 v[20:23], v[16:19], v[224:227], v[20:23]\n"
      "s_waitcnt lgkmcnt(6)\n"
      "v_mfma_f32_32x32x16_bf16 v[80:95], v[228:231], v[36:39], v[80:95]\n"
      "v_mfma_f32_16x16x32_bf16 v[20:23], v[16:19], v[228:231], v[20:23]\n"
      "s_barrier\n"
      "ds_read_b64_tr_b16 v[240:241], v65 offset:0\n"
      "ds_read_b64_tr_b16 v[242:243], v65 offset:1024\n"
      "ds_read_b64_tr_b16 v[244:245], v65 offset:4096\n"
      "ds_read_b64_tr_b16 v[246:247], v65 offset:5120\n"
      "s_waitcnt lgkmcnt(7)\n"
      "v_mfma_f32_32x32x16_bf16 v[80:95], v[232:235], v[40:43], v[80:95]\n"
      "ds_read_b64_tr_b16 v[248:249], v65 offset:8192\n"
      "ds_read_b64_tr_b16 v[250:251], v65 offset:9216\n"
      "ds_read_b64_tr_b16 v[252:253], v65 offset:12288\n"
      "ds_read_b64_tr_b16 v[254:255], v65 offset:13312\n"
      "v_mfma_f32_16x16x32_bf16 v[20:23], v[16:19], v[232:235], v[20:23]\n"
      "ds_read_b128 v[48:51], v66 offset:16384\n"
      "ds_read_b128 v[52:55], v67 offset:16384\n"
      "s_waitcnt lgkmcnt(10)\n"
      "v_mfma_f32_32x32x16_bf16 v[80:95], v[236:239], v[44:47], v[80:95]\n"
      "ds_read_b128 v[56:59], v68 offset:16384\n"
      "ds_read_b128 v[60:63], v69 offset:16384\n"
      "v_mfma_f32_16x16x32_bf16 v[20:23], v[16:19], v[236:239], v[20:23]\n"
      "ds_read_b32 v31, v2 offset:4144\n"
      "s_waitcnt lgkmcnt(11)\n"
      "v_mfma_f32_16x16x32_bf16 v[20:23], v[16:19], v[240:243], v[20:23]\n"
      "v_add_u32 v4, 81920, v4\n"
      "s_waitcnt lgkmcnt(9)\n"
      "v_mfma_f32_16x16x32_bf16 v[20:23], v[16:19], v[244:247], v[20:23]\n"
      "v_add_u32 v5, 81920, v5\n"
      "s_waitcnt lgkmcnt(7)\n"
      "v_mfma_f32_16x16x32_bf16 v[20:23], v[16:19], v[248:251], v[20:23]\n"
      "v_add_u32 v6, 81920, v6\n"
      "s_waitcnt lgkmcnt(5)\n"
      "v_mfma_f32_16x16x32_bf16 v[20:23], v[16:19], v[252:255], v[20:23]\n"
      "v_add_u32 v7, 81920, v7\n"
      "s_waitcnt lgkmcnt(0)\n"
      "v_mfma_f32_32x32x16_bf16 v[80:95], v[240:243], v[48:51], v[80:95]\n"
      "ds_read_b64 v[16:17], v30 offset:16\n"
      "v_readlane_b32 s64, v31, 0\n"
      "s_mov_b32 vcc_lo, s64\n"
      "s_mov_b32 vcc_hi, 0\n"
      "v_mfma_f32_32x32x16_bf16 v[80:95], v[244:247], v[52:55], v[80:95]\n"
      "v_cndmask_b32 v70, v20, v21, s[80:81]\n"
      "v_cvt_pk_bf16_f32 v71, 0, v70\n"
      "v_sub_f32 v70, v70, v71\n"
      "v_cvt_pk_bf16_f32 v72, 0, v70\n"
      "v_sub_f32 v70, v70, v72\n"
      "v_cvt_pk_bf16_f32 v71, v71, v72\n"
      "v_mfma_f32_32x32x16_bf16 v[80:95], v[248:251], v[56:59], v[80:95]\n"
      "v_cvt_pk_bf16_f32 v72, v70, v9\n"
      "v_cndmask_b32 v8, 0, v71, s[82:83]\n"
      "v_cndmask_b32 v9, 0, v72, s[82:83]\n"
      "ds_read_b128 v[24:27], v30\n"
      "ds_read_b64 v[12:13], v30 offset:1040\n"
      "v_mfma_f32_32x32x16_bf16 v[80:95], v[252:255], v[60:63], v[80:95]\n"
      "s_barrier\n"
      "ds_read_b128 v[32:35], v4\n"
      "ds_read_b128 v[36:39], v5\n"
      "ds_read_b128 v[40:43], v6\n"
      "ds_read_b128 v[44:47], v7\n"
      "s_waitcnt lgkmcnt(6)\n"
      "v_mfma_f32_32x32x16_bf16 v[64:79], v[8:11], v[16:19], v[80:95]\n"
      "ds_read_b128 v[48:51], v4 offset:4096\n"
      "ds_read_b128 v[52:55], v5 offset:4096\n"
      "ds_read_b128 v[56:59], v6 offset:4096\n"
      "ds_read_b128 v[60:63], v7 offset:4096\n"
      "s_waitcnt lgkmcnt(8)\n"
      "v_mfma_f32_32x32x16_bf16 v[80:95], v[8:11], v[12:15], 0\n"
      "s_cbranch_vccnz .Lg_121\n"
      ".Lg_122:\n"
      "ds_read_b64 v[12:13], v30 offset:2064\n"
      "s_nop 0\n"
      "s_waitcnt lgkmcnt(2)\n"
      "v_mfma_f32_32x32x16_bf16 v[80:95], v[96:99], v[32:35], v[80:95]\n"
      "ds_read_b128 v[32:35], v4 offset:8192\n"
      "v_mul_f32 v20, v64, v64\n"
      "v_fma_f32 v20, v20, v25, v24\n"
      "v_mul_f32 v20, v64, v20\n"
      "v_exp_f32 v20, v20\n"
      "v_mfma_f32_32x32x16_bf16 v[80:95], v[100:103], v[36:39], v[80:95]\n"
      "ds_read_b128 v[36:39], v5 offset:8192\n"
      "v_mul_f32 v21, v65, v65\n"
      "v_fma_f32 v20, v20, v26, v26\n"
      "v_fma_f32 v21, v21, v25, v24\n"
      "v_rcp_f32 v20, v20\n"
      "v_mfma_f32_32x32x16_bf16 v[80:95], v[104:107], v[40:43], v[80:95]\n"
      "ds_read_b128 v[40:43], v6 offset:8192\n"
      "v_mul_f32 v21, v65, v21\n"
      "v_mul_f32 v20, v64, v20\n"
      "v_exp_f32 v21, v21\n"
      "v_mfma_f32_32x32x16_bf16 v[80:95], v[108:111], v[44:47], v[80:95]\n"
      "ds_read_b128 v[44:47], v7 offset:8192\n"
      "v_mul_f32 v22, v66, v66\n"
      "v_fma_f32 v21, v21, v26, v26\n"
      "v_fma_f32 v22, v22, v25, v24\n"
      "s_waitcnt lgkmcnt(4)\n"
      "v_mfma_f32_32x32x16_bf16 v[80:95], v[112:115], v[48:51], v[80:95]\n"
      "ds_read_b128 v[48:51], v4 offset:12288\n"
      "v_rcp_f32 v21, v21\n"
      "v_mul_f32 v22, v66, v22\n"
      "v_mul_f32 v21, v65, v21\n"
      "v_cvt_pk_bf16_f32 v16, v20, v21\n"
      "v_mfma_f32_32x32x16_bf16 v[80:95], v[116:119], v[52:55], v[80:95]\n"
      "ds_read_b128 v[52:55], v5 offset:12288\n"
      "v_exp_f32 v22, v22\n"
      "v_mul_f32 v23, v67, v67\n"
      "v_fma_f32 v22, v22, v26, v26\n"
      "v_fma_f32 v23, v23, v25, v24\n"
      "v_mfma_f32_32x32x16_bf16 v[80:95], v[120:123], v[56:59], v[80:95]\n"
      "ds_read_b128 v[56:59], v6 offset:12288\n"
      "v_rcp_f32 v22, v22\n"
      "v_mul_f32 v23, v67, v23\n"
      "v_mul_f32 v22, v66, v22\n"
      "v_mfma_f32_32x32x16_bf16 v[80:95], v[124:127], v[60:63], v[80:95]\n"
      "ds_read_b128 v[60:63], v7 offset:12288\n"
      "v_exp_f32 v23, v23\n"
      "v_mul_f32 v20, v68, v68\n"
      "v_fma_f32 v23, v23, v26, v26\n"
      "s_waitcnt lgkmcnt(4)\n"
      "v_mfma_f32_32x32x16_bf16 v[80:95], v[128:131], v[32:35], v[80:95]\n"
      "ds_read_b128 v[32:35], v4 offset:16384\n"
      "v_fma_f32 v20, v20, v25, v24\n"
      "v_rcp_f32 v23, v23\n"
      "v_mul_f32 v20, v68, v20\n"
      "ds_read_b32 v31, v2 offset:24\n"
      "v_mfma_f32_32x32x16_bf16 v[80:95], v[132:135], v[36:39], v[80:95]\n"
      "ds_read_b128 v[36:39], v5 offset:16384\n"
      "v_mul_f32 v23, v67, v23\n"
      "v_cvt_pk_bf16_f32 v17, v22, v23\n"
      "v_exp_f32 v20, v20\n"
      "v_mfma_f32_32x32x16_bf16 v[80:95], v[136:139], v[40:43], v[80:95]\n"
      "ds_read_b128 v[40:43], v6 offset:16384\n"
      "v_mul_f32 v21, v69, v69\n"
      "v_fma_f32 v20, v20, v26, v26\n"
      "v_fma_f32 v21, v21, v25, v24\n"
      "v_rcp_f32 v20, v20\n"
      "v_mfma_f32_32x32x16_bf16 v[80:95], v[140:143], v[44:47], v[80:95]\n"
      "ds_read_b128 v[44:47], v7 offset:16384\n"
      "v_mul_f32 v21, v69, v21\n"
      "v_mul_f32 v20, v68, v20\n"
      "v_exp_f32 v21, v21\n"
      "s_waitcnt lgkmcnt(3)\n"
      "v_mfma_f32_32x32x16_bf16 v[80:95], v[144:147], v[48:51], v[80:95]\n"
      "v_readfirstlane_b32 s2, v31\n"
      "s_cmp_lt_u32 s2, 4\n"
      "s_cbranch_scc1 .Lw_123\n"
      ".Lw_124:\n"
      "ds_read_b128 v[48:51], v4 offset:20480\n"
      "v_mul_f32 v22, v70, v70\n"
      "v_mfma_f32_32x32x16_bf16 v[80:95], v[148:151], v[52:55], v[80:95]\n"
      "ds_read_b128 v[52:55], v5 offset:20480\n"
      "v_fma_f32 v21, v21, v26, v26\n"
      "v_fma_f32 v22, v22, v25, v24\n"
      "v_rcp_f32 v21, v21\n"
      "ds_add_u32 v2, v3 offset:1052\n"
      "v_mfma_f32_32x32x16_bf16 v[80:95], v[152:155], v[56:59], v[80:95]\n"
      "ds_read_b128 v[56:59], v6 offset:20480\n"
      "v_mul_f32 v22, v70, v22\n"
      "v_mul_f32 v21, v69, v21\n"
      "v_cvt_pk_bf16_f32 v18, v20, v21\n"
      "v_mfma_f32_32x32x16_bf16 v[80:95], v[156:159], v[60:63], v[80:95]\n"
      "ds_read_b128 v[60:63], v7 offset:20480\n"
      "v_exp_f32 v22, v22\n"
      "v_mul_f32 v23, v71, v71\n"
      "v_fma_f32 v22, v22, v26, v26\n"
      "v_fma_f32 v23, v23, v25, v24\n"
      "s_waitcnt lgkmcnt(4)\n"
      "v_mfma_f32_32x32x16_bf16 v[80:95], v[160:163], v[32:35], v[80:95]\n"
      "ds_read_b128 v[32:35], v4 offset:24576\n"
      "v_rcp_f32 v22, v22\n"
      "v_mul_f32 v23, v71, v23\n"
      "v_mul_f32 v22, v70, v22\n"
      "v_mfma_f32_32x32x16_bf16 v[80:95], v[164:167], v[36:39], v[80:95]\n"
      "ds_read_b128 v[36:39], v5 offset:24576\n"
      "v_exp_f32 v23, v23\n"
      "v_mul_f32 v20, v72, v72\n"
      "v_fma_f32 v23, v23, v26, v26\n"
      "v_fma_f32 v20, v20, v25, v24\n"
      "v_mfma_f32_32x32x16_bf16 v[80:95], v[168:171], v[40:43], v[80:95]\n"
      "ds_read_b128 v[40:43], v6 offset:24576\n"
      "v_rcp_f32 v23, v23\n"
      "v_mul_f32 v20, v72, v20\n"
      "v_mul_f32 v23, v71, v23\n"
      "v_cvt_pk_bf16_f32 v19, v22, v23\n"
      "v_mfma_f32_32x32x16_bf16 v[80:95], v[172:175], v[44:47], v[80:95]\n"
      "ds_read_b128 v[44:47], v7 offset:24576\n"
      "buffer_store_dwordx4 v[16:19], v29, s[20:23], s74 offen offset:0 sc1\n"
      "s_waitcnt lgkmcnt(4)\n"
      "v_mfma_f32_32x32x16_bf16 v[80:95], v[176:179], v[48:51], v[80:95]\n"
      "ds_read_b128 v[48:51], v4 offset:28672\n"
      "v_exp_f32 v20, v20\n"
      "v_mul_f32 v21, v73, v73\n"
      "v_fma_f32 v20, v20, v26, v26\n"
      "v_fma_f32 v21, v21, v25, v24\n"
      "v_mfma_f32_32x32x16_bf16 v[80:95], v[180:183], v[52:55], v[80:95]\n"
      "ds_read_b128 v[52:55], v5 offset:28672\n"
      "v_rcp_f32 v20, v20\n"
      "v_mul_f32 v21, v73, v21\n"
      "v_mul_f32 v20, v72, v20\n"
      "v_mfma_f32_32x32x16_bf16 v[80:95], v[184:187], v[56:59], v[80:95]\n"
      "ds_read_b128 v[56:59], v6 offset:28672\n"
      "v_exp_f32 v21, v21\n"
      "v_mul_f32 v22, v74, v74\n"
      "v_fma_f32 v21, v21, v26, v26\n"
      "v_fma_f32 v22, v22, v25, v24\n"
      "v_mfma_f32_32x32x16_bf16 v[80:95], v[188:191], v[60:63], v[80:95]\n"
      "ds_read_b128 v[60:63], v7 offset:28672\n"
      "v_rcp_f32 v21, v21\n"
      "v_mul_f32 v22, v74, v22\n"
      "v_mul_f32 v21, v73, v21\n"
      "s_waitcnt lgkmcnt(4)\n"
      "v_mfma_f32_32x32x16_bf16 v[80:95], v[192:195], v[32:35], v[80:95]\n"
      "ds_read_b128 v[32:35], v4 offset:32768\n"
      "v_cvt_pk_bf16_f32 v16, v20, v21\n"
      "v_exp_f32 v22, v22\n"
      "v_mul_f32 v23, v75, v75\n"
      "v_mfma_f32_32x32x16_bf16 v[80:95], v[196:199], v[36:39], v[80:95]\n"
      "ds_read_b128 v[36:39], v5 offset:32768\n"
      "v_fma_f32 v22, v22, v26, v26\n"
      "v_fma_f32 v23, v23, v25, v24\n"
      "v_rcp_f32 v22, v22\n"
      "v_mul_f32 v23, v75, v23\n"
      "v_mfma_f32_32x32x16_bf16 v[80:95], v[200:203], v[40:43], v[80:95]\n"
      "ds_read_b128 v[40:43], v6 offset:32768\n"
      "v_mul_f32 v22, v74, v22\n"
      "v_exp_f32 v23, v23\n"
      "v_mul_f32 v20, v76, v76\n"
      "v_fma_f32 v23, v23, v26, v26\n"
      "v_mfma_f32_32x32x16_bf16 v[80:95], v[204:207], v[44:47], v[80:95]\n"
      "ds_read_b128 v[44:47], v7 offset:32768\n"
      "v_fma_f32 v20, v20, v25, v24\n"
      "v_rcp_f32 v23, v23\n"
      "s_waitcnt lgkmcnt(4)\n"
      "v_mfma_f32_32x32x16_bf16 v[80:95], v[208:211], v[48:51], v[80:95]\n"
      "ds_read_b128 v[48:51], v4 offset:36864\n"
      "v_mul_f32 v20, v76, v20\n"
      "v_mul_f32 v23, v75, v23\n"
      "v_cvt_pk_bf16_f32 v17, v22, v23\n"
      "v_exp_f32 v20, v20\n"
      "ds_read_b32 v31, v2 offset:1048\n"
      "v_mfma_f32_32x32x16_bf16 v[80:95], v[212:215], v[52:55], v[80:95]\n"
      "ds_read_b128 v[52:55], v5 offset:36864\n"
      "v_mul_f32 v21, v77, v77\n"
      "v_fma_f32 v20, v20, v26, v26\n"
      "v_fma_f32 v21, v21, v25, v24\n"
      "v_add_u32 v4, 4294885376, v4\n"
      "v_mfma_f32_32x32x16_bf16 v[80:95], v[216:219], v[56:59], v[80:95]\n"
      "ds_read_b128 v[56:59], v6 offset:36864\n"
      "v_rcp_f32 v20, v20\n"
      "v_mul_f32 v21, v77, v21\n"
      "v_add_u32 v5, 4294885376, v5\n"
      "v_mfma_f32_32x32x16_bf16 v[80:95], v[220:223], v[60:63], v[80:95]\n"
      "ds_read_b128 v[60:63], v7 offset:36864\n"
      "v_mul_f32 v20, v76, v20\n"
      "v_exp_f32 v21, v21\n"
      "v_add_u32 v6, 4294885376, v6\n"
      "s_waitcnt lgkmcnt(3)\n"
      "v_mfma_f32_32x32x16_bf16 v[80:95], v[224:227], v[32:35], v[80:95]\n"
      "v_readfirstlane_b32 s2, v31\n"
      "s_cmp_lt_u32 s2, 4\n"
      "s_cbranch_scc1 .Lw_126\n"
      ".Lw_127:\n"
      "ds_read_b128 v[32:35], v4 offset:0\n"
      "v_add_u32 v7, 4294885376, v7\n"
      "v_mfma_f32_32x32x16_bf16 v[80:95], v[228:231], v[36:39], v[80:95]\n"
      "ds_read_b128 v[36:39], v5 offset:0\n"
      "v_mul_f32 v22, v78, v78\n"
      "v_fma_f32 v21, v21, v26, v26\n"
      "v_fma_f32 v22, v22, v25, v24\n"
      "v_rcp_f32 v21, v21\n"
      "v_mfma_f32_32x32x16_bf16 v[80:95], v[232:235], v[40:43], v[80:95]\n"
      "ds_read_b128 v[40:43], v6 offset:0\n"
      "v_mul_f32 v22, v78, v22\n"
      "v_mul_f32 v21, v77, v21\n"
      "v_cvt_pk_bf16_f32 v18, v20, v21\n"
      "v_exp_f32 v22, v22\n"
      "v_mfma_f32_32x32x16_bf16 v[80:95], v[236:239], v[44:47], v[80:95]\n"
      "ds_read_b128 v[44:47], v7 offset:0\n"
      "v_mul_f32 v23, v79, v79\n"
      "v_fma_f32 v22, v22, v26, v26\n"
      "v_fma_f32 v23, v23, v25, v24\n"
      "s_waitcnt lgkmcnt(4)\n"
      "v_mfma_f32_32x32x16_bf16 v[80:95], v[240:243], v[48:51], v[80:95]\n"
      "ds_read_b128 v[48:51], v4 offset:4096\n"
      "v_rcp_f32 v22, v22\n"
      "v_mul_f32 v23, v79, v23\n"
      "v_mul_f32 v22, v78, v22\n"
      "v_mfma_f32_32x32x16_bf16 v[80:95], v[244:247], v[52:55], v[80:95]\n"
      "ds_read_b128 v[52:55], v5 offset:4096\n"
      "v_exp_f32 v23, v23\n"
      "s_nop 0\n"
      "v_fma_f32 v23, v23, v26, v26\n"
      "v_rcp_f32 v23, v23\n"
      "v_mfma_f32_32x32x16_bf16 v[80:95], v[248:251], v[56:59], v[80:95]\n"
      "ds_read_b128 v[56:59], v6 offset:4096\n"
      "v_mul_f32 v23, v79, v23\n"
      "v_cvt_pk_bf16_f32 v19, v22, v23\n"
      "buffer_store_dwordx4 v[16:19], v29, s[20:23], s74 offen offset:256 sc1\n"
      "ds_read_b128 v[24:27], v30 offset:1024\n"
      "v_mfma_f32_32x32x16_bf16 v[64:79], v[252:255], v[60:63], v[80:95]\n"
      "ds_read_b128 v[60:63], v7 offset:4096\n"
      "s_nop 3\n"
      "v_mfma_f32_32x32x16_bf16 v[80:95], v[8:11], v[12:15], 0\n"
      "s_cbranch_vccnz .Lg_129\n"
      ".Lg_130:\n"
      "ds_read_b64 v[12:13], v30 offset:3088\n"
      "s_nop 0\n"
      "s_waitcnt lgkmcnt(2)\n"
      "v_mfma_f32_32x32x16_bf16 v[80:95], v[96:99], v[32:35], v[80:95]\n"
      "ds_read_b128 v[32:35], v4 offset:8192\n"
      "v_mul_f32 v20, v64, v64\n"
      "v_fma_f32 v20, v20, v25, v24\n"
      "v_mul_f32 v20, v64, v20\n"
      "v_exp_f32 v20, v20\n"
      "v_mfma_f32_32x32x16_bf16 v[80:95], v[100:103], v[36:39], v[80:95]\n"
      "ds_read_b128 v[36:39], v5 offset:8192\n"
      "v_mul_f32 v21, v65, v65\n"
      "v_fma_f32 v20, v20, v26, v26\n"
      "v_fma_f32 v21, v21, v25, v24\n"
      "v_mfma_f32_32x32x16_bf16 v[80:95], v[104:107], v[40:43], v[80:95]\n"
      "ds_read_b128 v[40:43], v6 offset:8192\n"
      "v_rcp_f32 v20, v20\n"
      "v_mul_f32 v21, v65, v21\n"
      "v_mul_f32 v20, v64, v20\n"
      "v_exp_f32 v21, v21\n"
      "v_mfma_f32_32x32x16_bf16 v[80:95], v[108:111], v[44:47], v[80:95]\n"
      "ds_read_b128 v[44:47], v7 offset:8192\n"
      "v_mul_f32 v22, v66, v66\n"
      "v_fma_f32 v21, v21, v26, v26\n"
      "v_fma_f32 v22, v22, v25, v24\n"
      "s_waitcnt lgkmcnt(4)\n"
      "v_mfma_f32_32x32x16_bf16 v[80:95], v[112:115], v[48:51], v[80:95]\n"
      "ds_read_b128 v[48:51], v4 offset:12288\n"
      "v_rcp_f32 v21, v21\n"
      "v_mul_f32 v22, v66, v22\n"
      "v_mul_f32 v21, v65, v21\n"
      "v_cvt_pk_bf16_f32 v16, v20, v21\n"
      "v_mfma_f32_32x32x16_bf16 v[80:95], v[116:119], v[52:55], v[80:95]\n"
      "ds_read_b128 v[52:55], v5 offset:12288\n"
      "v_exp_f32 v22, v22\n"
      "v_mul_f32 v23, v67, v67\n"
      "v_fma_f32 v22, v22, v26, v26\n"
      "v_mfma_f32_32x32x16_bf16 v[80:95], v[120:123], v[56:59], v[80:95]\n"
      "ds_read_b128 v[56:59], v6 offset:12288\n"
      "v_fma_f32 v23, v23, v25, v24\n"
      "v_rcp_f32 v22, v22\n"
      "v_mul_f32 v23, v67, v23\n"
      "v_mul_f32 v22, v66, v22\n"
      "v_mfma_f32_32x32x16_bf16 v[80:95], v[124:127], v[60:63], v[80:95]\n"
      "ds_read_b128 v[60:63], v7 offset:12288\n"
      "v_exp_f32 v23, v23\n"
      "v_mul_f32 v20, v68, v68\n"
      "s_waitcnt lgkmcnt(4)\n"
      "v_mfma_f32_32x32x16_bf16 v[80:95], v[128:131], v[32:35], v[80:95]\n"
      "ds_read_b128 v[32:35], v4 offset:16384\n"
      "v_fma_f32 v23, v23, v26, v26\n"
      "v_fma_f32 v20, v20, v25, v24\n"
      "v_rcp_f32 v23, v23\n"
      "ds_read_b32 v31, v2 offset:2072\n"
      "v_mfma_f32_32x32x16_bf16 v[80:95], v[132:135], v[36:39], v[80:95]\n"
      "ds_read_b128 v[36:39], v5 offset:16384\n"
      "v_mul_f32 v20, v68, v20\n"
      "v_mul_f32 v23, v67, v23\n"
      "v_cvt_pk_bf16_f32 v17, v22, v23\n"
      "v_exp_f32 v20, v20\n"
      "v_mfma_f32_32x32x16_bf16 v[80:95], v[136:139], v[40:43], v[80:95]\n"
      "ds_read_b128 v[40:43], v6 offset:16384\n"
      "v_mul_f32 v21, v69, v69\n"
      "v_fma_f32 v20, v20, v26, v26\n"
      "v_fma_f32 v21, v21, v25, v24\n"
      "v_mfma_f32_32x32x16_bf16 v[80:95], v[140:143], v[44:47], v[80:95]\n"
      "ds_read_b128 v[44:47], v7 offset:16384\n"
      "v_rcp_f32 v20, v20\n"
      "v_mul_f32 v21, v69, v21\n"
      "v_mul_f32 v20, v68, v20\n"
      "s_waitcnt lgkmcnt(3)\n"
      "v_mfma_f32_32x32x16_bf16 v[80:95], v[144:147], v[48:51], v[80:95]\n"
      "v_readfirstlane_b32 s2, v31\n"
      "s_cmp_lt_u32 s2, 4\n"
      "s_cbranch_scc1 .Lw_131\n"
      ".Lw_132:\n"
      "ds_read_b128 v[48:51], v4 offset:20480\n"
      "v_exp_f32 v21, v21\n"
      "v_mfma_f32_32x32x16_bf16 v[80:95], v[148:151], v[52:55], v[80:95]\n"
      "ds_read_b128 v[52:55], v5 offset:20480\n"
      "v_mul_f32 v22, v70, v70\n"
      "v_fma_f32 v21, v21, v26, v26\n"
      "v_fma_f32 v22, v22, v25, v24\n"
      "v_mfma_f32_32x32x16_bf16 v[80:95], v[152:155], v[56:59], v[80:95]\n"
      "ds_read_b128 v[56:59], v6 offset:20480\n"
      "v_rcp_f32 v21, v21\n"
      "v_mul_f32 v22, v70, v22\n"
      "v_mul_f32 v21, v69, v21\n"
      "v_cvt_pk_bf16_f32 v18, v20, v21\n"
      "v_mfma_f32_32x32x16_bf16 v[80:95], v[156:159], v[60:63], v[80:95]\n"
      "ds_read_b128 v[60:63], v7 offset:20480\n"
      "v_exp_f32 v22, v22\n"
      "v_mul_f32 v23, v71, v71\n"
      "v_fma_f32 v22, v22, v26, v26\n"
      "s_waitcnt lgkmcnt(4)\n"
      "v_mfma_f32_32x32x16_bf16 v[80:95], v[160:163], v[32:35], v[80:95]\n"
      "ds_read_b128 v[32:35], v4 offset:24576\n"
      "v_fma_f32 v23, v23, v25, v24\n"
      "v_rcp_f32 v22, v22\n"
      "v_mul_f32 v23, v71, v23\n"
      "v_mfma_f32_32x32x16_bf16 v[80:95], v[164:167], v[36:39], v[80:95]\n"
      "ds_read_b128 v[36:39], v5 offset:24576\n"
      "v_mul_f32 v22, v70, v22\n"
      "v_exp_f32 v23, v23\n"
      "v_mul_f32 v20, v72, v72\n"
      "v_fma_f32 v23, v23, v26, v26\n"
      "v_mfma_f32_32x32x16_bf16 v[80:95], v[168:171], v[40:43], v[80:95]\n"
      "ds_read_b128 v[40:43], v6 offset:24576\n"
      "v_fma_f32 v20, v20, v25, v24\n"
      "v_rcp_f32 v23, v23\n"
      "v_mul_f32 v20, v72, v20\n"
      "v_mfma_f32_32x32x16_bf16 v[80:95], v[172:175], v[44:47], v[80:95]\n"
      "ds_read_b128 v[44:47], v7 offset:24576\n"
      "v_mul_f32 v23, v71, v23\n"
      "v_cvt_pk_bf16_f32 v19, v22, v23\n"
      "buffer_store_dwordx4 v[16:19], v29, s[20:23], s75 offen offset:0 sc1\n"
      "s_waitcnt lgkmcnt(4)\n"
      "v_mfma_f32_32x32x16_bf16 v[80:95], v[176:179], v[48:51], v[80:95]\n"
      "ds_read_b128 v[48:51], v4 offset:28672\n"
      "v_exp_f32 v20, v20\n"
      "v_mul_f32 v21, v73, v73\n"
      "v_mfma_f32_32x32x16_bf16 v[80:95], v[180:183], v[52:55], v[80:95]\n"
      "ds_read_b128 v[52:55], v5 offset:28672\n"
      "v_fma_f32 v20, v20, v26, v26\n"
      "v_fma_f32 v21, v21, v25, v24\n"
      "v_rcp_f32 v20, v20\n"
      "v_mul_f32 v21, v73, v21\n"
      "v_mfma_f32_32x32x16_bf16 v[80:95], v[184:187], v[56:59], v[80:95]\n"
      "ds_read_b128 v[56:59], v6 offset:28672\n"
      "v_mul_f32 v20, v72, v20\n"
      "v_exp_f32 v21, v21\n"
      "v_mul_f32 v22, v74, v74\n"
      "v_mfma_f32_32x32x16_bf16 v[80:95], v[188:191], v[60:63], v[80:95]\n"
      "ds_read_b128 v[60:63], v7 offset:28672\n"
      "v_fma_f32 v21, v21, v26, v26\n"
      "v_fma_f32 v22, v22, v25, v24\n"
      "v_rcp_f32 v21, v21\n"
      "s_waitcnt lgkmcnt(4)\n"
      "v_mfma_f32_32x32x16_bf16 v[80:95], v[192:195], v[32:35], v[80:95]\n"
      "ds_read_b128 v[32:35], v4 offset:32768\n"
      "v_mul_f32 v22, v74, v22\n"
      "v_mul_f32 v21, v73, v21\n"
      "v_cvt_pk_bf16_f32 v16, v20, v21\n"
      "v_exp_f32 v22, v22\n"
      "v_mfma_f32_32x32x16_bf16 v[80:95], v[196:199], v[36:39], v[80:95]\n"
      "ds_read_b128 v[36:39], v5 offset:32768\n"
      "v_mul_f32 v23, v75, v75\n"
      "v_fma_f32 v22, v22, v26, v26\n"
      "v_fma_f32 v23, v23, v25, v24\n"
      "v_mfma_f32_32x32x16_bf16 v[80:95], v[200:203], v[40:43], v[80:95]\n"
      "ds_read_b128 v[40:43], v6 offset:32768\n"
      "v_rcp_f32 v22, v22\n"
      "v_mul_f32 v23, v75, v23\n"
      "v_mul_f32 v22, v74, v22\n"
      "v_exp_f32 v23, v23\n"
      "v_mfma_f32_32x32x16_bf16 v[80:95], v[204:207], v[44:47], v[80:95]\n"
      "ds_read_b128 v[44:47], v7 offset:32768\n"
      "v_mul_f32 v20, v76, v76\n"
      "v_fma_f32 v23, v23, v26, v26\n"
      "v_fma_f32 v20, v20, v25, v24\n"
      "s_waitcnt lgkmcnt(4)\n"
      "v_mfma_f32_32x32x16_bf16 v[80:95], v[208:211], v[48:51], v[80:95]\n"
      "ds_read_b128 v[48:51], v4 offset:36864\n"
      "v_rcp_f32 v23, v23\n"
      "v_mul_f32 v20, v76, v20\n"
      "ds_read_b32 v31, v2 offset:3096\n"
      "v_mfma_f32_32x32x16_bf16 v[80:95], v[212:215], v[52:55], v[80:95]\n"
      "ds_read_b128 v[52:55], v5 offset:36864\n"
      "v_mul_f32 v23, v75, v23\n"
      "v_cvt_pk_bf16_f32 v17, v22, v23\n"
      "v_exp_f32 v20, v20\n"
      "v_mul_f32 v21, v77, v77\n"
      "v_mfma_f32_32x32x16_bf16 v[80:95], v[216:219], v[56:59], v[80:95]\n"
      "ds_read_b128 v[56:59], v6 offset:36864\n"
      "v_fma_f32 v20, v20, v26, v26\n"
      "v_fma_f32 v21, v21, v25, v24\n"
      "v_rcp_f32 v20, v20\n"
      "v_mfma_f32_32x32x16_bf16 v[80:95], v[220:223], v[60:63], v[80:95]\n"
      "ds_read_b128 v[60:63], v7 offset:36864\n"
      "v_mul_f32 v21, v77, v21\n"
      "v_mul_f32 v20, v76, v20\n"
      "v_exp_f32 v21, v21\n"
      "s_waitcnt lgkmcnt(3)\n"
      "v_mfma_f32_32x32x16_bf16 v[80:95], v[224:227], v[32:35], v[80:95]\n"
      "v_readfirstlane_b32 s2, v31\n"
      "s_cmp_lt_u32 s2, 4\n"
      "s_cbranch_scc1 .Lw_134\n"
      ".Lw_135:\n"
      "ds_read_b128 v[32:35], v4 offset:40960\n"
      "v_mul_f32 v22, v78, v78\n"
      "v_mfma_f32_32x32x16_bf16 v[80:95], v[228:231], v[36:39], v[80:95]\n"
      "ds_read_b128 v[36:39], v5 offset:40960\n"
      "v_fma_f32 v21, v21, v26, v26\n"
      "v_fma_f32 v22, v22, v25, v24\n"
      "v_rcp_f32 v21, v21\n"
      "v_mfma_f32_32x32x16_bf16 v[80:95], v[232:235], v[40:43], v[80:95]\n"
      "ds_read_b128 v[40:43], v6 offset:40960\n"
      "v_mul_f32 v22, v78, v22\n"
      "v_mul_f32 v21, v77, v21\n"
      "v_cvt_pk_bf16_f32 v18, v20, v21\n"
      "v_exp_f32 v22, v22\n"
      "v_mfma_f32_32x32x16_bf16 v[80:95], v[236:239], v[44:47], v[80:95]\n"
      "ds_read_b128 v[44:47], v7 offset:40960\n"
      "v_mul_f32 v23, v79, v79\n"
      "v_fma_f32 v22, v22, v26, v26\n"
      "v_fma_f32 v23, v23, v25, v24\n"
      "s_waitcnt lgkmcnt(4)\n"
      "v_mfma_f32_32x32x16_bf16 v[80:95], v[240:243], v[48:51], v[80:95]\n"
      "ds_read_b128 v[48:51], v4 offset:45056\n"
      "v_rcp_f32 v22, v22\n"
      "v_mul_f32 v23, v79, v23\n"
      "v_mul_f32 v22, v78, v22\n"
      "v_exp_f32 v23, v23\n"
      "v_mfma_f32_32x32x16_bf16 v[80:95], v[244:247], v[52:55], v[80:95]\n"
      "ds_read_b128 v[52:55], v5 offset:45056\n"
      "v_fma_f32 v23, v23, v26, v26\n"
      "v_rcp_f32 v23, v23\n"
      "s_nop 0\n"
      "v_mul_f32 v23, v79, v23\n"
      "v_mfma_f32_32x32x16_bf16 v[80:95], v[248:251], v[56:59], v[80:95]\n"
      "ds_read_b128 v[56:59], v6 offset:45056\n"
      "v_cvt_pk_bf16_f32 v19, v22, v23\n"
      "buffer_store_dwordx4 v[16:19], v29, s[20:23], s75 offen offset:256 sc1\n"
      "ds_read_b128 v[24:27], v30 offset:2048\n"
      "v_mfma_f32_32x32x16_bf16 v[64:79], v[252:255], v[60:63], v[80:95]\n"
      "ds_read_b128 v[60:63], v7 offset:45056\n"
      "s_nop 3\n"
      "v_mfma_f32_32x32x16_bf16 v[80:95], v[8:11], v[12:15], 0\n"
      "s_cbranch_vccnz .Lg_137\n"
      ".Lg_138:\n"
      "s_nop 1\n"
      "s_waitcnt lgkmcnt(1)\n"
      "v_mfma_f32_32x32x16_bf16 v[80:95], v[96:99], v[32:35], v[80:95]\n"
      "ds_read_b128 v[32:35], v4 offset:49152\n"
      "v_mul_f32 v20, v64, v64\n"
      "v_fma_f32 v20, v20, v25, v24\n"
      "v_mul_f32 v20, v64, v20\n"
      "v_exp_f32 v20, v20\n"
      "v_mfma_f32_32x32x16_bf16 v[80:95], v[100:103], v[36:39], v[80:95]\n"
      "ds_read_b128 v[36:39], v5 offset:49152\n"
      "v_mul_f32 v21, v65, v65\n"
      "v_fma_f32 v20, v20, v26, v26\n"
      "v_fma_f32 v21, v21, v25, v24\n"
      "v_mov_b64 v[12:13], s[58:59]\n"
      "v_mfma_f32_32x32x16_bf16 v[80:95], v[104:107], v[40:43], v[80:95]\n"
      "ds_read_b128 v[40:43], v6 offset:49152\n"
      "v_rcp_f32 v20, v20\n"
      "v_mul_f32 v21, v65, v21\n"
      "v_mul_f32 v20, v64, v20\n"
      "v_mfma_f32_32x32x16_bf16 v[80:95], v[108:111], v[44:47], v[80:95]\n"
      "ds_read_b128 v[44:47], v7 offset:49152\n"
      "v_exp_f32 v21, v21\n"
      "v_mul_f32 v22, v66, v66\n"
      "v_fma_f32 v21, v21, v26, v26\n"
      "v_fma_f32 v22, v22, v25, v24\n"
      "s_waitcnt lgkmcnt(4)\n"
      "v_mfma_f32_32x32x16_bf16 v[80:95], v[112:115], v[48:51], v[80:95]\n"
      "ds_read_b128 v[48:51], v4 offset:53248\n"
      "v_rcp_f32 v21, v21\n"
      "v_mul_f32 v22, v66, v22\n"
      "v_mul_f32 v21, v65, v21\n"
      "v_mfma_f32_32x32x16_bf16 v[80:95], v[116:119], v[52:55], v[80:95]\n"
      "ds_read_b128 v[52:55], v5 offset:53248\n"
      "v_cvt_pk_bf16_f32 v16, v20, v21\n"
      "v_exp_f32 v22, v22\n"
      "v_mul_f32 v23, v67, v67\n"
      "v_fma_f32 v22, v22, v26, v26\n"
      "v_mfma_f32_32x32x16_bf16 v[80:95], v[120:123], v[56:59], v[80:95]\n"
      "ds_read_b128 v[56:59], v6 offset:53248\n"
      "v_fma_f32 v23, v23, v25, v24\n"
      "v_rcp_f32 v22, v22\n"
      "v_mul_f32 v23, v67, v23\n"
      "v_mfma_f32_32x32x16_bf16 v[80:95], v[124:127], v[60:63], v[80:95]\n"
      "ds_read_b128 v[60:63], v7 offset:53248\n"
      "v_mul_f32 v22, v66, v22\n"
      "v_exp_f32 v23, v23\n"
      "v_mul_f32 v20, v68, v68\n"
      "s_waitcnt lgkmcnt(4)\n"
      "v_mfma_f32_32x32x16_bf16 v[80:95], v[128:131], v[32:35], v[80:95]\n"
      "ds_read_b128 v[32:35], v4 offset:57344\n"
      "v_fma_f32 v23, v23, v26, v26\n"
      "v_fma_f32 v20, v20, v25, v24\n"
      "v_rcp_f32 v23, v23\n"
      "v_mul_f32 v20, v68, v20\n"
      "v_mfma_f32_32x32x16_bf16 v[80:95], v[132:135], v[36:39], v[80:95]\n"
      "ds_read_b128 v[36:39], v5 offset:57344\n"
      "v_mul_f32 v23, v67, v23\n"
      "v_cvt_pk_bf16_f32 v17, v22, v23\n"
      "v_exp_f32 v20, v20\n"
      "v_mfma_f32_32x32x16_bf16 v[80:95], v[136:139], v[40:43], v[80:95]\n"
      "ds_read_b128 v[40:43], v6 offset:57344\n"
      "v_mul_f32 v21, v69, v69\n"
      "v_fma_f32 v20, v20, v26, v26\n"
      "v_fma_f32 v21, v21, v25, v24\n"
      "v_rcp_f32 v20, v20\n"
      "v_mfma_f32_32x32x16_bf16 v[80:95], v[140:143], v[44:47], v[80:95]\n"
      "ds_read_b128 v[44:47], v7 offset:57344\n"
      "v_mul_f32 v21, v69, v21\n"
      "v_mul_f32 v20, v68, v20\n"
      "v_exp_f32 v21, v21\n"
      "s_waitcnt lgkmcnt(4)\n"
      "v_mfma_f32_32x32x16_bf16 v[80:95], v[144:147], v[48:51], v[80:95]\n"
      "ds_read_b128 v[48:51], v4 offset:61440\n"
      "v_mul_f32 v22, v70, v70\n"
      "v_fma_f32 v21, v21, v26, v26\n"
      "v_fma_f32 v22, v22, v25, v24\n"
      "v_rcp_f32 v21, v21\n"
      "v_mfma_f32_32x32x16_bf16 v[80:95], v[148:151], v[52:55], v[80:95]\n"
      "ds_read_b128 v[52:55], v5 offset:61440\n"
      "v_mul_f32 v22, v70, v22\n"
      "v_mul_f32 v21, v69, v21\n"
      "v_cvt_pk_bf16_f32 v18, v20, v21\n"
      "v_add_u32 v4, 40960, v4\n"
      "v_mfma_f32_32x32x16_bf16 v[80:95], v[152:155], v[56:59], v[80:95]\n"
      "ds_read_b128 v[56:59], v6 offset:61440\n"
      "v_exp_f32 v22, v22\n"
      "v_mul_f32 v23, v71, v71\n"
      "v_add_u32 v5, 40960, v5\n"
      "v_mfma_f32_32x32x16_bf16 v[80:95], v[156:159], v[60:63], v[80:95]\n"
      "ds_read_b128 v[60:63], v7 offset:61440\n"
      "v_fma_f32 v22, v22, v26, v26\n"
      "v_fma_f32 v23, v23, v25, v24\n"
      "v_rcp_f32 v22, v22\n"
      "v_add_u32 v6, 40960, v6\n"
      "s_waitcnt lgkmcnt(4)\n"
      "v_mfma_f32_32x32x16_bf16 v[80:95], v[160:163], v[32:35], v[80:95]\n"
      "ds_read_b128 v[32:35], v4 offset:24576\n"
      "v_mul_f32 v23, v71, v23\n"
      "v_mul_f32 v22, v70, v22\n"
      "v_add_u32 v7, 40960, v7\n"
      "v_mfma_f32_32x32x16_bf16 v[80:95], v[164:167], v[36:39], v[80:95]\n"
      "ds_read_b128 v[36:39], v5 offset:24576\n"
      "v_exp_f32 v23, v23\n"
      "v_mul_f32 v20, v72, v72\n"
      "v_fma_f32 v23, v23, v26, v26\n"
      "v_fma_f32 v20, v20, v25, v24\n"
      "v_mfma_f32_32x32x16_bf16 v[80:95], v[168:171], v[40:43], v[80:95]\n"
      "ds_read_b128 v[40:43], v6 offset:24576\n"
      "v_rcp_f32 v23, v23\n"
      "v_mul_f32 v20, v72, v20\n"
      "v_mul_f32 v23, v71, v23\n"
      "v_cvt_pk_bf16_f32 v19, v22, v23\n"
      "v_mfma_f32_32x32x16_bf16 v[80:95], v[172:175], v[44:47], v[80:95]\n"
      "ds_read_b128 v[44:47], v7 offset:24576\n"
      "buffer_store_dwordx4 v[16:19], v29, s[20:23], s76 offen offset:0 sc1\n"
      "s_waitcnt lgkmcnt(4)\n"
      "v_mfma_f32_32x32x16_bf16 v[80:95], v[176:179], v[48:51], v[80:95]\n"
      "ds_read_b128 v[48:51], v4 offset:28672\n"
      "v_exp_f32 v20, v20\n"
      "v_mul_f32 v21, v73, v73\n"
      "v_fma_f32 v20, v20, v26, v26\n"
      "v_fma_f32 v21, v21, v25, v24\n"
      "v_mfma_f32_32x32x16_bf16 v[80:95], v[180:183], v[52:55], v[80:95]\n"
      "ds_read_b128 v[52:55], v5 offset:28672\n"
      "v_rcp_f32 v20, v20\n"
      "v_mul_f32 v21, v73, v21\n"
      "v_mul_f32 v20, v72, v20\n"
      "v_mfma_f32_32x32x16_bf16 v[80:95], v[184:187], v[56:59], v[80:95]\n"
      "ds_read_b128 v[56:59], v6 offset:28672\n"
      "v_exp_f32 v21, v21\n"
      "v_mul_f32 v22, v74, v74\n"
      "v_fma_f32 v21, v21, v26, v26\n"
      "v_fma_f32 v22, v22, v25, v24\n"
      "v_mfma_f32_32x32x16_bf16 v[80:95], v[188:191], v[60:63], v[80:95]\n"
      "ds_read_b128 v[60:63], v7 offset:28672\n"
      "v_rcp_f32 v21, v21\n"
      "v_mul_f32 v22, v74, v22\n"
      "v_mul_f32 v21, v73, v21\n"
      "s_waitcnt lgkmcnt(4)\n"
      "v_mfma_f32_32x32x16_bf16 v[80:95], v[192:195], v[32:35], v[80:95]\n"
      "ds_read_b128 v[32:35], v4 offset:32768\n"
      "v_cvt_pk_bf16_f32 v16, v20, v21\n"
      "v_exp_f32 v22, v22\n"
      "v_mul_f32 v23, v75, v75\n"
      "v_fma_f32 v22, v22, v26, v26\n"
      "v_mfma_f32_32x32x16_bf16 v[80:95], v[196:199], v[36:39], v[80:95]\n"
      "ds_read_b128 v[36:39], v5 offset:32768\n"
      "s_waitcnt vmcnt(1)\n"
      "s_mov_b64 exec, 1\n"
      "buffer_store_dwordx2 v[12:13], off, s[20:23], s79 sc1\n"
      "s_mov_b64 exec, -1\n"
      "v_mfma_f32_32x32x16_bf16 v[80:95], v[200:203], v[40:43], v[80:95]\n"
      "ds_read_b128 v[40:43], v6 offset:32768\n"
      "v_fma_f32 v23, v23, v25, v24\n"
      "v_rcp_f32 v22, v22\n"
      "ds_add_u32 v2, v3 offset:4168\n"
      "v_mfma_f32_32x32x16_bf16 v[80:95], v[204:207], v[44:47], v[80:95]\n"
      "ds_read_b128 v[44:47], v7 offset:32768\n"
      "v_mul_f32 v23, v75, v23\n"
      "v_mul_f32 v22, v74, v22\n"
      "v_exp_f32 v23, v23\n"
      "s_waitcnt lgkmcnt(4)\n"
      "v_mfma_f32_32x32x16_bf16 v[80:95], v[208:211], v[48:51], v[80:95]\n"
      "ds_read_b128 v[48:51], v4 offset:36864\n"
      "v_mul_f32 v20, v76, v76\n"
      "v_fma_f32 v23, v23, v26, v26\n"
      "v_fma_f32 v20, v20, v25, v24\n"
      "v_rcp_f32 v23, v23\n"
      "v_mfma_f32_32x32x16_bf16 v[80:95], v[212:215], v[52:55], v[80:95]\n"
      "ds_read_b128 v[52:55], v5 offset:36864\n"
      "v_mul_f32 v20, v76, v20\n"
      "v_mul_f32 v23, v75, v23\n"
      "v_cvt_pk_bf16_f32 v17, v22, v23\n"
      "v_exp_f32 v20, v20\n"
      "v_mfma_f32_32x32x16_bf16 v[80:95], v[216:219], v[56:59], v[80:95]\n"
      "ds_read_b128 v[56:59], v6 offset:36864\n"
      "v_mul_f32 v21, v77, v77\n"
      "v_fma_f32 v20, v20, v26, v26\n"
      "v_fma_f32 v21, v21, v25, v24\n"
      "v_mfma_f32_32x32x16_bf16 v[80:95], v[220:223], v[60:63], v[80:95]\n"
      "ds_read_b128 v[60:63], v7 offset:36864\n"
      "v_rcp_f32 v20, v20\n"
      "v_mul_f32 v21, v77, v21\n"
      "v_mul_f32 v20, v76, v20\n"
      "s_waitcnt lgkmcnt(4)\n"
      "v_mfma_f32_32x32x16_bf16 v[80:95], v[224:227], v[32:35], v[80:95]\n"
      "v_exp_f32 v21, v21\n"
      "v_mul_f32 v22, v78, v78\n"
      "v_fma_f32 v21, v21, v26, v26\n"
      "v_fma_f32 v22, v22, v25, v24\n"
      "v_rcp_f32 v21, v21\n"
      "v_mfma_f32_32x32x16_bf16 v[80:95], v[228:231], v[36:39], v[80:95]\n"
      "v_mul_f32 v22, v78, v22\n"
      "v_mul_f32 v21, v77, v21\n"
      "v_cvt_pk_bf16_f32 v18, v20, v21\n"
      "v_xor_b32 v31, s86, v28\n"
      "ds_read_b64 v[218:219], v31\n"
      "v_mfma_f32_32x32x16_bf16 v[80:95], v[232:235], v[40:43], v[80:95]\n"
      "v_exp_f32 v22, v22\n"
      "v_mul_f32 v23, v79, v79\n"
      "v_xor_b32 v31, s87, v28\n"
      "ds_read_b64 v[220:221], v31\n"
      "v_mfma_f32_32x32x16_bf16 v[80:95], v[236:239], v[44:47], v[80:95]\n"
      "v_fma_f32 v22, v22, v26, v26\n"
      "v_fma_f32 v23, v23, v25, v24\n"
      "v_rcp_f32 v22, v22\n"
      "v_xor_b32 v31, s88, v28\n"
      "ds_read_b64 v[222:223], v31\n"
      "s_waitcnt lgkmcnt(3)\n"
      "v_mfma_f32_32x32x16_bf16 v[80:95], v[240:243], v[48:51], v[80:95]\n"
      "v_mul_f32 v23, v79, v23\n"
      "v_mul_f32 v22, v78, v22\n"
      "v_xor_b32 v31, s89, v28\n"
      "ds_read_b64 v[224:225], v31\n"
      "v_mfma_f32_32x32x16_bf16 v[80:95], v[244:247], v[52:55], v[80:95]\n"
      "v_exp_f32 v23, v23\n"
      "s_nop 0\n"
      "v_fma_f32 v23, v23, v26, v26\n"
      "v_rcp_f32 v23, v23\n"
      "v_xor_b32 v31, s90, v28\n"
      "ds_read_b64 v[226:227], v31\n"
      "v_mfma_f32_32x32x16_bf16 v[80:95], v[248:251], v[56:59], v[80:95]\n"
      "v_mul_f32 v23, v79, v23\n"
      "v_cvt_pk_bf16_f32 v19, v22, v23\n"
      "buffer_store_dwordx4 v[16:19], v29, s[20:23], s76 offen offset:256 sc1\n"
      "ds_read_b128 v[24:27], v30 offset:3072\n"
      "v_mfma_f32_32x32x16_bf16 v[64:79], v[252:255], v[60:63], v[80:95]\n"
      "ds_add_u32 v2, v3 offset:4160\n"
      "s_cbranch_vccnz .Lg_139\n"
      ".Lg_140:\n"
      "s_waitcnt lgkmcnt(0)\n"
      "v_mov_b32 v32, v24\n"
      "v_mov_b32 v33, v24\n"
      "v_mov_b32 v34, v25\n"
      "v_mov_b32 v35, v25\n"
      "v_mov_b32 v36, v26\n"
      "v_mov_b32 v37, v26\n"
      "s_nop 2\n"
      "v_pk_mul_f32 v[40:41], v[64:65], v[64:65]\n"
      "v_pk_mul_f32 v[42:43], v[66:67], v[66:67]\n"
      "v_pk_mul_f32 v[44:45], v[68:69], v[68:69]\n"
      "v_pk_mul_f32 v[46:47], v[70:71], v[70:71]\n"
      "v_pk_fma_f32 v[40:41], v[40:41], v[34:35], v[32:33]\n"
      "v_pk_fma_f32 v[42:43], v[42:43], v[34:35], v[32:33]\n"
      "v_pk_fma_f32 v[44:45], v[44:45], v[34:35], v[32:33]\n"
      "v_pk_fma_f32 v[46:47], v[46:47], v[34:35], v[32:33]\n"
      "v_pk_mul_f32 v[40:41], v[64:65], v[40:41]\n"
      "v_pk_mul_f32 v[42:43], v[66:67], v[42:43]\n"
      "v_pk_mul_f32 v[44:45], v[68:69], v[44:45]\n"
      "v_pk_mul_f32 v[46:47], v[70:71], v[46:47]\n"
      "v_exp_f32 v40, v40\n"
      "v_exp_f32 v41, v41\n"
      "v_exp_f32 v42, v42\n"
      "v_exp_f32 v43, v43\n"
      "v_exp_f32 v44, v44\n"
      "v_exp_f32 v45, v45\n"
      "v_exp_f32 v46, v46\n"
      "v_exp_f32 v47, v47\n"
      "v_pk_fma_f32 v[40:41], v[40:41], v[36:37], v[36:37]\n"
      "v_pk_fma_f32 v[42:43], v[42:43], v[36:37], v[36:37]\n"
      "v_pk_fma_f32 v[44:45], v[44:45], v[36:37], v[36:37]\n"
      "v_pk_fma_f32 v[46:47], v[46:47], v[36:37], v[36:37]\n"
      "v_rcp_f32 v40, v40\n"
      "v_rcp_f32 v41, v41\n"
      "v_rcp_f32 v42, v42\n"
      "v_rcp_f32 v43, v43\n"
      "v_rcp_f32 v44, v44\n"
      "v_rcp_f32 v45, v45\n"
      "v_rcp_f32 v46, v46\n"
      "v_rcp_f32 v47, v47\n"
      "v_pk_mul_f32 v[40:41], v[64:65], v[40:41]\n"
      "v_pk_mul_f32 v[42:43], v[66:67], v[42:43]\n"
      "v_pk_mul_f32 v[44:45], v[68:69], v[44:45]\n"
      "v_pk_mul_f32 v[46:47], v[70:71], v[46:47]\n"
      "v_cvt_pk_bf16_f32 v56, v40, v41\n"
      "v_cvt_pk_bf16_f32 v57, v42, v43\n"
      "v_cvt_pk_bf16_f32 v58, v44, v45\n"
      "v_cvt_pk_bf16_f32 v59, v46, v47\n"
      "buffer_store_dwordx4 v[56:59], v29, s[20:23], s77 offen offset:0 sc1\n"
      "v_pk_mul_f32 v[48:49], v[72:73], v[72:73]\n"
      "v_pk_mul_f32 v[50:51], v[74:75], v[74:75]\n"
      "v_pk_mul_f32 v[52:53], v[76:77], v[76:77]\n"
      "v_pk_mul_f32 v[54:55], v[78:79], v[78:79]\n"
      "v_pk_fma_f32 v[48:49], v[48:49], v[34:35], v[32:33]\n"
      "v_pk_fma_f32 v[50:51], v[50:51], v[34:35], v[32:33]\n"
      "v_pk_fma_f32 v[52:53], v[52:53], v[34:35], v[32:33]\n"
      "v_pk_fma_f32 v[54:55], v[54:55], v[34:35], v[32:33]\n"
      "v_pk_mul_f32 v[48:49], v[72:73], v[48:49]\n"
      "v_pk_mul_f32 v[50:51], v[74:75], v[50:51]\n"
      "v_pk_mul_f32 v[52:53], v[76:77], v[52:53]\n"
      "v_pk_mul_f32 v[54:55], v[78:79], v[54:55]\n"
      "v_exp_f32 v48, v48\n"
      "v_exp_f32 v49, v49\n"
      "v_exp_f32 v50, v50\n"
      "v_exp_f32 v51, v51\n"
      "v_exp_f32 v52, v52\n"
      "v_exp_f32 v53, v53\n"
      "v_exp_f32 v54, v54\n"
      "v_exp_f32 v55, v55\n"
      "v_pk_fma_f32 v[48:49], v[48:49], v[36:37], v[36:37]\n"
      "v_pk_fma_f32 v[50:51], v[50:51], v[36:37], v[36:37]\n"
      "v_pk_fma_f32 v[52:53], v[52:53], v[36:37], v[36:37]\n"
      "v_pk_fma_f32 v[54:55], v[54:55], v[36:37], v[36:37]\n"
      "ds_read_b32 v31, v2 offset:4128\n"
      "v_rcp_f32 v48, v48\n"
      "v_rcp_f32 v49, v49\n"
      "v_rcp_f32 v50, v50\n"
      "v_rcp_f32 v51, v51\n"
      "v_rcp_f32 v52, v52\n"
      "v_rcp_f32 v53, v53\n"
      "v_rcp_f32 v54, v54\n"
      "v_rcp_f32 v55, v55\n"
      "v_pk_mul_f32 v[48:49], v[72:73], v[48:49]\n"
      "v_pk_mul_f32 v[50:51], v[74:75], v[50:51]\n"
      "v_pk_mul_f32 v[52:53], v[76:77], v[52:53]\n"
      "v_pk_mul_f32 v[54:55], v[78:79], v[54:55]\n"
      "v_cvt_pk_bf16_f32 v60, v48, v49\n"
      "v_cvt_pk_bf16_f32 v61, v50, v51\n"
      "v_cvt_pk_bf16_f32 v62, v52, v53\n"
      "v_cvt_pk_bf16_f32 v63, v54, v55\n"
      "buffer_store_dwordx4 v[60:63], v29, s[20:23], s77 offen offset:256 sc1\n"
      ".LCONV:\n"
      "v_and_b32 v10, 1, v1\n"
      "v_xor_b32 v10, 1, v10\n"
      "v_mul_u32_u24 v10, 4160, v10\n"
      "v_and_b32 v11, 14, v1\n"
      "v_lshl_add_u32 v10, v11, 1, v10\n"
      "v_and_b32 v11, 48, v1\n"
      "v_sub_u32 v10, v10, v11\n"
      "v_and_b32 v12, 15, v1\n"
      "v_lshl_add_u32 v11, v12, 5, v11\n"
      "s_lshl_b32 s62, s53, 7\n"
      "s_add_u32 s63, s62, 82004\n"
      "v_add_u32 v208, s63, v10\n"
      "s_lshl_b32 s63, s54, 9\n"
      "s_sub_u32 s63, s63, s62\n"
      "s_add_u32 s63, s63, 149152\n"
      "v_add_u32 v216, s63, v11\n"
      "v_add_u32 v209, 1024, v208\n"
      "s_waitcnt lgkmcnt(0)\n"
      "v_readfirstlane_b32 s2, v31\n"
      "s_cmp_lt_u32 s2, 4\n"
      "s_cbranch_scc1 .Lw_141\n"
      ".Lw_142:\n"
      "ds_read2_b32 v[144:145], v208 offset0:3 offset1:2\n"
      "ds_read2_b32 v[146:147], v208 offset0:1 offset1:0\n"
      "ds_read_b128 v[204:207], v216 offset:3648\n"
      "ds_read_b128 v[196:199], v216 offset:2624\n"
      "ds_read_b128 v[188:191], v216 offset:1600\n"
      "ds_read_b128 v[180:183], v216 offset:576\n"
      "ds_read2_b32 v[148:149], v208 offset0:131 offset1:130\n"
      "ds_read2_b32 v[150:151], v208 offset0:129 offset1:128\n"
      "ds_read_b128 v[200:203], v216 offset:3136\n"
      "ds_read_b128 v[192:195], v216 offset:2112\n"
      "ds_read_b128 v[184:187], v216 offset:1088\n"
      "ds_read_b128 v[176:179], v216 offset:64\n"
      "ds_read2_b32 v[152:153], v209 offset0:3 offset1:2\n"
      "ds_read2_b32 v[154:155], v209 offset0:1 offset1:0\n"
      "v_add_u32 v210, 2048, v208\n"
      "v_add_u32 v211, 3072, v208\n"
      "s_lshr_b32 s64, s53, 1\n"
      "s_and_b32 s65, s53, 1\n"
      "s_lshl_b32 s66, s65, 8\n"
      "s_mul_i32 s62, s64, 8320\n"
      "s_add_u32 s62, s62, s66\n"
      "s_add_u32 s62, s62, 122964\n"
      "v_add_u32 v212, s62, v10\n"
      "s_mul_i32 s62, s64, 4608\n"
      "s_sub_u32 s63, s62, s66\n"
      "s_add_u32 s63, s63, 139808\n"
      "v_add_u32 v217, s63, v11\n"
      "v_add_u32 v213, 1024, v212\n"
      "v_add_u32 v214, 2048, v212\n"
      "v_add_u32 v215, 3072, v212\n"
      "s_waitcnt lgkmcnt(11)\n"
      "v_mfma_f32_16x16x32_bf16 v[140:143], v[144:147], v[204:207], 0\n"
      "ds_read2_b32 v[156:157], v209 offset0:131 offset1:130\n"
      "ds_read2_b32 v[158:159], v209 offset0:129 offset1:128\n"
      "s_waitcnt lgkmcnt(12)\n"
      "v_mfma_f32_16x16x32_bf16 v[136:139], v[144:147], v[196:199], 0\n"
      "ds_read2_b32 v[160:161], v210 offset0:3 offset1:2\n"
      "s_waitcnt lgkmcnt(12)\n"
      "v_mfma_f32_16x16x32_bf16 v[132:135], v[144:147], v[188:191], 0\n"
      "ds_read2_b32 v[162:163], v210 offset0:1 offset1:0\n"
      "s_waitcnt lgkmcnt(12)\n"
      "v_mfma_f32_16x16x32_bf16 v[128:131], v[144:147], v[180:183], 0\n"
      "ds_read2_b32 v[164:165], v210 offset0:131 offset1:130\n"
      "s_waitcnt lgkmcnt(10)\n"
      "v_mfma_f32_16x16x32_bf16 v[140:143], v[148:151], v[200:203], v[140:143]\n"
      "ds_read2_b32 v[166:167], v210 offset0:129 offset1:128\n"
      "v_lshlrev_b32 v9, 4, v1\n"
      "s_waitcnt lgkmcnt(10)\n"
      "v_mfma_f32_16x16x32_bf16 v[136:139], v[148:151], v[192:195], v[136:139]\n"
      "ds_read2_b32 v[168:169], v211 offset0:3 offset1:2\n"
      "s_lshl_b32 s62, s53, 12\n"
      "s_waitcnt lgkmcnt(7)\n"
      "v_mfma_f32_16x16x32_bf16 v[132:135], v[148:151], v[184:187], v[132:135]\n"
      "ds_read2_b32 v[170:171], v211 offset0:1 offset1:0\n"
      "v_add_u32 v14, s62, v9\n"
      "v_mfma_f32_16x16x32_bf16 v[128:131], v[148:151], v[176:179], v[128:131]\n"
      "ds_read2_b32 v[172:173], v211 offset0:131 offset1:130\n"
      "s_xor_b32 s62, s62, 4096\n"
      "v_mfma_f32_16x16x32_bf16 v[140:143], v[152:155], v[196:199], v[140:143]\n"
      "ds_read2_b32 v[174:175], v211 offset0:129 offset1:128\n"
      "v_add_u32 v15, s62, v9\n"
      "v_mfma_f32_16x16x32_bf16 v[136:139], v[152:155], v[188:191], v[136:139]\n"
      "ds_read2_b32 v[144:145], v208 offset0:19 offset1:18\n"
      "ds_read2_b32 v[146:147], v208 offset0:17 offset1:16\n"
      "v_mfma_f32_16x16x32_bf16 v[132:135], v[152:155], v[180:183], v[132:135]\n"
      "ds_read_b128 v[204:207], v216 offset:3584\n"
      "ds_read_b128 v[196:199], v216 offset:2560\n"
      "s_waitcnt lgkmcnt(9)\n"
      "v_mfma_f32_16x16x32_bf16 v[140:143], v[156:159], v[192:195], v[140:143]\n"
      "ds_read2_b32 v[148:149], v208 offset0:147 offset1:146\n"
      "s_lshl_b32 s62, s53, 10\n"
      "v_mfma_f32_16x16x32_bf16 v[136:139], v[156:159], v[184:187], v[136:139]\n"
      "ds_read2_b32 v[150:151], v208 offset0:145 offset1:144\n"
      "v_add_u32 v18, s62, v9\n"
      "v_mfma_f32_16x16x32_bf16 v[132:135], v[156:159], v[176:179], v[132:135]\n"
      "ds_read_b128 v[200:203], v216 offset:3072\n"
      "s_lshl_b32 s62, s53, 2\n"
      "v_mfma_f32_16x16x32_bf16 v[140:143], v[160:163], v[188:191], v[140:143]\n"
      "ds_read_b128 v[188:191], v216 offset:1536\n"
      "ds_read_b128 v[192:195], v216 offset:2048\n"
      "v_mfma_f32_16x16x32_bf16 v[136:139], v[160:163], v[180:183], v[136:139]\n"
      "ds_read2_b32 v[152:153], v209 offset0:19 offset1:18\n"
      "v_add_u32 v19, s62, v2\n"
      "s_waitcnt lgkmcnt(10)\n"
      "v_mfma_f32_16x16x32_bf16 v[140:143], v[164:167], v[184:187], v[140:143]\n"
      "ds_read_b128 v[184:187], v216 offset:1024\n"
      "s_xor_b32 s62, s62, 4\n"
      "v_mfma_f32_16x16x32_bf16 v[136:139], v[164:167], v[176:179], v[136:139]\n"
      "ds_read2_b32 v[154:155], v209 offset0:17 offset1:16\n"
      "v_add_u32 v20, s62, v2\n"
      "v_mfma_f32_16x16x32_bf16 v[140:143], v[168:171], v[180:183], v[140:143]\n"
      "ds_read_b128 v[180:183], v216 offset:512\n"
      "v_and_b32 v16, 15, v1\n"
      "v_mfma_f32_16x16x32_bf16 v[140:143], v[172:175], v[176:179], v[140:143]\n"
      "ds_read_b128 v[176:179], v216 offset:0\n"
      "v_lshrrev_b32 v17, 4, v1\n"
      "s_waitcnt lgkmcnt(8)\n"
      "v_mfma_f32_16x16x32_bf16 v[140:143], v[144:147], v[204:207], v[140:143]\n"
      "ds_read2_b32 v[156:157], v209 offset0:147 offset1:146\n"
      "ds_read2_b32 v[158:159], v209 offset0:145 offset1:144\n"
      "v_mfma_f32_16x16x32_bf16 v[136:139], v[144:147], v[196:199], v[136:139]\n"
      "ds_read2_b32 v[160:161], v210 offset0:19 offset1:18\n"
      "v_lshrrev_b32 v21, 5, v1\n"
      "s_waitcnt lgkmcnt(8)\n"
      "v_mfma_f32_16x16x32_bf16 v[132:135], v[144:147], v[188:191], v[132:135]\n"
      "ds_read2_b32 v[162:163], v210 offset0:17 offset1:16\n"
      "v_lshlrev_b32 v21, 4, v21\n"
      "s_waitcnt lgkmcnt(5)\n"
      "v_mfma_f32_16x16x32_bf16 v[128:131], v[144:147], v[180:183], v[128:131]\n"
      "ds_read2_b32 v[164:165], v210 offset0:147 offset1:146\n"
      "v_lshl_add_u32 v21, v16, 5, v21\n"
      "v_mfma_f32_16x16x32_bf16 v[140:143], v[148:151], v[200:203], v[140:143]\n"
      "ds_read2_b32 v[166:167], v210 offset0:145 offset1:144\n"
      "v_and_b32 v22, 1, v17\n"
      "v_mfma_f32_16x16x32_bf16 v[136:139], v[148:151], v[192:195], v[136:139]\n"
      "ds_read2_b32 v[168:169], v211 offset0:19 offset1:18\n"
      "v_lshl_add_u32 v5, v22, 9, v21\n"
      "v_mfma_f32_16x16x32_bf16 v[132:135], v[148:151], v[184:187], v[132:135]\n"
      "ds_read2_b32 v[170:171], v211 offset0:17 offset1:16\n"
      "v_lshl_add_u32 v24, v22, 30, v21\n"
      "s_waitcnt lgkmcnt(8)\n"
      "v_mfma_f32_16x16x32_bf16 v[128:131], v[148:151], v[176:179], v[128:131]\n"
      "ds_read2_b32 v[172:173], v211 offset0:147 offset1:146\n"
      "s_lshr_b32 s62, s60, 1\n"
      "v_mfma_f32_16x16x32_bf16 v[140:143], v[152:155], v[196:199], v[140:143]\n"
      "ds_read2_b32 v[174:175], v211 offset0:145 offset1:144\n"
      "s_mul_i32 s62, s62, 5\n"
      "v_mfma_f32_16x16x32_bf16 v[136:139], v[152:155], v[188:191], v[136:139]\n"
      "ds_read2_b32 v[144:145], v212 offset0:3 offset1:2\n"
      "ds_read2_b32 v[146:147], v212 offset0:1 offset1:0\n"
      "v_mfma_f32_16x16x32_bf16 v[132:135], v[152:155], v[180:183], v[132:135]\n"
      "ds_read_b128 v[204:207], v217 offset:3776\n"
      "ds_read_b128 v[200:203], v217 offset:3264\n"
      "s_waitcnt lgkmcnt(9)\n"
      "v_mfma_f32_16x16x32_bf16 v[140:143], v[156:159], v[192:195], v[140:143]\n"
      "ds_read_b128 v[196:199], v217 offset:2752\n"
      "s_mul_i32 s63, s54, 3\n"
      "v_mfma_f32_16x16x32_bf16 v[136:139], v[156:159], v[184:187], v[136:139]\n"
      "ds_read_b128 v[192:195], v217 offset:2240\n"
      "s_add_u32 s63, s63, s64\n"
      "v_mfma_f32_16x16x32_bf16 v[132:135], v[156:159], v[176:179], v[132:135]\n"
      "ds_read2_b32 v[148:149], v212 offset0:131 offset1:130\n"
      "s_add_u32 s63, s63, s62\n"
      "v_mfma_f32_16x16x32_bf16 v[140:143], v[160:163], v[188:191], v[140:143]\n"
      "ds_read_b128 v[188:191], v217 offset:1728\n"
      "s_mul_i32 s63, s63, 4224\n"
      "v_mfma_f32_16x16x32_bf16 v[136:139], v[160:163], v[180:183], v[136:139]\n"
      "ds_read2_b32 v[150:151], v212 offset0:129 offset1:128\n"
      "s_lshl_b32 s67, s65, 11\n"
      "s_waitcnt lgkmcnt(9)\n"
      "v_mfma_f32_16x16x32_bf16 v[140:143], v[164:167], v[184:187], v[140:143]\n"
      "ds_read_b128 v[184:187], v217 offset:1216\n"
      "s_add_u32 s63, s63, s67\n"
      "v_mfma_f32_16x16x32_bf16 v[136:139], v[164:167], v[176:179], v[136:139]\n"
      "ds_read2_b32 v[152:153], v213 offset0:3 offset1:2\n"
      "s_add_u32 s80, s63, 2818048\n"
      "v_mfma_f32_16x16x32_bf16 v[140:143], v[168:171], v[180:183], v[140:143]\n"
      "ds_read_b128 v[180:183], v217 offset:704\n"
      "s_mul_i32 s62, s62, 4224\n"
      "v_mfma_f32_16x16x32_bf16 v[140:143], v[172:175], v[176:179], v[140:143]\n"
      "ds_read_b128 v[176:179], v217 offset:192\n"
      "s_lshl1_add_u32 s67, s53, s54\n"
      "s_waitcnt lgkmcnt(7)\n"
      "v_mfma_f32_16x16x32_bf16 v[124:127], v[144:147], v[204:207], 0\n"
      "ds_read2_b32 v[154:155], v213 offset0:1 offset1:0\n"
      "s_lshl_b32 s67, s67, 9\n"
      "v_mfma_f32_16x16x32_bf16 v[120:123], v[144:147], v[200:203], 0\n"
      "ds_read2_b32 v[156:157], v213 offset0:131 offset1:130\n"
      "s_add_u32 s62, s62, s67\n"
      "v_mfma_f32_16x16x32_bf16 v[116:119], v[144:147], v[196:199], 0\n"
      "ds_read2_b32 v[158:159], v213 offset0:129 offset1:128\n"
      "s_add_u32 s81, s62, 2826496\n"
      "v_mfma_f32_16x16x32_bf16 v[112:115], v[144:147], v[192:195], 0\n"
      "ds_read2_b32 v[160:161], v214 offset0:3 offset1:2\n"
      "ds_read_b32 v31, v2 offset:4160\n"
      "s_waitcnt lgkmcnt(8)\n"
      "v_mfma_f32_16x16x32_bf16 v[108:111], v[144:147], v[188:191], 0\n"
      "ds_read2_b32 v[162:163], v214 offset0:1 offset1:0\n"
      "v_mov_b64 v[12:13], s[58:59]\n"
      "v_mfma_f32_16x16x32_bf16 v[104:107], v[144:147], v[184:187], 0\n"
      "s_waitcnt vmcnt(0)\n"
      "s_waitcnt lgkmcnt(7)\n"
      "v_mfma_f32_16x16x32_bf16 v[100:103], v[144:147], v[180:183], 0\n"
      "ds_read2_b32 v[164:165], v214 offset0:131 offset1:130\n"
      "s_waitcnt lgkmcnt(7)\n"
      "v_mfma_f32_16x16x32_bf16 v[96:99], v[144:147], v[176:179], 0\n"
      "ds_read2_b32 v[166:167], v214 offset0:129 offset1:128\n"
      "v_mfma_f32_16x16x32_bf16 v[124:127], v[148:151], v[200:203], v[124:127]\n"
      "s_mov_b64 exec, 1\n"
      "buffer_store_dwordx2 v[12:13], off, s[20:23], s79 offset:128 sc1\n"
      "s_mov_b64 exec, -1\n"
      "v_mfma_f32_16x16x32_bf16 v[120:123], v[148:151], v[196:199], v[120:123]\n"
      "ds_read2_b32 v[168:169], v215 offset0:3 offset1:2\n"
      "v_mfma_f32_16x16x32_bf16 v[116:119], v[148:151], v[192:195], v[116:119]\n"
      "ds_read2_b32 v[170:171], v215 offset0:1 offset1:0\n"
      "v_mfma_f32_16x16x32_bf16 v[112:115], v[148:151], v[188:191], v[112:115]\n"
      "ds_read2_b32 v[172:173], v215 offset0:131 offset1:130\n"
      "v_mfma_f32_16x16x32_bf16 v[108:111], v[148:151], v[184:187], v[108:111]\n"
      "ds_read2_b32 v[174:175], v215 offset0:129 offset1:128\n"
      "v_mfma_f32_16x16x32_bf16 v[104:107], v[148:151], v[180:183], v[104:107]\n"
      "ds_read2_b32 v[144:145], v212 offset0:19 offset1:18\n"
      "v_mfma_f32_16x16x32_bf16 v[100:103], v[148:151], v[176:179], v[100:103]\n"
      "ds_read2_b32 v[146:147], v212 offset0:17 offset1:16\n"
      "s_waitcnt lgkmcnt(6)\n"
      "v_mfma_f32_16x16x32_bf16 v[124:127], v[152:155], v[196:199], v[124:127]\n"
      "ds_read_b128 v[204:207], v217 offset:3712\n"
      "v_mfma_f32_16x16x32_bf16 v[120:123], v[152:155], v[192:195], v[120:123]\n"
      "ds_read_b128 v[200:203], v217 offset:3200\n"
      "v_mfma_f32_16x16x32_bf16 v[116:119], v[152:155], v[188:191], v[116:119]\n"
      "ds_read_b128 v[196:199], v217 offset:2688\n"
      "v_mfma_f32_16x16x32_bf16 v[112:115], v[152:155], v[184:187], v[112:115]\n"
      "v_readfirstlane_b32 s2, v31\n"
      "s_cmp_lt_u32 s2, 4\n"
      "s_cbranch_scc1 .Lw_144\n"
      ".Lw_145:\n"
      "v_mfma_f32_16x16x32_bf16 v[108:111], v[152:155], v[180:183], v[108:111]\n"
      "ds_write_b128 v14, v[128:131] offset:16384\n"
      "v_mfma_f32_16x16x32_bf16 v[104:107], v[152:155], v[176:179], v[104:107]\n"
      "ds_write_b128 v14, v[132:135] offset:17408\n"
      "v_mfma_f32_16x16x32_bf16 v[124:127], v[156:159], v[192:195], v[124:127]\n"
      "ds_write_b128 v14, v[136:139] offset:18432\n"
      "v_mfma_f32_16x16x32_bf16 v[120:123], v[156:159], v[188:191], v[120:123]\n"
      "ds_write_b128 v14, v[140:143] offset:19456\n"
      "v_mfma_f32_16x16x32_bf16 v[116:119], v[156:159], v[184:187], v[116:119]\n"
      "v_mfma_f32_16x16x32_bf16 v[112:115], v[156:159], v[180:183], v[112:115]\n"
      "ds_add_u32 v2, v3 offset:4132\n"
      "s_sub_u32 s82, 3, s53\n"
      "v_mfma_f32_16x16x32_bf16 v[108:111], v[156:159], v[176:179], v[108:111]\n"
      "ds_read_b128 v[192:195], v217 offset:2176\n"
      "s_lshr_b32 s63, s82, 1\n"
      "v_mfma_f32_16x16x32_bf16 v[124:127], v[160:163], v[188:191], v[124:127]\n"
      "ds_read_b128 v[188:191], v217 offset:1664\n"
      "s_lshl1_add_u32 s63, s54, s63\n"
      "v_mfma_f32_16x16x32_bf16 v[120:123], v[160:163], v[184:187], v[120:123]\n"
      "ds_read2_b32 v[148:149], v212 offset0:147 offset1:146\n"
      "s_xor_b32 s63, s63, s99\n"
      "v_mfma_f32_16x16x32_bf16 v[116:119], v[160:163], v[180:183], v[116:119]\n"
      "ds_read2_b32 v[150:151], v212 offset0:145 offset1:144\n"
      "s_lshl_b32 s63, s63, 10\n"
      "v_mfma_f32_16x16x32_bf16 v[112:115], v[160:163], v[176:179], v[112:115]\n"
      "ds_read2_b32 v[152:153], v213 offset0:19 offset1:18\n"
      "s_and_b32 s67, s82, 1\n"
      "v_mfma_f32_16x16x32_bf16 v[124:127], v[164:167], v[184:187], v[124:127]\n"
      "ds_read_b128 v[184:187], v217 offset:1152\n"
      "s_lshl_b32 s67, s67, 9\n"
      "v_mfma_f32_16x16x32_bf16 v[120:123], v[164:167], v[180:183], v[120:123]\n"
      "ds_read2_b32 v[154:155], v213 offset0:17 offset1:16\n"
      "s_add_u32 s63, s63, s67\n"
      "v_mfma_f32_16x16x32_bf16 v[116:119], v[164:167], v[176:179], v[116:119]\n"
      "s_add_u32 s63, s63, 157716\n"
      "v_lshl_add_u32 v21, v16, 5, s63\n"
      "s_waitcnt lgkmcnt(6)\n"
      "v_mfma_f32_16x16x32_bf16 v[124:127], v[168:171], v[180:183], v[124:127]\n"
      "ds_read_b128 v[180:183], v217 offset:640\n"
      "ds_read_b32 v23, v21\n"
      "v_mfma_f32_16x16x32_bf16 v[120:123], v[168:171], v[176:179], v[120:123]\n"
      "ds_read2_b32 v[156:157], v213 offset0:147 offset1:146\n"
      "s_lshl1_add_u32 s62, s56, s54\n"
      "v_mfma_f32_16x16x32_bf16 v[124:127], v[172:175], v[176:179], v[124:127]\n"
      "ds_read_b128 v[176:179], v217 offset:128\n"
      "s_lshl2_add_u32 s63, s62, s82\n"
      "v_mfma_f32_16x16x32_bf16 v[124:127], v[144:147], v[204:207], v[124:127]\n"
      "ds_read2_b32 v[158:159], v213 offset0:145 offset1:144\n"
      "s_mul_i32 s70, s63, 20480\n"
      "v_mfma_f32_16x16x32_bf16 v[120:123], v[144:147], v[200:203], v[120:123]\n"
      "s_mul_i32 s63, s55, 160\n"
      "s_add_u32 s70, s70, s63\n"
      "v_mfma_f32_16x16x32_bf16 v[116:119], v[144:147], v[196:199], v[116:119]\n"
      "ds_read2_b32 v[160:161], v214 offset0:19 offset1:18\n"
      "v_mul_u32_u24 v4, 1280, v16\n"
      "v_mfma_f32_16x16x32_bf16 v[112:115], v[144:147], v[192:195], v[112:115]\n"
      "ds_read2_b32 v[162:163], v214 offset0:17 offset1:16\n"
      "v_lshl_add_u32 v4, v17, 3, v4\n"
      "s_waitcnt lgkmcnt(7)\n"
      "v_mfma_f32_16x16x32_bf16 v[108:111], v[144:147], v[188:191], v[108:111]\n"
      "s_mul_i32 s63, s53, 5120\n"
      "s_lshl2_add_u32 s72, s60, s53\n"
      "v_mfma_f32_16x16x32_bf16 v[104:107], v[144:147], v[184:187], v[104:107]\n"
      "s_mul_i32 s72, s72, 3072\n"
      "s_add_u32 s72, s72, 15237120\n"
      "s_waitcnt lgkmcnt(4)\n"
      "v_mfma_f32_16x16x32_bf16 v[100:103], v[144:147], v[180:183], v[100:103]\n"
      "ds_read2_b32 v[164:165], v214 offset0:147 offset1:146\n"
      "s_add_u32 s63, s63, 61440\n"
      "s_waitcnt lgkmcnt(4)\n"
      "v_mfma_f32_16x16x32_bf16 v[96:99], v[144:147], v[176:179], v[96:99]\n"
      "ds_read2_b32 v[166:167], v214 offset0:145 offset1:144\n"
      "v_add_u32 v8, s63, v9\n"
      "v_mfma_f32_16x16x32_bf16 v[124:127], v[148:151], v[200:203], v[124:127]\n"
      "s_lshl_b32 s74, s93, 16\n"
      "s_lshl2_add_u32 s63, s55, s53\n"
      "v_mfma_f32_16x16x32_bf16 v[120:123], v[148:151], v[196:199], v[120:123]\n"
      "ds_read2_b32 v[168:169], v215 offset0:19 offset1:18\n"
      "s_mul_i32 s73, s63, 84480\n"
      "v_mfma_f32_16x16x32_bf16 v[116:119], v[148:151], v[192:195], v[116:119]\n"
      "ds_read2_b32 v[170:171], v215 offset0:17 offset1:16\n"
      "s_lshl_b32 s62, s62, 7\n"
      "v_mfma_f32_16x16x32_bf16 v[112:115], v[148:151], v[188:191], v[112:115]\n"
      "ds_read2_b32 v[172:173], v215 offset0:147 offset1:146\n"
      "s_add_u32 s73, s73, s62\n"
      "v_mfma_f32_16x16x32_bf16 v[108:111], v[148:151], v[184:187], v[108:111]\n"
      "ds_read2_b32 v[174:175], v215 offset0:145 offset1:144\n"
      "s_add_u32 s73, s73, 2818048\n"
      "v_mfma_f32_16x16x32_bf16 v[104:107], v[148:151], v[180:183], v[104:107]\n"
      "ds_read2_b32 v[144:145], v212 offset0:35 offset1:34\n"
      "s_lshl_b32 s63, s63, 10\n"
      "v_mfma_f32_16x16x32_bf16 v[100:103], v[148:151], v[176:179], v[100:103]\n"
      "ds_read2_b32 v[146:147], v212 offset0:33 offset1:32\n"
      "s_add_u32 s63, s63, 18864128\n"
      "v_mfma_f32_16x16x32_bf16 v[124:127], v[152:155], v[196:199], v[124:127]\n"
      "ds_read_b128 v[204:207], v217 offset:3648\n"
      "v_and_b32 v21, 7, v1\n"
      "v_mfma_f32_16x16x32_bf16 v[120:123], v[152:155], v[192:195], v[120:123]\n"
      "ds_read_b128 v[200:203], v217 offset:3136\n"
      "v_lshl_add_u32 v25, v21, 7, s63\n"
      "v_mfma_f32_16x16x32_bf16 v[116:119], v[152:155], v[188:191], v[116:119]\n"
      "ds_read_b128 v[196:199], v217 offset:2624\n"
      "v_lshrrev_b32 v22, 3, v1\n"
      "v_mfma_f32_16x16x32_bf16 v[112:115], v[152:155], v[184:187], v[112:115]\n"
      "v_mul_u32_u24 v6, 4224, v22\n"
      "v_lshl_add_u32 v6, v21, 4, v6\n"
      "v_mfma_f32_16x16x32_bf16 v[108:111], v[152:155], v[180:183], v[108:111]\n"
      "s_lshl_b32 s62, s82, 5\n"
      "s_add_u32 s62, s62, 139520\n"
      "v_mfma_f32_16x16x32_bf16 v[104:107], v[152:155], v[176:179], v[104:107]\n"
      "ds_read2_b32 v[148:149], v212 offset0:163 offset1:162\n"
      "v_lshrrev_b32 v21, 2, v1\n"
      "s_waitcnt lgkmcnt(5)\n"
      "v_mfma_f32_16x16x32_bf16 v[124:127], v[156:159], v[192:195], v[124:127]\n"
      "ds_read_b128 v[192:195], v217 offset:2112\n"
      "v_lshl_add_u32 v21, v21, 7, s62\n"
      "v_mfma_f32_16x16x32_bf16 v[120:123], v[156:159], v[188:191], v[120:123]\n"
      "ds_read2_b32 v[150:151], v212 offset0:161 offset1:160\n"
      "v_and_b32 v22, 3, v1\n"
      "v_mfma_f32_16x16x32_bf16 v[116:119], v[156:159], v[184:187], v[116:119]\n"
      "v_lshl_add_u32 v7, v22, 3, v21\n"
      "s_mov_b32 s76, 0xbfb8aa3b\n"
      "v_mfma_f32_16x16x32_bf16 v[112:115], v[156:159], v[180:183], v[112:115]\n"
      "s_mov_b32 s77, 0xbf317218\n"
      "v_and_b32 v21, s92, v23\n"
      "v_mfma_f32_16x16x32_bf16 v[108:111], v[156:159], v[176:179], v[108:111]\n"
      "v_rcp_f32 v21, v21\n"
      "v_mfma_f32_16x16x32_bf16 v[124:127], v[160:163], v[188:191], v[124:127]\n"
      "ds_read_b128 v[188:191], v217 offset:1600\n"
      "v_lshlrev_b32 v22, 16, v23\n"
      "v_mfma_f32_16x16x32_bf16 v[120:123], v[160:163], v[184:187], v[120:123]\n"
      "ds_read2_b32 v[152:153], v213 offset0:35 offset1:34\n"
      "v_mul_f32 v21, s77, v21\n"
      "v_mfma_f32_16x16x32_bf16 v[116:119], v[160:163], v[180:183], v[116:119]\n"
      "ds_read2_b32 v[154:155], v213 offset0:33 offset1:32\n"
      "v_mul_f32 v22, v22, v21\n"
      "v_mfma_f32_16x16x32_bf16 v[112:115], v[160:163], v[176:179], v[112:115]\n"
      "v_lshlrev_b32 v32, 16, v218\n"
      "v_and_b32 v33, s92, v218\n"
      "v_mfma_f32_16x16x32_bf16 v[124:127], v[164:167], v[184:187], v[124:127]\n"
      "ds_read_b128 v[184:187], v217 offset:1088\n"
      "v_lshlrev_b32 v34, 16, v219\n"
      "v_mfma_f32_16x16x32_bf16 v[120:123], v[164:167], v[180:183], v[120:123]\n"
      "v_and_b32 v35, s92, v219\n"
      "v_lshlrev_b32 v36, 16, v220\n"
      "v_mfma_f32_16x16x32_bf16 v[116:119], v[164:167], v[176:179], v[116:119]\n"
      "v_and_b32 v37, s92, v220\n"
      "v_lshlrev_b32 v38, 16, v221\n"
      "v_mfma_f32_16x16x32_bf16 v[124:127], v[168:171], v[180:183], v[124:127]\n"
      "ds_read_b128 v[180:183], v217 offset:576\n"
      "v_and_b32 v39, s92, v221\n"
      "v_mfma_f32_16x16x32_bf16 v[120:123], v[168:171], v[176:179], v[120:123]\n"
      "ds_read2_b32 v[156:157], v213 offset0:163 offset1:162\n"
      "v_lshlrev_b32 v40, 16, v222\n"
      "v_mfma_f32_16x16x32_bf16 v[124:127], v[172:175], v[176:179], v[124:127]\n"
      "ds_read_b128 v[176:179], v217 offset:64\n"
      "v_and_b32 v41, s92, v222\n"
      "s_waitcnt lgkmcnt(4)\n"
      "v_mfma_f32_16x16x32_bf16 v[124:127], v[144:147], v[204:207], v[124:127]\n"
      "ds_read2_b32 v[158:159], v213 offset0:161 offset1:160\n"
      "ds_read_b32 v31, v2 offset:4132\n"
      "v_mfma_f32_16x16x32_bf16 v[120:123], v[144:147], v[200:203], v[120:123]\n"
      "s_waitcnt lgkmcnt(0)\n"
      "v_readfirstlane_b32 s2, v31\n"
      "s_cmp_lt_u32 s2, 4\n"
      "s_cbranch_scc1 .Lw_147\n"
      ".Lw_148:\n"
      "v_mfma_f32_16x16x32_bf16 v[116:119], v[144:147], v[196:199], v[116:119]\n"
      "ds_read2_b32 v[160:161], v214 offset0:35 offset1:34\n"
      "ds_read_b128 v[128:131], v18 offset:16384\n"
      "v_mfma_f32_16x16x32_bf16 v[112:115], v[144:147], v[192:195], v[112:115]\n"
      "ds_read2_b32 v[162:163], v214 offset0:33 offset1:32\n"
      "ds_read_b128 v[132:135], v18 offset:20480\n"
      "v_mfma_f32_16x16x32_bf16 v[108:111], v[144:147], v[188:191], v[108:111]\n"
      "ds_read_b128 v[136:139], v18 offset:24576\n"
      "ds_read_b128 v[140:143], v18 offset:28672\n"
      "v_mfma_f32_16x16x32_bf16 v[104:107], v[144:147], v[184:187], v[104:107]\n"
      "v_lshlrev_b32 v42, 16, v223\n"
      "v_and_b32 v43, s92, v223\n"
      "v_mfma_f32_16x16x32_bf16 v[100:103], v[144:147], v[180:183], v[100:103]\n"
      "ds_read2_b32 v[164:165], v214 offset0:163 offset1:162\n"
      "v_lshlrev_b32 v44, 16, v224\n"
      "v_mfma_f32_16x16x32_bf16 v[96:99], v[144:147], v[176:179], v[96:99]\n"
      "ds_read2_b32 v[166:167], v214 offset0:161 offset1:160\n"
      "v_and_b32 v45, s92, v224\n"
      "v_mfma_f32_16x16x32_bf16 v[124:127], v[148:151], v[200:203], v[124:127]\n"
      "v_lshlrev_b32 v46, 16, v225\n"
      "v_and_b32 v47, s92, v225\n"
      "v_mfma_f32_16x16x32_bf16 v[120:123], v[148:151], v[196:199], v[120:123]\n"
      "ds_read2_b32 v[168:169], v215 offset0:35 offset1:34\n"
      "v_lshlrev_b32 v48, 16, v226\n"
      "v_mfma_f32_16x16x32_bf16 v[116:119], v[148:151], v[192:195], v[116:119]\n"
      "ds_read2_b32 v[170:171], v215 offset0:33 offset1:32\n"
      "v_and_b32 v49, s92, v226\n"
      "v_mfma_f32_16x16x32_bf16 v[112:115], v[148:151], v[188:191], v[112:115]\n"
      "ds_read2_b32 v[172:173], v215 offset0:163 offset1:162\n"
      "v_lshlrev_b32 v50, 16, v227\n"
      "v_mfma_f32_16x16x32_bf16 v[108:111], v[148:151], v[184:187], v[108:111]\n"
      "ds_read2_b32 v[174:175], v215 offset0:161 offset1:160\n"
      "v_and_b32 v51, s92, v227\n"
      "v_mfma_f32_16x16x32_bf16 v[104:107], v[148:151], v[180:183], v[104:107]\n"
      "ds_read2_b32 v[144:145], v212 offset0:51 offset1:50\n"
      "v_fma_f32 v52, v32, v21, v22\n"
      "v_mfma_f32_16x16x32_bf16 v[100:103], v[148:151], v[176:179], v[100:103]\n"
      "ds_read2_b32 v[146:147], v212 offset0:49 offset1:48\n"
      "v_fma_f32 v53, v33, v21, v22\n"
      "v_mfma_f32_16x16x32_bf16 v[124:127], v[152:155], v[196:199], v[124:127]\n"
      "ds_read_b128 v[204:207], v217 offset:3584\n"
      "v_fma_f32 v54, v34, v21, v22\n"
      "v_mfma_f32_16x16x32_bf16 v[120:123], v[152:155], v[192:195], v[120:123]\n"
      "ds_read_b128 v[200:203], v217 offset:3072\n"
      "v_fma_f32 v55, v35, v21, v22\n"
      "v_mfma_f32_16x16x32_bf16 v[116:119], v[152:155], v[188:191], v[116:119]\n"
      "ds_read_b128 v[196:199], v217 offset:2560\n"
      "v_fma_f32 v56, v36, v21, v22\n"
      "v_mfma_f32_16x16x32_bf16 v[112:115], v[152:155], v[184:187], v[112:115]\n"
      "v_fma_f32 v57, v37, v21, v22\n"
      "s_waitcnt lgkmcnt(6)\n"
      "v_add_f32 v136, v136, v140\n"
      "v_mfma_f32_16x16x32_bf16 v[108:111], v[152:155], v[180:183], v[108:111]\n"
      "v_add_f32 v137, v137, v141\n"
      "v_add_f32 v138, v138, v142\n"
      "v_mfma_f32_16x16x32_bf16 v[104:107], v[152:155], v[176:179], v[104:107]\n"
      "ds_read2_b32 v[148:149], v212 offset0:179 offset1:178\n"
      "v_add_f32 v139, v139, v143\n"
      "v_mfma_f32_16x16x32_bf16 v[124:127], v[156:159], v[192:195], v[124:127]\n"
      "ds_read_b128 v[192:195], v217 offset:2048\n"
      "v_add_f32 v128, v128, v132\n"
      "v_mfma_f32_16x16x32_bf16 v[120:123], v[156:159], v[188:191], v[120:123]\n"
      "ds_read2_b32 v[150:151], v212 offset0:177 offset1:176\n"
      "v_add_f32 v129, v129, v133\n"
      "v_mfma_f32_16x16x32_bf16 v[116:119], v[156:159], v[184:187], v[116:119]\n"
      "v_add_f32 v130, v130, v134\n"
      "v_add_f32 v131, v131, v135\n"
      "v_mfma_f32_16x16x32_bf16 v[112:115], v[156:159], v[180:183], v[112:115]\n"
      "v_add_f32 v128, v128, v136\n"
      "v_add_f32 v129, v129, v137\n"
      "v_mfma_f32_16x16x32_bf16 v[108:111], v[156:159], v[176:179], v[108:111]\n"
      "v_add_f32 v130, v130, v138\n"
      "v_add_f32 v131, v131, v139\n"
      "v_mfma_f32_16x16x32_bf16 v[124:127], v[160:163], v[188:191], v[124:127]\n"
      "ds_read_b128 v[188:191], v217 offset:1536\n"
      "v_cvt_pk_bf16_f32 v88, v128, v129\n"
      "v_mfma_f32_16x16x32_bf16 v[120:123], v[160:163], v[184:187], v[120:123]\n"
      "ds_read2_b32 v[152:153], v213 offset0:51 offset1:50\n"
      "v_cvt_pk_bf16_f32 v90, v128, v129\n"
      "v_mfma_f32_16x16x32_bf16 v[116:119], v[160:163], v[180:183], v[116:119]\n"
      "ds_read2_b32 v[154:155], v213 offset0:49 offset1:48\n"
      "v_cvt_pk_bf16_f32 v89, v130, v131\n"
      "v_mfma_f32_16x16x32_bf16 v[112:115], v[160:163], v[176:179], v[112:115]\n"
      "v_cvt_pk_bf16_f32 v91, v130, v131\n"
      "v_permlane16_swap_b32 v88, v90\n"
      "v_mfma_f32_16x16x32_bf16 v[124:127], v[164:167], v[184:187], v[124:127]\n"
      "ds_read_b128 v[184:187], v217 offset:1024\n"
      "v_permlane16_swap_b32 v89, v91\n"
      "v_mfma_f32_16x16x32_bf16 v[120:123], v[164:167], v[180:183], v[120:123]\n"
      "buffer_store_dwordx4 v[88:91], v24, s[20:23], s81 offen sc1\n"
      "v_mfma_f32_16x16x32_bf16 v[116:119], v[164:167], v[176:179], v[116:119]\n"
      "v_fma_f32 v58, v38, v21, v22\n"
      "v_fma_f32 v59, v39, v21, v22\n"
      "v_mfma_f32_16x16x32_bf16 v[124:127], v[168:171], v[180:183], v[124:127]\n"
      "ds_read_b128 v[180:183], v217 offset:512\n"
      "v_fma_f32 v60, v40, v21, v22\n"
      "v_mfma_f32_16x16x32_bf16 v[120:123], v[168:171], v[176:179], v[120:123]\n"
      "ds_read2_b32 v[156:157], v213 offset0:179 offset1:178\n"
      "v_fma_f32 v61, v41, v21, v22\n"
      "s_waitcnt lgkmcnt(4)\n"
      "v_mfma_f32_16x16x32_bf16 v[124:127], v[172:175], v[176:179], v[124:127]\n"
      "ds_read_b128 v[176:179], v217 offset:0\n"
      "v_fma_f32 v62, v42, v21, v22\n"
      "v_mfma_f32_16x16x32_bf16 v[124:127], v[144:147], v[204:207], v[124:127]\n"
      "ds_read2_b32 v[158:159], v213 offset0:177 offset1:176\n"
      "v_fma_f32 v63, v43, v21, v22\n"
      "v_mfma_f32_16x16x32_bf16 v[120:123], v[144:147], v[200:203], v[120:123]\n"
      "v_fma_f32 v64, v44, v21, v22\n"
      "v_fma_f32 v65, v45, v21, v22\n"
      "v_mfma_f32_16x16x32_bf16 v[116:119], v[144:147], v[196:199], v[116:119]\n"
      "ds_read2_b32 v[160:161], v214 offset0:51 offset1:50\n"
      "v_fma_f32 v66, v46, v21, v22\n"
      "v_mfma_f32_16x16x32_bf16 v[112:115], v[144:147], v[192:195], v[112:115]\n"
      "ds_read2_b32 v[162:163], v214 offset0:49 offset1:48\n"
      "v_fma_f32 v67, v47, v21, v22\n"
      "v_mfma_f32_16x16x32_bf16 v[108:111], v[144:147], v[188:191], v[108:111]\n"
      "v_fma_f32 v68, v48, v21, v22\n"
      "v_fma_f32 v69, v49, v21, v22\n"
      "s_waitcnt lgkmcnt(5)\n"
      "v_mfma_f32_16x16x32_bf16 v[104:107], v[144:147], v[184:187], v[104:107]\n"
      "v_fma_f32 v70, v50, v21, v22\n"
      "v_fma_f32 v71, v51, v21, v22\n"
      "v_mfma_f32_16x16x32_bf16 v[100:103], v[144:147], v[180:183], v[100:103]\n"
      "ds_read2_b32 v[164:165], v214 offset0:179 offset1:178\n"
      "s_waitcnt lgkmcnt(4)\n"
      "v_mfma_f32_16x16x32_bf16 v[96:99], v[144:147], v[176:179], v[96:99]\n"
      "ds_read2_b32 v[166:167], v214 offset0:177 offset1:176\n"
      "v_mfma_f32_16x16x32_bf16 v[124:127], v[148:151], v[200:203], v[124:127]\n"
      "v_mfma_f32_16x16x32_bf16 v[120:123], v[148:151], v[196:199], v[120:123]\n"
      "ds_read2_b32 v[168:169], v215 offset0:51 offset1:50\n"
      "v_mfma_f32_16x16x32_bf16 v[116:119], v[148:151], v[192:195], v[116:119]\n"
      "ds_read2_b32 v[170:171], v215 offset0:49 offset1:48\n"
      "v_mfma_f32_16x16x32_bf16 v[112:115], v[148:151], v[188:191], v[112:115]\n"
      "ds_read2_b32 v[172:173], v215 offset0:179 offset1:178\n"
      "v_mfma_f32_16x16x32_bf16 v[108:111], v[148:151], v[184:187], v[108:111]\n"
      "ds_read2_b32 v[174:175], v215 offset0:177 offset1:176\n"
      "v_mfma_f32_16x16x32_bf16 v[104:107], v[148:151], v[180:183], v[104:107]\n"
      "v_mfma_f32_16x16x32_bf16 v[100:103], v[148:151], v[176:179], v[100:103]\n"
      "v_mfma_f32_16x16x32_bf16 v[124:127], v[152:155], v[196:199], v[124:127]\n"
      "v_mfma_f32_16x16x32_bf16 v[120:123], v[152:155], v[192:195], v[120:123]\n"
      "v_mfma_f32_16x16x32_bf16 v[116:119], v[152:155], v[188:191], v[116:119]\n"
      "v_mfma_f32_16x16x32_bf16 v[112:115], v[152:155], v[184:187], v[112:115]\n"
      "v_mfma_f32_16x16x32_bf16 v[108:111], v[152:155], v[180:183], v[108:111]\n"
      "v_mfma_f32_16x16x32_bf16 v[104:107], v[152:155], v[176:179], v[104:107]\n"
      "s_waitcnt lgkmcnt(0)\n"
      "v_mfma_f32_16x16x32_bf16 v[124:127], v[156:159], v[192:195], v[124:127]\n"
      "v_mfma_f32_16x16x32_bf16 v[120:123], v[156:159], v[188:191], v[120:123]\n"
      "v_mfma_f32_16x16x32_bf16 v[116:119], v[156:159], v[184:187], v[116:119]\n"
      "v_mfma_f32_16x16x32_bf16 v[112:115], v[156:159], v[180:183], v[112:115]\n"
      "v_mfma_f32_16x16x32_bf16 v[108:111], v[156:159], v[176:179], v[108:111]\n"
      "v_mfma_f32_16x16x32_bf16 v[124:127], v[160:163], v[188:191], v[124:127]\n"
      "v_mfma_f32_16x16x32_bf16 v[120:123], v[160:163], v[184:187], v[120:123]\n"
      "v_mfma_f32_16x16x32_bf16 v[116:119], v[160:163], v[180:183], v[116:119]\n"
      "v_mfma_f32_16x16x32_bf16 v[112:115], v[160:163], v[176:179], v[112:115]\n"
      "v_mfma_f32_16x16x32_bf16 v[124:127], v[164:167], v[184:187], v[124:127]\n"
      "v_mfma_f32_16x16x32_bf16 v[120:123], v[164:167], v[180:183], v[120:123]\n"
      "v_mfma_f32_16x16x32_bf16 v[116:119], v[164:167], v[176:179], v[116:119]\n"
      "v_mfma_f32_16x16x32_bf16 v[124:127], v[168:171], v[180:183], v[124:127]\n"
      "v_mfma_f32_16x16x32_bf16 v[120:123], v[168:171], v[176:179], v[120:123]\n"
      "v_mfma_f32_16x16x32_bf16 v[124:127], v[172:175], v[176:179], v[124:127]\n"
      "s_setprio 3\n"
      "s_cmp_eq_u32 s65, 0\n"
      "s_cbranch_scc1 .Lx_150\n"
      "ds_write_b128 v14, v[96:99] offset:0\n"
      "ds_write_b128 v14, v[100:103] offset:1024\n"
      "ds_write_b128 v14, v[104:107] offset:2048\n"
      "ds_write_b128 v14, v[108:111] offset:3072\n"
      "ds_add_u32 v19, v3 offset:4176\n"
      "v_mov_b64 v[96:97], v[112:113]\n"
      "v_mov_b64 v[98:99], v[114:115]\n"
      "v_mov_b64 v[100:101], v[116:117]\n"
      "v_mov_b64 v[102:103], v[118:119]\n"
      "v_mov_b64 v[104:105], v[120:121]\n"
      "v_mov_b64 v[106:107], v[122:123]\n"
      "v_mov_b64 v[108:109], v[124:125]\n"
      "v_mov_b64 v[110:111], v[126:127]\n"
      "s_branch .Lx_151\n"
      ".Lx_150:\n"
      "ds_write_b128 v14, v[112:115] offset:0\n"
      "s_nop 0\n"
      "ds_write_b128 v14, v[116:119] offset:1024\n"
      "s_nop 0\n"
      "ds_write_b128 v14, v[120:123] offset:2048\n"
      "ds_write_b128 v14, v[124:127] offset:3072\n"
      "ds_add_u32 v19, v3 offset:4176\n"
      ".Lx_151:\n"
      "s_mov_b32 s3, 0x80000\n"
      ".Lp_152:\n"
      "ds_read_b32 v31, v20 offset:4176\n"
      "s_sub_u32 s3, s3, 1\n"
      "s_waitcnt lgkmcnt(0)\n"
      "v_readlane_b32 s2, v31, 0\n"
      "s_cselect_b32 s2, -1, s2\n"
      "s_cmp_lt_u32 s2, 1\n"
      "s_cbranch_scc1 .Lp_152\n"
      "ds_read_b128 v[72:75], v15 offset:0\n"
      "ds_read_b128 v[76:79], v15 offset:1024\n"
      "ds_read_b128 v[80:83], v15 offset:2048\n"
      "ds_read_b128 v[84:87], v15 offset:3072\n"
      "ds_add_u32 v2, v3 offset:4136\n"
      "s_waitcnt lgkmcnt(3)\n"
      "v_add_f32 v96, v96, v72\n"
      "v_add_f32 v97, v97, v73\n"
      "v_add_f32 v98, v98, v74\n"
      "v_add_f32 v99, v99, v75\n"
      "v_add_f32 v100, v100, v76\n"
      "v_add_f32 v101, v101, v77\n"
      "v_add_f32 v102, v102, v78\n"
      "v_add_f32 v103, v103, v79\n"
      "v_cvt_pk_bf16_f32 v88, v96, v97\n"
      "v_cvt_pk_bf16_f32 v90, v100, v101\n"
      "v_cvt_pk_bf16_f32 v89, v98, v99\n"
      "v_cvt_pk_bf16_f32 v91, v102, v103\n"
      "v_permlane16_swap_b32 v88, v90\n"
      "s_nop 0\n"
      "v_permlane16_swap_b32 v89, v91\n"
      "buffer_store_dwordx4 v[88:91], v5, s[20:23], s80 offen offset:0 sc1\n"
      "s_waitcnt lgkmcnt(1)\n"
      "v_add_f32 v104, v104, v80\n"
      "v_add_f32 v105, v105, v81\n"
      "v_add_f32 v106, v106, v82\n"
      "v_add_f32 v107, v107, v83\n"
      "v_add_f32 v108, v108, v84\n"
      "v_add_f32 v109, v109, v85\n"
      "v_add_f32 v110, v110, v86\n"
      "v_add_f32 v111, v111, v87\n"
      "v_cvt_pk_bf16_f32 v92, v104, v105\n"
      "v_cvt_pk_bf16_f32 v94, v108, v109\n"
      "v_cvt_pk_bf16_f32 v93, v106, v107\n"
      "v_cvt_pk_bf16_f32 v95, v110, v111\n"
      "v_permlane16_swap_b32 v92, v94\n"
      "s_nop 0\n"
      "v_permlane16_swap_b32 v93, v95\n"
      "buffer_store_dwordx4 v[92:95], v5, s[20:23], s80 offen offset:1024 sc1\n"
      "s_lshl_b32 s62, s60, 7\n"
      "s_add_u32 s62, s62, 18864128\n"
      "s_waitcnt vmcnt(0)\n"
      "ds_add_rtn_u32 v31, v2, v3 offset:4172\n"
      "s_waitcnt lgkmcnt(0)\n"
      "v_readfirstlane_b32 s2, v31\n"
      "s_cmp_eq_u32 s2, 3\n"
      "s_cselect_b32 s62, s62, 1073741824\n"
      "s_mov_b64 exec, 1\n"
      "buffer_store_dwordx2 v[12:13], off, s[20:23], s62 sc1\n"
      "s_mov_b64 exec, -1\n"
      "ds_read_b32 v31, v2 offset:4136\n"
      "s_waitcnt lgkmcnt(0)\n"
      "v_readfirstlane_b32 s2, v31\n"
      "s_cmp_lt_u32 s2, 4\n"
      "s_cbranch_scc1 .Lw_153\n"
      ".Lw_154:\n"
      "s_mul_i32 s62, s53, 2560\n"
      "s_add_u32 s63, s73, 32768\n"
      "s_add_u32 s64, s73, 65536\n"
      "s_add_u32 m0, s62, 139520\n"
      "s_mov_b32 s3, 0x80000\n"
      ".Lp_156:\n"
      "s_sleep 1\n"
      "ds_read_b32 v31, v2 offset:4192\n"
      "s_sub_u32 s3, s3, 1\n"
      "s_waitcnt lgkmcnt(0)\n"
      "v_readlane_b32 s2, v31, s53\n"
      "s_cselect_b32 s2, -1, s2\n"
      "s_cmp_lt_u32 s2, 18\n"
      "s_cbranch_scc1 .Lp_156\n"
      "buffer_load_dwordx2 v[26:27], v25, s[20:23], 0 offen sc1\n"
      "s_mov_b32 s3, 0x80000\n"
      ".Lp_157:\n"
      "s_sleep 1\n"
      "ds_read_b32 v31, v2 offset:4192\n"
      "s_sub_u32 s3, s3, 1\n"
      "s_waitcnt lgkmcnt(0)\n"
      "v_readlane_b32 s2, v31, s53\n"
      "s_cselect_b32 s2, -1, s2\n"
      "s_cmp_lt_u32 s2, 22\n"
      "s_cbranch_scc1 .Lp_157\n"
      "buffer_load_dwordx4 v[164:167], v9, s[20:23], s72 offen offset:0 sc1\n"
      "buffer_load_dwordx4 v[168:171], v9, s[20:23], s72 offen offset:1024 sc1\n"
      "buffer_load_dwordx4 v[172:175], v9, s[20:23], s72 offen offset:2048 sc1\n"
      "s_waitcnt vmcnt(3)\n"
      "v_cmp_ne_u64 vcc, s[58:59], v[26:27]\n"
      "s_mov_b32 s66, 0\n"
      "s_cbranch_vccnz .Lq_158\n"
      "buffer_load_dwordx4 v6, s[20:23], s73 offen sc1 lds\n"
      "buffer_load_dwordx4 v6, s[20:23], s63 offen offset:1024 sc1 lds\n"
      "s_mov_b32 exec_hi, 0\n"
      "buffer_load_dwordx4 v6, s[20:23], s64 offen offset:2048 sc1 lds\n"
      "s_mov_b32 exec_hi, -1\n"
      "s_mov_b32 s66, 1\n"
      "s_waitcnt vmcnt(3)\n"
      ".Lq_159:\n"
      "s_waitcnt vmcnt(63)\n"
      "v_xor_b32 v21, s74, v167\n"
      "v_bitop3_b32 v21, v21, v171, s74 bitop3:0xf6\n"
      "v_bitop3_b32 v21, v21, v175, s74 bitop3:0xf6\n"
      "v_cmp_lt_u32 vcc, 0xffff, v21\n"
      "s_cbranch_vccnz .Ly_160\n"
      ".Ly_161:\n"
      "v_lshlrev_b32 v144, 16, v164\n"
      "v_and_b32 v145, s92, v164\n"
      "v_lshlrev_b32 v146, 16, v165\n"
      "v_and_b32 v147, s92, v165\n"
      "v_lshlrev_b32 v148, 16, v166\n"
      "v_and_b32 v149, s92, v166\n"
      "v_lshlrev_b32 v150, 16, v167\n"
      "v_lshlrev_b32 v151, 16, v168\n"
      "v_and_b32 v152, s92, v168\n"
      "v_lshlrev_b32 v153, 16, v169\n"
      "v_and_b32 v154, s92, v169\n"
      "v_lshlrev_b32 v155, 16, v170\n"
      "v_and_b32 v156, s92, v170\n"
      "v_lshlrev_b32 v157, 16, v171\n"
      "v_lshlrev_b32 v158, 16, v172\n"
      "v_and_b32 v159, s92, v172\n"
      "v_lshlrev_b32 v160, 16, v173\n"
      "v_and_b32 v161, s92, v173\n"
      "v_lshlrev_b32 v162, 16, v174\n"
      "v_and_b32 v163, s92, v174\n"
      "s_mov_b32 s77, s76\n"
      "v_pk_mul_f32 v[144:145], v[144:145], s[76:77]\n"
      "v_pk_mul_f32 v[146:147], v[146:147], s[76:77]\n"
      "v_pk_mul_f32 v[148:149], v[148:149], s[76:77]\n"
      "v_pk_mul_f32 v[150:151], v[150:151], s[76:77]\n"
      "v_pk_mul_f32 v[152:153], v[152:153], s[76:77]\n"
      "v_pk_mul_f32 v[154:155], v[154:155], s[76:77]\n"
      "v_pk_mul_f32 v[156:157], v[156:157], s[76:77]\n"
      "v_pk_mul_f32 v[158:159], v[158:159], s[76:77]\n"
      "v_pk_mul_f32 v[160:161], v[160:161], s[76:77]\n"
      "v_pk_mul_f32 v[162:163], v[162:163], s[76:77]\n"
      "v_mov_b32 v22, 1.0\n"
      "v_mov_b32 v23, 1.0\n"
      "s_cmp_eq_u32 s66, 0\n"
      "s_cbranch_scc1 .Lq_163\n"
      ".Lq_164:\n"
      "s_mov_b32 s3, 0x80000\n"
      ".Lp_166:\n"
      "ds_read_b32 v31, v2 offset:4192\n"
      "s_sub_u32 s3, s3, 1\n"
      "s_waitcnt lgkmcnt(0)\n"
      "v_readlane_b32 s2, v31, s53\n"
      "s_cselect_b32 s2, -1, s2\n"
      "s_cmp_lt_u32 s2, 32\n"
      "s_cbranch_scc1 .Lp_166\n"
      "ds_read_b128 v[176:179], v8 offset:0\n"
      "ds_read_b128 v[180:183], v8 offset:1024\n"
      "s_waitcnt lgkmcnt(1)\n"
      "v_pk_fma_f32 v[176:177], v[176:177], s[76:77], v[144:145]\n"
      "v_pk_fma_f32 v[178:179], v[178:179], s[76:77], v[146:147]\n"
      "s_waitcnt lgkmcnt(0)\n"
      "v_pk_fma_f32 v[180:181], v[180:181], s[76:77], v[148:149]\n"
      "v_pk_fma_f32 v[182:183], v[182:183], s[76:77], v[150:151]\n"
      "v_exp_f32 v196, v176\n"
      "v_exp_f32 v197, v177\n"
      "v_exp_f32 v198, v178\n"
      "v_exp_f32 v199, v179\n"
      "v_exp_f32 v200, v180\n"
      "v_exp_f32 v201, v181\n"
      "v_exp_f32 v202, v182\n"
      "v_exp_f32 v203, v183\n"
      "v_pk_mul_f32 v[52:53], v[52:53], v[176:177]\n"
      "v_pk_mul_f32 v[54:55], v[54:55], v[178:179]\n"
      "v_pk_mul_f32 v[56:57], v[56:57], v[180:181]\n"
      "v_pk_mul_f32 v[58:59], v[58:59], v[182:183]\n"
      "v_pk_add_f32 v[196:197], v[196:197], v[22:23]\n"
      "v_pk_add_f32 v[198:199], v[198:199], v[22:23]\n"
      "v_pk_add_f32 v[200:201], v[200:201], v[22:23]\n"
      "v_pk_add_f32 v[202:203], v[202:203], v[22:23]\n"
      "v_rcp_f32 v196, v196\n"
      "v_rcp_f32 v197, v197\n"
      "v_rcp_f32 v198, v198\n"
      "v_rcp_f32 v199, v199\n"
      "v_rcp_f32 v200, v200\n"
      "v_rcp_f32 v201, v201\n"
      "v_rcp_f32 v202, v202\n"
      "v_rcp_f32 v203, v203\n"
      "v_pk_mul_f32 v[52:53], v[52:53], v[196:197]\n"
      "v_pk_mul_f32 v[54:55], v[54:55], v[198:199]\n"
      "v_pk_mul_f32 v[56:57], v[56:57], v[200:201]\n"
      "v_pk_mul_f32 v[58:59], v[58:59], v[202:203]\n"
      "s_cmp_eq_u32 s66, 0\n"
      "s_cbranch_scc1 .Lq_167\n"
      ".Lq_168:\n"
      "s_waitcnt vmcnt(0)\n"
      "ds_add_u32 v2, v3 offset:4140\n"
      "s_mov_b32 s3, 0x80000\n"
      ".Lp_171:\n"
      "ds_read_b32 v31, v2 offset:4140\n"
      "s_sub_u32 s3, s3, 1\n"
      "s_waitcnt lgkmcnt(0)\n"
      "v_readlane_b32 s2, v31, 0\n"
      "s_cselect_b32 s2, -1, s2\n"
      "s_cmp_lt_u32 s2, 4\n"
      "s_cbranch_scc1 .Lp_171\n"
      "ds_read_b64_tr_b16 v[218:219], v7 offset:0\n"
      "ds_read_b64_tr_b16 v[220:221], v7 offset:2048\n"
      "ds_read_b64_tr_b16 v[222:223], v7 offset:4096\n"
      "ds_read_b64_tr_b16 v[224:225], v7 offset:6144\n"
      "ds_read_b64_tr_b16 v[226:227], v7 offset:8192\n"
      "s_waitcnt lgkmcnt(3)\n"
      "v_lshlrev_b32 v72, 16, v218\n"
      "v_and_b32 v73, s92, v218\n"
      "v_lshlrev_b32 v74, 16, v219\n"
      "v_and_b32 v75, s92, v219\n"
      "v_lshlrev_b32 v76, 16, v220\n"
      "v_and_b32 v77, s92, v220\n"
      "v_lshlrev_b32 v78, 16, v221\n"
      "v_and_b32 v79, s92, v221\n"
      "s_waitcnt lgkmcnt(0)\n"
      "v_lshlrev_b32 v80, 16, v222\n"
      "v_and_b32 v81, s92, v222\n"
      "v_lshlrev_b32 v82, 16, v223\n"
      "v_and_b32 v83, s92, v223\n"
      "v_lshlrev_b32 v84, 16, v224\n"
      "v_and_b32 v85, s92, v224\n"
      "v_lshlrev_b32 v86, 16, v225\n"
      "v_and_b32 v87, s92, v225\n"
      "v_lshlrev_b32 v88, 16, v226\n"
      "v_and_b32 v89, s92, v226\n"
      "v_lshlrev_b32 v90, 16, v227\n"
      "v_and_b32 v91, s92, v227\n"
      "v_pk_fma_f32 v[52:53], v[52:53], v[72:73], v[32:33]\n"
      "v_pk_fma_f32 v[54:55], v[54:55], v[74:75], v[34:35]\n"
      "v_pk_fma_f32 v[56:57], v[56:57], v[76:77], v[36:37]\n"
      "v_pk_fma_f32 v[58:59], v[58:59], v[78:79], v[38:39]\n"
      "v_cvt_pk_bf16_f32 v218, v52, v53\n"
      "v_cvt_pk_bf16_f32 v219, v54, v55\n"
      "buffer_store_dwordx2 v[218:219], v4, s[48:51], s70 offen offset:0\n"
      "v_cvt_pk_bf16_f32 v220, v56, v57\n"
      "v_cvt_pk_bf16_f32 v221, v58, v59\n"
      "buffer_store_dwordx2 v[220:221], v4, s[48:51], s70 offen offset:32\n"
      "s_mov_b32 s3, 0x80000\n"
      ".Lp_172:\n"
      "ds_read_b32 v31, v2 offset:4192\n"
      "s_sub_u32 s3, s3, 1\n"
      "s_waitcnt lgkmcnt(0)\n"
      "v_readlane_b32 s2, v31, s53\n"
      "s_cselect_b32 s2, -1, s2\n"
      "s_cmp_lt_u32 s2, 34\n"
      "s_cbranch_scc1 .Lp_172\n"
      "ds_read_b128 v[184:187], v8 offset:2048\n"
      "ds_read_b128 v[188:191], v8 offset:3072\n"
      "s_waitcnt lgkmcnt(1)\n"
      "v_pk_fma_f32 v[184:185], v[184:185], s[76:77], v[152:153]\n"
      "v_pk_fma_f32 v[186:187], v[186:187], s[76:77], v[154:155]\n"
      "s_waitcnt lgkmcnt(0)\n"
      "v_pk_fma_f32 v[188:189], v[188:189], s[76:77], v[156:157]\n"
      "v_pk_fma_f32 v[190:191], v[190:191], s[76:77], v[158:159]\n"
      "v_exp_f32 v204, v184\n"
      "v_exp_f32 v205, v185\n"
      "v_exp_f32 v206, v186\n"
      "v_exp_f32 v207, v187\n"
      "v_exp_f32 v208, v188\n"
      "v_exp_f32 v209, v189\n"
      "v_exp_f32 v210, v190\n"
      "v_exp_f32 v211, v191\n"
      "v_pk_mul_f32 v[60:61], v[60:61], v[184:185]\n"
      "v_pk_mul_f32 v[62:63], v[62:63], v[186:187]\n"
      "v_pk_mul_f32 v[64:65], v[64:65], v[188:189]\n"
      "v_pk_mul_f32 v[66:67], v[66:67], v[190:191]\n"
      "v_pk_add_f32 v[204:205], v[204:205], v[22:23]\n"
      "v_pk_add_f32 v[206:207], v[206:207], v[22:23]\n"
      "v_pk_add_f32 v[208:209], v[208:209], v[22:23]\n"
      "v_pk_add_f32 v[210:211], v[210:211], v[22:23]\n"
      "v_rcp_f32 v204, v204\n"
      "v_rcp_f32 v205, v205\n"
      "v_rcp_f32 v206, v206\n"
      "v_rcp_f32 v207, v207\n"
      "v_rcp_f32 v208, v208\n"
      "v_rcp_f32 v209, v209\n"
      "v_rcp_f32 v210, v210\n"
      "v_rcp_f32 v211, v211\n"
      "v_pk_mul_f32 v[60:61], v[60:61], v[204:205]\n"
      "v_pk_mul_f32 v[62:63], v[62:63], v[206:207]\n"
      "v_pk_mul_f32 v[64:65], v[64:65], v[208:209]\n"
      "v_pk_mul_f32 v[66:67], v[66:67], v[210:211]\n"
      "v_pk_fma_f32 v[60:61], v[60:61], v[80:81], v[40:41]\n"
      "v_pk_fma_f32 v[62:63], v[62:63], v[82:83], v[42:43]\n"
      "v_pk_fma_f32 v[64:65], v[64:65], v[84:85], v[44:45]\n"
      "v_pk_fma_f32 v[66:67], v[66:67], v[86:87], v[46:47]\n"
      "v_cvt_pk_bf16_f32 v222, v60, v61\n"
      "v_cvt_pk_bf16_f32 v223, v62, v63\n"
      "buffer_store_dwordx2 v[222:223], v4, s[48:51], s70 offen offset:64\n"
      "v_cvt_pk_bf16_f32 v224, v64, v65\n"
      "v_cvt_pk_bf16_f32 v225, v66, v67\n"
      "buffer_store_dwordx2 v[224:225], v4, s[48:51], s70 offen offset:96\n"
      "s_mov_b32 s3, 0x80000\n"
      ".Lp_173:\n"
      "ds_read_b32 v31, v2 offset:4192\n"
      "s_sub_u32 s3, s3, 1\n"
      "s_waitcnt lgkmcnt(0)\n"
      "v_readlane_b32 s2, v31, s53\n"
      "s_cselect_b32 s2, -1, s2\n"
      "s_cmp_lt_u32 s2, 35\n"
      "s_cbranch_scc1 .Lp_173\n"
      "ds_read_b128 v[192:195], v8 offset:4096\n"
      "s_waitcnt lgkmcnt(0)\n"
      "v_pk_fma_f32 v[192:193], v[192:193], s[76:77], v[160:161]\n"
      "v_pk_fma_f32 v[194:195], v[194:195], s[76:77], v[162:163]\n"
      "v_exp_f32 v212, v192\n"
      "v_exp_f32 v213, v193\n"
      "v_exp_f32 v214, v194\n"
      "v_exp_f32 v215, v195\n"
      "v_pk_mul_f32 v[68:69], v[68:69], v[192:193]\n"
      "v_pk_mul_f32 v[70:71], v[70:71], v[194:195]\n"
      "v_pk_add_f32 v[212:213], v[212:213], v[22:23]\n"
      "v_pk_add_f32 v[214:215], v[214:215], v[22:23]\n"
      "v_rcp_f32 v212, v212\n"
      "v_rcp_f32 v213, v213\n"
      "v_rcp_f32 v214, v214\n"
      "v_rcp_f32 v215, v215\n"
      "v_pk_mul_f32 v[68:69], v[68:69], v[212:213]\n"
      "v_pk_mul_f32 v[70:71], v[70:71], v[214:215]\n"
      "v_pk_fma_f32 v[68:69], v[68:69], v[88:89], v[48:49]\n"
      "v_pk_fma_f32 v[70:71], v[70:71], v[90:91], v[50:51]\n"
      "v_cvt_pk_bf16_f32 v226, v68, v69\n"
      "v_cvt_pk_bf16_f32 v227, v70, v71\n"
      "buffer_store_dwordx2 v[226:227], v4, s[48:51], s70 offen offset:128\n"
      "s_endpgm\n"
      ".LSPARE:\n"
      "s_and_b32 s70, s60, 7\n"
      "s_lshl2_add_u32 s70, s70, s61\n"
      "s_sub_u32 s70, s70, 28\n"
      "s_xor_b32 s71, s52, 4\n"
      "s_lshl_b32 s71, s71, 5\n"
      "s_add_u32 s71, s71, s70\n"
      "s_add_u32 s71, s71, 224\n"
      "v_lshrrev_b32 v20, 4, v1\n"
      "v_mul_u32_u24 v20, 1280, v20\n"
      "v_and_b32 v21, 15, v1\n"
      "v_lshl_add_u32 v20, v21, 4, v20\n"
      "v_add_u32 v21, 4080, v20\n"
      "v_add_u32 v22, 8160, v20\n"
      "v_add_u32 v23, 12240, v20\n"
      "s_mul_i32 s72, s71, 205\n"
      "s_lshr_b32 s72, s72, 10\n"
      "s_mul_i32 s73, s72, 5\n"
      "s_sub_u32 s73, s71, s73\n"
      "s_mul_i32 s74, s53, 8320\n"
      "s_add_u32 s74, s74, 53248\n"
      "s_cmp_lt_u32 s52, 4\n"
      "s_cbranch_scc1 .Ls_174\n"
      "s_waitcnt lgkmcnt(0)\n"
      "s_barrier\n"
      "buffer_load_dwordx2 v[42:43], off, s[20:23], 0 sc1\n"
      "s_cmp_lt_u32 s71, 270\n"
      "s_cbranch_scc0 .Ls_174\n"
      "s_mul_i32 s62, s72, 40960\n"
      "s_lshl_b32 s63, s73, 8\n"
      "s_add_u32 s62, s62, s63\n"
      "s_mov_b32 m0, s74\n"
      "s_add_u32 s63, s62, 20480\n"
      "buffer_load_dwordx4 v20, s[40:43], s62 offen offset:0 nt lds\n"
      "buffer_load_dwordx4 v21, s[40:43], s62 offen offset:1040 nt lds\n"
      "buffer_load_dwordx4 v22, s[40:43], s62 offen offset:2080 nt lds\n"
      "buffer_load_dwordx4 v23, s[40:43], s62 offen offset:3120 nt lds\n"
      "s_add_u32 m0, s74, 4160\n"
      "s_nop 0\n"
      "buffer_load_dwordx4 v20, s[40:43], s63 offen offset:0 nt lds\n"
      "buffer_load_dwordx4 v21, s[40:43], s63 offen offset:1040 nt lds\n"
      "buffer_load_dwordx4 v22, s[40:43], s63 offen offset:2080 nt lds\n"
      "buffer_load_dwordx4 v23, s[40:43], s63 offen offset:3120 nt lds\n"
      ".Ls_174:\n"
      "v_lshrrev_b32 v24, 3, v1\n"
      "v_mul_u32_u24 v25, 1280, v24\n"
      "v_and_b32 v26, 7, v1\n"
      "v_lshl_add_u32 v25, v26, 4, v25\n"
      "v_cmp_gt_u32 vcc, 40, v1\n"
      "v_mov_b32 v26, 1073741824\n"
      "v_lshlrev_b32 v24, 4, v24\n"
      "v_cndmask_b32 v26, v25, v26, vcc\n"
      "s_mul_i32 s62, s70, 3\n"
      "s_add_u32 s62, s62, 224\n"
      "s_mul_i32 s64, s52, 1040\n"
      "s_mul_i32 s65, s52, 10240\n"
      "s_mul_i32 s80, s62, 205\n"
      "s_lshr_b32 s80, s80, 11\n"
      "s_mul_i32 s81, s80, 10\n"
      "s_sub_u32 s81, s62, s81\n"
      "s_mul_i32 s86, s80, 81920\n"
      "s_lshl_b32 s63, s81, 7\n"
      "s_add_u32 s86, s86, s63\n"
      "s_add_u32 m0, s64, 1040\n"
      "s_add_u32 s63, s86, s65\n"
      "buffer_load_dwordx4 v25, s[12:15], s63 offen nt lds\n"
      "s_add_u32 m0, s64, 9360\n"
      "s_add_u32 s62, s62, 1\n"
      "buffer_load_dwordx4 v25, s[16:19], s63 offen nt lds\n"
      "s_mul_i32 s82, s62, 205\n"
      "s_lshr_b32 s82, s82, 11\n"
      "s_mul_i32 s83, s82, 10\n"
      "s_sub_u32 s83, s62, s83\n"
      "s_mul_i32 s87, s82, 81920\n"
      "s_lshl_b32 s63, s83, 7\n"
      "s_add_u32 s87, s87, s63\n"
      "s_add_u32 m0, s64, 18720\n"
      "s_add_u32 s63, s87, s65\n"
      "buffer_load_dwordx4 v25, s[12:15], s63 offen nt lds\n"
      "s_add_u32 m0, s64, 27040\n"
      "s_add_u32 s62, s62, 1\n"
      "buffer_load_dwordx4 v25, s[16:19], s63 offen nt lds\n"
      "s_mul_i32 s84, s62, 205\n"
      "s_lshr_b32 s84, s84, 11\n"
      "s_mul_i32 s85, s84, 10\n"
      "s_sub_u32 s85, s62, s85\n"
      "s_mul_i32 s88, s84, 81920\n"
      "s_lshl_b32 s63, s85, 7\n"
      "s_add_u32 s88, s88, s63\n"
      "s_add_u32 m0, s64, 36400\n"
      "s_add_u32 s63, s88, s65\n"
      "buffer_load_dwordx4 v25, s[12:15], s63 offen nt lds\n"
      "s_add_u32 m0, s64, 44720\n"
      "s_add_u32 s62, s62, 1\n"
      "buffer_load_dwordx4 v25, s[16:19], s63 offen nt lds\n"
      "s_cmp_eq_u32 s52, 0\n"
      "s_cselect_b32 s62, s86, s88\n"
      "s_cselect_b32 s63, 0, 35360\n"
      "s_cmp_eq_u32 s52, 1\n"
      "s_cselect_b32 s62, s87, s62\n"
      "s_cselect_b32 m0, 17680, s63\n"
      "s_sub_u32 s62, s62, 10240\n"
      "buffer_load_dwordx4 v26, s[12:15], s62 offen nt lds\n"
      "s_cmp_lt_u32 s52, 4\n"
      "s_cselect_b32 s80, s80, s82\n"
      "s_cselect_b32 s81, s81, s83\n"
      "s_cselect_b32 s89, 0, 17680\n"
      "s_lshl2_add_u32 s81, s81, s53\n"
      "s_lshl2_add_u32 s85, s85, s53\n"
      "s_lshl_b32 s62, s81, 7\n"
      "s_lshl_b32 s63, s85, 7\n"
      "buffer_load_dwordx4 v[176:179], v24, s[24:27], s62 offen\n"
      "buffer_load_dwordx4 v[180:183], v24, s[24:27], s63 offen\n"
      "v_mov_b64 v[36:37], 0\n"
      "v_mov_b64 v[38:39], 0\n"
      "s_lshl_b32 s63, s53, 10\n"
      "s_add_u32 s63, s63, 157696\n"
      "v_lshl_add_u32 v40, v1, 4, s63\n"
      "ds_write_b128 v40, v[36:39]\n"
      "v_lshlrev_b32 v40, 2, v1\n"
      "v_add_u32 v40, 161792, v40\n"
      "ds_write_b32 v40, v36\n"
      "v_lshrrev_b32 v20, 4, v1\n"
      "v_and_b32 v21, 1, v20\n"
      "v_lshrrev_b32 v20, 1, v20\n"
      "v_lshl_add_u32 v20, v21, 2, v20\n"
      "v_mul_u32_u24 v20, 1040, v20\n"
      "v_bfe_u32 v21, v1, 2, 2\n"
      "v_lshl_add_u32 v20, v21, 8, v20\n"
      "v_and_b32 v21, 3, v1\n"
      "v_lshl_add_u32 v20, v21, 3, v20\n"
      "v_lshlrev_b32 v22, 4, v1\n"
      "s_lshl3_add_u32 s62, s72, s73\n"
      "s_lshl_b32 s62, s62, 7\n"
      "s_add_u32 s62, s62, 18808832\n"
      "s_cmp_lt_u32 s71, 270\n"
      "s_cselect_b32 s62, s62, 1073741824\n"
      "v_mov_b32 v23, 1073741824\n"
      "v_writelane_b32 v23, s62, 0\n"
      "v_add_u32 v21, s74, v20\n"
      "s_lshr_b32 s63, s72, 2\n"
      "s_and_b32 s62, s72, 3\n"
      "s_mul_i32 s62, s62, 7\n"
      "s_add_u32 s62, s62, s63\n"
      "s_cmp_ge_u32 s63, 7\n"
      "s_cselect_b32 s63, 21, 0\n"
      "s_add_u32 s62, s62, s63\n"
      "s_lshl_b32 s62, s62, 10\n"
      "s_mul_i32 s63, s73, 458752\n"
      "s_add_u32 s62, s62, s63\n"
      "s_add_u32 s62, s62, 5603328\n"
      "s_waitcnt vmcnt(9)\n"
      "v_cmp_eq_u32 vcc, 0, v1\n"
      "v_lshlrev_b32 v2, 2, v1\n"
      "v_add_u32 v2, 157696, v2\n"
      "v_cndmask_b32 v3, 0, 1, vcc\n"
      "v_readfirstlane_b32 s58, v42\n"
      "v_readfirstlane_b32 s59, v43\n"
      "s_add_u32 s58, s58, 1\n"
      "s_addc_u32 s59, s59, 0\n"
      "v_mov_b64 v[12:13], s[58:59]\n"
      "s_cmp_lt_u32 s71, 270\n"
      "s_cbranch_scc0 .Ls_175\n"
      "ds_read_b64_tr_b16 v[96:97], v21 offset:0\n"
      "ds_read_b64_tr_b16 v[98:99], v21 offset:2080\n"
      "ds_read_b64_tr_b16 v[100:101], v21 offset:32\n"
      "ds_read_b64_tr_b16 v[102:103], v21 offset:2112\n"
      "ds_read_b64_tr_b16 v[104:105], v21 offset:64\n"
      "ds_read_b64_tr_b16 v[106:107], v21 offset:2144\n"
      "ds_read_b64_tr_b16 v[108:109], v21 offset:96\n"
      "ds_read_b64_tr_b16 v[110:111], v21 offset:2176\n"
      "s_waitcnt lgkmcnt(6)\n"
      "ds_read_b64_tr_b16 v[112:113], v21 offset:128\n"
      "ds_read_b64_tr_b16 v[114:115], v21 offset:2208\n"
      "ds_read_b64_tr_b16 v[116:117], v21 offset:160\n"
      "ds_read_b64_tr_b16 v[118:119], v21 offset:2240\n"
      "ds_read_b64_tr_b16 v[120:121], v21 offset:192\n"
      "ds_read_b64_tr_b16 v[122:123], v21 offset:2272\n"
      "ds_read_b64_tr_b16 v[124:125], v21 offset:224\n"
      "ds_read_b64_tr_b16 v[126:127], v21 offset:2304\n"
      "s_waitcnt lgkmcnt(14)\n"
      "buffer_store_dwordx4 v[96:99], v22, s[20:23], s62 offen sc1\n"
      "s_add_u32 s62, s62, 57344\n"
      "s_waitcnt lgkmcnt(12)\n"
      "buffer_store_dwordx4 v[100:103], v22, s[20:23], s62 offen sc1\n"
      "s_add_u32 s62, s62, 57344\n"
      "s_waitcnt lgkmcnt(10)\n"
      "buffer_store_dwordx4 v[104:107], v22, s[20:23], s62 offen sc1\n"
      "s_add_u32 s62, s62, 57344\n"
      "s_waitcnt lgkmcnt(8)\n"
      "buffer_store_dwordx4 v[108:111], v22, s[20:23], s62 offen sc1\n"
      "s_add_u32 s62, s62, 57344\n"
      "s_waitcnt lgkmcnt(6)\n"
      "buffer_store_dwordx4 v[112:115], v22, s[20:23], s62 offen sc1\n"
      "s_add_u32 s62, s62, 57344\n"
      "s_waitcnt lgkmcnt(4)\n"
      "buffer_store_dwordx4 v[116:119], v22, s[20:23], s62 offen sc1\n"
      "s_add_u32 s62, s62, 57344\n"
      "s_waitcnt lgkmcnt(2)\n"
      "buffer_store_dwordx4 v[120:123], v22, s[20:23], s62 offen sc1\n"
      "s_add_u32 s62, s62, 57344\n"
      "s_waitcnt lgkmcnt(0)\n"
      "buffer_store_dwordx4 v[124:127], v22, s[20:23], s62 offen sc1\n"
      "s_waitcnt vmcnt(8)\n"
      "s_branch .Ls_176\n"
      ".Ls_175:\n"
      "s_waitcnt vmcnt(0)\n"
      ".Ls_176:\n"
      "s_lshl_b32 s62, s53, 5\n"
      "s_add_u32 s62, s62, s89\n"
      "s_add_u32 s62, s62, 640\n"
      "v_and_b32 v53, 7, v1\n"
      "v_lshrrev_b32 v54, 3, v1\n"
      "v_mul_u32_u24 v78, 1040, v53\n"
      "v_lshl_add_u32 v78, v54, 2, v78\n"
      "v_add_u32 v78, s62, v78\n"
      "v_add_u32 v79, 8720, v78\n"
      "v_mul_u32_u24 v8, 8448, v54\n"
      "v_lshl_add_u32 v8, v53, 4, v8\n"
      "v_lshlrev_b32 v70, 16, v176\n"
      "v_lshlrev_b32 v71, 16, v178\n"
      "v_and_b32 v72, s92, v176\n"
      "v_and_b32 v73, s92, v178\n"
      "v_lshlrev_b32 v74, 16, v177\n"
      "v_lshlrev_b32 v75, 16, v179\n"
      "v_and_b32 v76, s92, v177\n"
      "v_and_b32 v77, s92, v179\n"
      "s_mul_i32 s62, s81, 67584\n"
      "s_lshl_b32 s63, s80, 7\n"
      "s_add_u32 s62, s62, s63\n"
      "s_add_u32 s62, s62, 32768\n"
      "s_add_u32 s63, s62, 4224\n"
      "s_mov_b32 s64, 0\n"
      "s_waitcnt lgkmcnt(0)\n"
      "s_barrier\n"
      ".LSKV:\n"
      "ds_read2_b32 v[196:197], v78 offset0:0 offset1:32\n"
      "ds_read2_b32 v[198:199], v78 offset0:64 offset1:100\n"
      "ds_read2_b32 v[200:201], v78 offset0:132 offset1:164\n"
      "ds_read2_b32 v[202:203], v78 offset0:196 offset1:228\n"
      "ds_read_b32 v204, v78 offset:1040\n"
      "ds_read_b32 v205, v78 offset:1168\n"
      "ds_read_b32 v206, v78 offset:1296\n"
      "ds_read2_b32 v[208:209], v79 offset0:0 offset1:32\n"
      "ds_read2_b32 v[210:211], v79 offset0:64 offset1:96\n"
      "ds_read2_b32 v[212:213], v79 offset0:128 offset1:160\n"
      "ds_read2_b32 v[214:215], v79 offset0:192 offset1:224\n"
      "s_waitcnt lgkmcnt(9)\n"
      "v_lshlrev_b32 v216, 16, v196\n"
      "v_and_b32 v217, s92, v196\n"
      "v_lshlrev_b32 v218, 16, v197\n"
      "v_and_b32 v219, s92, v197\n"
      "v_lshlrev_b32 v220, 16, v198\n"
      "v_and_b32 v221, s92, v198\n"
      "v_lshlrev_b32 v222, 16, v199\n"
      "v_and_b32 v223, s92, v199\n"
      "s_waitcnt lgkmcnt(7)\n"
      "v_lshlrev_b32 v224, 16, v200\n"
      "v_and_b32 v225, s92, v200\n"
      "v_lshlrev_b32 v226, 16, v201\n"
      "v_and_b32 v227, s92, v201\n"
      "v_lshlrev_b32 v228, 16, v202\n"
      "v_and_b32 v229, s92, v202\n"
      "v_lshlrev_b32 v230, 16, v203\n"
      "v_and_b32 v231, s92, v203\n"
      "s_waitcnt lgkmcnt(4)\n"
      "v_lshlrev_b32 v232, 16, v204\n"
      "v_and_b32 v233, s92, v204\n"
      "v_lshlrev_b32 v234, 16, v205\n"
      "v_and_b32 v235, s92, v205\n"
      "v_lshlrev_b32 v236, 16, v206\n"
      "v_and_b32 v237, s92, v206\n"
      "v_pk_mul_f32 v[240:241], v[216:217], v[76:77]\n"
      "v_pk_mul_f32 v[242:243], v[218:219], v[76:77]\n"
      "v_pk_mul_f32 v[244:245], v[220:221], v[76:77]\n"
      "v_pk_mul_f32 v[246:247], v[222:223], v[76:77]\n"
      "v_pk_mul_f32 v[248:249], v[224:225], v[76:77]\n"
      "v_pk_mul_f32 v[250:251], v[226:227], v[76:77]\n"
      "v_pk_mul_f32 v[252:253], v[228:229], v[76:77]\n"
      "v_pk_mul_f32 v[254:255], v[230:231], v[76:77]\n"
      "v_pk_fma_f32 v[240:241], v[218:219], v[74:75], v[240:241]\n"
      "v_pk_fma_f32 v[242:243], v[220:221], v[74:75], v[242:243]\n"
      "v_pk_fma_f32 v[244:245], v[222:223], v[74:75], v[244:245]\n"
      "v_pk_fma_f32 v[246:247], v[224:225], v[74:75], v[246:247]\n"
      "v_pk_fma_f32 v[248:249], v[226:227], v[74:75], v[248:249]\n"
      "v_pk_fma_f32 v[250:251], v[228:229], v[74:75], v[250:251]\n"
      "v_pk_fma_f32 v[252:253], v[230:231], v[74:75], v[252:253]\n"
      "v_pk_fma_f32 v[254:255], v[232:233], v[74:75], v[254:255]\n"
      "v_pk_fma_f32 v[240:241], v[220:221], v[72:73], v[240:241]\n"
      "v_pk_fma_f32 v[242:243], v[222:223], v[72:73], v[242:243]\n"
      "v_pk_fma_f32 v[244:245], v[224:225], v[72:73], v[244:245]\n"
      "v_pk_fma_f32 v[246:247], v[226:227], v[72:73], v[246:247]\n"
      "v_pk_fma_f32 v[248:249], v[228:229], v[72:73], v[248:249]\n"
      "v_pk_fma_f32 v[250:251], v[230:231], v[72:73], v[250:251]\n"
      "v_pk_fma_f32 v[252:253], v[232:233], v[72:73], v[252:253]\n"
      "v_pk_fma_f32 v[254:255], v[234:235], v[72:73], v[254:255]\n"
      "v_pk_fma_f32 v[240:241], v[222:223], v[70:71], v[240:241]\n"
      "v_pk_fma_f32 v[242:243], v[224:225], v[70:71], v[242:243]\n"
      "v_pk_fma_f32 v[244:245], v[226:227], v[70:71], v[244:245]\n"
      "v_pk_fma_f32 v[246:247], v[228:229], v[70:71], v[246:247]\n"
      "v_pk_fma_f32 v[248:249], v[230:231], v[70:71], v[248:249]\n"
      "v_pk_fma_f32 v[250:251], v[232:233], v[70:71], v[250:251]\n"
      "v_pk_fma_f32 v[252:253], v[234:235], v[70:71], v[252:253]\n"
      "v_pk_fma_f32 v[254:255], v[236:237], v[70:71], v[254:255]\n"
      "s_waitcnt lgkmcnt(0)\n"
      "v_lshlrev_b32 v216, 16, v208\n"
      "v_and_b32 v217, s92, v208\n"
      "v_lshlrev_b32 v218, 16, v209\n"
      "v_and_b32 v219, s92, v209\n"
      "v_lshlrev_b32 v220, 16, v210\n"
      "v_and_b32 v221, s92, v210\n"
      "v_lshlrev_b32 v222, 16, v211\n"
      "v_and_b32 v223, s92, v211\n"
      "v_lshlrev_b32 v224, 16, v212\n"
      "v_and_b32 v225, s92, v212\n"
      "v_lshlrev_b32 v226, 16, v213\n"
      "v_and_b32 v227, s92, v213\n"
      "v_lshlrev_b32 v228, 16, v214\n"
      "v_and_b32 v229, s92, v214\n"
      "v_lshlrev_b32 v230, 16, v215\n"
      "v_and_b32 v231, s92, v215\n"
      "v_pk_mul_f32 v[240:241], v[240:241], v[216:217]\n"
      "v_pk_mul_f32 v[242:243], v[242:243], v[218:219]\n"
      "v_pk_mul_f32 v[244:245], v[244:245], v[220:221]\n"
      "v_pk_mul_f32 v[246:247], v[246:247], v[222:223]\n"
      "v_pk_mul_f32 v[248:249], v[248:249], v[224:225]\n"
      "v_pk_mul_f32 v[250:251], v[250:251], v[226:227]\n"
      "v_pk_mul_f32 v[252:253], v[252:253], v[228:229]\n"
      "v_pk_mul_f32 v[254:255], v[254:255], v[230:231]\n"
      "v_cvt_pk_bf16_f32 v196, v242, v240\n"
      "v_cvt_pk_bf16_f32 v197, v246, v244\n"
      "v_cvt_pk_bf16_f32 v198, v250, v248\n"
      "v_cvt_pk_bf16_f32 v199, v254, v252\n"
      "buffer_store_dwordx4 v[196:199], v8, s[20:23], s62 offen sc1\n"
      "v_cvt_pk_bf16_f32 v200, v243, v241\n"
      "v_cvt_pk_bf16_f32 v201, v247, v245\n"
      "v_cvt_pk_bf16_f32 v202, v251, v249\n"
      "v_cvt_pk_bf16_f32 v203, v255, v253\n"
      "buffer_store_dwordx4 v[200:203], v8, s[20:23], s63 offen sc1\n"
      "s_waitcnt vmcnt(2)\n"
      "buffer_store_dwordx2 v[12:13], v23, s[20:23], 0 offen sc1\n"
      "s_lshl_b32 s62, s81, 5\n"
      "s_add_u32 s62, s62, s80\n"
      "s_lshl_b32 s62, s62, 7\n"
      "s_add_u32 s62, s62, 18644992\n"
      "v_mov_b32 v23, 1073741824\n"
      "v_writelane_b32 v23, s62, 0\n"
      "s_mov_b32 s80, s84\n"
      "s_mov_b32 s81, s85\n"
      "v_lshlrev_b32 v70, 16, v180\n"
      "v_lshlrev_b32 v71, 16, v182\n"
      "v_and_b32 v72, s92, v180\n"
      "v_and_b32 v73, s92, v182\n"
      "v_lshlrev_b32 v74, 16, v181\n"
      "v_lshlrev_b32 v75, 16, v183\n"
      "v_and_b32 v76, s92, v181\n"
      "v_and_b32 v77, s92, v183\n"
      "v_add_u32 v78, 35360, v78\n"
      "v_add_u32 v79, 35360, v79\n"
      "s_mul_i32 s62, s81, 67584\n"
      "s_lshl_b32 s63, s80, 7\n"
      "s_add_u32 s62, s62, s63\n"
      "s_add_u32 s62, s62, 32768\n"
      "s_add_u32 s63, s62, 4224\n"
      "s_add_u32 s64, s64, 4\n"
      "s_add_u32 s65, s64, s52\n"
      "s_cmp_lt_u32 s65, 8\n"
      "s_cbranch_scc1 .LSKV\n"
      "s_lshl3_add_u32 s65, s60, 4096\n"
      "s_cmp_eq_u32 s52, 0\n"
      "s_cselect_b32 s65, s65, 1073741824\n"
      "v_writelane_b32 v23, s65, 1\n"
      "s_waitcnt vmcnt(1) lgkmcnt(0)\n"
      "buffer_store_dwordx2 v[12:13], v23, s[20:23], 0 offen sc1\n"
      "s_barrier\n"
      "s_lshl_b32 s94, s53, 12\n"
      "s_lshl_b32 s95, s53, 10\n"
      "s_cmp_lt_u32 s52, 4\n"
      "s_cbranch_scc1 .Ls_177\n"
      "v_mov_b64 v[46:47], v[12:13]\n"
      "v_lshlrev_b32 v5, 4, v1\n"
      "v_add_u32 v6, 4096, v5\n"
      "s_lshr_b32 s62, s60, 1\n"
      "s_mul_i32 s62, s62, 5\n"
      "s_mul_i32 s63, s54, 3\n"
      "s_add_u32 s63, s63, s62\n"
      "s_lshl_b32 s24, s63, 12\n"
      "s_add_u32 s24, s24, s95\n"
      "s_add_u32 s25, s24, 4096\n"
      "s_add_u32 s63, s62, 2\n"
      "s_lshl_b32 s26, s63, 12\n"
      "s_add_u32 s26, s26, s95\n"
      "v_mov_b32 v32, s26\n"
      "v_writelane_b32 v32, s24, 0\n"
      "v_writelane_b32 v32, s25, 1\n"
      "s_cmp_eq_u32 s53, 0\n"
      "s_cselect_b32 s63, 1073741824, -4\n"
      "v_add_u32 v32, s63, v32\n"
      "v_lshrrev_b32 v33, 11, v32\n"
      "v_xor_b32 v33, 524288, v33\n"
      "s_lshr_b32 s91, s62, 4\n"
      "s_add_u32 s63, s62, 4\n"
      "s_lshr_b32 s63, s63, 4\n"
      "s_sub_u32 s63, s63, s91\n"
      "s_lshl_b32 s63, s63, 9\n"
      "v_and_b32 v72, 8, v1\n"
      "v_mul_u32_u24 v72, s63, v72\n"
      "v_and_b32 v73, 7, v1\n"
      "v_lshl_add_u32 v30, v73, 7, v72\n"
      "s_lshl_b32 s91, s91, 12\n"
      "s_add_u32 s91, s91, s95\n"
      "s_add_u32 s91, s91, 18644992\n"
      "v_add_u32 v30, s91, v30\n"
      "s_mul_i32 s63, s53, 4608\n"
      "s_cmp_eq_u32 s53, 3\n"
      "s_cselect_b32 s63, 9728, s63\n"
      "s_add_u32 s63, s63, 139520\n"
      "v_lshl_add_u32 v34, v1, 3, s63\n"
      "v_lshrrev_b32 v72, 1, v1\n"
      "v_min_u32 v72, 6, v72\n"
      "s_mul_i32 s62, s54, 28\n"
      "s_add_u32 s62, s62, s53\n"
      "v_lshl_add_u32 v72, v72, 2, s62\n"
      "v_min_u32 v72, 53, v72\n"
      "s_lshl_b32 s63, s56, 14\n"
      "s_add_u32 s63, s63, 18382848\n"
      "v_lshl_add_u32 v13, v72, 8, s63\n"
      "s_mul_i32 s63, s55, 80\n"
      "s_add_u32 s91, s63, 79\n"
      "s_lshr_b32 s63, s63, 7\n"
      "s_lshr_b32 s91, s91, 7\n"
      "s_sub_u32 s91, s91, s63\n"
      "v_and_b32 v73, s91, v1\n"
      "v_lshl_add_u32 v72, v72, 3, v73\n"
      "s_lshl_b32 s63, s63, 7\n"
      "s_add_u32 s63, s63, 18808832\n"
      "v_lshl_add_u32 v31, v72, 7, s63\n"
      "v_lshrrev_b32 v72, 3, v1\n"
      "v_min_u32 v72, 5, v72\n"
      "v_mul_u32_u24 v73, 4160, v72\n"
      "v_lshrrev_b32 v72, 2, v72\n"
      "v_mul_u32_u24 v72, 57600, v72\n"
      "v_sub_u32 v73, v73, v72\n"
      "v_add_u32 v73, 122880, v73\n"
      "v_and_b32 v72, 7, v1\n"
      "v_lshl_add_u32 v35, v72, 3, v73\n"
      "s_add_u32 s62, s95, 122944\n"
      "v_add_u32 v40, s62, v5\n"
      "v_subrev_u32 v41, 40960, v40\n"
      "v_and_b32 v72, 31, v1\n"
      "v_lshlrev_b32 v42, 3, v72\n"
      "s_add_u32 s62, s95, 157696\n"
      "v_lshl_add_u32 v43, v72, 5, s62\n"
      "s_xor_b32 s31, s99, s53\n"
      "s_lshl2_add_u32 s31, s56, s31\n"
      "s_lshl_b32 s31, s31, 8\n"
      "s_add_u32 s31, s31, 8192\n"
      "s_lshl2_add_u32 s62, s54, s53\n"
      "s_cmp_ge_u32 s62, 6\n"
      "s_cselect_b32 s63, 1073741824, 6144\n"
      "v_add_u32 v7, s63, v5\n"
      "s_mul_i32 s62, s62, 7168\n"
      "s_mul_i32 s63, s55, 286720\n"
      "s_add_u32 s63, s63, s62\n"
      "s_add_u32 s27, s63, 5603328\n"
      "s_mul_i32 s63, s56, 458752\n"
      "s_add_u32 s63, s63, s62\n"
      "s_add_u32 s63, s63, 7897088\n"
      "s_mul_i32 s62, s54, 229376\n"
      "s_add_u32 s91, s63, s62\n"
      "s_sub_u32 s62, 229376, s62\n"
      "s_add_u32 s93, s63, s62\n"
      "s_sub_u32 s62, 4, s53\n"
      "s_and_b32 s62, s62, 3\n"
      "s_mul_i32 s62, s62, 57344\n"
      "s_add_u32 s44, s93, s62\n"
      "s_add_u32 s48, s91, s62\n"
      "s_sub_u32 s62, 5, s53\n"
      "s_and_b32 s62, s62, 3\n"
      "s_mul_i32 s62, s62, 57344\n"
      "s_add_u32 s45, s93, s62\n"
      "s_add_u32 s49, s91, s62\n"
      "s_sub_u32 s62, 6, s53\n"
      "s_and_b32 s62, s62, 3\n"
      "s_mul_i32 s62, s62, 57344\n"
      "s_add_u32 s46, s93, s62\n"
      "s_add_u32 s50, s91, s62\n"
      "s_sub_u32 s62, 7, s53\n"
      "s_and_b32 s62, s62, 3\n"
      "s_mul_i32 s62, s62, 57344\n"
      "s_add_u32 s47, s93, s62\n"
      "s_add_u32 s51, s91, s62\n"
      "s_add_u32 s91, s53, 3\n"
      "s_and_b32 s91, s91, 3\n"
      "s_lshl2_add_u32 s62, s91, 161888\n"
      "v_mov_b32 v15, s62\n"
      "s_mul_i32 s62, s53, 5120\n"
      "s_add_u32 s63, s62, 40960\n"
      "v_add_u32 v8, s63, v5\n"
      "s_mul_i32 s63, s91, 5120\n"
      "s_add_u32 s63, s63, 40960\n"
      "v_add_u32 v9, s63, v5\n"
      "s_xor_b32 s63, s60, 1\n"
      "s_lshl2_add_u32 s63, s63, s53\n"
      "s_mul_i32 s28, s63, 3072\n"
      "s_add_u32 s28, s28, 15237120\n"
      "v_cmp_eq_u32 vcc, s53, v1\n"
      "v_and_b32 v14, 48, v1\n"
      "v_add_u32 v14, 163072, v14\n"
      "v_cndmask_b32 v55, 0, 1, vcc\n"
      "buffer_load_dwordx2 v[76:77], v42, s[20:23], s31 offen sc1\n"
      "v_readfirstlane_b32 s58, v46\n"
      "v_readfirstlane_b32 s59, v47\n"
      "v_mov_b32 v1, v55\n"
      "buffer_load_dwordx4 v[56:59], v5, s[36:39], s24 offen nt\n"
      "buffer_load_dwordx4 v[60:63], v5, s[36:39], s25 offen nt\n"
      "buffer_load_dwordx4 v[64:67], v5, s[36:39], s26 offen nt\n"
      "buffer_load_dword v52, v32, s[36:39], 0 offen nt\n"
      "buffer_load_ushort v53, v33, s[32:35], 0 offen\n"
      "s_waitcnt vmcnt(5)\n"
      "v_cmp_ne_u32 vcc, s58, v77\n"
      "s_cbranch_vccnz .Lt_178\n"
      ".Lt_179:\n"
      "v_and_b32 v82, s92, v76\n"
      "v_rcp_f32 v77, v82\n"
      "v_xor_b32 v85, 0x8000, v76\n"
      "s_mov_b32 s62, 0x01000100\n"
      "v_mul_f32 v80, 0xc0135761, v77\n"
      "v_mul_f32 v81, v77, v77\n"
      "v_cmp_nle_f32 vcc, 0x3089705f, v77\n"
      "v_mul_f32 v81, 0x3d372713, v81\n"
      "v_perm_b32 v84, v85, v85, s62\n"
      "v_mul_f32 v81, v81, v80\n"
      "v_mov_b32 v83, 1.0\n"
      "ds_write_b64 v43, v[84:85] offset:16\n"
      "s_cbranch_vccnz .Lb_181\n"
      ".Lb_182:\n"
      "ds_write_b128 v43, v[80:83]\n"
      "s_waitcnt vmcnt(0)\n"
      "s_cmp_eq_u32 s53, 0\n"
      "s_cbranch_scc0 .Ld_183\n"
      "v_readlane_b32 s62, v53, 0\n"
      "v_readlane_b32 s63, v53, 1\n"
      "v_readlane_b32 s91, v53, 2\n"
      "s_mov_b64 exec, 1\n"
      "v_lshlrev_b32 v72, 16, v56\n"
      "s_lshl_b32 s62, s62, 16\n"
      "v_and_b32 v73, s92, v56\n"
      "v_add_f32 v72, s62, v72\n"
      "v_cvt_pk_bf16_f32 v56, v72, v73\n"
      "v_lshlrev_b32 v72, 16, v60\n"
      "s_lshl_b32 s63, s63, 16\n"
      "v_and_b32 v73, s92, v60\n"
      "v_add_f32 v72, s63, v72\n"
      "v_cvt_pk_bf16_f32 v60, v72, v73\n"
      "v_lshlrev_b32 v72, 16, v64\n"
      "s_lshl_b32 s91, s91, 16\n"
      "v_and_b32 v73, s92, v64\n"
      "v_add_f32 v72, s91, v72\n"
      "v_cvt_pk_bf16_f32 v64, v72, v73\n"
      "s_mov_b64 exec, -1\n"
      ".Ld_183:\n"
      "v_readlane_b32 s62, v52, 2\n"
      "v_alignbyte_b32 v107, v67, v66, 2\n"
      "v_alignbyte_b32 v105, v65, v64, 2\n"
      "v_writelane_b32 v72, s62, 0\n"
      "v_alignbyte_b32 v106, v66, v65, 2\n"
      "s_nop 0\n"
      "v_mov_b32_dpp v72, v67 wave_shr:1 row_mask:0xf bank_mask:0xf\n"
      "v_alignbyte_b32 v104, v64, v72, 2\n"
      "ds_write_b128 v40, v[60:63] offset:8320\n"
      "v_readlane_b32 s62, v52, 1\n"
      "v_alignbyte_b32 v103, v63, v62, 2\n"
      "v_alignbyte_b32 v101, v61, v60, 2\n"
      "v_writelane_b32 v72, s62, 0\n"
      "v_alignbyte_b32 v102, v62, v61, 2\n"
      "s_nop 0\n"
      "v_mov_b32_dpp v72, v63 wave_shr:1 row_mask:0xf bank_mask:0xf\n"
      "v_alignbyte_b32 v100, v60, v72, 2\n"
      "ds_write_b128 v40, v[100:103] offset:12480\n"
      "ds_write_b128 v40, v[56:59] offset:0\n"
      "v_alignbyte_b32 v99, v59, v58, 2\n"
      "v_alignbyte_b32 v97, v57, v56, 2\n"
      "v_alignbyte_b32 v98, v58, v57, 2\n"
      "v_mov_b32_dpp v52, v59 wave_shr:1 row_mask:0xf bank_mask:0xf\n"
      "v_alignbyte_b32 v96, v56, v52, 2\n"
      "ds_write_b128 v40, v[96:99] offset:4160\n"
      "s_branch .LS2\n"
      ".Ls_177:\n"
      "ds_read_b32 v26, v2 offset:5120\n"
      "v_lshlrev_b32 v31, 3, v1\n"
      "v_add_u32 v31, 163072, v31\n"
      "s_waitcnt lgkmcnt(0)\n"
      "v_and_b32 v27, s92, v26\n"
      "v_lshlrev_b32 v26, 16, v26\n"
      "ds_write_b64 v31, v[26:27]\n"
      "ds_add_u32 v2, v3 offset:1052\n"
      "ds_add_u32 v2, v3 offset:4168\n"
      "ds_add_u32 v2, v3 offset:4160\n"
      "s_lshl3_add_u32 s64, s60, s52\n"
      "s_cmp_eq_u32 s64, 2040\n"
      "s_cbranch_scc0 .Le_184\n"
      "v_lshlrev_b32 v25, 3, v1\n"
      "s_mov_b32 s64, 4096\n"
      "buffer_load_dwordx2 v[26:27], v25, s[20:23], s64 offen sc1\n"
      "s_waitcnt vmcnt(0)\n"
      "v_cmp_ne_u64 vcc, s[58:59], v[26:27]\n"
      "s_cbranch_vccnz .Lf_185\n"
      ".Lf_186:\n"
      "s_mov_b32 s64, 4608\n"
      "buffer_load_dwordx2 v[26:27], v25, s[20:23], s64 offen sc1\n"
      "s_waitcnt vmcnt(0)\n"
      "v_cmp_ne_u64 vcc, s[58:59], v[26:27]\n"
      "s_cbranch_vccnz .Lf_188\n"
      ".Lf_189:\n"
      "s_mov_b32 s64, 5120\n"
      "buffer_load_dwordx2 v[26:27], v25, s[20:23], s64 offen sc1\n"
      "s_waitcnt vmcnt(0)\n"
      "v_cmp_ne_u64 vcc, s[58:59], v[26:27]\n"
      "s_cbranch_vccnz .Lf_191\n"
      ".Lf_192:\n"
      "s_mov_b32 s64, 5632\n"
      "buffer_load_dwordx2 v[26:27], v25, s[20:23], s64 offen sc1\n"
      "s_waitcnt vmcnt(0)\n"
      "v_cmp_ne_u64 vcc, s[58:59], v[26:27]\n"
      "s_cbranch_vccnz .Lf_194\n"
      ".Lf_195:\n"
      "s_mov_b64 exec, 1\n"
      "buffer_store_dwordx2 v[12:13], off, s[20:23], 0 sc1\n"
      "s_mov_b64 exec, -1\n"
      ".Le_184:\n"
      "s_sub_u32 s66, 3, s53\n"
      "s_lshl1_add_u32 s62, s56, s54\n"
      "s_lshl2_add_u32 s63, s62, s66\n"
      "s_mul_i32 s62, s63, 20480\n"
      "s_mul_i32 s63, s55, 160\n"
      "s_add_u32 s62, s62, s63\n"
      "v_and_b32 v21, 15, v1\n"
      "v_mul_u32_u24 v21, 1280, v21\n"
      "v_lshrrev_b32 v22, 4, v1\n"
      "v_lshl_add_u32 v21, v22, 3, v21\n"
      "buffer_load_dwordx2 v[218:219], v21, s[4:7], s62 offen offset:0\n"
      "buffer_load_dwordx2 v[220:221], v21, s[4:7], s62 offen offset:32\n"
      "buffer_load_dwordx2 v[222:223], v21, s[4:7], s62 offen offset:64\n"
      "buffer_load_dwordx2 v[224:225], v21, s[4:7], s62 offen offset:96\n"
      "buffer_load_dwordx2 v[226:227], v21, s[4:7], s62 offen offset:128\n"
      "s_mov_b32 s79, 1073741824\n"
      "ds_read_b32 v31, v2 offset:4128\n"
      "s_setprio 3\n"
      "s_branch .LCONV\n"
      ".Lt_2:\n"
      "s_mov_b32 s3, 0x10000\n"
      ".Lt_4:\n"
      "s_sleep 4\n"
      "buffer_load_dwordx2 v[76:77], v42, s[20:23], s31 offen sc1\n"
      "s_waitcnt vmcnt(0)\n"
      "v_cmp_ne_u32 vcc, s58, v77\n"
      "s_sub_u32 s3, s3, 1\n"
      "s_cselect_b64 vcc, 0, vcc\n"
      "s_cbranch_vccnz .Lt_4\n"
      "s_branch .Lt_3\n"
      ".Lb_5:\n"
      "s_mov_b64 exec, vcc\n"
      "v_mov_b32 v80, 0xc0135761\n"
      "v_mov_b32 v81, 0xbdd2d3e8\n"
      "v_mov_b32 v82, 1.0\n"
      "v_mov_b32 v83, v77\n"
      "s_mov_b64 exec, -1\n"
      "ds_add_u32 v2, v3 offset:4144\n"
      "s_branch .Lb_6\n"
      ".Lw_7:\n"
      "s_mov_b32 s3, 0x80000\n"
      ".Lw_9:\n"
      "ds_read_b32 v4, v2 offset:4152\n"
      "s_waitcnt lgkmcnt(0)\n"
      "v_readfirstlane_b32 s2, v4\n"
      "s_sub_u32 s3, s3, 1\n"
      "s_cselect_b32 s2, -1, s2\n"
      "s_cmp_lt_u32 s2, 4\n"
      "s_cbranch_scc1 .Lw_9\n"
      "s_branch .Lw_8\n"
      ".Lf_10:\n"
      "s_mov_b32 s3, 0x10000\n"
      ".Lf_12:\n"
      "s_sleep 4\n"
      "buffer_load_dwordx2 v[50:51], v31, s[20:23], 0 offen sc1\n"
      "s_waitcnt vmcnt(0)\n"
      "v_cmp_ne_u64 vcc, s[58:59], v[50:51]\n"
      "s_sub_u32 s3, s3, 1\n"
      "s_cselect_b64 vcc, 0, vcc\n"
      "s_cbranch_vccnz .Lf_12\n"
      "s_branch .Lf_11\n"
      ".Lf_13:\n"
      "s_mov_b32 s3, 0x10000\n"
      ".Lf_15:\n"
      "s_sleep 4\n"
      "buffer_load_dwordx2 v[48:49], v30, s[20:23], 0 offen sc1\n"
      "s_waitcnt vmcnt(0)\n"
      "v_cmp_ne_u64 vcc, s[58:59], v[48:49]\n"
      "s_sub_u32 s3, s3, 1\n"
      "s_cselect_b64 vcc, 0, vcc\n"
      "s_cbranch_vccnz .Lf_15\n"
      "s_branch .Lf_14\n"
      ".Lw_16:\n"
      "s_mov_b32 s3, 0x80000\n"
      ".Lw_18:\n"
      "ds_read_b32 v4, v2 offset:1052\n"
      "s_waitcnt lgkmcnt(0)\n"
      "v_readfirstlane_b32 s2, v4\n"
      "s_sub_u32 s3, s3, 1\n"
      "s_cselect_b32 s2, -1, s2\n"
      "s_cmp_lt_u32 s2, 4\n"
      "s_cbranch_scc1 .Lw_18\n"
      "s_branch .Lw_17\n"
      ".Lf_19:\n"
      "s_mov_b32 s3, 0x10000\n"
      ".Lf_21:\n"
      "s_sleep 4\n"
      "buffer_load_dwordx2 v[10:11], v13, s[20:23], 0 offen sc1\n"
      "s_waitcnt vmcnt(0)\n"
      "v_cmp_ne_u64 vcc, s[58:59], v[10:11]\n"
      "s_sub_u32 s3, s3, 1\n"
      "s_cselect_b64 vcc, 0, vcc\n"
      "s_cbranch_vccnz .Lf_21\n"
      "s_branch .Lf_20\n"
      ".Lw_22:\n"
      "s_mov_b32 s3, 0x80000\n"
      ".Lw_24:\n"
      "ds_read_b32 v4, v2 offset:4160\n"
      "s_waitcnt lgkmcnt(0)\n"
      "v_readfirstlane_b32 s2, v4\n"
      "s_sub_u32 s3, s3, 1\n"
      "s_cselect_b32 s2, -1, s2\n"
      "s_cmp_lt_u32 s2, 4\n"
      "s_cbranch_scc1 .Lw_24\n"
      "s_branch .Lw_23\n"
      ".Lf_35:\n"
      "s_mov_b32 s3, 0x10000\n"
      ".Lf_37:\n"
      "s_sleep 4\n"
      "buffer_load_dwordx2 v[10:11], v13, s[20:23], s74 offen sc1\n"
      "s_waitcnt vmcnt(0)\n"
      "v_cmp_ne_u64 vcc, s[58:59], v[10:11]\n"
      "s_sub_u32 s3, s3, 1\n"
      "s_cselect_b64 vcc, 0, vcc\n"
      "s_cbranch_vccnz .Lf_37\n"
      "s_branch .Lf_36\n"
      ".Lw_58:\n"
      "s_mov_b32 s3, 0x80000\n"
      ".Lw_60:\n"
      "ds_read_b32 v4, v2 offset:4136\n"
      "s_waitcnt lgkmcnt(0)\n"
      "v_readfirstlane_b32 s2, v4\n"
      "s_sub_u32 s3, s3, 1\n"
      "s_cselect_b32 s2, -1, s2\n"
      "s_cmp_lt_u32 s2, 4\n"
      "s_cbranch_scc1 .Lw_60\n"
      "s_branch .Lw_59\n"
      ".Lg_121:\n"
      "s_waitcnt lgkmcnt(0)\n"
      "s_nop 11\n"
      "v_mul_f32 v64, v64, v27\n"
      "v_mul_f32 v65, v65, v27\n"
      "v_mul_f32 v66, v66, v27\n"
      "v_mul_f32 v67, v67, v27\n"
      "v_mul_f32 v68, v68, v27\n"
      "v_mul_f32 v69, v69, v27\n"
      "v_mul_f32 v70, v70, v27\n"
      "v_mul_f32 v71, v71, v27\n"
      "v_mul_f32 v72, v72, v27\n"
      "v_mul_f32 v73, v73, v27\n"
      "v_mul_f32 v74, v74, v27\n"
      "v_mul_f32 v75, v75, v27\n"
      "v_mul_f32 v76, v76, v27\n"
      "v_mul_f32 v77, v77, v27\n"
      "v_mul_f32 v78, v78, v27\n"
      "v_mul_f32 v79, v79, v27\n"
      "s_branch .Lg_122\n"
      ".Lw_123:\n"
      "s_mov_b32 s3, 0x80000\n"
      ".Lw_125:\n"
      "ds_read_b32 v31, v2 offset:24\n"
      "s_waitcnt lgkmcnt(0)\n"
      "v_readfirstlane_b32 s2, v31\n"
      "s_sub_u32 s3, s3, 1\n"
      "s_cselect_b32 s2, -1, s2\n"
      "s_cmp_lt_u32 s2, 4\n"
      "s_cbranch_scc1 .Lw_125\n"
      "s_branch .Lw_124\n"
      ".Lw_126:\n"
      "s_mov_b32 s3, 0x80000\n"
      ".Lw_128:\n"
      "ds_read_b32 v31, v2 offset:1048\n"
      "s_waitcnt lgkmcnt(0)\n"
      "v_readfirstlane_b32 s2, v31\n"
      "s_sub_u32 s3, s3, 1\n"
      "s_cselect_b32 s2, -1, s2\n"
      "s_cmp_lt_u32 s2, 4\n"
      "s_cbranch_scc1 .Lw_128\n"
      "s_branch .Lw_127\n"
      ".Lg_129:\n"
      "s_waitcnt lgkmcnt(0)\n"
      "s_nop 11\n"
      "v_mul_f32 v64, v64, v27\n"
      "v_mul_f32 v65, v65, v27\n"
      "v_mul_f32 v66, v66, v27\n"
      "v_mul_f32 v67, v67, v27\n"
      "v_mul_f32 v68, v68, v27\n"
      "v_mul_f32 v69, v69, v27\n"
      "v_mul_f32 v70, v70, v27\n"
      "v_mul_f32 v71, v71, v27\n"
      "v_mul_f32 v72, v72, v27\n"
      "v_mul_f32 v73, v73, v27\n"
      "v_mul_f32 v74, v74, v27\n"
      "v_mul_f32 v75, v75, v27\n"
      "v_mul_f32 v76, v76, v27\n"
      "v_mul_f32 v77, v77, v27\n"
      "v_mul_f32 v78, v78, v27\n"
      "v_mul_f32 v79, v79, v27\n"
      "s_branch .Lg_130\n"
      ".Lw_131:\n"
      "s_mov_b32 s3, 0x80000\n"
      ".Lw_133:\n"
      "ds_read_b32 v31, v2 offset:2072\n"
      "s_waitcnt lgkmcnt(0)\n"
      "v_readfirstlane_b32 s2, v31\n"
      "s_sub_u32 s3, s3, 1\n"
      "s_cselect_b32 s2, -1, s2\n"
      "s_cmp_lt_u32 s2, 4\n"
      "s_cbranch_scc1 .Lw_133\n"
      "s_branch .Lw_132\n"
      ".Lw_134:\n"
      "s_mov_b32 s3, 0x80000\n"
      ".Lw_136:\n"
      "ds_read_b32 v31, v2 offset:3096\n"
      "s_waitcnt lgkmcnt(0)\n"
      "v_readfirstlane_b32 s2, v31\n"
      "s_sub_u32 s3, s3, 1\n"
      "s_cselect_b32 s2, -1, s2\n"
      "s_cmp_lt_u32 s2, 4\n"
      "s_cbranch_scc1 .Lw_136\n"
      "s_branch .Lw_135\n"
      ".Lg_137:\n"
      "s_waitcnt lgkmcnt(0)\n"
      "s_nop 11\n"
      "v_mul_f32 v64, v64, v27\n"
      "v_mul_f32 v65, v65, v27\n"
      "v_mul_f32 v66, v66, v27\n"
      "v_mul_f32 v67, v67, v27\n"
      "v_mul_f32 v68, v68, v27\n"
      "v_mul_f32 v69, v69, v27\n"
      "v_mul_f32 v70, v70, v27\n"
      "v_mul_f32 v71, v71, v27\n"
      "v_mul_f32 v72, v72, v27\n"
      "v_mul_f32 v73, v73, v27\n"
      "v_mul_f32 v74, v74, v27\n"
      "v_mul_f32 v75, v75, v27\n"
      "v_mul_f32 v76, v76, v27\n"
      "v_mul_f32 v77, v77, v27\n"
      "v_mul_f32 v78, v78, v27\n"
      "v_mul_f32 v79, v79, v27\n"
      "s_branch .Lg_138\n"
      ".Lg_139:\n"
      "s_waitcnt lgkmcnt(0)\n"
      "s_nop 11\n"
      "v_mul_f32 v64, v64, v27\n"
      "v_mul_f32 v65, v65, v27\n"
      "v_mul_f32 v66, v66, v27\n"
      "v_mul_f32 v67, v67, v27\n"
      "v_mul_f32 v68, v68, v27\n"
      "v_mul_f32 v69, v69, v27\n"
      "v_mul_f32 v70, v70, v27\n"
      "v_mul_f32 v71, v71, v27\n"
      "v_mul_f32 v72, v72, v27\n"
      "v_mul_f32 v73, v73, v27\n"
      "v_mul_f32 v74, v74, v27\n"
      "v_mul_f32 v75, v75, v27\n"
      "v_mul_f32 v76, v76, v27\n"
      "v_mul_f32 v77, v77, v27\n"
      "v_mul_f32 v78, v78, v27\n"
      "v_mul_f32 v79, v79, v27\n"
      "s_branch .Lg_140\n"
      ".Lw_141:\n"
      "s_mov_b32 s3, 0x80000\n"
      ".Lw_143:\n"
      "ds_read_b32 v31, v2 offset:4128\n"
      "s_waitcnt lgkmcnt(0)\n"
      "v_readfirstlane_b32 s2, v31\n"
      "s_sub_u32 s3, s3, 1\n"
      "s_cselect_b32 s2, -1, s2\n"
      "s_cmp_lt_u32 s2, 4\n"
      "s_cbranch_scc1 .Lw_143\n"
      "s_branch .Lw_142\n"
      ".Lw_144:\n"
      "s_mov_b32 s3, 0x80000\n"
      ".Lw_146:\n"
      "ds_read_b32 v31, v2 offset:4160\n"
      "s_waitcnt lgkmcnt(0)\n"
      "v_readfirstlane_b32 s2, v31\n"
      "s_sub_u32 s3, s3, 1\n"
      "s_cselect_b32 s2, -1, s2\n"
      "s_cmp_lt_u32 s2, 4\n"
      "s_cbranch_scc1 .Lw_146\n"
      "s_branch .Lw_145\n"
      ".Lw_147:\n"
      "s_mov_b32 s3, 0x80000\n"
      ".Lw_149:\n"
      "ds_read_b32 v31, v2 offset:4132\n"
      "s_waitcnt lgkmcnt(0)\n"
      "v_readfirstlane_b32 s2, v31\n"
      "s_sub_u32 s3, s3, 1\n"
      "s_cselect_b32 s2, -1, s2\n"
      "s_cmp_lt_u32 s2, 4\n"
      "s_cbranch_scc1 .Lw_149\n"
      "s_branch .Lw_148\n"
      ".Lw_153:\n"
      "s_mov_b32 s3, 0x80000\n"
      ".Lw_155:\n"
      "ds_read_b32 v31, v2 offset:4136\n"
      "s_waitcnt lgkmcnt(0)\n"
      "v_readfirstlane_b32 s2, v31\n"
      "s_sub_u32 s3, s3, 1\n"
      "s_cselect_b32 s2, -1, s2\n"
      "s_cmp_lt_u32 s2, 4\n"
      "s_cbranch_scc1 .Lw_155\n"
      "s_branch .Lw_154\n"
      ".Lq_158:\n"
      "buffer_load_dwordx2 v[26:27], v25, s[20:23], 0 offen sc1\n"
      "s_waitcnt vmcnt(1)\n"
      "s_branch .Lq_159\n"
      ".Ly_160:\n"
      "s_mov_b32 s3, 0x10000\n"
      ".Ly_162:\n"
      "s_sleep 4\n"
      "buffer_load_dwordx4 v[164:167], v9, s[20:23], s72 offen offset:0 sc1\n"
      "buffer_load_dwordx4 v[168:171], v9, s[20:23], s72 offen offset:1024 sc1\n"
      "buffer_load_dwordx4 v[172:175], v9, s[20:23], s72 offen offset:2048 sc1\n"
      "s_waitcnt vmcnt(0)\n"
      "v_xor_b32 v21, s74, v167\n"
      "v_bitop3_b32 v21, v21, v171, s74 bitop3:0xf6\n"
      "v_bitop3_b32 v21, v21, v175, s74 bitop3:0xf6\n"
      "v_cmp_lt_u32 vcc, 0xffff, v21\n"
      "s_sub_u32 s3, s3, 1\n"
      "s_cselect_b64 vcc, 0, vcc\n"
      "s_cbranch_vccnz .Ly_162\n"
      "s_branch .Ly_161\n"
      ".Lq_163:\n"
      "s_waitcnt vmcnt(0)\n"
      "v_cmp_ne_u64 vcc, s[58:59], v[26:27]\n"
      "s_cbranch_vccz .Lq_165\n"
      "buffer_load_dwordx2 v[26:27], v25, s[20:23], 0 offen sc1\n"
      "s_branch .Lq_164\n"
      ".Lq_165:\n"
      "buffer_load_dwordx4 v6, s[20:23], s73 offen sc1 lds\n"
      "buffer_load_dwordx4 v6, s[20:23], s63 offen offset:1024 sc1 lds\n"
      "s_mov_b32 exec_hi, 0\n"
      "buffer_load_dwordx4 v6, s[20:23], s64 offen offset:2048 sc1 lds\n"
      "s_mov_b32 exec_hi, -1\n"
      "s_mov_b32 s66, 1\n"
      "s_branch .Lq_164\n"
      ".Lq_167:\n"
      "s_mov_b32 s3, 0x10000\n"
      ".Lq_169:\n"
      "s_waitcnt vmcnt(0)\n"
      "v_cmp_ne_u64 vcc, s[58:59], v[26:27]\n"
      "s_sub_u32 s3, s3, 1\n"
      "s_cselect_b64 vcc, 0, vcc\n"
      "s_cbranch_vccz .Lq_170\n"
      "s_sleep 2\n"
      "buffer_load_dwordx2 v[26:27], v25, s[20:23], 0 offen sc1\n"
      "s_branch .Lq_169\n"
      ".Lq_170:\n"
      "buffer_load_dwordx4 v6, s[20:23], s73 offen sc1 lds\n"
      "buffer_load_dwordx4 v6, s[20:23], s63 offen offset:1024 sc1 lds\n"
      "s_mov_b32 exec_hi, 0\n"
      "buffer_load_dwordx4 v6, s[20:23], s64 offen offset:2048 sc1 lds\n"
      "s_mov_b32 exec_hi, -1\n"
      "s_branch .Lq_168\n"
      ".Lt_178:\n"
      "s_mov_b32 s3, 0x10000\n"
      ".Lt_180:\n"
      "s_sleep 4\n"
      "buffer_load_dwordx2 v[76:77], v42, s[20:23], s31 offen sc1\n"
      "s_waitcnt vmcnt(0)\n"
      "v_cmp_ne_u32 vcc, s58, v77\n"
      "s_sub_u32 s3, s3, 1\n"
      "s_cselect_b64 vcc, 0, vcc\n"
      "s_cbranch_vccnz .Lt_180\n"
      "s_branch .Lt_179\n"
      ".Lb_181:\n"
      "s_mov_b64 exec, vcc\n"
      "v_mov_b32 v80, 0xc0135761\n"
      "v_mov_b32 v81, 0xbdd2d3e8\n"
      "v_mov_b32 v82, 1.0\n"
      "v_mov_b32 v83, v77\n"
      "s_mov_b64 exec, -1\n"
      "ds_add_u32 v2, v3 offset:4144\n"
      "s_branch .Lb_182\n"
      ".Lf_185:\n"
      "s_mov_b32 s3, 0x10000\n"
      ".Lf_187:\n"
      "s_sleep 4\n"
      "buffer_load_dwordx2 v[26:27], v25, s[20:23], s64 offen sc1\n"
      "s_waitcnt vmcnt(0)\n"
      "v_cmp_ne_u64 vcc, s[58:59], v[26:27]\n"
      "s_sub_u32 s3, s3, 1\n"
      "s_cselect_b64 vcc, 0, vcc\n"
      "s_cbranch_vccnz .Lf_187\n"
      "s_branch .Lf_186\n"
      ".Lf_188:\n"
      "s_mov_b32 s3, 0x10000\n"
      ".Lf_190:\n"
      "s_sleep 4\n"
      "buffer_load_dwordx2 v[26:27], v25, s[20:23], s64 offen sc1\n"
      "s_waitcnt vmcnt(0)\n"
      "v_cmp_ne_u64 vcc, s[58:59], v[26:27]\n"
      "s_sub_u32 s3, s3, 1\n"
      "s_cselect_b64 vcc, 0, vcc\n"
      "s_cbranch_vccnz .Lf_190\n"
      "s_branch .Lf_189\n"
      ".Lf_191:\n"
      "s_mov_b32 s3, 0x10000\n"
      ".Lf_193:\n"
      "s_sleep 4\n"
      "buffer_load_dwordx2 v[26:27], v25, s[20:23], s64 offen sc1\n"
      "s_waitcnt vmcnt(0)\n"
      "v_cmp_ne_u64 vcc, s[58:59], v[26:27]\n"
      "s_sub_u32 s3, s3, 1\n"
      "s_cselect_b64 vcc, 0, vcc\n"
      "s_cbranch_vccnz .Lf_193\n"
      "s_branch .Lf_192\n"
      ".Lf_194:\n"
      "s_mov_b32 s3, 0x10000\n"
      ".Lf_196:\n"
      "s_sleep 4\n"
      "buffer_load_dwordx2 v[26:27], v25, s[20:23], s64 offen sc1\n"
      "s_waitcnt vmcnt(0)\n"
      "v_cmp_ne_u64 vcc, s[58:59], v[26:27]\n"
      "s_sub_u32 s3, s3, 1\n"
      "s_cselect_b64 vcc, 0, vcc\n"
      "s_cbranch_vccnz .Lf_196\n"
      "s_branch .Lf_195\n"
      ".Lfz_end:\n"
      ".fill 320, 4, 0xBF800000\n"
      : "+{v0}"(tid) : "{s[0:1]}"(__builtin_amdgcn_kernarg_segment_ptr()), "{s2}"(blockIdx.x) : "v1", "v2", "v3", "v4", "v5", "v6", "v7", "v8", "v9", "v10", "v11", "v12", "v13", "v14", "v15", "v16", "v17", "v18", "v19", "v20", "v21", "v22", "v23", "v24", "v25", "v26", "v27", "v28", "v29", "v30", "v31", "v32", "v33", "v34", "v35", "v36", "v37", "v38", "v39", "v40", "v41", "v42", "v43", "v44", "v45", "v46", "v47", "v48", "v49", "v50", "v51", "v52", "v53", "v54", "v55", "v56", "v57", "v58", "v59", "v60", "v61", "v62", "v63", "v64", "v65", "v66", "v67", "v68", "v69", "v70", "v71", "v72", "v73", "v74", "v75", "v76", "v77", "v78", "v79", "v80", "v81", "v82", "v83", "v84", "v85", "v86", "v87", "v88", "v89", "v90", "v91", "v92", "v93", "v94", "v95", "v96", "v97", "v98", "v99", "v100", "v101", "v102", "v103", "v104", "v105", "v106", "v107", "v108", "v109", "v110", "v111", "v112", "v113", "v114", "v115", "v116", "v117", "v118", "v119", "v120", "v121", "v122", "v123", "v124", "v125", "v126", "v127", "v128", "v129", "v130", "v131", "v132", "v133", "v134", "v135", "v136", "v137", "v138", "v139", "v140", "v141", "v142", "v143", "v144", "v145", "v146", "v147", "v148", "v149", "v150", "v151", "v152", "v153", "v154", "v155", "v156", "v157", "v158", "v159", "v160", "v161", "v162", "v163", "v164", "v165", "v166", "v167", "v168", "v169", "v170", "v171", "v172", "v173", "v174", "v175", "v176", "v177", "v178", "v179", "v180", "v181", "v182", "v183", "v184", "v185", "v186", "v187", "v188", "v189", "v190", "v191", "v192", "v193", "v194", "v195", "v196", "v197", "v198", "v199", "v200", "v201", "v202", "v203", "v204", "v205", "v206", "v207", "v208", "v209", "v210", "v211", "v212", "v213", "v214", "v215", "v216", "v217", "v218", "v219", "v220", "v221", "v222", "v223", "v224", "v225", "v226", "v227", "v228", "v229", "v230", "v231", "v232", "v233", "v234", "v235", "v236", "v237", "v238", "v239", "v240", "v241", "v242", "v243", "v244", "v245", "v246", "v247", "v248", "v249", "v250", "v251", "v252", "v253", "v254", "v255", "s3", "s4", "s5", "s6", "s7", "s8", "s9", "s10", "s11", "s12", "s13", "s14", "s15", "s16", "s17", "s18", "s19", "s20", "s21", "s22", "s23", "s24", "s25", "s26", "s27", "s28", "s29", "s30", "s31", "s33", "s34", "s35", "s36", "s37", "s38", "s39", "s40", "s41", "s42", "s43", "s44", "s45", "s46", "s47", "s48", "s49", "s50", "s51", "s52", "s53", "s54", "s55", "s56", "s57", "s58", "s59", "s60", "s61", "s62", "s63", "s64", "s65", "s66", "s67", "s68", "s69", "s70", "s71", "s72", "s73", "s74", "s75", "s76", "s77", "s78", "s79", "s80", "s81", "s82", "s83", "s84", "s85", "s86", "s87", "s88", "s89", "s90", "s91", "s92", "s93", "s94", "s95", "s96", "s97", "s98", "s99", "vcc", "scc", "memory");
#endif
}
}

extern "C" void kernel_launch(void* const* d_in, const int* in_sizes, int n_in, void* d_out, int out_size, void* d_ws,
                              size_t ws_size, hipStream_t stream) {
  (void)hipFuncSetAttribute((const void*)fused_kernel, hipFuncAttributeMaxDynamicSharedMemorySize, 163840);
  fused_kernel<<<256, 512, 163840, stream>>>(d_in[0], 2621440u, 0x20000u, d_in[7], 2211840u, 0x20000u, d_in[1], 2621440u, 0x20000u, d_in[2], 2621440u, 0x20000u, d_ws, 18896896u, 0x20000u, d_in[3], 5120u, 0x20000u, d_in[8], 3456u, 0x20000u, d_in[5], 1280u, 0x20000u, d_in[4], 2621440u, 0x20000u, d_in[9], 2211840u, 0x20000u, d_in[10], 1280u, 0x20000u, d_out, 2621440u, 0x20000u);
}
```

```cpp
#include <hip/hip_runtime.h>
#include <stdint.h>
namespace {
constexpr unsigned WS_STATS = 8192, WS_KV = 32768, WS_Y2 = 2818048, WS_W2T = 5603328, WS_HT = 7897088, WS_EX = 15237120, WS_END = 18896896;
constexpr unsigned KVPI = 4224, Y2PI = 4224;
#define BUF const void*, unsigned, unsigned
__global__ __launch_bounds__(512, 2) void fused_kernel(BUF, BUF, BUF, BUF, BUF, BUF, BUF, BUF, BUF, BUF, BUF, BUF) {
#if defined(__HIP_DEVICE_COMPILE__)
  unsigned tid = threadIdx.x;
  asm volatile(
      "s_load_dwordx16 s[4:19], s[0:1], 0x0\n"
      "s_load_dwordx16 s[20:35], s[0:1], 0x40\n"
      "s_load_dwordx16 s[36:51], s[0:1], 0x80\n"
      "s_getpc_b64 s[64:65]\n"
      ".Lfz_pc:\n"
      "v_lshrrev_b32 v2, 6, v0\n"
      "v_and_b32 v1, 63, v0\n"
      "s_mov_b32 s60, s2\n"
      "v_readfirstlane_b32 s52, v2\n"
      "s_lshr_b32 s61, s2, 3\n"
      "s_mov_b32 s66, .Lfz_end-.Lfz_pc\n"
      "s_mov_b32 s67, 0x20000\n"
      "s_lshl3_add_u32 s62, s61, s52\n"
      "s_lshl_b32 s62, s62, 7\n"
      "v_and_b32 v2, 1, v0\n"
      "v_lshlrev_b32 v2, 15, v2\n"
      "buffer_load_dword v3, v2, s[64:67], s62 offen\n"
      "s_and_b32 s53, s52, 3\n"
      "s_and_b32 s54, s2, 1\n"
      "s_mul_i32 s62, s61, 37\n"
      "s_lshr_b32 s62, s62, 8\n"
      "s_mul_i32 s63, s62, 7\n"
      "s_sub_u32 s63, s61, s63\n"
      "s_cmp_lt_u32 s61, 28\n"
      "s_cselect_b32 s62, s62, s63\n"
      "s_cselect_b32 s55, s63, 7\n"
      "s_bfe_u32 s63, s2, 0x20001\n"
      "s_lshl2_add_u32 s56, s63, s62\n"
      "s_mul_i32 s63, s54, 7\n"
      "s_add_u32 s57, s63, s55\n"
      "s_mov_b32 s92, 0xffff0000\n"
      "s_and_b32 s62, s57, 1\n"
      "s_xor_b32 s63, s54, 1\n"
      "s_lshl1_add_u32 s99, s63, s62\n"
      "s_cmp_lt_u32 s52, 4\n"
      "s_cbranch_scc1 .LM\n"
      "s_cmp_lt_u32 s61, 28\n"
      "s_cbranch_scc0 .LSPARE\n"
      "v_lshrrev_b32 v72, 3, v1\n"
      "v_and_b32 v73, 7, v1\n"
      "v_mul_u32_u24 v74, 1280, v72\n"
      "v_lshl_add_u32 v24, v73, 4, v74\n"
      "v_lshrrev_b32 v75, 4, v1\n"
      "s_and_b32 s62, s53, 1\n"
      "s_lshl_b32 s62, s62, 2\n"
      "v_xor_b32 v73, v73, v75\n"
      "v_xor_b32 v73, s62, v73\n"
      "v_lshl_add_u32 v74, v73, 4, v74\n"
      "s_mul_i32 s62, s99, 40960\n"
      "v_add_u32 v20, s62, v74\n"
      "s_xor_b32 s62, s99, 1\n"
      "s_mul_i32 s62, s62, 40960\n"
      "v_add_u32 v21, s62, v74\n"
      "v_and_b32 v73, 15, v1\n"
      "v_lshlrev_b32 v76, 2, v75\n"
      "v_xor_b32 v73, v73, v76\n"
      "s_cmp_eq_u32 s57, 13\n"
      "s_cselect_b32 s63, 7, 15\n"
      "v_and_b32 v73, s63, v73\n"
      "v_mul_u32_u24 v75, 3456, v75\n"
      "v_lshl_add_u32 v16, v73, 4, v75\n"
      "v_add_u32 v17, 12800, v16\n"
      "v_add_u32 v18, 25600, v16\n"
      "v_add_u32 v19, 38400, v16\n"
      "s_mul_i32 s63, s56, 3\n"
      "s_add_u32 s63, s63, s57\n"
      "s_mul_i32 s64, s63, 205\n"
      "s_lshr_b32 s64, s64, 11\n"
      "s_mul_i32 s64, s64, 10\n"
      "s_sub_u32 s63, s63, s64\n"
      "s_lshl_b32 s64, s56, 4\n"
      "s_add_u32 s64, s64, s53\n"
      "s_mul_i32 s68, s64, 10240\n"
      "s_mul_i32 s69, s53, 55296\n"
      "s_lshl_b32 s64, s57, 8\n"
      "s_add_u32 s69, s69, s64\n"
      "s_lshl_b32 s64, s63, 7\n"
      "s_add_u32 s70, s68, s64\n"
      "s_mul_i32 s64, s63, 221184\n"
      "s_add_u32 s80, s69, s64\n"
      "s_add_u32 s64, s68, 1280\n"
      "s_and_b32 s91, s2, 7\n"
      "s_mul_i32 s91, s91, 28\n"
      "s_add_u32 s91, s91, s61\n"
      "s_mul_i32 s93, s91, 205\n"
      "s_lshr_b32 s90, s93, 10\n"
      "s_lshr_b32 s93, s93, 11\n"
      "s_mul_i32 s65, s93, 80640\n"
      "s_lshl_b32 s67, s91, 7\n"
      "s_add_u32 s65, s65, s67\n"
      "s_sub_u32 s66, s65, 10240\n"
      "s_cselect_b32 s66, 1073741824, s66\n"
      "s_mul_i32 s67, s53, 20480\n"
      "s_add_u32 s65, s65, s67\n"
      "s_lshr_b32 s67, s67, 1\n"
      "s_mul_i32 s90, s90, 39680\n"
      "s_add_u32 s90, s90, s67\n"
      "s_lshl_b32 s67, s91, 8\n"
      "s_add_u32 s90, s90, s67\n"
      "v_add_u32 v25, 9200, v24\n"
      "v_cmp_gt_u32 vcc, 40, v1\n"
      "v_mov_b32 v72, 1073741824\n"
      "s_mul_i32 s67, s53, 2080\n"
      "v_cndmask_b32 v26, v24, v72, vcc\n"
      "v_lshrrev_b32 v72, 4, v1\n"
      "v_mul_u32_u24 v72, 1280, v72\n"
      "v_and_b32 v73, 15, v1\n"
      "v_lshl_add_u32 v27, v73, 4, v72\n"
      "v_add_u32 v28, 4080, v27\n"
      "s_add_u32 s71, s70, 128\n"
      "s_add_u32 s81, s80, 221184\n"
      "s_cmp_eq_u32 s71, s64\n"
      "s_cselect_b32 s71, s68, s71\n"
      "s_cselect_b32 s81, s69, s81\n"
      "s_lshl_b32 s94, s53, 12\n"
      "s_lshl_b32 s95, s53, 10\n"
      "s_setprio 2\n"
      "s_waitcnt lgkmcnt(0)\n"
      "s_barrier\n"
      "s_add_u32 m0, s94, 0\n"
      "s_nop 0\n"
      "buffer_load_dwordx4 v16, s[8:11], s80 offen offset:0 lds\n"
      "buffer_load_dwordx4 v17, s[8:11], s80 offen offset:1024 lds\n"
      "buffer_load_dwordx4 v18, s[8:11], s80 offen offset:2048 lds\n"
      "buffer_load_dwordx4 v19, s[8:11], s80 offen offset:3072 lds\n"
      "s_add_u32 m0, s95, 16384\n"
      "s_nop 0\n"
      "buffer_load_dwordx4 v20, s[4:7], s70 offen lds\n"
      "s_add_u32 m0, s95, 20480\n"
      "s_nop 0\n"
      "buffer_load_dwordx4 v21, s[4:7], s70 offen lds\n"
      "s_mov_b32 m0, 139520\n"
      "s_nop 0\n"
      "buffer_load_dwordx4 v26, s[12:15], s66 offen nt lds\n"
      "s_add_u32 m0, s67, 140560\n"
      "s_nop 0\n"
      "buffer_load_dwordx4 v24, s[12:15], s65 offen nt lds\n"
      "buffer_load_dwordx4 v25, s[12:15], s65 offen offset:1040 nt lds\n"
      "s_add_u32 m0, s67, 148880\n"
      "s_nop 0\n"
      "buffer_load_dwordx4 v24, s[16:19], s65 offen nt lds\n"
      "buffer_load_dwordx4 v25, s[16:19], s65 offen offset:1040 nt lds\n"
      "s_add_u32 m0, s67, 122880\n"
      "s_nop 0\n"
      "buffer_load_dwordx4 v27, s[40:43], s90 offen nt lds\n"
      "buffer_load_dwordx4 v28, s[40:43], s90 offen offset:1040 nt lds\n"
      "s_add_u32 s72, s71, 128\n"
      "s_add_u32 s82, s81, 221184\n"
      "s_cmp_eq_u32 s72, s64\n"
      "s_cselect_b32 s72, s68, s72\n"
      "s_cselect_b32 s82, s69, s82\n"
      "s_add_u32 s73, s72, 128\n"
      "s_add_u32 s83, s82, 221184\n"
      "s_cmp_eq_u32 s73, s64\n"
      "s_cselect_b32 s73, s68, s73\n"
      "s_cselect_b32 s83, s69, s83\n"
      "s_add_u32 s74, s73, 128\n"
      "s_add_u32 s84, s83, 221184\n"
      "s_cmp_eq_u32 s74, s64\n"
      "s_cselect_b32 s74, s68, s74\n"
      "s_cselect_b32 s84, s69, s84\n"
      "s_add_u32 s75, s74, 128\n"
      "s_add_u32 s85, s84, 221184\n"
      "s_cmp_eq_u32 s75, s64\n"
      "s_cselect_b32 s75, s68, s75\n"
      "s_cselect_b32 s85, s69, s85\n"
      "s_add_u32 s76, s75, 128\n"
      "s_add_u32 s86, s85, 221184\n"
      "s_cmp_eq_u32 s76, s64\n"
      "s_cselect_b32 s76, s68, s76\n"
      "s_cselect_b32 s86, s69, s86\n"
      "s_add_u32 s77, s76, 128\n"
      "s_add_u32 s87, s86, 221184\n"
      "s_cmp_eq_u32 s77, s64\n"
      "s_cselect_b32 s77, s68, s77\n"
      "s_cselect_b32 s87, s69, s87\n"
      "s_add_u32 s78, s77, 128\n"
      "s_add_u32 s88, s87, 221184\n"
      "s_cmp_eq_u32 s78, s64\n"
      "s_cselect_b32 s78, s68, s78\n"
      "s_cselect_b32 s88, s69, s88\n"
      "s_add_u32 s79, s78, 128\n"
      "s_add_u32 s89, s88, 221184\n"
      "s_cmp_eq_u32 s79, s64\n"
      "s_cselect_b32 s79, s68, s79\n"
      "s_cselect_b32 s89, s69, s89\n"
      "s_xor_b32 s62, s99, 2\n"
      "s_mul_i32 s62, s62, 40960\n"
      "v_add_u32 v22, s62, v74\n"
      "s_xor_b32 s62, s99, 3\n"
      "s_mul_i32 s62, s62, 40960\n"
      "v_add_u32 v23, s62, v74\n"
      "s_mul_i32 s62, s93, 10\n"
      "s_sub_u32 s62, s91, s62\n"
      "s_lshl2_add_u32 s62, s62, s53\n"
      "s_lshl_b32 s62, s62, 5\n"
      "s_add_u32 s62, s62, s93\n"
      "s_lshl_b32 s62, s62, 7\n"
      "s_add_u32 s62, s62, 18644992\n"
      "v_mov_b32 v29, 1073741824\n"
      "v_writelane_b32 v29, s62, 0\n"
      "s_mul_i32 s93, s91, 205\n"
      "s_lshr_b32 s93, s93, 10\n"
      "s_mul_i32 s62, s93, 5\n"
      "s_sub_u32 s91, s91, s62\n"
      "s_lshr_b32 s30, s93, 2\n"
      "s_and_b32 s29, s93, 3\n"
      "s_mul_i32 s29, s29, 7\n"
      "s_add_u32 s29, s29, s30\n"
      "s_cmp_ge_u32 s30, 7\n"
      "s_cselect_b32 s30, 21, 0\n"
      "s_add_u32 s29, s29, s30\n"
      "s_lshl_b32 s29, s29, 10\n"
      "s_mul_i32 s30, s91, 458752\n"
      "s_add_u32 s29, s29, s30\n"
      "s_add_u32 s29, s29, 5603328\n"
      "s_mul_i32 s30, s53, 114688\n"
      "s_add_u32 s29, s29, s30\n"
      "s_add_u32 s30, s29, 57344\n"
      "s_lshl3_add_u32 s62, s93, s91\n"
      "s_lshl_b32 s62, s62, 7\n"
      "s_add_u32 s62, s62, 18808832\n"
      "s_lshl3_add_u32 s63, s60, 4096\n"
      "s_cmp_eq_u32 s53, 0\n"
      "s_cselect_b32 s62, s62, 1073741824\n"
      "s_cselect_b32 s63, s63, 1073741824\n"
      "v_writelane_b32 v29, s62, 1\n"
      "v_writelane_b32 v29, s63, 2\n"
      "v_lshrrev_b32 v44, 4, v1\n"
      "v_and_b32 v72, 1, v44\n"
      "v_lshrrev_b32 v44, 1, v44\n"
      "v_lshl_add_u32 v44, v72, 2, v44\n"
      "v_mul_u32_u24 v44, 1040, v44\n"
      "v_bfe_u32 v72, v1, 2, 2\n"
      "v_lshl_add_u32 v44, v72, 8, v44\n"
      "v_and_b32 v72, 3, v1\n"
      "v_lshl_add_u32 v44, v72, 3, v44\n"
      "s_lshl_b32 s64, s53, 6\n"
      "s_add_u32 s64, s64, 122880\n"
      "v_add_u32 v44, s64, v44\n"
      "v_mov_b64 v[36:37], 0\n"
      "v_mov_b64 v[38:39], 0\n"
      "s_lshl_b32 s63, s53, 10\n"
      "s_add_u32 s63, s63, 157696\n"
      "v_lshl_add_u32 v40, v1, 4, s63\n"
      "ds_write_b128 v40, v[36:39]\n"
      "v_lshlrev_b32 v40, 2, v1\n"
      "v_add_u32 v40, 161792, v40\n"
      "ds_write_b32 v40, v36\n"
      "v_lshlrev_b32 v5, 4, v1\n"
      "v_add_u32 v6, 4096, v5\n"
      "s_lshr_b32 s62, s60, 1\n"
      "s_mul_i32 s62, s62, 5\n"
      "s_mul_i32 s63, s54, 3\n"
      "s_add_u32 s63, s63, s62\n"
      "s_lshl_b32 s24, s63, 12\n"
      "s_add_u32 s24, s24, s95\n"
      "s_add_u32 s25, s24, 4096\n"
      "s_add_u32 s63, s62, 2\n"
      "s_lshl_b32 s26, s63, 12\n"
      "s_add_u32 s26, s26, s95\n"
      "v_mov_b32 v32, s26\n"
      "v_writelane_b32 v32, s24, 0\n"
      "v_writelane_b32 v32, s25, 1\n"
      "s_cmp_eq_u32 s53, 0\n"
      "s_cselect_b32 s63, 1073741824, -4\n"
      "v_add_u32 v32, s63, v32\n"
      "v_lshrrev_b32 v33, 11, v32\n"
      "v_xor_b32 v33, 524288, v33\n"
      "s_lshr_b32 s91, s62, 4\n"
      "s_add_u32 s63, s62, 4\n"
      "s_lshr_b32 s63, s63, 4\n"
      "s_sub_u32 s63, s63, s91\n"
      "s_lshl_b32 s63, s63, 9\n"
      "v_and_b32 v72, 8, v1\n"
      "v_mul_u32_u24 v72, s63, v72\n"
      "v_and_b32 v73, 7, v1\n"
      "v_lshl_add_u32 v30, v73, 7, v72\n"
      "s_lshl_b32 s91, s91, 12\n"
      "s_add_u32 s91, s91, s95\n"
      "s_add_u32 s91, s91, 18644992\n"
      "v_add_u32 v30, s91, v30\n"
      "s_mul_i32 s63, s53, 4608\n"
      "s_cmp_eq_u32 s53, 3\n"
      "s_cselect_b32 s63, 9728, s63\n"
      "s_add_u32 s63, s63, 139520\n"
      "v_lshl_add_u32 v34, v1, 3, s63\n"
      "v_lshrrev_b32 v72, 1, v1\n"
      "v_min_u32 v72, 6, v72\n"
      "s_mul_i32 s62, s54, 28\n"
      "s_add_u32 s62, s62, s53\n"
      "v_lshl_add_u32 v72, v72, 2, s62\n"
      "v_min_u32 v72, 53, v72\n"
      "s_lshl_b32 s63, s56, 14\n"
      "s_add_u32 s63, s63, 18382848\n"
      "v_lshl_add_u32 v13, v72, 8, s63\n"
      "s_mul_i32 s63, s55, 80\n"
      "s_add_u32 s91, s63, 79\n"
      "s_lshr_b32 s63, s63, 7\n"
      "s_lshr_b32 s91, s91, 7\n"
      "s_sub_u32 s91, s91, s63\n"
      "v_and_b32 v73, s91, v1\n"
      "v_lshl_add_u32 v72, v72, 3, v73\n"
      "s_lshl_b32 s63, s63, 7\n"
      "s_add_u32 s63, s63, 18808832\n"
      "v_lshl_add_u32 v31, v72, 7, s63\n"
      "v_lshrrev_b32 v72, 3, v1\n"
      "v_min_u32 v72, 5, v72\n"
      "v_mul_u32_u24 v73, 4160, v72\n"
      "v_lshrrev_b32 v72, 2, v72\n"
      "v_mul_u32_u24 v72, 41216, v72\n"
      "v_sub_u32 v73, v73, v72\n"
      "v_add_u32 v73, 122880, v73\n"
      "v_and_b32 v72, 7, v1\n"
      "v_lshl_add_u32 v35, v72, 3, v73\n"
      "s_add_u32 s62, s95, 122944\n"
      "v_add_u32 v40, s62, v5\n"
      "v_subrev_u32 v41, 24576, v40\n"
      "v_and_b32 v72, 31, v1\n"
      "v_lshlrev_b32 v42, 3, v72\n"
      "s_add_u32 s62, s95, 157696\n"
      "v_lshl_add_u32 v43, v72, 5, s62\n"
      "s_xor_b32 s31, s99, s53\n"
      "s_lshl2_add_u32 s31, s56, s31\n"
      "s_lshl_b32 s31, s31, 8\n"
      "s_add_u32 s31, s31, 8192\n"
      "s_lshl2_add_u32 s62, s54, s53\n"
      "s_cmp_ge_u32 s62, 6\n"
      "s_cselect_b32 s63, 1073741824, 6144\n"
      "v_add_u32 v7, s63, v5\n"
      "s_mul_i32 s62, s62, 7168\n"
      "s_mul_i32 s63, s55, 286720\n"
      "s_add_u32 s63, s63, s62\n"
      "s_add_u32 s27, s63, 5603328\n"
      "s_mul_i32 s63, s56, 458752\n"
      "s_add_u32 s63, s63, s62\n"
      "s_add_u32 s63, s63, 7897088\n"
      "s_mul_i32 s62, s54, 229376\n"
      "s_add_u32 s91, s63, s62\n"
      "s_sub_u32 s62, 229376, s62\n"
      "s_add_u32 s93, s63, s62\n"
      "s_sub_u32 s62, 4, s53\n"
      "s_and_b32 s62, s62, 3\n"
      "s_mul_i32 s62, s62, 57344\n"
      "s_add_u32 s44, s93, s62\n"
      "s_add_u32 s48, s91, s62\n"
      "s_sub_u32 s62, 5, s53\n"
      "s_and_b32 s62, s62, 3\n"
      "s_mul_i32 s62, s62, 57344\n"
      "s_add_u32 s45, s93, s62\n"
      "s_add_u32 s49, s91, s62\n"
      "s_sub_u32 s62, 6, s53\n"
      "s_and_b32 s62, s62, 3\n"
      "s_mul_i32 s62, s62, 57344\n"
      "s_add_u32 s46, s93, s62\n"
      "s_add_u32 s50, s91, s62\n"
      "s_sub_u32 s62, 7, s53\n"
      "s_and_b32 s62, s62, 3\n"
      "s_mul_i32 s62, s62, 57344\n"
      "s_add_u32 s47, s93, s62\n"
      "s_add_u32 s51, s91, s62\n"
      "s_add_u32 s91, s53, 3\n"
      "s_and_b32 s91, s91, 3\n"
      "s_lshl2_add_u32 s62, s91, 161888\n"
      "v_mov_b32 v15, s62\n"
      "s_mul_i32 s62, s53, 5120\n"
      "s_add_u32 s63, s62, 32768\n"
      "v_add_u32 v8, s63, v5\n"
      "s_mul_i32 s63, s91, 5120\n"
      "s_add_u32 s63, s63, 32768\n"
      "v_add_u32 v9, s63, v5\n"
      "s_xor_b32 s63, s60, 1\n"
      "s_lshl2_add_u32 s63, s63, s53\n"
      "s_mul_i32 s28, s63, 3072\n"
      "s_add_u32 s28, s28, 15237120\n"
      "v_cmp_eq_u32 vcc, s53, v1\n"
      "v_and_b32 v14, 48, v1\n"
      "v_add_u32 v14, 163072, v14\n"
      "v_cndmask_b32 v55, 0, 1, vcc\n"
      "v_mov_b32 v45, 161920\n"
      "s_add_u32 m0, s94, 24576\n"
      "s_nop 0\n"
      "buffer_load_dwordx4 v16, s[8:11], s81 offen offset:0 lds\n"
      "buffer_load_dwordx4 v17, s[8:11], s81 offen offset:1024 lds\n"
      "buffer_load_dwordx4 v18, s[8:11], s81 offen offset:2048 lds\n"
      "buffer_load_dwordx4 v19, s[8:11], s81 offen offset:3072 lds\n"
      "s_add_u32 m0, s95, 40960\n"
      "s_nop 0\n"
      "buffer_load_dwordx4 v20, s[4:7], s71 offen lds\n"
      "s_add_u32 m0, s95, 45056\n"
      "s_nop 0\n"
      "buffer_load_dwordx4 v21, s[4:7], s71 offen lds\n"
      "s_waitcnt vmcnt(13) lgkmcnt(0)\n"
      "v_cmp_eq_u32 vcc, 0, v1\n"
      "v_lshlrev_b32 v2, 2, v1\n"
      "v_add_u32 v2, 157696, v2\n"
      "v_cndmask_b32 v3, 0, 1, vcc\n"
      "s_barrier\n"
      "s_add_u32 m0, s94, 49152\n"
      "s_nop 0\n"
      "buffer_load_dwordx4 v16, s[8:11], s82 offen offset:0 lds\n"
      "buffer_load_dwordx4 v17, s[8:11], s82 offen offset:1024 lds\n"
      "buffer_load_dwordx4 v18, s[8:11], s82 offen offset:2048 lds\n"
      "buffer_load_dwordx4 v19, s[8:11], s82 offen offset:3072 lds\n"
      "s_add_u32 m0, s95, 65536\n"
      "s_nop 0\n"
      "buffer_load_dwordx4 v20, s[4:7], s72 offen lds\n"
      "s_add_u32 m0, s95, 69632\n"
      "s_nop 0\n"
      "buffer_load_dwordx4 v21, s[4:7], s72 offen lds\n"
      "s_waitcnt vmcnt(12)\n"
      "s_barrier\n"
      "ds_read_b64_tr_b16 v[88:89], v44 offset:0\n"
      "ds_read_b64_tr_b16 v[90:91], v44 offset:2080\n"
      "ds_read_b64_tr_b16 v[92:93], v44 offset:32\n"
      "ds_read_b64_tr_b16 v[94:95], v44 offset:2112\n"
      "ds_read_b64 v[46:47], v45\n"
      "s_waitcnt lgkmcnt(0)\n"
      "buffer_store_dwordx4 v[88:91], v5, s[20:23], s29 offen sc1\n"
      "buffer_store_dwordx4 v[92:95], v5, s[20:23], s30 offen sc1\n"
      "s_waitcnt lgkmcnt(0)\n"
      "v_readfirstlane_b32 s58, v46\n"
      "v_readfirstlane_b32 s59, v47\n"
      "v_mov_b32 v1, v55\n"
      "s_add_u32 m0, s94, 73728\n"
      "s_nop 0\n"
      "buffer_load_dwordx4 v16, s[8:11], s83 offen offset:0 lds\n"
      "buffer_load_dwordx4 v17, s[8:11], s83 offen offset:1024 lds\n"
      "buffer_load_dwordx4 v18, s[8:11], s83 offen offset:2048 lds\n"
      "buffer_load_dwordx4 v19, s[8:11], s83 offen offset:3072 lds\n"
      "s_add_u32 m0, s95, 90112\n"
      "s_nop 0\n"
      "buffer_load_dwordx4 v20, s[4:7], s73 offen lds\n"
      "s_add_u32 m0, s95, 94208\n"
      "s_nop 0\n"
      "buffer_load_dwordx4 v21, s[4:7], s73 offen lds\n"
      "s_waitcnt vmcnt(14)\n"
      "s_barrier\n"
      "s_add_u32 m0, s94, 98304\n"
      "s_nop 0\n"
      "buffer_load_dwordx4 v16, s[8:11], s84 offen offset:0 lds\n"
      "buffer_load_dwordx4 v17, s[8:11], s84 offen offset:1024 lds\n"
      "buffer_load_dwordx4 v18, s[8:11], s84 offen offset:2048 lds\n"
      "buffer_load_dwordx4 v19, s[8:11], s84 offen offset:3072 lds\n"
      "s_add_u32 m0, s95, 114688\n"
      "s_nop 0\n"
      "buffer_load_dwordx4 v20, s[4:7], s74 offen lds\n"
      "s_add_u32 m0, s95, 118784\n"
      "s_nop 0\n"
      "buffer_load_dwordx4 v21, s[4:7], s74 offen lds\n"
      "s_waitcnt vmcnt(14)\n"
      "s_barrier\n"
      "s_add_u32 m0, s94, 0\n"
      "s_nop 0\n"
      "buffer_load_dwordx4 v16, s[8:11], s85 offen offset:0 lds\n"
      "buffer_load_dwordx4 v17, s[8:11], s85 offen offset:1024 lds\n"
      "buffer_load_dwordx4 v18, s[8:11], s85 offen offset:2048 lds\n"
      "buffer_load_dwordx4 v19, s[8:11], s85 offen offset:3072 lds\n"
      "s_add_u32 m0, s95, 16384\n"
      "s_nop 0\n"
      "buffer_load_dwordx4 v20, s[4:7], s75 offen lds\n"
      "s_add_u32 m0, s95, 20480\n"
      "s_nop 0\n"
      "buffer_load_dwordx4 v21, s[4:7], s75 offen lds\n"
      "buffer_load_dwordx2 v[76:77], v42, s[20:23], s31 offen sc1\n"
      "s_waitcnt vmcnt(13)\n"
      "s_barrier\n"
      "s_add_u32 m0, s94, 24576\n"
      "s_nop 0\n"
      "buffer_load_dwordx4 v16, s[8:11], s86 offen offset:0 lds\n"
      "buffer_load_dwordx4 v17, s[8:11], s86 offen offset:1024 lds\n"
      "buffer_load_dwordx4 v18, s[8:11], s86 offen offset:2048 lds\n"
      "buffer_load_dwordx4 v19, s[8:11], s86 offen offset:3072 lds\n"
      "s_add_u32 m0, s95, 40960\n"
      "s_nop 0\n"
      "buffer_load_dwordx4 v20, s[4:7], s76 offen lds\n"
      "s_add_u32 m0, s95, 45056\n"
      "s_nop 0\n"
      "buffer_load_dwordx4 v21, s[4:7], s76 offen lds\n"
      "s_waitcnt vmcnt(13)\n"
      "s_barrier\n"
      "s_add_u32 m0, s94, 49152\n"
      "s_nop 0\n"
      "buffer_load_dwordx4 v16, s[8:11], s87 offen offset:0 lds\n"
      "buffer_load_dwordx4 v17, s[8:11], s87 offen offset:1024 lds\n"
      "buffer_load_dwordx4 v18, s[8:11], s87 offen offset:2048 lds\n"
      "buffer_load_dwordx4 v19, s[8:11], s87 offen offset:3072 lds\n"
      "s_add_u32 m0, s95, 65536\n"
      "s_nop 0\n"
      "buffer_load_dwordx4 v20, s[4:7], s77 offen lds\n"
      "s_add_u32 m0, s95, 69632\n"
      "s_nop 0\n"
      "buffer_load_dwordx4 v21, s[4:7], s77 offen lds\n"
      "s_waitcnt vmcnt(12)\n"
      "s_barrier\n"
      "buffer_store_dwordx2 v[46:47], v29, s[20:23], 0 offen sc1\n"
      "v_cmp_ne_u32 vcc, s58, v77\n"
      "s_cbranch_vccnz .Lt_1\n"
      ".Lt_2:\n"
      "v_and_b32 v82, s92, v76\n"
      "v_rcp_f32 v77, v82\n"
      "v_xor_b32 v85, 0x8000, v76\n"
      "s_mov_b32 s62, 0x01000100\n"
      "v_mul_f32 v80, 0xc0135761, v77\n"
      "v_mul_f32 v81, v77, v77\n"
      "v_cmp_nle_f32 vcc, 0x3089705f, v77\n"
      "v_mul_f32 v81, 0x3d372713, v81\n"
      "v_perm_b32 v84, v85, v85, s62\n"
      "v_mul_f32 v81, v81, v80\n"
      "v_mov_b32 v83, 1.0\n"
      "ds_write_b64 v43, v[84:85] offset:16\n"
      "s_cbranch_vccnz .Lb_4\n"
      ".Lb_5:\n"
      "ds_write_b128 v43, v[80:83]\n"
      "s_add_u32 m0, s94, 73728\n"
      "s_nop 0\n"
      "buffer_load_dwordx4 v16, s[8:11], s88 offen offset:0 lds\n"
      "buffer_load_dwordx4 v17, s[8:11], s88 offen offset:1024 lds\n"
      "buffer_load_dwordx4 v18, s[8:11], s88 offen offset:2048 lds\n"
      "buffer_load_dwordx4 v19, s[8:11], s88 offen offset:3072 lds\n"
      "s_add_u32 m0, s95, 90112\n"
      "s_nop 0\n"
      "buffer_load_dwordx4 v20, s[4:7], s78 offen lds\n"
      "s_add_u32 m0, s95, 94208\n"
      "s_nop 0\n"
      "buffer_load_dwordx4 v21, s[4:7], s78 offen lds\n"
      "s_waitcnt vmcnt(13)\n"
      "s_barrier\n"
      "s_add_u32 m0, s94, 98304\n"
      "s_nop 0\n"
      "buffer_load_dwordx4 v16, s[8:11], s89 offen offset:0 lds\n"
      "buffer_load_dwordx4 v17, s[8:11], s89 offen offset:1024 lds\n"
      "buffer_load_dwordx4 v18, s[8:11], s89 offen offset:2048 lds\n"
      "buffer_load_dwordx4 v19, s[8:11], s89 offen offset:3072 lds\n"
      "s_add_u32 m0, s95, 114688\n"
      "s_nop 0\n"
      "buffer_load_dwordx4 v20, s[4:7], s79 offen lds\n"
      "s_add_u32 m0, s95, 118784\n"
      "s_nop 0\n"
      "buffer_load_dwordx4 v21, s[4:7], s79 offen lds\n"
      "s_waitcnt vmcnt(13)\n"
      "s_barrier\n"
      "s_add_u32 m0, s95, 0\n"
      "s_nop 0\n"
      "buffer_load_dwordx4 v22, s[4:7], s70 offen lds\n"
      "s_add_u32 m0, s95, 4096\n"
      "s_nop 0\n"
      "buffer_load_dwordx4 v22, s[4:7], s71 offen lds\n"
      "s_add_u32 m0, s95, 8192\n"
      "s_nop 0\n"
      "buffer_load_dwordx4 v22, s[4:7], s72 offen lds\n"
      "s_add_u32 m0, s95, 12288\n"
      "s_nop 0\n"
      "buffer_load_dwordx4 v22, s[4:7], s73 offen lds\n"
      "s_add_u32 m0, s95, 16384\n"
      "s_nop 0\n"
      "buffer_load_dwordx4 v22, s[4:7], s74 offen lds\n"
      "s_waitcnt vmcnt(11)\n"
      "s_barrier\n"
      "s_add_u32 m0, s95, 20480\n"
      "s_nop 0\n"
      "buffer_load_dwordx4 v22, s[4:7], s75 offen lds\n"
      "s_add_u32 m0, s95, 24576\n"
      "s_nop 0\n"
      "buffer_load_dwordx4 v22, s[4:7], s76 offen lds\n"
      "s_add_u32 m0, s95, 28672\n"
      "s_nop 0\n"
      "buffer_load_dwordx4 v22, s[4:7], s77 offen lds\n"
      "s_add_u32 m0, s95, 32768\n"
      "s_nop 0\n"
      "buffer_load_dwordx4 v22, s[4:7], s78 offen lds\n"
      "s_add_u32 m0, s95, 36864\n"
      "s_nop 0\n"
      "buffer_load_dwordx4 v22, s[4:7], s79 offen lds\n"
      "s_waitcnt vmcnt(10) lgkmcnt(0)\n"
      "s_barrier\n"
      "s_add_u32 m0, s95, 49152\n"
      "s_nop 0\n"
      "buffer_load_dwordx4 v23, s[4:7], s70 offen lds\n"
      "s_add_u32 m0, s95, 53248\n"
      "s_nop 0\n"
      "buffer_load_dwordx4 v23, s[4:7], s71 offen lds\n"
      "s_add_u32 m0, s95, 57344\n"
      "s_nop 0\n"
      "buffer_load_dwordx4 v23, s[4:7], s72 offen lds\n"
      "s_add_u32 m0, s95, 61440\n"
      "s_nop 0\n"
      "buffer_load_dwordx4 v23, s[4:7], s73 offen lds\n"
      "s_add_u32 m0, s95, 65536\n"
      "s_nop 0\n"
      "buffer_load_dwordx4 v23, s[4:7], s74 offen lds\n"
      "buffer_load_dwordx2 v[50:51], v31, s[20:23], 0 offen sc1\n"
      "buffer_load_dwordx2 v[48:49], v30, s[20:23], 0 offen sc1\n"
      "s_waitcnt vmcnt(12)\n"
      "s_barrier\n"
      "s_add_u32 m0, s95, 69632\n"
      "s_nop 0\n"
      "buffer_load_dwordx4 v23, s[4:7], s75 offen lds\n"
      "s_add_u32 m0, s95, 73728\n"
      "s_nop 0\n"
      "buffer_load_dwordx4 v23, s[4:7], s76 offen lds\n"
      "s_add_u32 m0, s95, 77824\n"
      "s_nop 0\n"
      "buffer_load_dwordx4 v23, s[4:7], s77 offen lds\n"
      "s_add_u32 m0, s95, 81920\n"
      "s_nop 0\n"
      "buffer_load_dwordx4 v23, s[4:7], s78 offen lds\n"
      "s_add_u32 m0, s95, 86016\n"
      "s_nop 0\n"
      "buffer_load_dwordx4 v23, s[4:7], s79 offen lds\n"
      "ds_add_u32 v2, v3 offset:4152\n"
      "s_waitcnt vmcnt(12)\n"
      "ds_add_u32 v2, v3 offset:2072\n"
      "ds_read_b32 v4, v2 offset:4152\n"
      "s_waitcnt lgkmcnt(0)\n"
      "v_readfirstlane_b32 s2, v4\n"
      "s_cmp_lt_u32 s2, 4\n"
      "s_cbranch_scc1 .Lw_6\n"
      ".Lw_7:\n"
      ".LS2:\n"
      "buffer_load_dwordx4 v[56:59], v5, s[36:39], s24 offen nt\n"
      "buffer_load_dwordx4 v[60:63], v5, s[36:39], s25 offen nt\n"
      "buffer_load_dwordx4 v[64:67], v5, s[36:39], s26 offen nt\n"
      "buffer_load_dword v52, v32, s[36:39], 0 offen nt\n"
      "buffer_load_ushort v53, v33, s[32:35], 0 offen\n"
      "ds_write_b64 v34, v[36:37]\n"
      "s_waitcnt vmcnt(5)\n"
      "ds_add_u32 v2, v3 offset:3096\n"
      "s_waitcnt vmcnt(11)\n"
      "v_cmp_ne_u64 vcc, s[58:59], v[50:51]\n"
      "s_cbranch_vccnz .Lf_9\n"
      ".Lf_10:\n"
      "s_add_u32 s66, s27, 57344\n"
      "s_add_u32 s67, s27, 114688\n"
      "s_add_u32 s68, s27, 172032\n"
      "s_add_u32 s69, s27, 229376\n"
      "s_waitcnt vmcnt(10)\n"
      "v_cmp_ne_u64 vcc, s[58:59], v[48:49]\n"
      "s_cbranch_vccnz .Lf_12\n"
      ".Lf_13:\n"
      "s_add_u32 m0, s95, 140032\n"
      "s_lshr_b32 s62, s24, 12\n"
      "s_mul_i32 s62, s62, 128\n"
      "s_add_u32 s24, s24, s62\n"
      "s_add_u32 s24, s24, 32768\n"
      "buffer_load_dwordx4 v5, s[20:23], s24 offen sc1 lds\n"
      "s_add_u32 m0, s95, 144640\n"
      "s_lshr_b32 s62, s25, 12\n"
      "s_mul_i32 s62, s62, 128\n"
      "s_add_u32 s25, s25, s62\n"
      "s_add_u32 s25, s25, 32768\n"
      "buffer_load_dwordx4 v5, s[20:23], s25 offen sc1 lds\n"
      "s_add_u32 m0, s95, 149760\n"
      "s_lshr_b32 s62, s26, 12\n"
      "s_mul_i32 s62, s62, 128\n"
      "s_add_u32 s26, s26, s62\n"
      "s_add_u32 s26, s26, 32768\n"
      "buffer_load_dwordx4 v5, s[20:23], s26 offen sc1 lds\n"
      "s_waitcnt vmcnt(6)\n"
      "buffer_load_dwordx4 v[116:119], v5, s[20:23], s27 offen offset:0 sc1\n"
      "buffer_load_dwordx4 v[136:139], v5, s[20:23], s27 offen offset:1024 sc1\n"
      "s_waitcnt vmcnt(6)\n"
      "buffer_load_dwordx4 v[156:159], v5, s[20:23], s27 offen offset:2048 sc1\n"
      "buffer_load_dwordx4 v[176:179], v5, s[20:23], s27 offen offset:3072 sc1\n"
      "s_waitcnt vmcnt(7)\n"
      "s_cmp_eq_u32 s53, 0\n"
      "s_cbranch_scc0 .Ld_15\n"
      "v_readlane_b32 s62, v53, 0\n"
      "v_readlane_b32 s63, v53, 1\n"
      "v_readlane_b32 s91, v53, 2\n"
      "s_mov_b64 exec, 1\n"
      "v_lshlrev_b32 v72, 16, v56\n"
      "s_lshl_b32 s62, s62, 16\n"
      "v_and_b32 v73, s92, v56\n"
      "v_add_f32 v72, s62, v72\n"
      "v_cvt_pk_bf16_f32 v56, v72, v73\n"
      "v_lshlrev_b32 v72, 16, v60\n"
      "s_lshl_b32 s63, s63, 16\n"
      "v_and_b32 v73, s92, v60\n"
      "v_add_f32 v72, s63, v72\n"
      "v_cvt_pk_bf16_f32 v60, v72, v73\n"
      "v_lshlrev_b32 v72, 16, v64\n"
      "s_lshl_b32 s91, s91, 16\n"
      "v_and_b32 v73, s92, v64\n"
      "v_add_f32 v72, s91, v72\n"
      "v_cvt_pk_bf16_f32 v64, v72, v73\n"
      "s_mov_b64 exec, -1\n"
      ".Ld_15:\n"
      "ds_write_b128 v40, v[60:63] offset:8320\n"
      "v_readlane_b32 s62, v52, 1\n"
      "v_alignbyte_b32 v103, v63, v62, 2\n"
      "v_alignbyte_b32 v101, v61, v60, 2\n"
      "v_writelane_b32 v72, s62, 0\n"
      "v_alignbyte_b32 v102, v62, v61, 2\n"
      "s_nop 0\n"
      "v_mov_b32_dpp v72, v63 wave_shr:1 row_mask:0xf bank_mask:0xf\n"
      "v_alignbyte_b32 v100, v60, v72, 2\n"
      "ds_write_b128 v40, v[100:103] offset:12480\n"
      "s_waitcnt vmcnt(6)\n"
      "buffer_load_dwordx4 v[196:199], v6, s[20:23], s27 offen offset:0 sc1\n"
      "buffer_load_dwordx4 v[216:219], v6, s[20:23], s27 offen offset:1024 sc1\n"
      "ds_write_b64 v35, v[36:37]\n"
      "ds_write_b128 v41, v[64:67] offset:0\n"
      "v_readlane_b32 s62, v52, 2\n"
      "v_alignbyte_b32 v107, v67, v66, 2\n"
      "v_alignbyte_b32 v105, v65, v64, 2\n"
      "v_writelane_b32 v72, s62, 0\n"
      "v_alignbyte_b32 v106, v66, v65, 2\n"
      "s_nop 0\n"
      "v_mov_b32_dpp v72, v67 wave_shr:1 row_mask:0xf bank_mask:0xf\n"
      "v_alignbyte_b32 v104, v64, v72, 2\n"
      "ds_write_b128 v41, v[104:107] offset:4160\n"
      "s_waitcnt vmcnt(6)\n"
      "buffer_load_dwordx4 v[236:239], v7, s[20:23], s27 offen offset:0 sc1\n"
      "buffer_load_dwordx4 v[120:123], v5, s[20:23], s66 offen offset:0 sc1\n"
      "ds_write_b128 v40, v[56:59] offset:0\n"
      "v_alignbyte_b32 v99, v59, v58, 2\n"
      "v_alignbyte_b32 v97, v57, v56, 2\n"
      "v_alignbyte_b32 v98, v58, v57, 2\n"
      "v_mov_b32_dpp v52, v59 wave_shr:1 row_mask:0xf bank_mask:0xf\n"
      "v_alignbyte_b32 v96, v56, v52, 2\n"
      "ds_write_b128 v40, v[96:99] offset:4160\n"
      "s_waitcnt vmcnt(8)\n"
      "ds_add_u32 v2, v3 offset:4128\n"
      "s_waitcnt vmcnt(6)\n"
      "buffer_load_dwordx4 v[140:143], v5, s[20:23], s66 offen offset:1024 sc1\n"
      "buffer_load_dwordx4 v[160:163], v5, s[20:23], s66 offen offset:2048 sc1\n"
      "s_waitcnt vmcnt(6)\n"
      "buffer_load_dwordx4 v[180:183], v5, s[20:23], s66 offen offset:3072 sc1\n"
      "buffer_load_dwordx4 v[200:203], v6, s[20:23], s66 offen offset:0 sc1\n"
      "s_waitcnt vmcnt(6)\n"
      "buffer_load_dwordx4 v[220:223], v6, s[20:23], s66 offen offset:1024 sc1\n"
      "buffer_load_dwordx4 v[240:243], v7, s[20:23], s66 offen offset:0 sc1\n"
      "s_waitcnt vmcnt(6)\n"
      "buffer_load_dwordx4 v[124:127], v5, s[20:23], s67 offen offset:0 sc1\n"
      "buffer_load_dwordx4 v[144:147], v5, s[20:23], s67 offen offset:1024 sc1\n"
      "s_waitcnt vmcnt(6)\n"
      "buffer_load_dwordx4 v[164:167], v5, s[20:23], s67 offen offset:2048 sc1\n"
      "buffer_load_dwordx4 v[184:187], v5, s[20:23], s67 offen offset:3072 sc1\n"
      "s_waitcnt vmcnt(6)\n"
      "buffer_load_dwordx4 v[204:207], v6, s[20:23], s67 offen offset:0 sc1\n"
      "buffer_load_dwordx4 v[224:227], v6, s[20:23], s67 offen offset:1024 sc1\n"
      "s_waitcnt vmcnt(6)\n"
      "buffer_load_dwordx4 v[244:247], v7, s[20:23], s67 offen offset:0 sc1\n"
      "buffer_load_dwordx4 v[128:131], v5, s[20:23], s68 offen offset:0 sc1\n"
      "s_waitcnt vmcnt(6)\n"
      "buffer_load_dwordx4 v[148:151], v5, s[20:23], s68 offen offset:1024 sc1\n"
      "buffer_load_dwordx4 v[168:171], v5, s[20:23], s68 offen offset:2048 sc1\n"
      "s_waitcnt vmcnt(6)\n"
      "buffer_load_dwordx4 v[188:191], v5, s[20:23], s68 offen offset:3072 sc1\n"
      "buffer_load_dwordx4 v[208:211], v6, s[20:23], s68 offen offset:0 sc1\n"
      "s_waitcnt vmcnt(6)\n"
      "buffer_load_dwordx4 v[228:231], v6, s[20:23], s68 offen offset:1024 sc1\n"
      "buffer_load_dwordx4 v[248:251], v7, s[20:23], s68 offen offset:0 sc1\n"
      "s_waitcnt vmcnt(6)\n"
      "buffer_load_dwordx4 v[132:135], v5, s[20:23], s69 offen offset:0 sc1\n"
      "buffer_load_dwordx4 v[152:155], v5, s[20:23], s69 offen offset:1024 sc1\n"
      "s_waitcnt vmcnt(6)\n"
      "buffer_load_dwordx4 v[172:175], v5, s[20:23], s69 offen offset:2048 sc1\n"
      "buffer_load_dwordx4 v[192:195], v5, s[20:23], s69 offen offset:3072 sc1\n"
      "s_waitcnt vmcnt(6)\n"
      "buffer_load_dwordx4 v[212:215], v6, s[20:23], s69 offen offset:0 sc1\n"
      "buffer_load_dwordx4 v[232:235], v6, s[20:23], s69 offen offset:1024 sc1\n"
      "s_waitcnt vmcnt(6)\n"
      "buffer_load_dwordx4 v[252:255], v7, s[20:23], s69 offen offset:0 sc1\n"
      "s_and_b32 s75, s58, 0x1fff\n"
      "s_or_b32 s75, s75, 0x4000\n"
      "s_lshl_b32 s75, s75, 16\n"
      "s_mov_b32 s74, 128\n"
      "s_mov_b32 s73, 0x100000\n"
      "s_mov_b64 vcc, 0\n"
      "s_branch .LWARM\n"
      ".LWARME:\n"
      "ds_read_b32 v4, v2 offset:4168\n"
      "s_waitcnt lgkmcnt(0)\n"
      "v_readfirstlane_b32 s2, v4\n"
      "s_cmp_lt_u32 s2, 4\n"
      "s_cbranch_scc1 .Lw_16\n"
      ".Lw_17:\n"
      "buffer_load_dwordx2 v[10:11], v13, s[20:23], 0 offen sc1\n"
      "s_waitcnt vmcnt(0)\n"
      "v_cmp_ne_u64 vcc, s[58:59], v[10:11]\n"
      "s_cbranch_vccnz .Lf_19\n"
      ".Lf_20:\n"
      "buffer_load_dwordx4 v[32:35], v5, s[20:23], s44 offen offset:0 sc1\n"
      "buffer_load_dwordx4 v[36:39], v5, s[20:23], s44 offen offset:1024 sc1\n"
      "buffer_load_dwordx4 v[40:43], v5, s[20:23], s44 offen offset:2048 sc1\n"
      "buffer_load_dwordx4 v[44:47], v5, s[20:23], s44 offen offset:3072 sc1\n"
      "buffer_load_dwordx4 v[48:51], v6, s[20:23], s44 offen offset:0 sc1\n"
      "buffer_load_dwordx4 v[52:55], v6, s[20:23], s44 offen offset:1024 sc1\n"
      "buffer_load_dwordx4 v[56:59], v7, s[20:23], s44 offen offset:0 sc1\n"
      "buffer_load_dwordx4 v[60:63], v5, s[20:23], s45 offen offset:0 sc1\n"
      "s_waitcnt vmcnt(6)\n"
      "buffer_load_dwordx4 v[64:67], v5, s[20:23], s45 offen offset:1024 sc1\n"
      "s_setprio 1\n"
      "s_waitcnt vmcnt(6)\n"
      "v_mfma_f32_16x16x32_bf16 v[16:19], v[116:119], v[32:35], 0\n"
      "v_mfma_f32_16x16x32_bf16 v[16:19], v[136:139], v[36:39], v[16:19]\n"
      "buffer_load_dwordx4 v[68:71], v5, s[20:23], s45 offen offset:2048 sc1\n"
      "v_mfma_f32_16x16x32_bf16 v[16:19], v[156:159], v[40:43], v[16:19]\n"
      "s_waitcnt vmcnt(3)\n"
      "v_mfma_f32_16x16x32_bf16 v[16:19], v[176:179], v[44:47], v[16:19]\n"
      "buffer_load_dwordx4 v[72:75], v5, s[20:23], s45 offen offset:3072 sc1\n"
      "ds_read_b32 v4, v2 offset:4160\n"
      "v_mfma_f32_16x16x32_bf16 v[16:19], v[196:199], v[48:51], v[16:19]\n"
      "v_mfma_f32_16x16x32_bf16 v[16:19], v[216:219], v[52:55], v[16:19]\n"
      "buffer_load_dwordx4 v[76:79], v6, s[20:23], s45 offen offset:0 sc1\n"
      "v_mfma_f32_16x16x32_bf16 v[16:19], v[236:239], v[56:59], v[16:19]\n"
      "v_mfma_f32_16x16x32_bf16 v[20:23], v[120:123], v[32:35], 0\n"
      "buffer_load_dwordx4 v[80:83], v6, s[20:23], s45 offen offset:1024 sc1\n"
      "v_mfma_f32_16x16x32_bf16 v[20:23], v[140:143], v[36:39], v[20:23]\n"
      "s_waitcnt lgkmcnt(0)\n"
      "v_readfirstlane_b32 s2, v4\n"
      "s_cmp_lt_u32 s2, 4\n"
      "s_cbranch_scc1 .Lw_22\n"
      ".Lw_23:\n"
      "v_mfma_f32_16x16x32_bf16 v[20:23], v[160:163], v[40:43], v[20:23]\n"
      "buffer_load_dwordx4 v[84:87], v7, s[20:23], s45 offen offset:0 sc1\n"
      "v_mfma_f32_16x16x32_bf16 v[20:23], v[180:183], v[44:47], v[20:23]\n"
      "v_mfma_f32_16x16x32_bf16 v[20:23], v[200:203], v[48:51], v[20:23]\n"
      "buffer_load_dwordx4 v[88:91], v5, s[20:23], s46 offen offset:0 sc1\n"
      "ds_write_b128 v8, v[16:19] offset:0\n"
      "v_mfma_f32_16x16x32_bf16 v[20:23], v[220:223], v[52:55], v[20:23]\n"
      "v_mfma_f32_16x16x32_bf16 v[20:23], v[240:243], v[56:59], v[20:23]\n"
      "buffer_load_dwordx4 v[92:95], v5, s[20:23], s46 offen offset:1024 sc1\n"
      "v_mfma_f32_16x16x32_bf16 v[24:27], v[124:127], v[32:35], 0\n"
      "v_mfma_f32_16x16x32_bf16 v[24:27], v[144:147], v[36:39], v[24:27]\n"
      "buffer_load_dwordx4 v[96:99], v5, s[20:23], s46 offen offset:2048 sc1\n"
      "ds_add_u32 v2, v1 offset:4192\n"
      "v_mfma_f32_16x16x32_bf16 v[24:27], v[164:167], v[40:43], v[24:27]\n"
      "v_mfma_f32_16x16x32_bf16 v[24:27], v[184:187], v[44:47], v[24:27]\n"
      "buffer_load_dwordx4 v[100:103], v5, s[20:23], s46 offen offset:3072 sc1\n"
      "v_mfma_f32_16x16x32_bf16 v[24:27], v[204:207], v[48:51], v[24:27]\n"
      "ds_write_b128 v8, v[20:23] offset:1024\n"
      "v_mfma_f32_16x16x32_bf16 v[24:27], v[224:227], v[52:55], v[24:27]\n"
      "buffer_load_dwordx4 v[104:107], v6, s[20:23], s46 offen offset:0 sc1\n"
      "v_mfma_f32_16x16x32_bf16 v[24:27], v[244:247], v[56:59], v[24:27]\n"
      "v_mfma_f32_16x16x32_bf16 v[28:31], v[128:131], v[32:35], 0\n"
      "buffer_load_dwordx4 v[108:111], v6, s[20:23], s46 offen offset:1024 sc1\n"
      "v_mfma_f32_16x16x32_bf16 v[28:31], v[148:151], v[36:39], v[28:31]\n"
      "ds_add_u32 v2, v1 offset:4192\n"
      "v_mfma_f32_16x16x32_bf16 v[28:31], v[168:171], v[40:43], v[28:31]\n"
      "buffer_load_dwordx4 v[112:115], v7, s[20:23], s46 offen offset:0 sc1\n"
      "v_mfma_f32_16x16x32_bf16 v[28:31], v[188:191], v[44:47], v[28:31]\n"
      "v_mfma_f32_16x16x32_bf16 v[28:31], v[208:211], v[48:51], v[28:31]\n"
      "ds_write_b128 v8, v[24:27] offset:2048\n"
      "v_mfma_f32_16x16x32_bf16 v[28:31], v[228:231], v[52:55], v[28:31]\n"
      "v_mfma_f32_16x16x32_bf16 v[28:31], v[248:251], v[56:59], v[28:31]\n"
      "buffer_load_dwordx2 v[10:11], v13, s[20:23], s74 offen sc1\n"
      "v_mfma_f32_16x16x32_bf16 v[16:19], v[132:135], v[32:35], 0\n"
      "buffer_load_dwordx4 v[32:35], v5, s[20:23], s47 offen offset:0 sc1\n"
      "ds_read_b32 v4, v15\n"
      "ds_read_b128 v[20:23], v9 offset:0\n"
      "v_mfma_f32_16x16x32_bf16 v[16:19], v[152:155], v[36:39], v[16:19]\n"
      "ds_add_u32 v2, v1 offset:4192\n"
      "v_mfma_f32_16x16x32_bf16 v[16:19], v[172:175], v[40:43], v[16:19]\n"
      "v_mfma_f32_16x16x32_bf16 v[16:19], v[192:195], v[44:47], v[16:19]\n"
      "s_waitcnt lgkmcnt(2)\n"
      "v_cmp_gt_u32 vcc, 1, v4\n"
      "v_mfma_f32_16x16x32_bf16 v[16:19], v[212:215], v[48:51], v[16:19]\n"
      "buffer_load_dwordx4 v[36:39], v5, s[20:23], s47 offen offset:1024 sc1\n"
      "ds_write_b128 v8, v[28:31] offset:3072\n"
      "v_mfma_f32_16x16x32_bf16 v[16:19], v[232:235], v[52:55], v[16:19]\n"
      "s_cbranch_vccnz .Lp_25\n"
      ".Lp_26:\n"
      "v_mfma_f32_16x16x32_bf16 v[16:19], v[252:255], v[56:59], v[16:19]\n"
      "s_waitcnt vmcnt(14)\n"
      "s_waitcnt lgkmcnt(2)\n"
      "v_mfma_f32_16x16x32_bf16 v[20:23], v[116:119], v[60:63], v[20:23]\n"
      "ds_read_b32 v4, v15\n"
      "ds_read_b128 v[24:27], v9 offset:1024\n"
      "v_mfma_f32_16x16x32_bf16 v[20:23], v[136:139], v[64:67], v[20:23]\n"
      "buffer_load_dwordx4 v[40:43], v5, s[20:23], s47 offen offset:2048 sc1\n"
      "ds_add_u32 v2, v1 offset:4192\n"
      "v_mfma_f32_16x16x32_bf16 v[20:23], v[156:159], v[68:71], v[20:23]\n"
      "s_waitcnt vmcnt(11)\n"
      "v_mfma_f32_16x16x32_bf16 v[20:23], v[176:179], v[72:75], v[20:23]\n"
      "s_waitcnt lgkmcnt(2)\n"
      "v_cmp_gt_u32 vcc, 2, v4\n"
      "v_mfma_f32_16x16x32_bf16 v[20:23], v[196:199], v[76:79], v[20:23]\n"
      "ds_write_b128 v8, v[16:19] offset:4096\n"
      "v_mfma_f32_16x16x32_bf16 v[20:23], v[216:219], v[80:83], v[20:23]\n"
      "buffer_load_dwordx4 v[44:47], v5, s[20:23], s47 offen offset:3072 sc1\n"
      "s_cbranch_vccnz .Lp_27\n"
      ".Lp_28:\n"
      "v_mfma_f32_16x16x32_bf16 v[20:23], v[236:239], v[84:87], v[20:23]\n"
      "s_waitcnt lgkmcnt(2)\n"
      "v_mfma_f32_16x16x32_bf16 v[24:27], v[120:123], v[60:63], v[24:27]\n"
      "ds_read_b32 v4, v15\n"
      "ds_read_b128 v[28:31], v9 offset:2048\n"
      "v_mfma_f32_16x16x32_bf16 v[24:27], v[140:143], v[64:67], v[24:27]\n"
      "ds_add_u32 v2, v1 offset:4192\n"
      "v_mfma_f32_16x16x32_bf16 v[24:27], v[160:163], v[68:71], v[24:27]\n"
      "buffer_load_dwordx4 v[48:51], v6, s[20:23], s47 offen offset:0 sc1\n"
      "v_mfma_f32_16x16x32_bf16 v[24:27], v[180:183], v[72:75], v[24:27]\n"
      "s_waitcnt lgkmcnt(2)\n"
      "v_cmp_gt_u32 vcc, 3, v4\n"
      "v_mfma_f32_16x16x32_bf16 v[24:27], v[200:203], v[76:79], v[24:27]\n"
      "ds_write_b128 v8, v[20:23] offset:20480\n"
      "v_mfma_f32_16x16x32_bf16 v[24:27], v[220:223], v[80:83], v[24:27]\n"
      "s_cbranch_vccnz .Lp_29\n"
      ".Lp_30:\n"
      "v_mfma_f32_16x16x32_bf16 v[24:27], v[240:243], v[84:87], v[24:27]\n"
      "buffer_load_dwordx4 v[52:55], v6, s[20:23], s47 offen offset:1024 sc1\n"
      "s_waitcnt lgkmcnt(2)\n"
      "v_mfma_f32_16x16x32_bf16 v[28:31], v[124:127], v[60:63], v[28:31]\n"
      "ds_read_b32 v4, v15\n"
      "ds_read_b128 v[16:19], v9 offset:3072\n"
      "v_mfma_f32_16x16x32_bf16 v[28:31], v[144:147], v[64:67], v[28:31]\n"
      "ds_add_u32 v2, v1 offset:4192\n"
      "v_mfma_f32_16x16x32_bf16 v[28:31], v[164:167], v[68:71], v[28:31]\n"
      "v_mfma_f32_16x16x32_bf16 v[28:31], v[184:187], v[72:75], v[28:31]\n"
      "buffer_load_dwordx4 v[56:59], v7, s[20:23], s47 offen offset:0 sc1\n"
      "s_waitcnt lgkmcnt(2)\n"
      "v_cmp_gt_u32 vcc, 4, v4\n"
      "v_mfma_f32_16x16x32_bf16 v[28:31], v[204:207], v[76:79], v[28:31]\n"
      "ds_write_b128 v8, v[24:27] offset:21504\n"
      "v_mfma_f32_16x16x32_bf16 v[28:31], v[224:227], v[80:83], v[28:31]\n"
      "s_cbranch_vccnz .Lp_31\n"
      ".Lp_32:\n"
      "v_mfma_f32_16x16x32_bf16 v[28:31], v[244:247], v[84:87], v[28:31]\n"
      "s_waitcnt lgkmcnt(2)\n"
      "v_mfma_f32_16x16x32_bf16 v[16:19], v[128:131], v[60:63], v[16:19]\n"
      "ds_read_b32 v4, v15\n"
      "ds_read_b128 v[20:23], v9 offset:4096\n"
      "v_mfma_f32_16x16x32_bf16 v[16:19], v[148:151], v[64:67], v[16:19]\n"
      "ds_add_u32 v2, v1 offset:4192\n"
      "v_mfma_f32_16x16x32_bf16 v[16:19], v[168:171], v[68:71], v[16:19]\n"
      "v_mfma_f32_16x16x32_bf16 v[16:19], v[188:191], v[72:75], v[16:19]\n"
      "s_waitcnt lgkmcnt(2)\n"
      "v_cmp_gt_u32 vcc, 5, v4\n"
      "v_mfma_f32_16x16x32_bf16 v[16:19], v[208:211], v[76:79], v[16:19]\n"
      "ds_write_b128 v8, v[28:31] offset:22528\n"
      "v_mfma_f32_16x16x32_bf16 v[16:19], v[228:231], v[80:83], v[16:19]\n"
      "s_cbranch_vccnz .Lp_33\n"
      ".Lp_34:\n"
      "v_mfma_f32_16x16x32_bf16 v[16:19], v[248:251], v[84:87], v[16:19]\n"
      "s_waitcnt lgkmcnt(2)\n"
      "v_mfma_f32_16x16x32_bf16 v[20:23], v[132:135], v[60:63], v[20:23]\n"
      "ds_read_b32 v4, v15\n"
      "ds_read_b128 v[24:27], v9 offset:20480\n"
      "v_mfma_f32_16x16x32_bf16 v[20:23], v[152:155], v[64:67], v[20:23]\n"
      "ds_add_u32 v2, v1 offset:4192\n"
      "v_mfma_f32_16x16x32_bf16 v[20:23], v[172:175], v[68:71], v[20:23]\n"
      "v_mfma_f32_16x16x32_bf16 v[20:23], v[192:195], v[72:75], v[20:23]\n"
      "s_waitcnt lgkmcnt(2)\n"
      "v_cmp_gt_u32 vcc, 6, v4\n"
      "v_mfma_f32_16x16x32_bf16 v[20:23], v[212:215], v[76:79], v[20:23]\n"
      "ds_write_b128 v8, v[16:19] offset:23552\n"
      "v_mfma_f32_16x16x32_bf16 v[20:23], v[232:235], v[80:83], v[20:23]\n"
      "s_cbranch_vccnz .Lp_35\n"
      ".Lp_36:\n"
      "v_mfma_f32_16x16x32_bf16 v[20:23], v[252:255], v[84:87], v[20:23]\n"
      "s_waitcnt vmcnt(12)\n"
      "s_waitcnt lgkmcnt(2)\n"
      "v_mfma_f32_16x16x32_bf16 v[24:27], v[116:119], v[88:91], v[24:27]\n"
      "ds_read_b32 v4, v15\n"
      "ds_read_b128 v[28:31], v9 offset:21504\n"
      "v_mfma_f32_16x16x32_bf16 v[24:27], v[136:139], v[92:95], v[24:27]\n"
      "ds_add_u32 v2, v1 offset:4192\n"
      "v_mfma_f32_16x16x32_bf16 v[24:27], v[156:159], v[96:99], v[24:27]\n"
      "s_waitcnt vmcnt(8)\n"
      "v_mfma_f32_16x16x32_bf16 v[24:27], v[176:179], v[100:103], v[24:27]\n"
      "s_waitcnt lgkmcnt(2)\n"
      "v_cmp_gt_u32 vcc, 7, v4\n"
      "v_mfma_f32_16x16x32_bf16 v[24:27], v[196:199], v[104:107], v[24:27]\n"
      "ds_write_b128 v8, v[20:23] offset:24576\n"
      "v_mfma_f32_16x16x32_bf16 v[24:27], v[216:219], v[108:111], v[24:27]\n"
      "s_cbranch_vccnz .Lp_37\n"
      ".Lp_38:\n"
      "v_mfma_f32_16x16x32_bf16 v[24:27], v[236:239], v[112:115], v[24:27]\n"
      "s_waitcnt lgkmcnt(2)\n"
      "v_mfma_f32_16x16x32_bf16 v[28:31], v[120:123], v[88:91], v[28:31]\n"
      "s_waitcnt vmcnt(7)\n"
      "v_cmp_ne_u64 vcc, s[58:59], v[10:11]\n"
      "s_cbranch_vccnz .Lf_39\n"
      ".Lf_40:\n"
      "buffer_load_dwordx4 v[60:63], v5, s[20:23], s48 offen offset:0 sc1\n"
      "ds_read_b32 v4, v15\n"
      "ds_read_b128 v[16:19], v9 offset:22528\n"
      "v_mfma_f32_16x16x32_bf16 v[28:31], v[140:143], v[92:95], v[28:31]\n"
      "ds_add_u32 v2, v1 offset:4192\n"
      "v_mfma_f32_16x16x32_bf16 v[28:31], v[160:163], v[96:99], v[28:31]\n"
      "buffer_load_dwordx4 v[64:67], v5, s[20:23], s48 offen offset:1024 sc1\n"
      "v_mfma_f32_16x16x32_bf16 v[28:31], v[180:183], v[100:103], v[28:31]\n"
      "s_waitcnt lgkmcnt(2)\n"
      "v_cmp_gt_u32 vcc, 8, v4\n"
      "v_mfma_f32_16x16x32_bf16 v[28:31], v[200:203], v[104:107], v[28:31]\n"
      "buffer_load_dwordx4 v[68:71], v5, s[20:23], s48 offen offset:2048 sc1\n"
      "ds_write_b128 v8, v[24:27] offset:40960\n"
      "v_mfma_f32_16x16x32_bf16 v[28:31], v[220:223], v[108:111], v[28:31]\n"
      "s_cbranch_vccnz .Lp_42\n"
      ".Lp_43:\n"
      "v_mfma_f32_16x16x32_bf16 v[28:31], v[240:243], v[112:115], v[28:31]\n"
      "buffer_load_dwordx4 v[72:75], v5, s[20:23], s48 offen offset:3072 sc1\n"
      "s_waitcnt lgkmcnt(2)\n"
      "v_mfma_f32_16x16x32_bf16 v[16:19], v[124:127], v[88:91], v[16:19]\n"
      "ds_read_b32 v4, v15\n"
      "ds_read_b128 v[20:23], v9 offset:23552\n"
      "v_mfma_f32_16x16x32_bf16 v[16:19], v[144:147], v[92:95], v[16:19]\n"
      "buffer_load_dwordx4 v[76:79], v6, s[20:23], s48 offen offset:0 sc1\n"
      "ds_add_u32 v2, v1 offset:4192\n"
      "v_mfma_f32_16x16x32_bf16 v[16:19], v[164:167], v[96:99], v[16:19]\n"
      "v_mfma_f32_16x16x32_bf16 v[16:19], v[184:187], v[100:103], v[16:19]\n"
      "buffer_load_dwordx4 v[80:83], v6, s[20:23], s48 offen offset:1024 sc1\n"
      "s_waitcnt lgkmcnt(2)\n"
      "v_cmp_gt_u32 vcc, 9, v4\n"
      "v_mfma_f32_16x16x32_bf16 v[16:19], v[204:207], v[104:107], v[16:19]\n"
      "ds_write_b128 v8, v[28:31] offset:41984\n"
      "v_mfma_f32_16x16x32_bf16 v[16:19], v[224:227], v[108:111], v[16:19]\n"
      "buffer_load_dwordx4 v[84:87], v7, s[20:23], s48 offen offset:0 sc1\n"
      "s_cbranch_vccnz .Lp_44\n"
      ".Lp_45:\n"
      "v_mfma_f32_16x16x32_bf16 v[16:19], v[244:247], v[112:115], v[16:19]\n"
      "s_waitcnt lgkmcnt(2)\n"
      "v_mfma_f32_16x16x32_bf16 v[20:23], v[128:131], v[88:91], v[20:23]\n"
      "ds_read_b32 v4, v15\n"
      "ds_read_b128 v[24:27], v9 offset:24576\n"
      "v_mfma_f32_16x16x32_bf16 v[20:23], v[148:151], v[92:95], v[20:23]\n"
      "ds_add_u32 v2, v1 offset:4192\n"
      "v_mfma_f32_16x16x32_bf16 v[20:23], v[168:171], v[96:99], v[20:23]\n"
      "v_mfma_f32_16x16x32_bf16 v[20:23], v[188:191], v[100:103], v[20:23]\n"
      "s_waitcnt lgkmcnt(2)\n"
      "v_cmp_gt_u32 vcc, 10, v4\n"
      "v_mfma_f32_16x16x32_bf16 v[20:23], v[208:211], v[104:107], v[20:23]\n"
      "ds_write_b128 v8, v[16:19] offset:43008\n"
      "v_mfma_f32_16x16x32_bf16 v[20:23], v[228:231], v[108:111], v[20:23]\n"
      "s_cbranch_vccnz .Lp_46\n"
      ".Lp_47:\n"
      "v_mfma_f32_16x16x32_bf16 v[20:23], v[248:251], v[112:115], v[20:23]\n"
      "s_waitcnt lgkmcnt(2)\n"
      "v_mfma_f32_16x16x32_bf16 v[24:27], v[132:135], v[88:91], v[24:27]\n"
      "buffer_load_dwordx4 v[88:91], v5, s[20:23], s49 offen offset:0 sc1\n"
      "ds_read_b32 v4, v15\n"
      "ds_read_b128 v[28:31], v9 offset:40960\n"
      "v_mfma_f32_16x16x32_bf16 v[24:27], v[152:155], v[92:95], v[24:27]\n"
      "ds_add_u32 v2, v1 offset:4192\n"
      "v_mfma_f32_16x16x32_bf16 v[24:27], v[172:175], v[96:99], v[24:27]\n"
      "v_mfma_f32_16x16x32_bf16 v[24:27], v[192:195], v[100:103], v[24:27]\n"
      "s_waitcnt lgkmcnt(2)\n"
      "v_cmp_gt_u32 vcc, 11, v4\n"
      "v_mfma_f32_16x16x32_bf16 v[24:27], v[212:215], v[104:107], v[24:27]\n"
      "buffer_load_dwordx4 v[92:95], v5, s[20:23], s49 offen offset:1024 sc1\n"
      "ds_write_b128 v8, v[20:23] offset:44032\n"
      "v_mfma_f32_16x16x32_bf16 v[24:27], v[232:235], v[108:111], v[24:27]\n"
      "s_cbranch_vccnz .Lp_48\n"
      ".Lp_49:\n"
      "v_mfma_f32_16x16x32_bf16 v[24:27], v[252:255], v[112:115], v[24:27]\n"
      "s_waitcnt vmcnt(13)\n"
      "s_waitcnt lgkmcnt(2)\n"
      "v_mfma_f32_16x16x32_bf16 v[28:31], v[116:119], v[32:35], v[28:31]\n"
      "ds_read_b32 v4, v15\n"
      "ds_read_b128 v[16:19], v9 offset:41984\n"
      "v_mfma_f32_16x16x32_bf16 v[28:31], v[136:139], v[36:39], v[28:31]\n"
      "buffer_load_dwordx4 v[96:99], v5, s[20:23], s49 offen offset:2048 sc1\n"
      "ds_add_u32 v2, v1 offset:4192\n"
      "v_mfma_f32_16x16x32_bf16 v[28:31], v[156:159], v[40:43], v[28:31]\n"
      "s_waitcnt vmcnt(10)\n"
      "v_mfma_f32_16x16x32_bf16 v[28:31], v[176:179], v[44:47], v[28:31]\n"
      "s_waitcnt lgkmcnt(2)\n"
      "v_cmp_gt_u32 vcc, 12, v4\n"
      "v_mfma_f32_16x16x32_bf16 v[28:31], v[196:199], v[48:51], v[28:31]\n"
      "ds_write_b128 v8, v[24:27] offset:45056\n"
      "v_mfma_f32_16x16x32_bf16 v[28:31], v[216:219], v[52:55], v[28:31]\n"
      "buffer_load_dwordx4 v[100:103], v5, s[20:23], s49 offen offset:3072 sc1\n"
      "s_cbranch_vccnz .Lp_50\n"
      ".Lp_51:\n"
      "v_mfma_f32_16x16x32_bf16 v[28:31], v[236:239], v[56:59], v[28:31]\n"
      "s_waitcnt lgkmcnt(2)\n"
      "v_mfma_f32_16x16x32_bf16 v[16:19], v[120:123], v[32:35], v[16:19]\n"
      "ds_read_b32 v4, v15\n"
      "ds_read_b128 v[20:23], v9 offset:43008\n"
      "v_mfma_f32_16x16x32_bf16 v[16:19], v[140:143], v[36:39], v[16:19]\n"
      "ds_add_u32 v2, v1 offset:4192\n"
      "v_mfma_f32_16x16x32_bf16 v[16:19], v[160:163], v[40:43], v[16:19]\n"
      "buffer_load_dwordx4 v[104:107], v6, s[20:23], s49 offen offset:0 sc1\n"
      "v_mfma_f32_16x16x32_bf16 v[16:19], v[180:183], v[44:47], v[16:19]\n"
      "s_waitcnt lgkmcnt(2)\n"
      "v_cmp_gt_u32 vcc, 13, v4\n"
      "v_mfma_f32_16x16x32_bf16 v[16:19], v[200:203], v[48:51], v[16:19]\n"
      "v_cvt_pk_bf16_f32 v10, v28, v29\n"
      "v_cvt_pk_bf16_f32 v11, v30, v31\n"
      "v_mfma_f32_16x16x32_bf16 v[16:19], v[220:223], v[52:55], v[16:19]\n"
      "s_cbranch_vccnz .Lp_52\n"
      ".Lp_53:\n"
      "v_mfma_f32_16x16x32_bf16 v[16:19], v[240:243], v[56:59], v[16:19]\n"
      "buffer_load_dwordx4 v[108:111], v6, s[20:23], s49 offen offset:1024 sc1\n"
      "s_waitcnt lgkmcnt(1)\n"
      "v_mfma_f32_16x16x32_bf16 v[20:23], v[124:127], v[32:35], v[20:23]\n"
      "ds_read_b32 v4, v15\n"
      "ds_read_b128 v[24:27], v9 offset:44032\n"
      "v_mfma_f32_16x16x32_bf16 v[20:23], v[144:147], v[36:39], v[20:23]\n"
      "v_mfma_f32_16x16x32_bf16 v[20:23], v[164:167], v[40:43], v[20:23]\n"
      "v_mfma_f32_16x16x32_bf16 v[20:23], v[184:187], v[44:47], v[20:23]\n"
      "buffer_load_dwordx4 v[112:115], v7, s[20:23], s49 offen offset:0 sc1\n"
      "s_waitcnt lgkmcnt(1)\n"
      "v_cmp_gt_u32 vcc, 14, v4\n"
      "v_mfma_f32_16x16x32_bf16 v[20:23], v[204:207], v[48:51], v[20:23]\n"
      "v_cvt_pk_bf16_f32 v12, v16, v17\n"
      "v_cvt_pk_bf16_f32 v13, v18, s75\n"
      "v_mfma_f32_16x16x32_bf16 v[20:23], v[224:227], v[52:55], v[20:23]\n"
      "s_cbranch_vccnz .Lp_54\n"
      ".Lp_55:\n"
      "v_mfma_f32_16x16x32_bf16 v[20:23], v[244:247], v[56:59], v[20:23]\n"
      "buffer_store_dwordx4 v[10:13], v5, s[20:23], s28 offen offset:0 sc1\n"
      "s_waitcnt lgkmcnt(0)\n"
      "v_mfma_f32_16x16x32_bf16 v[24:27], v[128:131], v[32:35], v[24:27]\n"
      "ds_read_b32 v4, v15\n"
      "ds_read_b128 v[28:31], v9 offset:45056\n"
      "v_mfma_f32_16x16x32_bf16 v[24:27], v[148:151], v[36:39], v[24:27]\n"
      "v_mfma_f32_16x16x32_bf16 v[24:27], v[168:171], v[40:43], v[24:27]\n"
      "v_mfma_f32_16x16x32_bf16 v[24:27], v[188:191], v[44:47], v[24:27]\n"
      "s_waitcnt lgkmcnt(1)\n"
      "v_cmp_gt_u32 vcc, 15, v4\n"
      "v_mfma_f32_16x16x32_bf16 v[24:27], v[208:211], v[48:51], v[24:27]\n"
      "v_cvt_pk_bf16_f32 v10, v19, v20\n"
      "v_cvt_pk_bf16_f32 v11, v21, v22\n"
      "v_mfma_f32_16x16x32_bf16 v[24:27], v[228:231], v[52:55], v[24:27]\n"
      "s_cbranch_vccnz .Lp_56\n"
      ".Lp_57:\n"
      "v_mfma_f32_16x16x32_bf16 v[24:27], v[248:251], v[56:59], v[24:27]\n"
      "s_waitcnt lgkmcnt(0)\n"
      "v_mfma_f32_16x16x32_bf16 v[28:31], v[132:135], v[32:35], v[28:31]\n"
      "buffer_load_dwordx4 v[32:35], v5, s[20:23], s50 offen offset:0 sc1\n"
      "ds_read_b128 v[16:19], v14 offset:0\n"
      "v_mfma_f32_16x16x32_bf16 v[28:31], v[152:155], v[36:39], v[28:31]\n"
      "v_mfma_f32_16x16x32_bf16 v[28:31], v[172:175], v[40:43], v[28:31]\n"
      "ds_read_b32 v4, v2 offset:4136\n"
      "v_mfma_f32_16x16x32_bf16 v[28:31], v[192:195], v[44:47], v[28:31]\n"
      "v_mfma_f32_16x16x32_bf16 v[28:31], v[212:215], v[48:51], v[28:31]\n"
      "buffer_load_dwordx4 v[36:39], v5, s[20:23], s50 offen offset:1024 sc1\n"
      "v_cvt_pk_bf16_f32 v12, v23, v24\n"
      "v_cvt_pk_bf16_f32 v13, v25, s75\n"
      "v_mfma_f32_16x16x32_bf16 v[28:31], v[232:235], v[52:55], v[28:31]\n"
      "v_mfma_f32_16x16x32_bf16 v[28:31], v[252:255], v[56:59], v[28:31]\n"
      "buffer_store_dwordx4 v[10:13], v5, s[20:23], s28 offen offset:1024 sc1\n"
      "s_waitcnt vmcnt(15)\n"
      "s_waitcnt lgkmcnt(1)\n"
      "v_mfma_f32_16x16x32_bf16 v[16:19], v[116:119], v[60:63], v[16:19]\n"
      "ds_read_b128 v[20:23], v14 offset:64\n"
      "v_mfma_f32_16x16x32_bf16 v[16:19], v[136:139], v[64:67], v[16:19]\n"
      "buffer_load_dwordx4 v[40:43], v5, s[20:23], s50 offen offset:2048 sc1\n"
      "v_mfma_f32_16x16x32_bf16 v[16:19], v[156:159], v[68:71], v[16:19]\n"
      "s_waitcnt vmcnt(12)\n"
      "v_mfma_f32_16x16x32_bf16 v[16:19], v[176:179], v[72:75], v[16:19]\n"
      "s_waitcnt lgkmcnt(1)\n"
      "v_readfirstlane_b32 s2, v4\n"
      "s_cmp_lt_u32 s2, 4\n"
      "s_cbranch_scc1 .Lw_58\n"
      ".Lw_59:\n"
      "v_mfma_f32_16x16x32_bf16 v[16:19], v[196:199], v[76:79], v[16:19]\n"
      "v_cvt_pk_bf16_f32 v10, v26, v27\n"
      "v_cvt_pk_bf16_f32 v11, v28, v29\n"
      "v_mfma_f32_16x16x32_bf16 v[16:19], v[216:219], v[80:83], v[16:19]\n"
      "buffer_load_dwordx4 v[44:47], v5, s[20:23], s50 offen offset:3072 sc1\n"
      "v_cvt_pk_bf16_f32 v12, v30, v31\n"
      "v_cvt_pk_bf16_f32 v13, v31, s75\n"
      "v_mfma_f32_16x16x32_bf16 v[16:19], v[236:239], v[84:87], v[16:19]\n"
      "buffer_store_dwordx4 v[10:13], v5, s[20:23], s28 offen offset:2048 sc1\n"
      "s_waitcnt lgkmcnt(0)\n"
      "v_mfma_f32_16x16x32_bf16 v[20:23], v[120:123], v[60:63], v[20:23]\n"
      "ds_read_b128 v[24:27], v14 offset:128\n"
      "v_mfma_f32_16x16x32_bf16 v[20:23], v[140:143], v[64:67], v[20:23]\n"
      "v_mfma_f32_16x16x32_bf16 v[20:23], v[160:163], v[68:71], v[20:23]\n"
      "buffer_load_dwordx4 v[48:51], v6, s[20:23], s50 offen offset:0 sc1\n"
      "v_mfma_f32_16x16x32_bf16 v[20:23], v[180:183], v[72:75], v[20:23]\n"
      "v_mfma_f32_16x16x32_bf16 v[20:23], v[200:203], v[76:79], v[20:23]\n"
      "v_add_u32 v8, 4294934528, v8\n"
      "ds_write_b128 v8, v[16:19] offset:0\n"
      "v_mfma_f32_16x16x32_bf16 v[20:23], v[220:223], v[80:83], v[20:23]\n"
      "v_mfma_f32_16x16x32_bf16 v[20:23], v[240:243], v[84:87], v[20:23]\n"
      "buffer_load_dwordx4 v[52:55], v6, s[20:23], s50 offen offset:1024 sc1\n"
      "s_waitcnt lgkmcnt(1)\n"
      "v_mfma_f32_16x16x32_bf16 v[24:27], v[124:127], v[60:63], v[24:27]\n"
      "ds_read_b128 v[28:31], v14 offset:192\n"
      "v_mfma_f32_16x16x32_bf16 v[24:27], v[144:147], v[64:67], v[24:27]\n"
      "ds_add_u32 v2, v1 offset:4192\n"
      "v_mfma_f32_16x16x32_bf16 v[24:27], v[164:167], v[68:71], v[24:27]\n"
      "v_mfma_f32_16x16x32_bf16 v[24:27], v[184:187], v[72:75], v[24:27]\n"
      "buffer_load_dwordx4 v[56:59], v7, s[20:23], s50 offen offset:0 sc1\n"
      "v_mfma_f32_16x16x32_bf16 v[24:27], v[204:207], v[76:79], v[24:27]\n"
      "ds_write_b128 v8, v[20:23] offset:1024\n"
      "v_mfma_f32_16x16x32_bf16 v[24:27], v[224:227], v[80:83], v[24:27]\n"
      "v_mfma_f32_16x16x32_bf16 v[24:27], v[244:247], v[84:87], v[24:27]\n"
      "s_waitcnt lgkmcnt(2)\n"
      "v_mfma_f32_16x16x32_bf16 v[28:31], v[128:131], v[60:63], v[28:31]\n"
      "ds_read_b128 v[16:19], v14 offset:256\n"
      "v_mfma_f32_16x16x32_bf16 v[28:31], v[148:151], v[64:67], v[28:31]\n"
      "ds_add_u32 v2, v1 offset:4192\n"
      "v_mfma_f32_16x16x32_bf16 v[28:31], v[168:171], v[68:71], v[28:31]\n"
      "v_mfma_f32_16x16x32_bf16 v[28:31], v[188:191], v[72:75], v[28:31]\n"
      "v_mfma_f32_16x16x32_bf16 v[28:31], v[208:211], v[76:79], v[28:31]\n"
      "ds_write_b128 v8, v[24:27] offset:2048\n"
      "v_mfma_f32_16x16x32_bf16 v[28:31], v[228:231], v[80:83], v[28:31]\n"
      "v_mfma_f32_16x16x32_bf16 v[28:31], v[248:251], v[84:87], v[28:31]\n"
      "s_waitcnt lgkmcnt(2)\n"
      "v_mfma_f32_16x16x32_bf16 v[16:19], v[132:135], v[60:63], v[16:19]\n"
      "buffer_load_dwordx4 v[60:63], v5, s[20:23], s51 offen offset:0 sc1\n"
      "v_add_u32 v9, 4294934528, v9\n"
      "ds_read_b32 v4, v15\n"
      "ds_read_b128 v[20:23], v9 offset:0\n"
      "v_mfma_f32_16x16x32_bf16 v[16:19], v[152:155], v[64:67], v[16:19]\n"
      "ds_add_u32 v2, v1 offset:4192\n"
      "v_mfma_f32_16x16x32_bf16 v[16:19], v[172:175], v[68:71], v[16:19]\n"
      "v_mfma_f32_16x16x32_bf16 v[16:19], v[192:195], v[72:75], v[16:19]\n"
      "s_waitcnt lgkmcnt(2)\n"
      "v_cmp_gt_u32 vcc, 16, v4\n"
      "v_mfma_f32_16x16x32_bf16 v[16:19], v[212:215], v[76:79], v[16:19]\n"
      "buffer_load_dwordx4 v[64:67], v5, s[20:23], s51 offen offset:1024 sc1\n"
      "ds_write_b128 v8, v[28:31] offset:3072\n"
      "v_mfma_f32_16x16x32_bf16 v[16:19], v[232:235], v[80:83], v[16:19]\n"
      "s_cbranch_vccnz .Lp_61\n"
      ".Lp_62:\n"
      "v_mfma_f32_16x16x32_bf16 v[16:19], v[252:255], v[84:87], v[16:19]\n"
      "s_waitcnt vmcnt(16)\n"
      "s_waitcnt lgkmcnt(2)\n"
      "v_mfma_f32_16x16x32_bf16 v[20:23], v[116:119], v[88:91], v[20:23]\n"
      "ds_read_b32 v4, v15\n"
      "ds_read_b128 v[24:27], v9 offset:1024\n"
      "v_mfma_f32_16x16x32_bf16 v[20:23], v[136:139], v[92:95], v[20:23]\n"
      "buffer_load_dwordx4 v[68:71], v5, s[20:23], s51 offen offset:2048 sc1\n"
      "ds_add_u32 v2, v1 offset:4192\n"
      "v_mfma_f32_16x16x32_bf16 v[20:23], v[156:159], v[96:99], v[20:23]\n"
      "s_waitcnt vmcnt(13)\n"
      "v_mfma_f32_16x16x32_bf16 v[20:23], v[176:179], v[100:103], v[20:23]\n"
      "s_waitcnt lgkmcnt(2)\n"
      "v_cmp_gt_u32 vcc, 17, v4\n"
      "v_mfma_f32_16x16x32_bf16 v[20:23], v[196:199], v[104:107], v[20:23]\n"
      "ds_write_b128 v8, v[16:19] offset:4096\n"
      "v_mfma_f32_16x16x32_bf16 v[20:23], v[216:219], v[108:111], v[20:23]\n"
      "buffer_load_dwordx4 v[72:75], v5, s[20:23], s51 offen offset:3072 sc1\n"
      "s_cbranch_vccnz .Lp_63\n"
      ".Lp_64:\n"
      "v_mfma_f32_16x16x32_bf16 v[20:23], v[236:239], v[112:115], v[20:23]\n"
      "s_waitcnt lgkmcnt(2)\n"
      "v_mfma_f32_16x16x32_bf16 v[24:27], v[120:123], v[88:91], v[24:27]\n"
      "ds_read_b32 v4, v15\n"
      "ds_read_b128 v[28:31], v9 offset:2048\n"
      "v_mfma_f32_16x16x32_bf16 v[24:27], v[140:143], v[92:95], v[24:27]\n"
      "ds_add_u32 v2, v1 offset:4192\n"
      "v_mfma_f32_16x16x32_bf16 v[24:27], v[160:163], v[96:99], v[24:27]\n"
      "buffer_load_dwordx4 v[76:79], v6, s[20:23], s51 offen offset:0 sc1\n"
      "v_mfma_f32_16x16x32_bf16 v[24:27], v[180:183], v[100:103], v[24:27]\n"
      "s_waitcnt lgkmcnt(2)\n"
      "v_cmp_gt_u32 vcc, 18, v4\n"
      "v_mfma_f32_16x16x32_bf16 v[24:27], v[200:203], v[104:107], v[24:27]\n"
      "v_add_u32 v8, 98304, v8\n"
      "ds_write_b128 v8, v[20:23] offset:0\n"
      "v_mfma_f32_16x16x32_bf16 v[24:27], v[220:223], v[108:111], v[24:27]\n"
      "s_cbranch_vccnz .Lp_65\n"
      ".Lp_66:\n"
      "v_mfma_f32_16x16x32_bf16 v[24:27], v[240:243], v[112:115], v[24:27]\n"
      "buffer_load_dwordx4 v[80:83], v6, s[20:23], s51 offen offset:1024 sc1\n"
      "s_waitcnt lgkmcnt(2)\n"
      "v_mfma_f32_16x16x32_bf16 v[28:31], v[124:127], v[88:91], v[28:31]\n"
      "ds_read_b32 v4, v15\n"
      "ds_read_b128 v[16:19], v9 offset:3072\n"
      "v_mfma_f32_16x16x32_bf16 v[28:31], v[144:147], v[92:95], v[28:31]\n"
      "ds_add_u32 v2, v1 offset:4192\n"
      "v_mfma_f32_16x16x32_bf16 v[28:31], v[164:167], v[96:99], v[28:31]\n"
      "v_mfma_f32_16x16x32_bf16 v[28:31], v[184:187], v[100:103], v[28:31]\n"
      "buffer_load_dwordx4 v[84:87], v7, s[20:23], s51 offen offset:0 sc1\n"
      "s_waitcnt lgkmcnt(2)\n"
      "v_cmp_gt_u32 vcc, 19, v4\n"
      "v_mfma_f32_16x16x32_bf16 v[28:31], v[204:207], v[104:107], v[28:31]\n"
      "ds_write_b128 v8, v[24:27] offset:1024\n"
      "v_mfma_f32_16x16x32_bf16 v[28:31], v[224:227], v[108:111], v[28:31]\n"
      "s_cbranch_vccnz .Lp_67\n"
      ".Lp_68:\n"
      "v_mfma_f32_16x16x32_bf16 v[28:31], v[244:247], v[112:115], v[28:31]\n"
      "s_waitcnt lgkmcnt(2)\n"
      "v_mfma_f32_16x16x32_bf16 v[16:19], v[128:131], v[88:91], v[16:19]\n"
      "ds_read_b32 v4, v15\n"
      "ds_read_b128 v[20:23], v9 offset:4096\n"
      "v_mfma_f32_16x16x32_bf16 v[16:19], v[148:151], v[92:95], v[16:19]\n"
      "ds_add_u32 v2, v1 offset:4192\n"
      "v_mfma_f32_16x16x32_bf16 v[16:19], v[168:171], v[96:99], v[16:19]\n"
      "v_mfma_f32_16x16x32_bf16 v[16:19], v[188:191], v[100:103], v[16:19]\n"
      "s_waitcnt lgkmcnt(2)\n"
      "v_cmp_gt_u32 vcc, 20, v4\n"
      "v_mfma_f32_16x16x32_bf16 v[16:19], v[208:211], v[104:107], v[16:19]\n"
      "ds_write_b128 v8, v[28:31] offset:2048\n"
      "v_mfma_f32_16x16x32_bf16 v[16:19], v[228:231], v[108:111], v[16:19]\n"
      "s_cbranch_vccnz .Lp_69\n"
      ".Lp_70:\n"
      "v_mfma_f32_16x16x32_bf16 v[16:19], v[248:251], v[112:115], v[16:19]\n"
      "s_waitcnt lgkmcnt(2)\n"
      "v_mfma_f32_16x16x32_bf16 v[20:23], v[132:135], v[88:91], v[20:23]\n"
      "v_add_u32 v9, 98304, v9\n"
      "ds_read_b32 v4, v15\n"
      "ds_read_b128 v[24:27], v9 offset:0\n"
      "v_mfma_f32_16x16x32_bf16 v[20:23], v[152:155], v[92:95], v[20:23]\n"
      "ds_add_u32 v2, v1 offset:4192\n"
      "v_mfma_f32_16x16x32_bf16 v[20:23], v[172:175], v[96:99], v[20:23]\n"
      "v_mfma_f32_16x16x32_bf16 v[20:23], v[192:195], v[100:103], v[20:23]\n"
      "s_waitcnt lgkmcnt(2)\n"
      "v_cmp_gt_u32 vcc, 21, v4\n"
      "v_mfma_f32_16x16x32_bf16 v[20:23], v[212:215], v[104:107], v[20:23]\n"
      "ds_write_b128 v8, v[16:19] offset:3072\n"
      "v_mfma_f32_16x16x32_bf16 v[20:23], v[232:235], v[108:111], v[20:23]\n"
      "s_cbranch_vccnz .Lp_71\n"
      ".Lp_72:\n"
      "v_mfma_f32_16x16x32_bf16 v[20:23], v[252:255], v[112:115], v[20:23]\n"
      "s_waitcnt vmcnt(12)\n"
      "s_waitcnt lgkmcnt(2)\n"
      "v_mfma_f32_16x16x32_bf16 v[24:27], v[116:119], v[32:35], v[24:27]\n"
      "ds_read_b32 v4, v15\n"
      "ds_read_b128 v[28:31], v9 offset:1024\n"
      "v_mfma_f32_16x16x32_bf16 v[24:27], v[136:139], v[36:39], v[24:27]\n"
      "ds_add_u32 v2, v1 offset:4192\n"
      "v_mfma_f32_16x16x32_bf16 v[24:27], v[156:159], v[40:43], v[24:27]\n"
      "s_waitcnt vmcnt(7)\n"
      "v_mfma_f32_16x16x32_bf16 v[24:27], v[176:179], v[44:47], v[24:27]\n"
      "s_waitcnt lgkmcnt(2)\n"
      "v_cmp_gt_u32 vcc, 22, v4\n"
      "v_mfma_f32_16x16x32_bf16 v[24:27], v[196:199], v[48:51], v[24:27]\n"
      "ds_write_b128 v8, v[20:23] offset:4096\n"
      "v_mfma_f32_16x16x32_bf16 v[24:27], v[216:219], v[52:55], v[24:27]\n"
      "s_cbranch_vccnz .Lp_73\n"
      ".Lp_74:\n"
      "v_mfma_f32_16x16x32_bf16 v[24:27], v[236:239], v[56:59], v[24:27]\n"
      "s_waitcnt lgkmcnt(2)\n"
      "v_mfma_f32_16x16x32_bf16 v[28:31], v[120:123], v[32:35], v[28:31]\n"
      "ds_read_b32 v4, v15\n"
      "ds_read_b128 v[16:19], v9 offset:2048\n"
      "v_mfma_f32_16x16x32_bf16 v[28:31], v[140:143], v[36:39], v[28:31]\n"
      "ds_add_u32 v2, v1 offset:4192\n"
      "v_mfma_f32_16x16x32_bf16 v[28:31], v[160:163], v[40:43], v[28:31]\n"
      "v_mfma_f32_16x16x32_bf16 v[28:31], v[180:183], v[44:47], v[28:31]\n"
      "s_waitcnt lgkmcnt(2)\n"
      "v_cmp_gt_u32 vcc, 23, v4\n"
      "v_mfma_f32_16x16x32_bf16 v[28:31], v[200:203], v[48:51], v[28:31]\n"
      "v_add_u32 v8, 4294901760, v8\n"
      "ds_write_b128 v8, v[24:27] offset:0\n"
      "v_mfma_f32_16x16x32_bf16 v[28:31], v[220:223], v[52:55], v[28:31]\n"
      "s_cbranch_vccnz .Lp_75\n"
      ".Lp_76:\n"
      "v_mfma_f32_16x16x32_bf16 v[28:31], v[240:243], v[56:59], v[28:31]\n"
      "s_waitcnt lgkmcnt(2)\n"
      "v_mfma_f32_16x16x32_bf16 v[16:19], v[124:127], v[32:35], v[16:19]\n"
      "ds_read_b32 v4, v15\n"
      "ds_read_b128 v[20:23], v9 offset:3072\n"
      "v_mfma_f32_16x16x32_bf16 v[16:19], v[144:147], v[36:39], v[16:19]\n"
      "ds_add_u32 v2, v1 offset:4192\n"
      "v_mfma_f32_16x16x32_bf16 v[16:19], v[164:167], v[40:43], v[16:19]\n"
      "v_mfma_f32_16x16x32_bf16 v[16:19], v[184:187], v[44:47], v[16:19]\n"
      "s_waitcnt lgkmcnt(2)\n"
      "v_cmp_gt_u32 vcc, 24, v4\n"
      "v_mfma_f32_16x16x32_bf16 v[16:19], v[204:207], v[48:51], v[16:19]\n"
      "ds_write_b128 v8, v[28:31] offset:1024\n"
      "v_mfma_f32_16x16x32_bf16 v[16:19], v[224:227], v[52:55], v[16:19]\n"
      "s_cbranch_vccnz .Lp_77\n"
      ".Lp_78:\n"
      "v_mfma_f32_16x16x32_bf16 v[16:19], v[244:247], v[56:59], v[16:19]\n"
      "s_waitcnt lgkmcnt(2)\n"
      "v_mfma_f32_16x16x32_bf16 v[20:23], v[128:131], v[32:35], v[20:23]\n"
      "ds_read_b32 v4, v15\n"
      "ds_read_b128 v[24:27], v9 offset:4096\n"
      "v_mfma_f32_16x16x32_bf16 v[20:23], v[148:151], v[36:39], v[20:23]\n"
      "ds_add_u32 v2, v1 offset:4192\n"
      "v_mfma_f32_16x16x32_bf16 v[20:23], v[168:171], v[40:43], v[20:23]\n"
      "v_mfma_f32_16x16x32_bf16 v[20:23], v[188:191], v[44:47], v[20:23]\n"
      "s_waitcnt lgkmcnt(2)\n"
      "v_cmp_gt_u32 vcc, 25, v4\n"
      "v_mfma_f32_16x16x32_bf16 v[20:23], v[208:211], v[48:51], v[20:23]\n"
      "ds_write_b128 v8, v[16:19] offset:2048\n"
      "v_mfma_f32_16x16x32_bf16 v[20:23], v[228:231], v[52:55], v[20:23]\n"
      "s_cbranch_vccnz .Lp_79\n"
      ".Lp_80:\n"
      "v_mfma_f32_16x16x32_bf16 v[20:23], v[248:251], v[56:59], v[20:23]\n"
      "s_waitcnt lgkmcnt(2)\n"
      "v_mfma_f32_16x16x32_bf16 v[24:27], v[132:135], v[32:35], v[24:27]\n"
      "v_add_u32 v9, 4294901760, v9\n"
      "ds_read_b32 v4, v15\n"
      "ds_read_b128 v[28:31], v9 offset:0\n"
      "v_mfma_f32_16x16x32_bf16 v[24:27], v[152:155], v[36:39], v[24:27]\n"
      "ds_add_u32 v2, v1 offset:4192\n"
      "v_mfma_f32_16x16x32_bf16 v[24:27], v[172:175], v[40:43], v[24:27]\n"
      "v_mfma_f32_16x16x32_bf16 v[24:27], v[192:195], v[44:47], v[24:27]\n"
      "s_waitcnt lgkmcnt(2)\n"
      "v_cmp_gt_u32 vcc, 26, v4\n"
      "v_mfma_f32_16x16x32_bf16 v[24:27], v[212:215], v[48:51], v[24:27]\n"
      "ds_write_b128 v8, v[20:23] offset:3072\n"
      "v_mfma_f32_16x16x32_bf16 v[24:27], v[232:235], v[52:55], v[24:27]\n"
      "s_cbranch_vccnz .Lp_81\n"
      ".Lp_82:\n"
      "v_mfma_f32_16x16x32_bf16 v[24:27], v[252:255], v[56:59], v[24:27]\n"
      "s_waitcnt vmcnt(4)\n"
      "s_waitcnt lgkmcnt(2)\n"
      "v_mfma_f32_16x16x32_bf16 v[28:31], v[116:119], v[60:63], v[28:31]\n"
      "ds_read_b32 v4, v15\n"
      "ds_read_b128 v[16:19], v9 offset:1024\n"
      "v_mfma_f32_16x16x32_bf16 v[28:31], v[136:139], v[64:67], v[28:31]\n"
      "ds_add_u32 v2, v1 offset:4192\n"
      "v_mfma_f32_16x16x32_bf16 v[28:31], v[156:159], v[68:71], v[28:31]\n"
      "s_waitcnt vmcnt(0)\n"
      "v_mfma_f32_16x16x32_bf16 v[28:31], v[176:179], v[72:75], v[28:31]\n"
      "s_waitcnt lgkmcnt(2)\n"
      "v_cmp_gt_u32 vcc, 27, v4\n"
      "v_mfma_f32_16x16x32_bf16 v[28:31], v[196:199], v[76:79], v[28:31]\n"
      "ds_write_b128 v8, v[24:27] offset:4096\n"
      "v_mfma_f32_16x16x32_bf16 v[28:31], v[216:219], v[80:83], v[28:31]\n"
      "s_cbranch_vccnz .Lp_83\n"
      ".Lp_84:\n"
      "v_mfma_f32_16x16x32_bf16 v[28:31], v[236:239], v[84:87], v[28:31]\n"
      "s_waitcnt lgkmcnt(2)\n"
      "v_mfma_f32_16x16x32_bf16 v[16:19], v[120:123], v[60:63], v[16:19]\n"
      "ds_read_b32 v4, v15\n"
      "ds_read_b128 v[20:23], v9 offset:2048\n"
      "v_mfma_f32_16x16x32_bf16 v[16:19], v[140:143], v[64:67], v[16:19]\n"
      "ds_add_u32 v2, v1 offset:4192\n"
      "v_mfma_f32_16x16x32_bf16 v[16:19], v[160:163], v[68:71], v[16:19]\n"
      "v_mfma_f32_16x16x32_bf16 v[16:19], v[180:183], v[72:75], v[16:19]\n"
      "s_waitcnt lgkmcnt(2)\n"
      "v_cmp_gt_u32 vcc, 28, v4\n"
      "v_mfma_f32_16x16x32_bf16 v[16:19], v[200:203], v[76:79], v[16:19]\n"
      "ds_write_b128 v8, v[28:31] offset:20480\n"
      "v_mfma_f32_16x16x32_bf16 v[16:19], v[220:223], v[80:83], v[16:19]\n"
      "s_cbranch_vccnz .Lp_85\n"
      ".Lp_86:\n"
      "v_mfma_f32_16x16x32_bf16 v[16:19], v[240:243], v[84:87], v[16:19]\n"
      "s_waitcnt lgkmcnt(2)\n"
      "v_mfma_f32_16x16x32_bf16 v[20:23], v[124:127], v[60:63], v[20:23]\n"
      "ds_read_b32 v4, v15\n"
      "ds_read_b128 v[24:27], v9 offset:3072\n"
      "v_mfma_f32_16x16x32_bf16 v[20:23], v[144:147], v[64:67], v[20:23]\n"
      "ds_add_u32 v2, v1 offset:4192\n"
      "v_mfma_f32_16x16x32_bf16 v[20:23], v[164:167], v[68:71], v[20:23]\n"
      "v_mfma_f32_16x16x32_bf16 v[20:23], v[184:187], v[72:75], v[20:23]\n"
      "s_waitcnt lgkmcnt(2)\n"
      "v_cmp_gt_u32 vcc, 29, v4\n"
      "v_mfma_f32_16x16x32_bf16 v[20:23], v[204:207], v[76:79], v[20:23]\n"
      "ds_write_b128 v8, v[16:19] offset:21504\n"
      "v_mfma_f32_16x16x32_bf16 v[20:23], v[224:227], v[80:83], v[20:23]\n"
      "s_cbranch_vccnz .Lp_87\n"
      ".Lp_88:\n"
      "v_mfma_f32_16x16x32_bf16 v[20:23], v[244:247], v[84:87], v[20:23]\n"
      "s_waitcnt lgkmcnt(2)\n"
      "v_mfma_f32_16x16x32_bf16 v[24:27], v[128:131], v[60:63], v[24:27]\n"
      "ds_read_b32 v4, v15\n"
      "ds_read_b128 v[28:31], v9 offset:4096\n"
      "v_mfma_f32_16x16x32_bf16 v[24:27], v[148:151], v[64:67], v[24:27]\n"
      "ds_add_u32 v2, v1 offset:4192\n"
      "v_mfma_f32_16x16x32_bf16 v[24:27], v[168:171], v[68:71], v[24:27]\n"
      "v_mfma_f32_16x16x32_bf16 v[24:27], v[188:191], v[72:75], v[24:27]\n"
      "s_waitcnt lgkmcnt(2)\n"
      "v_cmp_gt_u32 vcc, 30, v4\n"
      "v_mfma_f32_16x16x32_bf16 v[24:27], v[208:211], v[76:79], v[24:27]\n"
      "ds_write_b128 v8, v[20:23] offset:22528\n"
      "v_mfma_f32_16x16x32_bf16 v[24:27], v[228:231], v[80:83], v[24:27]\n"
      "s_cbranch_vccnz .Lp_89\n"
      ".Lp_90:\n"
      "v_mfma_f32_16x16x32_bf16 v[24:27], v[248:251], v[84:87], v[24:27]\n"
      "s_waitcnt lgkmcnt(2)\n"
      "v_mfma_f32_16x16x32_bf16 v[28:31], v[132:135], v[60:63], v[28:31]\n"
      "v_mfma_f32_16x16x32_bf16 v[28:31], v[152:155], v[64:67], v[28:31]\n"
      "ds_add_u32 v2, v1 offset:4192\n"
      "v_mfma_f32_16x16x32_bf16 v[28:31], v[172:175], v[68:71], v[28:31]\n"
      "v_mfma_f32_16x16x32_bf16 v[28:31], v[192:195], v[72:75], v[28:31]\n"
      "v_mfma_f32_16x16x32_bf16 v[28:31], v[212:215], v[76:79], v[28:31]\n"
      "s_nop 0\n"
      "ds_write_b128 v8, v[24:27] offset:23552\n"
      "v_mfma_f32_16x16x32_bf16 v[28:31], v[232:235], v[80:83], v[28:31]\n"
      "v_mfma_f32_16x16x32_bf16 v[28:31], v[252:255], v[84:87], v[28:31]\n"
      "ds_add_u32 v2, v1 offset:4192\n"
      "s_nop 6\n"
      "ds_write_b128 v8, v[28:31] offset:24576\n"
      "ds_add_u32 v2, v1 offset:4192\n"
      "s_endpgm\n"
      ".LWARM:\n"
      ".Lp_25:\n"
      "s_cbranch_vccz .Lp_27\n"
      ".Lp_91:\n"
      "ds_read_b32 v4, v15\n"
      "ds_read_b128 v[20:23], v9 offset:0\n"
      "s_sub_u32 s73, s73, 1\n"
      "s_cselect_b32 s73, 0, s73\n"
      "s_waitcnt lgkmcnt(0)\n"
      "v_cmp_gt_u32 vcc, 1, v4\n"
      "s_cselect_b64 vcc, 0, vcc\n"
      "s_cbranch_vccnz .Lp_91\n"
      "s_branch .Lp_26\n"
      ".Lp_27:\n"
      "s_cbranch_vccz .Lp_29\n"
      ".Lp_92:\n"
      "ds_read_b32 v4, v15\n"
      "ds_read_b128 v[24:27], v9 offset:1024\n"
      "s_sub_u32 s73, s73, 1\n"
      "s_cselect_b32 s73, 0, s73\n"
      "s_waitcnt lgkmcnt(0)\n"
      "v_cmp_gt_u32 vcc, 2, v4\n"
      "s_cselect_b64 vcc, 0, vcc\n"
      "s_cbranch_vccnz .Lp_92\n"
      "s_branch .Lp_28\n"
      ".Lp_29:\n"
      "s_cbranch_vccz .Lp_31\n"
      ".Lp_93:\n"
      "ds_read_b32 v4, v15\n"
      "ds_read_b128 v[28:31], v9 offset:2048\n"
      "s_sub_u32 s73, s73, 1\n"
      "s_cselect_b32 s73, 0, s73\n"
      "s_waitcnt lgkmcnt(0)\n"
      "v_cmp_gt_u32 vcc, 3, v4\n"
      "s_cselect_b64 vcc, 0, vcc\n"
      "s_cbranch_vccnz .Lp_93\n"
      "s_branch .Lp_30\n"
      ".Lp_31:\n"
      "s_cbranch_vccz .Lp_33\n"
      ".Lp_94:\n"
      "ds_read_b32 v4, v15\n"
      "ds_read_b128 v[16:19], v9 offset:3072\n"
      "s_sub_u32 s73, s73, 1\n"
      "s_cselect_b32 s73, 0, s73\n"
      "s_waitcnt lgkmcnt(0)\n"
      "v_cmp_gt_u32 vcc, 4, v4\n"
      "s_cselect_b64 vcc, 0, vcc\n"
      "s_cbranch_vccnz .Lp_94\n"
      "s_branch .Lp_32\n"
      ".Lp_33:\n"
      "s_cbranch_vccz .Lp_35\n"
      ".Lp_95:\n"
      "ds_read_b32 v4, v15\n"
      "ds_read_b128 v[20:23], v9 offset:4096\n"
      "s_sub_u32 s73, s73, 1\n"
      "s_cselect_b32 s73, 0, s73\n"
      "s_waitcnt lgkmcnt(0)\n"
      "v_cmp_gt_u32 vcc, 5, v4\n"
      "s_cselect_b64 vcc, 0, vcc\n"
      "s_cbranch_vccnz .Lp_95\n"
      "s_branch .Lp_34\n"
      ".Lp_35:\n"
      "s_cbranch_vccz .Lp_37\n"
      ".Lp_96:\n"
      "ds_read_b32 v4, v15\n"
      "ds_read_b128 v[24:27], v9 offset:20480\n"
      "s_sub_u32 s73, s73, 1\n"
      "s_cselect_b32 s73, 0, s73\n"
      "s_waitcnt lgkmcnt(0)\n"
      "v_cmp_gt_u32 vcc, 6, v4\n"
      "s_cselect_b64 vcc, 0, vcc\n"
      "s_cbranch_vccnz .Lp_96\n"
      "s_branch .Lp_36\n"
      ".Lp_37:\n"
      "s_cbranch_vccz .Lp_42\n"
      ".Lp_97:\n"
      "ds_read_b32 v4, v15\n"
      "ds_read_b128 v[28:31], v9 offset:21504\n"
      "s_sub_u32 s73, s73, 1\n"
      "s_cselect_b32 s73, 0, s73\n"
      "s_waitcnt lgkmcnt(0)\n"
      "v_cmp_gt_u32 vcc, 7, v4\n"
      "s_cselect_b64 vcc, 0, vcc\n"
      "s_cbranch_vccnz .Lp_97\n"
      "s_branch .Lp_38\n"
      ".Lp_42:\n"
      "s_cbranch_vccz .Lp_44\n"
      ".Lp_98:\n"
      "ds_read_b32 v4, v15\n"
      "ds_read_b128 v[16:19], v9 offset:22528\n"
      "s_sub_u32 s73, s73, 1\n"
      "s_cselect_b32 s73, 0, s73\n"
      "s_waitcnt lgkmcnt(0)\n"
      "v_cmp_gt_u32 vcc, 8, v4\n"
      "s_cselect_b64 vcc, 0, vcc\n"
      "s_cbranch_vccnz .Lp_98\n"
      "s_branch .Lp_43\n"
      ".Lp_44:\n"
      "s_cbranch_vccz .Lp_46\n"
      ".Lp_99:\n"
      "ds_read_b32 v4, v15\n"
      "ds_read_b128 v[20:23], v9 offset:23552\n"
      "s_sub_u32 s73, s73, 1\n"
      "s_cselect_b32 s73, 0, s73\n"
      "s_waitcnt lgkmcnt(0)\n"
      "v_cmp_gt_u32 vcc, 9, v4\n"
      "s_cselect_b64 vcc, 0, vcc\n"
      "s_cbranch_vccnz .Lp_99\n"
      "s_branch .Lp_45\n"
      ".Lp_46:\n"
      "s_cbranch_vccz .Lp_48\n"
      ".Lp_100:\n"
      "ds_read_b32 v4, v15\n"
      "ds_read_b128 v[24:27], v9 offset:24576\n"
      "s_sub_u32 s73, s73, 1\n"
      "s_cselect_b32 s73, 0, s73\n"
      "s_waitcnt lgkmcnt(0)\n"
      "v_cmp_gt_u32 vcc, 10, v4\n"
      "s_cselect_b64 vcc, 0, vcc\n"
      "s_cbranch_vccnz .Lp_100\n"
      "s_branch .Lp_47\n"
      ".Lp_48:\n"
      "s_cbranch_vccz .Lp_50\n"
      ".Lp_101:\n"
      "ds_read_b32 v4, v15\n"
      "ds_read_b128 v[28:31], v9 offset:40960\n"
      "s_sub_u32 s73, s73, 1\n"
      "s_cselect_b32 s73, 0, s73\n"
      "s_waitcnt lgkmcnt(0)\n"
      "v_cmp_gt_u32 vcc, 11, v4\n"
      "s_cselect_b64 vcc, 0, vcc\n"
      "s_cbranch_vccnz .Lp_101\n"
      "s_branch .Lp_49\n"
      ".Lp_50:\n"
      "s_cbranch_vccz .Lp_52\n"
      ".Lp_102:\n"
      "ds_read_b32 v4, v15\n"
      "ds_read_b128 v[16:19], v9 offset:41984\n"
      "s_sub_u32 s73, s73, 1\n"
      "s_cselect_b32 s73, 0, s73\n"
      "s_waitcnt lgkmcnt(0)\n"
      "v_cmp_gt_u32 vcc, 12, v4\n"
      "s_cselect_b64 vcc, 0, vcc\n"
      "s_cbranch_vccnz .Lp_102\n"
      "s_branch .Lp_51\n"
      ".Lp_52:\n"
      "s_cbranch_vccz .Lp_54\n"
      ".Lp_103:\n"
      "ds_read_b32 v4, v15\n"
      "ds_read_b128 v[20:23], v9 offset:43008\n"
      "s_sub_u32 s73, s73, 1\n"
      "s_cselect_b32 s73, 0, s73\n"
      "s_waitcnt lgkmcnt(0)\n"
      "v_cmp_gt_u32 vcc, 13, v4\n"
      "s_cselect_b64 vcc, 0, vcc\n"
      "s_cbranch_vccnz .Lp_103\n"
      "s_branch .Lp_53\n"
      ".Lp_54:\n"
      "s_cbranch_vccz .Lp_56\n"
      ".Lp_104:\n"
      "ds_read_b32 v4, v15\n"
      "ds_read_b128 v[24:27], v9 offset:44032\n"
      "s_sub_u32 s73, s73, 1\n"
      "s_cselect_b32 s73, 0, s73\n"
      "s_waitcnt lgkmcnt(0)\n"
      "v_cmp_gt_u32 vcc, 14, v4\n"
      "s_cselect_b64 vcc, 0, vcc\n"
      "s_cbranch_vccnz .Lp_104\n"
      "s_branch .Lp_55\n"
      ".Lp_56:\n"
      "s_cbranch_vccz .Lp_61\n"
      ".Lp_105:\n"
      "ds_read_b32 v4, v15\n"
      "ds_read_b128 v[28:31], v9 offset:45056\n"
      "s_sub_u32 s73, s73, 1\n"
      "s_cselect_b32 s73, 0, s73\n"
      "s_waitcnt lgkmcnt(0)\n"
      "v_cmp_gt_u32 vcc, 15, v4\n"
      "s_cselect_b64 vcc, 0, vcc\n"
      "s_cbranch_vccnz .Lp_105\n"
      "s_branch .Lp_57\n"
      ".Lp_61:\n"
      "s_cbranch_vccz .Lp_63\n"
      ".Lp_106:\n"
      "ds_read_b32 v4, v15\n"
      "ds_read_b128 v[20:23], v9 offset:0\n"
      "s_sub_u32 s73, s73, 1\n"
      "s_cselect_b32 s73, 0, s73\n"
      "s_waitcnt lgkmcnt(0)\n"
      "v_cmp_gt_u32 vcc, 16, v4\n"
      "s_cselect_b64 vcc, 0, vcc\n"
      "s_cbranch_vccnz .Lp_106\n"
      "s_branch .Lp_62\n"
      ".Lp_63:\n"
      "s_cbranch_vccz .Lp_65\n"
      ".Lp_107:\n"
      "ds_read_b32 v4, v15\n"
      "ds_read_b128 v[24:27], v9 offset:1024\n"
      "s_sub_u32 s73, s73, 1\n"
      "s_cselect_b32 s73, 0, s73\n"
      "s_waitcnt lgkmcnt(0)\n"
      "v_cmp_gt_u32 vcc, 17, v4\n"
      "s_cselect_b64 vcc, 0, vcc\n"
      "s_cbranch_vccnz .Lp_107\n"
      "s_branch .Lp_64\n"
      ".Lp_65:\n"
      "s_cbranch_vccz .Lp_67\n"
      ".Lp_108:\n"
      "ds_read_b32 v4, v15\n"
      "ds_read_b128 v[28:31], v9 offset:2048\n"
      "s_sub_u32 s73, s73, 1\n"
      "s_cselect_b32 s73, 0, s73\n"
      "s_waitcnt lgkmcnt(0)\n"
      "v_cmp_gt_u32 vcc, 18, v4\n"
      "s_cselect_b64 vcc, 0, vcc\n"
      "s_cbranch_vccnz .Lp_108\n"
      "s_branch .Lp_66\n"
      ".Lp_67:\n"
      "s_cbranch_vccz .Lp_69\n"
      ".Lp_109:\n"
      "ds_read_b32 v4, v15\n"
      "ds_read_b128 v[16:19], v9 offset:3072\n"
      "s_sub_u32 s73, s73, 1\n"
      "s_cselect_b32 s73, 0, s73\n"
      "s_waitcnt lgkmcnt(0)\n"
      "v_cmp_gt_u32 vcc, 19, v4\n"
      "s_cselect_b64 vcc, 0, vcc\n"
      "s_cbranch_vccnz .Lp_109\n"
      "s_branch .Lp_68\n"
      ".Lp_69:\n"
      "s_cbranch_vccz .Lp_71\n"
      ".Lp_110:\n"
      "ds_read_b32 v4, v15\n"
      "ds_read_b128 v[20:23], v9 offset:4096\n"
      "s_sub_u32 s73, s73, 1\n"
      "s_cselect_b32 s73, 0, s73\n"
      "s_waitcnt lgkmcnt(0)\n"
      "v_cmp_gt_u32 vcc, 20, v4\n"
      "s_cselect_b64 vcc, 0, vcc\n"
      "s_cbranch_vccnz .Lp_110\n"
      "s_branch .Lp_70\n"
      ".Lp_71:\n"
      "s_cbranch_vccz .Lp_73\n"
      ".Lp_111:\n"
      "ds_read_b32 v4, v15\n"
      "ds_read_b128 v[24:27], v9 offset:0\n"
      "s_sub_u32 s73, s73, 1\n"
      "s_cselect_b32 s73, 0, s73\n"
      "s_waitcnt lgkmcnt(0)\n"
      "v_cmp_gt_u32 vcc, 21, v4\n"
      "s_cselect_b64 vcc, 0, vcc\n"
      "s_cbranch_vccnz .Lp_111\n"
      "s_branch .Lp_72\n"
      ".Lp_73:\n"
      "s_cbranch_vccz .Lp_75\n"
      ".Lp_112:\n"
      "ds_read_b32 v4, v15\n"
      "ds_read_b128 v[28:31], v9 offset:1024\n"
      "s_sub_u32 s73, s73, 1\n"
      "s_cselect_b32 s73, 0, s73\n"
      "s_waitcnt lgkmcnt(0)\n"
      "v_cmp_gt_u32 vcc, 22, v4\n"
      "s_cselect_b64 vcc, 0, vcc\n"
      "s_cbranch_vccnz .Lp_112\n"
      "s_branch .Lp_74\n"
      ".Lp_75:\n"
      "s_cbranch_vccz .Lp_77\n"
      ".Lp_113:\n"
      "ds_read_b32 v4, v15\n"
      "ds_read_b128 v[16:19], v9 offset:2048\n"
      "s_sub_u32 s73, s73, 1\n"
      "s_cselect_b32 s73, 0, s73\n"
      "s_waitcnt lgkmcnt(0)\n"
      "v_cmp_gt_u32 vcc, 23, v4\n"
      "s_cselect_b64 vcc, 0, vcc\n"
      "s_cbranch_vccnz .Lp_113\n"
      "s_branch .Lp_76\n"
      ".Lp_77:\n"
      "s_cbranch_vccz .Lp_79\n"
      ".Lp_114:\n"
      "ds_read_b32 v4, v15\n"
      "ds_read_b128 v[20:23], v9 offset:3072\n"
      "s_sub_u32 s73, s73, 1\n"
      "s_cselect_b32 s73, 0, s73\n"
      "s_waitcnt lgkmcnt(0)\n"
      "v_cmp_gt_u32 vcc, 24, v4\n"
      "s_cselect_b64 vcc, 0, vcc\n"
      "s_cbranch_vccnz .Lp_114\n"
      "s_branch .Lp_78\n"
      ".Lp_79:\n"
      "s_cbranch_vccz .Lp_81\n"
      ".Lp_115:\n"
      "ds_read_b32 v4, v15\n"
      "ds_read_b128 v[24:27], v9 offset:4096\n"
      "s_sub_u32 s73, s73, 1\n"
      "s_cselect_b32 s73, 0, s73\n"
      "s_waitcnt lgkmcnt(0)\n"
      "v_cmp_gt_u32 vcc, 25, v4\n"
      "s_cselect_b64 vcc, 0, vcc\n"
      "s_cbranch_vccnz .Lp_115\n"
      "s_branch .Lp_80\n"
      ".Lp_81:\n"
      "s_cbranch_vccz .Lp_83\n"
      ".Lp_116:\n"
      "ds_read_b32 v4, v15\n"
      "ds_read_b128 v[28:31], v9 offset:0\n"
      "s_sub_u32 s73, s73, 1\n"
      "s_cselect_b32 s73, 0, s73\n"
      "s_waitcnt lgkmcnt(0)\n"
      "v_cmp_gt_u32 vcc, 26, v4\n"
      "s_cselect_b64 vcc, 0, vcc\n"
      "s_cbranch_vccnz .Lp_116\n"
      "s_branch .Lp_82\n"
      ".Lp_83:\n"
      "s_cbranch_vccz .Lp_85\n"
      ".Lp_117:\n"
      "ds_read_b32 v4, v15\n"
      "ds_read_b128 v[16:19], v9 offset:1024\n"
      "s_sub_u32 s73, s73, 1\n"
      "s_cselect_b32 s73, 0, s73\n"
      "s_waitcnt lgkmcnt(0)\n"
      "v_cmp_gt_u32 vcc, 27, v4\n"
      "s_cselect_b64 vcc, 0, vcc\n"
      "s_cbranch_vccnz .Lp_117\n"
      "s_branch .Lp_84\n"
      ".Lp_85:\n"
      "s_cbranch_vccz .Lp_87\n"
      ".Lp_118:\n"
      "ds_read_b32 v4, v15\n"
      "ds_read_b128 v[20:23], v9 offset:2048\n"
      "s_sub_u32 s73, s73, 1\n"
      "s_cselect_b32 s73, 0, s73\n"
      "s_waitcnt lgkmcnt(0)\n"
      "v_cmp_gt_u32 vcc, 28, v4\n"
      "s_cselect_b64 vcc, 0, vcc\n"
      "s_cbranch_vccnz .Lp_118\n"
      "s_branch .Lp_86\n"
      ".Lp_87:\n"
      "s_cbranch_vccz .Lp_89\n"
      ".Lp_119:\n"
      "ds_read_b32 v4, v15\n"
      "ds_read_b128 v[24:27], v9 offset:3072\n"
      "s_sub_u32 s73, s73, 1\n"
      "s_cselect_b32 s73, 0, s73\n"
      "s_waitcnt lgkmcnt(0)\n"
      "v_cmp_gt_u32 vcc, 29, v4\n"
      "s_cselect_b64 vcc, 0, vcc\n"
      "s_cbranch_vccnz .Lp_119\n"
      "s_branch .Lp_88\n"
      ".Lp_89:\n"
      "s_cbranch_vccz .LWARME\n"
      ".Lp_120:\n"
      "ds_read_b32 v4, v15\n"
      "ds_read_b128 v[28:31], v9 offset:4096\n"
      "s_sub_u32 s73, s73, 1\n"
      "s_cselect_b32 s73, 0, s73\n"
      "s_waitcnt lgkmcnt(0)\n"
      "v_cmp_gt_u32 vcc, 30, v4\n"
      "s_cselect_b64 vcc, 0, vcc\n"
      "s_cbranch_vccnz .Lp_120\n"
      "s_branch .Lp_90\n"
      ".LM:\n"
      "s_cmp_lt_u32 s61, 28\n"
      "s_cselect_b32 s62, s57, 14\n"
      "s_cselect_b32 s63, 0, s54\n"
      "s_add_u32 s62, s62, s63\n"
      "s_lshl4_add_u32 s62, s56, s62\n"
      "s_lshl2_add_u32 s62, s62, s53\n"
      "s_mul_i32 s63, s62, 2560\n"
      "s_lshl_b32 s69, s62, 4\n"
      "v_lshrrev_b32 v5, 5, v1\n"
      "v_and_b32 v6, 31, v1\n"
      "v_mul_u32_u24 v7, 1280, v5\n"
      "v_lshl_add_u32 v18, v6, 4, v7\n"
      "v_lshl_add_u32 v19, v6, 3, v7\n"
      "v_lshlrev_b32 v11, 2, v1\n"
      "s_mul_i32 s64, s55, 160\n"
      "s_lshl2_add_u32 s65, s57, s53\n"
      "s_lshl_b32 s65, s65, 6\n"
      "v_lshlrev_b32 v12, 1, v6\n"
      "s_and_b32 s66, s2, 7\n"
      "s_mul_i32 s66, s66, 28\n"
      "s_add_u32 s66, s66, s61\n"
      "s_mul_i32 s67, s66, 205\n"
      "s_lshr_b32 s67, s67, 11\n"
      "s_mul_i32 s68, s67, 10\n"
      "s_sub_u32 s68, s66, s68\n"
      "s_lshl2_add_u32 s66, s68, s53\n"
      "s_lshl_b32 s70, s66, 7\n"
      "v_lshrrev_b32 v13, 3, v1\n"
      "v_lshlrev_b32 v14, 4, v13\n"
      "s_mov_b32 m0, 162816\n"
      "s_waitcnt lgkmcnt(0)\n"
      "buffer_load_dwordx4 v[96:99], v18, s[4:7], s63 offen\n"
      "buffer_load_dwordx4 v[100:103], v18, s[4:7], s63 offen offset:512\n"
      "buffer_load_dwordx2 v[104:105], v19, s[4:7], s63 offen offset:1024\n"
      "buffer_load_dwordx2 v[42:43], off, s[20:23], 0 sc1\n"
      "buffer_load_dword v11, s[44:47], s64 offen lds\n"
      "buffer_load_dwordx4 v[44:47], v14, s[24:27], s70 offen\n"
      "buffer_load_ushort v9, v12, s[28:31], s65 offen\n"
      "s_barrier\n"
      "s_setprio 3\n"
      "s_lshl_b32 s62, s53, 5\n"
      "s_add_u32 s62, s62, 140160\n"
      "v_and_b32 v53, 7, v1\n"
      "v_lshrrev_b32 v54, 3, v1\n"
      "v_mul_u32_u24 v192, 1040, v53\n"
      "v_lshl_add_u32 v192, v54, 2, v192\n"
      "v_add_u32 v192, s62, v192\n"
      "v_add_u32 v193, 8720, v192\n"
      "v_mul_u32_u24 v8, 8448, v54\n"
      "v_lshl_add_u32 v8, v53, 4, v8\n"
      "s_mul_i32 s84, s66, 67584\n"
      "s_lshl_b32 s63, s67, 7\n"
      "s_add_u32 s84, s84, s63\n"
      "s_add_u32 s84, s84, 32768\n"
      "s_add_u32 s85, s84, 4224\n"
      "v_lshrrev_b32 v14, 5, v1\n"
      "v_bfe_u32 v15, v1, 2, 2\n"
      "v_lshl_add_u32 v16, v14, 3, v15\n"
      "v_lshlrev_b32 v16, 8, v16\n"
      "v_bfe_u32 v17, v1, 4, 1\n"
      "v_mul_u32_u24 v29, 57344, v17\n"
      "s_lshl_b32 s64, s53, 1\n"
      "v_or_b32 v17, s64, v17\n"
      "v_lshlrev_b32 v15, 1, v15\n"
      "v_xor_b32 v17, v17, v15\n"
      "v_lshl_add_u32 v16, v17, 5, v16\n"
      "v_and_b32 v17, 3, v1\n"
      "v_lshl_add_u32 v24, v17, 3, v16\n"
      "v_and_b32 v15, 0x2f, v1\n"
      "v_lshl_add_u32 v29, v15, 4, v29\n"
      "v_and_b32 v15, 31, v1\n"
      "v_bfe_u32 v16, v1, 1, 3\n"
      "v_xor_b32 v16, v16, v14\n"
      "v_lshlrev_b32 v16, 4, v16\n"
      "v_lshl_add_u32 v4, v15, 7, v16\n"
      "v_xor_b32 v5, 32, v4\n"
      "v_xor_b32 v6, 64, v4\n"
      "v_xor_b32 v7, 96, v4\n"
      "v_lshlrev_b32 v30, 5, v15\n"
      "v_add_u32 v30, 157696, v30\n"
      "v_lshrrev_b32 v16, 4, v1\n"
      "v_xor_b32 v16, v16, v1\n"
      "v_and_b32 v16, 1, v16\n"
      "v_cmp_eq_u32 vcc, 0, v16\n"
      "v_mov_b32 v17, 0x3f803f80\n"
      "v_mov_b64 v[10:11], 0\n"
      "v_cndmask_b32 v16, 0, v17, vcc\n"
      "v_mov_b32 v17, v16\n"
      "v_mov_b32 v18, v16\n"
      "v_mov_b32 v19, v16\n"
      "v_mov_b64 v[14:15], 0\n"
      "s_lshl2_add_u32 s64, s54, s53\n"
      "s_mul_i32 s64, s64, 7\n"
      "s_add_u32 s64, s64, s55\n"
      "s_lshl_b32 s65, s64, 10\n"
      "s_mul_i32 s78, s56, 458752\n"
      "s_add_u32 s78, s78, s65\n"
      "s_add_u32 s78, s78, 7897088\n"
      "s_lshl2_add_u32 s65, s57, s53\n"
      "s_cmp_ge_u32 s65, 54\n"
      "s_cselect_b32 s78, 1073741824, s78\n"
      "s_lshl_b32 s64, s56, 6\n"
      "s_add_u32 s64, s64, s65\n"
      "s_lshl_b32 s64, s64, 8\n"
      "s_add_u32 s79, s64, 18382848\n"
      "s_xor_b32 s74, s99, 0\n"
      "s_mul_i32 s74, s74, 114688\n"
      "s_add_u32 s74, s74, s78\n"
      "s_xor_b32 s75, s99, 1\n"
      "s_mul_i32 s75, s75, 114688\n"
      "s_add_u32 s75, s75, s78\n"
      "s_xor_b32 s76, s99, 2\n"
      "s_mul_i32 s76, s76, 114688\n"
      "s_add_u32 s76, s76, s78\n"
      "s_xor_b32 s77, s99, 3\n"
      "s_mul_i32 s77, s77, 114688\n"
      "s_add_u32 s77, s77, s78\n"
      "s_mul_i32 s63, s56, 3\n"
      "s_add_u32 s63, s63, s57\n"
      "s_mul_i32 s64, s63, 205\n"
      "s_lshr_b32 s64, s64, 11\n"
      "s_mul_i32 s64, s64, 10\n"
      "s_sub_u32 s63, s63, s64\n"
      "s_sub_u32 s64, 3, s53\n"
      "s_lshr_b32 s65, s64, 1\n"
      "s_xor_b32 s65, s65, s99\n"
      "s_and_b32 s65, s65, 1\n"
      "s_mul_i32 s65, s65, 49152\n"
      "s_and_b32 s64, s64, 1\n"
      "s_lshl_b32 s64, s64, 11\n"
      "s_add_u32 s65, s65, s64\n"
      "s_mul_i32 s64, s55, 5\n"
      "s_lshr_b32 s71, s64, 2\n"
      "s_sub_u32 s71, s71, s63\n"
      "s_cselect_b32 s72, 10, 0\n"
      "s_add_u32 s71, s71, s72\n"
      "s_lshl_b32 s71, s71, 12\n"
      "s_add_u32 s71, s71, s65\n"
      "s_and_b32 s72, s64, 3\n"
      "s_lshl_b32 s72, s72, 5\n"
      "s_or_b32 s86, s71, s72\n"
      "s_add_u32 s64, s64, 1\n"
      "s_lshr_b32 s71, s64, 2\n"
      "s_sub_u32 s71, s71, s63\n"
      "s_cselect_b32 s72, 10, 0\n"
      "s_add_u32 s71, s71, s72\n"
      "s_lshl_b32 s71, s71, 12\n"
      "s_add_u32 s71, s71, s65\n"
      "s_and_b32 s72, s64, 3\n"
      "s_lshl_b32 s72, s72, 5\n"
      "s_or_b32 s87, s71, s72\n"
      "s_add_u32 s64, s64, 1\n"
      "s_lshr_b32 s71, s64, 2\n"
      "s_sub_u32 s71, s71, s63\n"
      "s_cselect_b32 s72, 10, 0\n"
      "s_add_u32 s71, s71, s72\n"
      "s_lshl_b32 s71, s71, 12\n"
      "s_add_u32 s71, s71, s65\n"
      "s_and_b32 s72, s64, 3\n"
      "s_lshl_b32 s72, s72, 5\n"
      "s_or_b32 s88, s71, s72\n"
      "s_add_u32 s64, s64, 1\n"
      "s_lshr_b32 s71, s64, 2\n"
      "s_sub_u32 s71, s71, s63\n"
      "s_cselect_b32 s72, 10, 0\n"
      "s_add_u32 s71, s71, s72\n"
      "s_lshl_b32 s71, s71, 12\n"
      "s_add_u32 s71, s71, s65\n"
      "s_and_b32 s72, s64, 3\n"
      "s_lshl_b32 s72, s72, 5\n"
      "s_or_b32 s89, s71, s72\n"
      "s_add_u32 s64, s64, 1\n"
      "s_lshr_b32 s71, s64, 2\n"
      "s_sub_u32 s71, s71, s63\n"
      "s_cselect_b32 s72, 10, 0\n"
      "s_add_u32 s71, s71, s72\n"
      "s_lshl_b32 s71, s71, 12\n"
      "s_add_u32 s71, s71, s65\n"
      "s_and_b32 s72, s64, 3\n"
      "s_lshl_b32 s72, s72, 5\n"
      "s_or_b32 s90, s71, s72\n"
      "s_add_u32 s64, s64, 1\n"
      "v_and_b32 v50, 15, v1\n"
      "v_bfe_u32 v51, v1, 1, 3\n"
      "v_lshrrev_b32 v52, 5, v1\n"
      "v_xor_b32 v51, v51, v52\n"
      "v_lshlrev_b32 v51, 4, v51\n"
      "v_lshl_add_u32 v51, v50, 7, v51\n"
      "v_bfe_u32 v52, v1, 4, 1\n"
      "v_lshl_add_u32 v28, v52, 3, v51\n"
      "s_mov_b32 s80, s92\n"
      "s_mov_b32 s81, s92\n"
      "s_mov_b32 s82, -1\n"
      "s_mov_b32 s83, 0\n"
      "s_waitcnt vmcnt(3)\n"
      "ds_swizzle_b32 v106, v96 offset:0\n"
      "v_lshlrev_b32 v116, 16, v96\n"
      "v_and_b32 v117, s92, v96\n"
      "v_lshlrev_b32 v118, 16, v97\n"
      "v_and_b32 v119, s92, v97\n"
      "v_lshlrev_b32 v120, 16, v98\n"
      "v_and_b32 v121, s92, v98\n"
      "v_lshlrev_b32 v122, 16, v99\n"
      "v_and_b32 v123, s92, v99\n"
      "v_lshlrev_b32 v124, 16, v100\n"
      "v_and_b32 v125, s92, v100\n"
      "v_lshlrev_b32 v126, 16, v101\n"
      "v_and_b32 v127, s92, v101\n"
      "v_lshlrev_b32 v128, 16, v102\n"
      "v_and_b32 v129, s92, v102\n"
      "v_lshlrev_b32 v130, 16, v103\n"
      "v_and_b32 v131, s92, v103\n"
      "v_lshlrev_b32 v132, 16, v104\n"
      "v_and_b32 v133, s92, v104\n"
      "v_lshlrev_b32 v134, 16, v105\n"
      "v_and_b32 v135, s92, v105\n"
      "v_readfirstlane_b32 s58, v42\n"
      "v_readfirstlane_b32 s59, v43\n"
      "s_waitcnt lgkmcnt(0)\n"
      "v_lshlrev_b32 v106, 16, v106\n"
      "v_mov_b32 v107, v106\n"
      "s_add_u32 s58, s58, 1\n"
      "s_addc_u32 s59, s59, 0\n"
      "v_pk_add_f32 v[116:117], v[116:117], v[106:107] neg_lo:[0,1] neg_hi:[0,1]\n"
      "v_pk_add_f32 v[118:119], v[118:119], v[106:107] neg_lo:[0,1] neg_hi:[0,1]\n"
      "v_pk_add_f32 v[120:121], v[120:121], v[106:107] neg_lo:[0,1] neg_hi:[0,1]\n"
      "v_pk_add_f32 v[122:123], v[122:123], v[106:107] neg_lo:[0,1] neg_hi:[0,1]\n"
      "v_pk_add_f32 v[124:125], v[124:125], v[106:107] neg_lo:[0,1] neg_hi:[0,1]\n"
      "v_pk_add_f32 v[126:127], v[126:127], v[106:107] neg_lo:[0,1] neg_hi:[0,1]\n"
      "v_pk_add_f32 v[128:129], v[128:129], v[106:107] neg_lo:[0,1] neg_hi:[0,1]\n"
      "v_pk_add_f32 v[130:131], v[130:131], v[106:107] neg_lo:[0,1] neg_hi:[0,1]\n"
      "v_pk_add_f32 v[132:133], v[132:133], v[106:107] neg_lo:[0,1] neg_hi:[0,1]\n"
      "v_pk_add_f32 v[134:135], v[134:135], v[106:107] neg_lo:[0,1] neg_hi:[0,1]\n"
      "v_pk_mul_f32 v[108:109], v[116:117], v[116:117]\n"
      "v_pk_mul_f32 v[110:111], v[118:119], v[118:119]\n"
      "v_pk_fma_f32 v[108:109], v[120:121], v[120:121], v[108:109]\n"
      "v_pk_add_f32 v[116:117], v[116:117], v[120:121]\n"
      "v_pk_fma_f32 v[110:111], v[122:123], v[122:123], v[110:111]\n"
      "v_pk_add_f32 v[118:119], v[118:119], v[122:123]\n"
      "v_pk_fma_f32 v[108:109], v[124:125], v[124:125], v[108:109]\n"
      "v_pk_add_f32 v[116:117], v[116:117], v[124:125]\n"
      "v_pk_fma_f32 v[110:111], v[126:127], v[126:127], v[110:111]\n"
      "v_pk_add_f32 v[118:119], v[118:119], v[126:127]\n"
      "v_pk_fma_f32 v[108:109], v[128:129], v[128:129], v[108:109]\n"
      "v_pk_add_f32 v[116:117], v[116:117], v[128:129]\n"
      "v_pk_fma_f32 v[110:111], v[130:131], v[130:131], v[110:111]\n"
      "v_pk_add_f32 v[118:119], v[118:119], v[130:131]\n"
      "v_pk_fma_f32 v[108:109], v[132:133], v[132:133], v[108:109]\n"
      "v_pk_add_f32 v[116:117], v[116:117], v[132:133]\n"
      "v_pk_fma_f32 v[110:111], v[134:135], v[134:135], v[110:111]\n"
      "v_pk_add_f32 v[118:119], v[118:119], v[134:135]\n"
      "v_pk_add_f32 v[108:109], v[108:109], v[110:111]\n"
      "v_pk_add_f32 v[116:117], v[116:117], v[118:119]\n"
      "v_add_f32 v108, v108, v109\n"
      "v_add_f32 v109, v116, v117\n"
      "s_and_b32 s93, s58, 0x1fff\n"
      "v_add_f32_dpp v108, v108, v108 quad_perm:[1,0,3,2] row_mask:0xf bank_mask:0xf\n"
      "v_add_f32_dpp v109, v109, v109 quad_perm:[1,0,3,2] row_mask:0xf bank_mask:0xf\n"
      "s_or_b32 s93, s93, 0x4000\n"
      "v_add_f32_dpp v108, v108, v108 quad_perm:[2,3,0,1] row_mask:0xf bank_mask:0xf\n"
      "v_add_f32_dpp v109, v109, v109 quad_perm:[2,3,0,1] row_mask:0xf bank_mask:0xf\n"
      "v_mov_b32 v114, 1073741824\n"
      "v_add_f32_dpp v108, v108, v108 row_half_mirror row_mask:0xf bank_mask:0xf\n"
      "v_add_f32_dpp v109, v109, v109 row_half_mirror row_mask:0xf bank_mask:0xf\n"
      "v_writelane_b32 v114, 0, 16\n"
      "v_add_f32_dpp v108, v108, v108 row_mirror row_mask:0xf bank_mask:0xf\n"
      "v_add_f32_dpp v109, v109, v109 row_mirror row_mask:0xf bank_mask:0xf\n"
      "v_writelane_b32 v114, 8, 48\n"
      "v_add_f32_dpp v108, v108, v108 row_bcast:15 row_mask:0xa bank_mask:0xf\n"
      "v_add_f32_dpp v109, v109, v109 row_bcast:15 row_mask:0xa bank_mask:0xf\n"
      "v_mul_f32 v109, 0x3acccccd, v109\n"
      "v_mul_f32 v108, 0x3acccccd, v108\n"
      "v_fma_f32 v108, -v109, v109, v108\n"
      "v_max_f32 v108, 0, v108\n"
      "v_cvt_pk_bf16_f32 v108, 0, v108\n"
      "v_add_f32 v108, 0x3727c5ac, v108\n"
      "v_cvt_pk_bf16_f32 v108, 0, v108\n"
      "v_sqrt_f32 v108, v108\n"
      "v_add_f32 v109, v106, v109\n"
      "s_add_u32 s69, s69, 8192\n"
      "v_cvt_pk_bf16_f32 v112, v109, v108\n"
      "v_mov_b32 v113, s58\n"
      "buffer_store_dwordx2 v[112:113], v114, s[20:23], s69 offen sc1\n"
      "s_cmp_lt_u32 s61, 28\n"
      "s_cbranch_scc0 .LSPARE\n"
      "v_mov_b64 v[12:13], s[58:59]\n"
      "v_cmp_eq_u32 vcc, 0, v1\n"
      "v_lshlrev_b32 v2, 2, v1\n"
      "v_add_u32 v2, 157696, v2\n"
      "v_cndmask_b32 v3, 0, 1, vcc\n"
      "s_waitcnt vmcnt(1)\n"
      "v_lshlrev_b32 v184, 16, v44\n"
      "v_lshlrev_b32 v185, 16, v46\n"
      "v_and_b32 v186, s92, v44\n"
      "v_and_b32 v187, s92, v46\n"
      "v_lshlrev_b32 v188, 16, v45\n"
      "v_lshlrev_b32 v189, 16, v47\n"
      "v_and_b32 v190, s92, v45\n"
      "v_and_b32 v191, s92, v47\n"
      "v_lshlrev_b32 v9, 16, v9\n"
      "s_barrier\n"
      "v_mov_b32 v31, 161920\n"
      "ds_write_b64 v31, v[12:13]\n"
      "ds_read_b32 v26, v2 offset:5120\n"
      "v_lshlrev_b32 v31, 3, v1\n"
      "v_add_u32 v31, 163072, v31\n"
      "s_waitcnt lgkmcnt(0)\n"
      "v_and_b32 v27, s92, v26\n"
      "v_lshlrev_b32 v26, 16, v26\n"
      "ds_write_b64 v31, v[26:27]\n"
      "ds_read_b64_tr_b16 v[96:97], v24 offset:0\n"
      "ds_read_b64_tr_b16 v[98:99], v24 offset:1024\n"
      "ds_read_b128 v[32:35], v4 offset:16384\n"
      "ds_read_b128 v[36:39], v4 offset:20480\n"
      "ds_read_b64_tr_b16 v[100:101], v24 offset:4096\n"
      "ds_read_b64_tr_b16 v[102:103], v24 offset:5120\n"
      "ds_read_b128 v[40:43], v5 offset:16384\n"
      "ds_read_b128 v[44:47], v5 offset:20480\n"
      "ds_read_b64_tr_b16 v[104:105], v24 offset:8192\n"
      "ds_read_b64_tr_b16 v[106:107], v24 offset:9216\n"
      "ds_read_b128 v[48:51], v6 offset:16384\n"
      "ds_read_b128 v[52:55], v6 offset:20480\n"
      "ds_read_b64_tr_b16 v[108:109], v24 offset:12288\n"
      "ds_read_b64_tr_b16 v[110:111], v24 offset:13312\n"
      "ds_read_b128 v[56:59], v7 offset:16384\n"
      "ds_read_b128 v[60:63], v7 offset:20480\n"
      "s_waitcnt lgkmcnt(13)\n"
      "v_mfma_f32_32x32x16_bf16 v[80:95], v[96:99], v[32:35], 0\n"
      "s_waitcnt lgkmcnt(12)\n"
      "v_mfma_f32_32x32x16_bf16 v[64:79], v[96:99], v[36:39], 0\n"
      "v_mfma_f32_16x16x32_bf16 v[20:23], v[16:19], v[96:99], 0\n"
      "s_waitcnt lgkmcnt(10)\n"
      "v_mfma_f32_16x16x32_bf16 v[20:23], v[16:19], v[100:103], v[20:23]\n"
      "s_waitcnt lgkmcnt(9)\n"
      "v_mfma_f32_32x32x16_bf16 v[80:95], v[100:103], v[40:43], v[80:95]\n"
      "s_waitcnt lgkmcnt(0)\n"
      "v_mfma_f32_32x32x16_bf16 v[64:79], v[100:103], v[44:47], v[64:79]\n"
      "s_barrier\n"
      "ds_read2_b32 v[196:197], v192 offset0:0 offset1:32\n"
      "ds_read2_b32 v[198:199], v192 offset0:64 offset1:100\n"
      "ds_read2_b32 v[200:201], v192 offset0:132 offset1:164\n"
      "ds_read2_b32 v[202:203], v192 offset0:196 offset1:228\n"
      "ds_read_b32 v204, v192 offset:1040\n"
      "ds_read_b32 v205, v192 offset:1168\n"
      "ds_read_b32 v206, v192 offset:1296\n"
      "ds_read2_b32 v[208:209], v193 offset0:0 offset1:32\n"
      "ds_read2_b32 v[210:211], v193 offset0:64 offset1:96\n"
      "ds_read2_b32 v[212:213], v193 offset0:128 offset1:160\n"
      "ds_read2_b32 v[214:215], v193 offset0:192 offset1:224\n"
      "s_waitcnt lgkmcnt(11)\n"
      "v_mfma_f32_32x32x16_bf16 v[80:95], v[104:107], v[48:51], v[80:95]\n"
      "s_waitcnt lgkmcnt(9)\n"
      "v_lshlrev_b32 v216, 16, v196\n"
      "v_and_b32 v217, s92, v196\n"
      "v_lshlrev_b32 v218, 16, v197\n"
      "v_and_b32 v219, s92, v197\n"
      "v_mfma_f32_32x32x16_bf16 v[64:79], v[104:107], v[52:55], v[64:79]\n"
      "v_lshlrev_b32 v220, 16, v198\n"
      "v_and_b32 v221, s92, v198\n"
      "v_lshlrev_b32 v222, 16, v199\n"
      "v_and_b32 v223, s92, v199\n"
      "v_mfma_f32_32x32x16_bf16 v[80:95], v[108:111], v[56:59], v[80:95]\n"
      "s_waitcnt lgkmcnt(7)\n"
      "v_lshlrev_b32 v224, 16, v200\n"
      "v_and_b32 v225, s92, v200\n"
      "v_lshlrev_b32 v226, 16, v201\n"
      "v_and_b32 v227, s92, v201\n"
      "v_mfma_f32_32x32x16_bf16 v[64:79], v[108:111], v[60:63], v[64:79]\n"
      "v_lshlrev_b32 v228, 16, v202\n"
      "v_and_b32 v229, s92, v202\n"
      "v_lshlrev_b32 v230, 16, v203\n"
      "v_and_b32 v231, s92, v203\n"
      "v_mfma_f32_16x16x32_bf16 v[20:23], v[16:19], v[104:107], v[20:23]\n"
      "s_waitcnt lgkmcnt(4)\n"
      "v_lshlrev_b32 v232, 16, v204\n"
      "v_and_b32 v233, s92, v204\n"
      "v_lshlrev_b32 v234, 16, v205\n"
      "v_and_b32 v235, s92, v205\n"
      "v_mfma_f32_16x16x32_bf16 v[20:23], v[16:19], v[108:111], v[20:23]\n"
      "v_lshlrev_b32 v236, 16, v206\n"
      "v_and_b32 v237, s92, v206\n"
      "v_pk_mul_f32 v[240:241], v[216:217], v[190:191]\n"
      "v_pk_mul_f32 v[242:243], v[218:219], v[190:191]\n"
      "v_pk_mul_f32 v[244:245], v[220:221], v[190:191]\n"
      "v_pk_mul_f32 v[246:247], v[222:223], v[190:191]\n"
      "v_pk_mul_f32 v[248:249], v[224:225], v[190:191]\n"
      "v_pk_mul_f32 v[250:251], v[226:227], v[190:191]\n"
      "v_pk_mul_f32 v[252:253], v[228:229], v[190:191]\n"
      "v_pk_mul_f32 v[254:255], v[230:231], v[190:191]\n"
      "v_pk_fma_f32 v[240:241], v[218:219], v[188:189], v[240:241]\n"
      "v_pk_fma_f32 v[242:243], v[220:221], v[188:189], v[242:243]\n"
      "v_pk_fma_f32 v[244:245], v[222:223], v[188:189], v[244:245]\n"
      "v_pk_fma_f32 v[246:247], v[224:225], v[188:189], v[246:247]\n"
      "v_pk_fma_f32 v[248:249], v[226:227], v[188:189], v[248:249]\n"
      "v_pk_fma_f32 v[250:251], v[228:229], v[188:189], v[250:251]\n"
      "v_pk_fma_f32 v[252:253], v[230:231], v[188:189], v[252:253]\n"
      "v_pk_fma_f32 v[254:255], v[232:233], v[188:189], v[254:255]\n"
      "v_pk_fma_f32 v[240:241], v[220:221], v[186:187], v[240:241]\n"
      "v_pk_fma_f32 v[242:243], v[222:223], v[186:187], v[242:243]\n"
      "v_pk_fma_f32 v[244:245], v[224:225], v[186:187], v[244:245]\n"
      "v_pk_fma_f32 v[246:247], v[226:227], v[186:187], v[246:247]\n"
      "v_pk_fma_f32 v[248:249], v[228:229], v[186:187], v[248:249]\n"
      "v_pk_fma_f32 v[250:251], v[230:231], v[186:187], v[250:251]\n"
      "v_pk_fma_f32 v[252:253], v[232:233], v[186:187], v[252:253]\n"
      "v_pk_fma_f32 v[254:255], v[234:235], v[186:187], v[254:255]\n"
      "v_pk_fma_f32 v[240:241], v[222:223], v[184:185], v[240:241]\n"
      "v_pk_fma_f32 v[242:243], v[224:225], v[184:185], v[242:243]\n"
      "v_pk_fma_f32 v[244:245], v[226:227], v[184:185], v[244:245]\n"
      "v_pk_fma_f32 v[246:247], v[228:229], v[184:185], v[246:247]\n"
      "v_pk_fma_f32 v[248:249], v[230:231], v[184:185], v[248:249]\n"
      "v_pk_fma_f32 v[250:251], v[232:233], v[184:185], v[250:251]\n"
      "v_pk_fma_f32 v[252:253], v[234:235], v[184:185], v[252:253]\n"
      "v_pk_fma_f32 v[254:255], v[236:237], v[184:185], v[254:255]\n"
      "s_waitcnt lgkmcnt(0)\n"
      "v_lshlrev_b32 v216, 16, v208\n"
      "v_and_b32 v217, s92, v208\n"
      "v_lshlrev_b32 v218, 16, v209\n"
      "v_and_b32 v219, s92, v209\n"
      "v_lshlrev_b32 v220, 16, v210\n"
      "v_and_b32 v221, s92, v210\n"
      "v_lshlrev_b32 v222, 16, v211\n"
      "v_and_b32 v223, s92, v211\n"
      "v_lshlrev_b32 v224, 16, v212\n"
      "v_and_b32 v225, s92, v212\n"
      "v_lshlrev_b32 v226, 16, v213\n"
      "v_and_b32 v227, s92, v213\n"
      "v_lshlrev_b32 v228, 16, v214\n"
      "v_and_b32 v229, s92, v214\n"
      "v_lshlrev_b32 v230, 16, v215\n"
      "v_and_b32 v231, s92, v215\n"
      "v_pk_mul_f32 v[240:241], v[240:241], v[216:217]\n"
      "v_pk_mul_f32 v[242:243], v[242:243], v[218:219]\n"
      "v_pk_mul_f32 v[244:245], v[244:245], v[220:221]\n"
      "v_pk_mul_f32 v[246:247], v[246:247], v[222:223]\n"
      "v_pk_mul_f32 v[248:249], v[248:249], v[224:225]\n"
      "v_pk_mul_f32 v[250:251], v[250:251], v[226:227]\n"
      "v_pk_mul_f32 v[252:253], v[252:253], v[228:229]\n"
      "v_pk_mul_f32 v[254:255], v[254:255], v[230:231]\n"
      "v_cvt_pk_bf16_f32 v196, v242, v240\n"
      "v_cvt_pk_bf16_f32 v197, v246, v244\n"
      "v_cvt_pk_bf16_f32 v198, v250, v248\n"
      "v_cvt_pk_bf16_f32 v199, v254, v252\n"
      "buffer_store_dwordx4 v[196:199], v8, s[20:23], s84 offen sc1\n"
      "v_cvt_pk_bf16_f32 v200, v243, v241\n"
      "v_cvt_pk_bf16_f32 v201, v247, v245\n"
      "v_cvt_pk_bf16_f32 v202, v251, v249\n"
      "v_cvt_pk_bf16_f32 v203, v255, v253\n"
      "buffer_store_dwordx4 v[200:203], v8, s[20:23], s85 offen sc1\n"
      "s_barrier\n"
      "ds_read_b64_tr_b16 v[112:113], v24 offset:24576\n"
      "ds_read_b64_tr_b16 v[114:115], v24 offset:25600\n"
      "ds_read_b128 v[32:35], v4 offset:40960\n"
      "ds_read_b128 v[36:39], v4 offset:45056\n"
      "ds_read_b64_tr_b16 v[116:117], v24 offset:28672\n"
      "ds_read_b64_tr_b16 v[118:119], v24 offset:29696\n"
      "ds_read_b128 v[40:43], v5 offset:40960\n"
      "ds_read_b128 v[44:47], v5 offset:45056\n"
      "ds_read_b64_tr_b16 v[120:121], v24 offset:32768\n"
      "ds_read_b64_tr_b16 v[122:123], v24 offset:33792\n"
      "ds_read_b128 v[48:51], v6 offset:40960\n"
      "ds_read_b128 v[52:55], v6 offset:45056\n"
      "ds_read_b64_tr_b16 v[124:125], v24 offset:36864\n"
      "ds_read_b64_tr_b16 v[126:127], v24 offset:37888\n"
      "ds_read_b128 v[56:59], v7 offset:40960\n"
      "ds_read_b128 v[60:63], v7 offset:45056\n"
      "s_waitcnt lgkmcnt(13)\n"
      "v_mfma_f32_32x32x16_bf16 v[80:95], v[112:115], v[32:35], v[80:95]\n"
      "s_waitcnt lgkmcnt(12)\n"
      "v_mfma_f32_32x32x16_bf16 v[64:79], v[112:115], v[36:39], v[64:79]\n"
      "v_mfma_f32_16x16x32_bf16 v[20:23], v[16:19], v[112:115], v[20:23]\n"
      "s_waitcnt lgkmcnt(10)\n"
      "v_mfma_f32_16x16x32_bf16 v[20:23], v[16:19], v[116:119], v[20:23]\n"
      "s_waitcnt lgkmcnt(9)\n"
      "v_mfma_f32_32x32x16_bf16 v[80:95], v[116:119], v[40:43], v[80:95]\n"
      "s_waitcnt lgkmcnt(0)\n"
      "v_add_u32 v24, 49152, v24\n"
      "v_add_u32 v4, 49152, v4\n"
      "v_add_u32 v5, 49152, v5\n"
      "v_add_u32 v6, 49152, v6\n"
      "v_add_u32 v7, 49152, v7\n"
      "v_mfma_f32_32x32x16_bf16 v[64:79], v[116:119], v[44:47], v[64:79]\n"
      "s_barrier\n"
      "v_mfma_f32_32x32x16_bf16 v[80:95], v[120:123], v[48:51], v[80:95]\n"
      "ds_read_b64_tr_b16 v[128:129], v24 offset:0\n"
      "ds_read_b64_tr_b16 v[130:131], v24 offset:1024\n"
      "ds_read_b128 v[32:35], v4 offset:16384\n"
      "ds_read_b128 v[36:39], v4 offset:20480\n"
      "v_mfma_f32_32x32x16_bf16 v[64:79], v[120:123], v[52:55], v[64:79]\n"
      "ds_read_b64_tr_b16 v[132:133], v24 offset:4096\n"
      "ds_read_b64_tr_b16 v[134:135], v24 offset:5120\n"
      "ds_read_b128 v[40:43], v5 offset:16384\n"
      "ds_read_b128 v[44:47], v5 offset:20480\n"
      "v_mfma_f32_32x32x16_bf16 v[80:95], v[124:127], v[56:59], v[80:95]\n"
      "ds_read_b64_tr_b16 v[136:137], v24 offset:8192\n"
      "ds_read_b64_tr_b16 v[138:139], v24 offset:9216\n"
      "ds_read_b128 v[48:51], v6 offset:16384\n"
      "ds_read_b128 v[52:55], v6 offset:20480\n"
      "v_mfma_f32_32x32x16_bf16 v[64:79], v[124:127], v[60:63], v[64:79]\n"
      "ds_read_b64_tr_b16 v[140:141], v24 offset:12288\n"
      "ds_read_b64_tr_b16 v[142:143], v24 offset:13312\n"
      "ds_read_b128 v[56:59], v7 offset:16384\n"
      "ds_read_b128 v[60:63], v7 offset:20480\n"
      "v_mfma_f32_16x16x32_bf16 v[20:23], v[16:19], v[120:123], v[20:23]\n"
      "v_mfma_f32_16x16x32_bf16 v[20:23], v[16:19], v[124:127], v[20:23]\n"
      "s_waitcnt lgkmcnt(13)\n"
      "v_mfma_f32_32x32x16_bf16 v[80:95], v[128:131], v[32:35], v[80:95]\n"
      "s_waitcnt lgkmcnt(12)\n"
      "v_mfma_f32_32x32x16_bf16 v[64:79], v[128:131], v[36:39], v[64:79]\n"
      "v_mfma_f32_16x16x32_bf16 v[20:23], v[16:19], v[128:131], v[20:23]\n"
      "s_waitcnt lgkmcnt(8)\n"
      "v_mfma_f32_16x16x32_bf16 v[20:23], v[16:19], v[132:135], v[20:23]\n"
      "v_mfma_f32_32x32x16_bf16 v[80:95], v[132:135], v[40:43], v[80:95]\n"
      "s_waitcnt lgkmcnt(0)\n"
      "v_mfma_f32_32x32x16_bf16 v[64:79], v[132:135], v[44:47], v[64:79]\n"
      "s_barrier\n"
      "v_mfma_f32_32x32x16_bf16 v[80:95], v[136:139], v[48:51], v[80:95]\n"
      "ds_read_b64_tr_b16 v[144:145], v24 offset:24576\n"
      "ds_read_b64_tr_b16 v[146:147], v24 offset:25600\n"
      "ds_read_b128 v[32:35], v4 offset:40960\n"
      "ds_read_b128 v[36:39], v4 offset:45056\n"
      "v_mfma_f32_32x32x16_bf16 v[64:79], v[136:139], v[52:55], v[64:79]\n"
      "ds_read_b64_tr_b16 v[148:149], v24 offset:28672\n"
      "ds_read_b64_tr_b16 v[150:151], v24 offset:29696\n"
      "ds_read_b128 v[40:43], v5 offset:40960\n"
      "ds_read_b128 v[44:47], v5 offset:45056\n"
      "v_mfma_f32_32x32x16_bf16 v[80:95], v[140:143], v[56:59], v[80:95]\n"
      "ds_read_b64_tr_b16 v[152:153], v24 offset:32768\n"
      "ds_read_b64_tr_b16 v[154:155], v24 offset:33792\n"
      "ds_read_b128 v[48:51], v6 offset:40960\n"
      "ds_read_b128 v[52:55], v6 offset:45056\n"
      "v_mfma_f32_32x32x16_bf16 v[64:79], v[140:143], v[60:63], v[64:79]\n"
      "ds_read_b64_tr_b16 v[156:157], v24 offset:36864\n"
      "ds_read_b64_tr_b16 v[158:159], v24 offset:37888\n"
      "ds_read_b128 v[56:59], v7 offset:40960\n"
      "ds_read_b128 v[60:63], v7 offset:45056\n"
      "v_mfma_f32_16x16x32_bf16 v[20:23], v[16:19], v[136:139], v[20:23]\n"
      "v_mfma_f32_16x16x32_bf16 v[20:23], v[16:19], v[140:143], v[20:23]\n"
      "s_waitcnt lgkmcnt(13)\n"
      "v_mfma_f32_32x32x16_bf16 v[80:95], v[144:147], v[32:35], v[80:95]\n"
      "s_waitcnt lgkmcnt(12)\n"
      "v_mfma_f32_32x32x16_bf16 v[64:79], v[144:147], v[36:39], v[64:79]\n"
      "v_mfma_f32_16x16x32_bf16 v[20:23], v[16:19], v[144:147], v[20:23]\n"
      "s_waitcnt lgkmcnt(8)\n"
      "v_mfma_f32_16x16x32_bf16 v[20:23], v[16:19], v[148:151], v[20:23]\n"
      "v_mfma_f32_32x32x16_bf16 v[80:95], v[148:151], v[40:43], v[80:95]\n"
      "s_waitcnt lgkmcnt(0)\n"
      "v_add_u32 v24, 49152, v24\n"
      "v_add_u32 v4, 49152, v4\n"
      "v_add_u32 v5, 49152, v5\n"
      "v_add_u32 v6, 49152, v6\n"
      "v_add_u32 v7, 49152, v7\n"
      "v_mfma_f32_32x32x16_bf16 v[64:79], v[148:151], v[44:47], v[64:79]\n"
      "s_barrier\n"
      "v_mfma_f32_32x32x16_bf16 v[80:95], v[152:155], v[48:51], v[80:95]\n"
      "ds_read_b64_tr_b16 v[160:161], v24 offset:0\n"
      "ds_read_b64_tr_b16 v[162:163], v24 offset:1024\n"
      "ds_read_b128 v[32:35], v4 offset:16384\n"
      "ds_read_b128 v[36:39], v4 offset:20480\n"
      "v_mfma_f32_32x32x16_bf16 v[64:79], v[152:155], v[52:55], v[64:79]\n"
      "ds_read_b64_tr_b16 v[164:165], v24 offset:4096\n"
      "ds_read_b64_tr_b16 v[166:167], v24 offset:5120\n"
      "ds_read_b128 v[40:43], v5 offset:16384\n"
      "ds_read_b128 v[44:47], v5 offset:20480\n"
      "v_mfma_f32_32x32x16_bf16 v[80:95], v[156:159], v[56:59], v[80:95]\n"
      "ds_read_b64_tr_b16 v[168:169], v24 offset:8192\n"
      "ds_read_b64_tr_b16 v[170:171], v24 offset:9216\n"
      "ds_read_b128 v[48:51], v6 offset:16384\n"
      "ds_read_b128 v[52:55], v6 offset:20480\n"
      "v_mfma_f32_32x32x16_bf16 v[64:79], v[156:159], v[60:63], v[64:79]\n"
      "ds_read_b64_tr_b16 v[172:173], v24 offset:12288\n"
      "ds_read_b64_tr_b16 v[174:175], v24 offset:13312\n"
      "ds_read_b128 v[56:59], v7 offset:16384\n"
      "ds_read_b128 v[60:63], v7 offset:20480\n"
      "v_mfma_f32_16x16x32_bf16 v[20:23], v[16:19], v[152:155], v[20:23]\n"
      "v_mfma_f32_16x16x32_bf16 v[20:23], v[16:19], v[156:159], v[20:23]\n"
      "s_waitcnt lgkmcnt(13)\n"
      "v_mfma_f32_32x32x16_bf16 v[80:95], v[160:163], v[32:35], v[80:95]\n"
      "s_waitcnt lgkmcnt(12)\n"
      "v_mfma_f32_32x32x16_bf16 v[64:79], v[160:163], v[36:39], v[64:79]\n"
      "v_mfma_f32_16x16x32_bf16 v[20:23], v[16:19], v[160:163], v[20:23]\n"
      "s_waitcnt lgkmcnt(8)\n"
      "v_mfma_f32_16x16x32_bf16 v[20:23], v[16:19], v[164:167], v[20:23]\n"
      "v_mfma_f32_32x32x16_bf16 v[80:95], v[164:167], v[40:43], v[80:95]\n"
      "s_waitcnt lgkmcnt(0)\n"
      "v_add_u32 v24, 4294868992, v24\n"
      "v_add_u32 v4, 4294868992, v4\n"
      "v_add_u32 v5, 4294868992, v5\n"
      "v_add_u32 v6, 4294868992, v6\n"
      "v_add_u32 v7, 4294868992, v7\n"
      "s_waitcnt vmcnt(0)\n"
      "v_mfma_f32_32x32x16_bf16 v[64:79], v[164:167], v[44:47], v[64:79]\n"
      "s_barrier\n"
      "v_mfma_f32_32x32x16_bf16 v[80:95], v[168:171], v[48:51], v[80:95]\n"
      "ds_read_b64_tr_b16 v[176:177], v24 offset:0\n"
      "ds_read_b64_tr_b16 v[178:179], v24 offset:1024\n"
      "ds_read_b128 v[32:35], v4 offset:16384\n"
      "ds_read_b128 v[36:39], v4 offset:20480\n"
      "v_mfma_f32_32x32x16_bf16 v[64:79], v[168:171], v[52:55], v[64:79]\n"
      "ds_read_b64_tr_b16 v[180:181], v24 offset:4096\n"
      "ds_read_b64_tr_b16 v[182:183], v24 offset:5120\n"
      "ds_read_b128 v[40:43], v5 offset:16384\n"
      "ds_read_b128 v[44:47], v5 offset:20480\n"
      "v_mfma_f32_32x32x16_bf16 v[80:95], v[172:175], v[56:59], v[80:95]\n"
      "ds_read_b64_tr_b16 v[184:185], v24 offset:8192\n"
      "ds_read_b64_tr_b16 v[186:187], v24 offset:9216\n"
      "ds_read_b128 v[48:51], v6 offset:16384\n"
      "ds_read_b128 v[52:55], v6 offset:20480\n"
      "v_mfma_f32_32x32x16_bf16 v[64:79], v[172:175], v[60:63], v[64:79]\n"
      "ds_read_b64_tr_b16 v[188:189], v24 offset:12288\n"
      "ds_read_b64_tr_b16 v[190:191], v24 offset:13312\n"
      "ds_read_b128 v[56:59], v7 offset:16384\n"
      "ds_read_b128 v[60:63], v7 offset:20480\n"
      "v_mfma_f32_16x16x32_bf16 v[20:23], v[16:19], v[168:171], v[20:23]\n"
      "v_mfma_f32_16x16x32_bf16 v[20:23], v[16:19], v[172:175], v[20:23]\n"
      "s_waitcnt lgkmcnt(13)\n"
      "v_mfma_f32_32x32x16_bf16 v[80:95], v[176:179], v[32:35], v[80:95]\n"
      "s_waitcnt lgkmcnt(12)\n"
      "v_mfma_f32_32x32x16_bf16 v[64:79], v[176:179], v[36:39], v[64:79]\n"
      "v_mfma_f32_16x16x32_bf16 v[20:23], v[16:19], v[176:179], v[20:23]\n"
      "s_waitcnt lgkmcnt(8)\n"
      "v_mfma_f32_16x16x32_bf16 v[20:23], v[16:19], v[180:183], v[20:23]\n"
      "v_mfma_f32_32x32x16_bf16 v[80:95], v[180:183], v[40:43], v[80:95]\n"
      "s_waitcnt lgkmcnt(0)\n"
      "v_mfma_f32_32x32x16_bf16 v[64:79], v[180:183], v[44:47], v[64:79]\n"
      "s_barrier\n"
      "v_mfma_f32_32x32x16_bf16 v[80:95], v[184:187], v[48:51], v[80:95]\n"
      "ds_read_b64_tr_b16 v[192:193], v24 offset:24576\n"
      "ds_read_b64_tr_b16 v[194:195], v24 offset:25600\n"
      "ds_read_b128 v[32:35], v4 offset:40960\n"
      "ds_read_b128 v[36:39], v4 offset:45056\n"
      "v_mfma_f32_32x32x16_bf16 v[64:79], v[184:187], v[52:55], v[64:79]\n"
      "ds_read_b64_tr_b16 v[196:197], v24 offset:28672\n"
      "ds_read_b64_tr_b16 v[198:199], v24 offset:29696\n"
      "ds_read_b128 v[40:43], v5 offset:40960\n"
      "ds_read_b128 v[44:47], v5 offset:45056\n"
      "v_mfma_f32_32x32x16_bf16 v[80:95], v[188:191], v[56:59], v[80:95]\n"
      "ds_read_b64_tr_b16 v[200:201], v24 offset:32768\n"
      "ds_read_b64_tr_b16 v[202:203], v24 offset:33792\n"
      "ds_read_b128 v[48:51], v6 offset:40960\n"
      "ds_read_b128 v[52:55], v6 offset:45056\n"
      "v_mfma_f32_32x32x16_bf16 v[64:79], v[188:191], v[60:63], v[64:79]\n"
      "ds_read_b64_tr_b16 v[204:205], v24 offset:36864\n"
      "ds_read_b64_tr_b16 v[206:207], v24 offset:37888\n"
      "ds_read_b128 v[56:59], v7 offset:40960\n"
      "ds_read_b128 v[60:63], v7 offset:45056\n"
      "v_mfma_f32_16x16x32_bf16 v[20:23], v[16:19], v[184:187], v[20:23]\n"
      "v_mfma_f32_16x16x32_bf16 v[20:23], v[16:19], v[188:191], v[20:23]\n"
      "s_waitcnt lgkmcnt(13)\n"
      "v_mfma_f32_32x32x16_bf16 v[80:95], v[192:195], v[32:35], v[80:95]\n"
      "s_waitcnt lgkmcnt(12)\n"
      "v_mfma_f32_32x32x16_bf16 v[64:79], v[192:195], v[36:39], v[64:79]\n"
      "v_mfma_f32_16x16x32_bf16 v[20:23], v[16:19], v[192:195], v[20:23]\n"
      "s_waitcnt lgkmcnt(8)\n"
      "v_mfma_f32_16x16x32_bf16 v[20:23], v[16:19], v[196:199], v[20:23]\n"
      "v_mfma_f32_32x32x16_bf16 v[80:95], v[196:199], v[40:43], v[80:95]\n"
      "s_waitcnt lgkmcnt(0)\n"
      "v_add_u32 v24, 49152, v24\n"
      "v_add_u32 v4, 49152, v4\n"
      "v_add_u32 v5, 49152, v5\n"
      "v_add_u32 v6, 49152, v6\n"
      "v_add_u32 v7, 49152, v7\n"
      "v_mfma_f32_32x32x16_bf16 v[64:79], v[196:199], v[44:47], v[64:79]\n"
      "s_barrier\n"
      "v_mfma_f32_32x32x16_bf16 v[80:95], v[200:203], v[48:51], v[80:95]\n"
      "ds_read_b64_tr_b16 v[208:209], v24 offset:0\n"
      "ds_read_b64_tr_b16 v[210:211], v24 offset:1024\n"
      "ds_read_b128 v[32:35], v4 offset:16384\n"
      "ds_read_b128 v[36:39], v4 offset:20480\n"
      "v_mfma_f32_32x32x16_bf16 v[64:79], v[200:203], v[52:55], v[64:79]\n"
      "ds_read_b64_tr_b16 v[212:213], v24 offset:4096\n"
      "ds_read_b64_tr_b16 v[214:215], v24 offset:5120\n"
      "ds_read_b128 v[40:43], v5 offset:16384\n"
      "ds_read_b128 v[44:47], v5 offset:20480\n"
      "v_mfma_f32_32x32x16_bf16 v[80:95], v[204:207], v[56:59], v[80:95]\n"
      "ds_read_b64_tr_b16 v[216:217], v24 offset:8192\n"
      "ds_read_b64_tr_b16 v[218:219], v24 offset:9216\n"
      "ds_read_b128 v[48:51], v6 offset:16384\n"
      "ds_read_b128 v[52:55], v6 offset:20480\n"
      "v_mfma_f32_32x32x16_bf16 v[64:79], v[204:207], v[60:63], v[64:79]\n"
      "ds_read_b64_tr_b16 v[220:221], v24 offset:12288\n"
      "ds_read_b64_tr_b16 v[222:223], v24 offset:13312\n"
      "ds_read_b128 v[56:59], v7 offset:16384\n"
      "ds_read_b128 v[60:63], v7 offset:20480\n"
      "v_mfma_f32_16x16x32_bf16 v[20:23], v[16:19], v[200:203], v[20:23]\n"
      "v_mfma_f32_16x16x32_bf16 v[20:23], v[16:19], v[204:207], v[20:23]\n"
      "s_waitcnt lgkmcnt(13)\n"
      "v_mfma_f32_32x32x16_bf16 v[80:95], v[208:211], v[32:35], v[80:95]\n"
      "s_waitcnt lgkmcnt(12)\n"
      "v_mfma_f32_32x32x16_bf16 v[64:79], v[208:211], v[36:39], v[64:79]\n"
      "v_mfma_f32_16x16x32_bf16 v[20:23], v[16:19], v[208:211], v[20:23]\n"
      "s_waitcnt lgkmcnt(8)\n"
      "v_mfma_f32_16x16x32_bf16 v[20:23], v[16:19], v[212:215], v[20:23]\n"
      "v_mfma_f32_32x32x16_bf16 v[80:95], v[212:215], v[40:43], v[80:95]\n"
      "s_waitcnt lgkmcnt(0)\n"
      "v_mfma_f32_32x32x16_bf16 v[64:79], v[212:215], v[44:47], v[64:79]\n"
      "s_barrier\n"
      "v_mfma_f32_32x32x16_bf16 v[80:95], v[216:219], v[48:51], v[80:95]\n"
      "ds_read_b64_tr_b16 v[224:225], v24 offset:24576\n"
      "ds_read_b64_tr_b16 v[226:227], v24 offset:25600\n"
      "ds_read_b128 v[32:35], v4 offset:40960\n"
      "ds_read_b128 v[36:39], v4 offset:45056\n"
      "v_mfma_f32_32x32x16_bf16 v[64:79], v[216:219], v[52:55], v[64:79]\n"
      "ds_read_b64_tr_b16 v[228:229], v24 offset:28672\n"
      "ds_read_b64_tr_b16 v[230:231], v24 offset:29696\n"
      "ds_read_b128 v[40:43], v5 offset:40960\n"
      "ds_read_b128 v[44:47], v5 offset:45056\n"
      "v_mfma_f32_32x32x16_bf16 v[80:95], v[220:223], v[56:59], v[80:95]\n"
      "ds_read_b64_tr_b16 v[232:233], v24 offset:32768\n"
      "ds_read_b64_tr_b16 v[234:235], v24 offset:33792\n"
      "ds_read_b128 v[48:51], v6 offset:40960\n"
      "ds_read_b128 v[52:55], v6 offset:45056\n"
      "v_mfma_f32_32x32x16_bf16 v[64:79], v[220:223], v[60:63], v[64:79]\n"
      "ds_read_b64_tr_b16 v[236:237], v24 offset:36864\n"
      "ds_read_b64_tr_b16 v[238:239], v24 offset:37888\n"
      "ds_read_b128 v[56:59], v7 offset:40960\n"
      "ds_read_b128 v[60:63], v7 offset:45056\n"
      "v_mfma_f32_16x16x32_bf16 v[20:23], v[16:19], v[216:219], v[20:23]\n"
      "v_mfma_f32_16x16x32_bf16 v[20:23], v[16:19], v[220:223], v[20:23]\n"
      "s_waitcnt lgkmcnt(13)\n"
      "v_mfma_f32_32x32x16_bf16 v[80:95], v[224:227], v[32:35], v[80:95]\n"
      "s_waitcnt lgkmcnt(12)\n"
      "v_mfma_f32_32x32x16_bf16 v[64:79], v[224:227], v[36:39], v[64:79]\n"
      "v_mfma_f32_16x16x32_bf16 v[20:23], v[16:19], v[224:227], v[20:23]\n"
      "s_waitcnt lgkmcnt(8)\n"
      "v_mfma_f32_16x16x32_bf16 v[20:23], v[16:19], v[228:231], v[20:23]\n"
      "v_mfma_f32_32x32x16_bf16 v[80:95], v[228:231], v[40:43], v[80:95]\n"
      "s_waitcnt lgkmcnt(0)\n"
      "v_add_u32 v24, 49152, v24\n"
      "v_add_u32 v4, 49152, v4\n"
      "v_add_u32 v5, 49152, v5\n"
      "v_add_u32 v6, 49152, v6\n"
      "v_add_u32 v7, 49152, v7\n"
      "v_mfma_f32_32x32x16_bf16 v[64:79], v[228:231], v[44:47], v[64:79]\n"
      "s_barrier\n"
      "v_mfma_f32_32x32x16_bf16 v[80:95], v[232:235], v[48:51], v[80:95]\n"
      "ds_read_b64_tr_b16 v[240:241], v24 offset:0\n"
      "ds_read_b64_tr_b16 v[242:243], v24 offset:1024\n"
      "ds_read_b128 v[32:35], v4 offset:16384\n"
      "ds_read_b128 v[36:39], v4 offset:20480\n"
      "v_mfma_f32_32x32x16_bf16 v[64:79], v[232:235], v[52:55], v[64:79]\n"
      "ds_read_b64_tr_b16 v[244:245], v24 offset:4096\n"
      "ds_read_b64_tr_b16 v[246:247], v24 offset:5120\n"
      "ds_read_b128 v[40:43], v5 offset:16384\n"
      "ds_read_b128 v[44:47], v5 offset:20480\n"
      "v_mfma_f32_32x32x16_bf16 v[80:95], v[236:239], v[56:59], v[80:95]\n"
      "ds_read_b64_tr_b16 v[248:249], v24 offset:8192\n"
      "ds_read_b64_tr_b16 v[250:251], v24 offset:9216\n"
      "ds_read_b128 v[48:51], v6 offset:16384\n"
      "ds_read_b128 v[52:55], v6 offset:20480\n"
      "v_mfma_f32_32x32x16_bf16 v[64:79], v[236:239], v[60:63], v[64:79]\n"
      "ds_read_b64_tr_b16 v[252:253], v24 offset:12288\n"
      "ds_read_b64_tr_b16 v[254:255], v24 offset:13312\n"
      "ds_read_b128 v[56:59], v7 offset:16384\n"
      "ds_read_b128 v[60:63], v7 offset:20480\n"
      "v_mfma_f32_16x16x32_bf16 v[20:23], v[16:19], v[232:235], v[20:23]\n"
      "v_mfma_f32_16x16x32_bf16 v[20:23], v[16:19], v[236:239], v[20:23]\n"
      "ds_read_b32 v31, v2 offset:4144\n"
      "ds_read_b64 v[12:13], v30 offset:1040\n"
      "s_waitcnt lgkmcnt(14)\n"
      "v_mfma_f32_16x16x32_bf16 v[20:23], v[16:19], v[240:243], v[20:23]\n"
      "v_subrev_u32 v4, 98304, v4\n"
      "s_waitcnt lgkmcnt(12)\n"
      "v_mfma_f32_16x16x32_bf16 v[20:23], v[16:19], v[244:247], v[20:23]\n"
      "v_subrev_u32 v5, 98304, v5\n"
      "s_waitcnt lgkmcnt(8)\n"
      "v_mfma_f32_16x16x32_bf16 v[20:23], v[16:19], v[248:251], v[20:23]\n"
      "v_subrev_u32 v6, 98304, v6\n"
      "s_waitcnt lgkmcnt(4)\n"
      "v_mfma_f32_16x16x32_bf16 v[20:23], v[16:19], v[252:255], v[20:23]\n"
      "v_subrev_u32 v7, 98304, v7\n"
      "s_waitcnt lgkmcnt(0)\n"
      "v_mfma_f32_32x32x16_bf16 v[80:95], v[240:243], v[32:35], v[80:95]\n"
      "ds_read_b64 v[16:17], v30 offset:16\n"
      "v_readlane_b32 s64, v31, 0\n"
      "s_mov_b32 vcc_hi, 0\n"
      "s_mov_b32 vcc_lo, s64\n"
      "s_nop 0\n"
      "v_mfma_f32_32x32x16_bf16 v[80:95], v[244:247], v[40:43], v[80:95]\n"
      "v_cndmask_b32 v24, v20, v21, s[80:81]\n"
      "v_cvt_pk_bf16_f32 v25, 0, v24\n"
      "v_sub_f32 v24, v24, v25\n"
      "v_cvt_pk_bf16_f32 v26, 0, v24\n"
      "v_sub_f32 v24, v24, v26\n"
      "v_mfma_f32_32x32x16_bf16 v[80:95], v[248:251], v[48:51], v[80:95]\n"
      "v_cvt_pk_bf16_f32 v25, v25, v26\n"
      "v_cvt_pk_bf16_f32 v26, v24, v9\n"
      "v_cndmask_b32 v8, 0, v25, s[82:83]\n"
      "v_cndmask_b32 v9, 0, v26, s[82:83]\n"
      "ds_read_b128 v[24:27], v30\n"
      "v_mfma_f32_32x32x16_bf16 v[80:95], v[252:255], v[56:59], v[80:95]\n"
      "s_waitcnt lgkmcnt(1)\n"
      "v_mfma_f32_32x32x16_bf16 v[80:95], v[8:11], v[16:19], v[80:95]\n"
      "v_mfma_f32_32x32x16_bf16 v[64:79], v[240:243], v[36:39], v[64:79]\n"
      "v_mfma_f32_32x32x16_bf16 v[64:79], v[244:247], v[44:47], v[64:79]\n"
      "v_mfma_f32_32x32x16_bf16 v[64:79], v[248:251], v[52:55], v[64:79]\n"
      "v_mfma_f32_32x32x16_bf16 v[64:79], v[252:255], v[60:63], v[64:79]\n"
      "v_mfma_f32_32x32x16_bf16 v[64:79], v[8:11], v[12:15], v[64:79]\n"
      "ds_read_b64 v[12:13], v30 offset:2064\n"
      "s_barrier\n"
      "s_mov_b64 s[84:85], 0\n"
      "s_waitcnt lgkmcnt(1)\n"
      "s_cbranch_vccnz .Lg_121\n"
      ".Lg_122:\n"
      "v_mov_b32 v48, v24\n"
      "v_mov_b32 v49, v24\n"
      "v_mov_b32 v50, v25\n"
      "v_mov_b32 v51, v25\n"
      "v_mov_b32 v52, v26\n"
      "v_mov_b32 v53, v26\n"
      "ds_read_b128 v[24:27], v30 offset:1024\n"
      "ds_read_b128 v[32:35], v4\n"
      "ds_read_b128 v[36:39], v5\n"
      "ds_read_b128 v[40:43], v6\n"
      "ds_read_b128 v[44:47], v7\n"
      "v_pk_mul_f32 v[54:55], v[80:81], v[80:81]\n"
      "v_pk_mul_f32 v[56:57], v[82:83], v[82:83]\n"
      "v_pk_mul_f32 v[58:59], v[84:85], v[84:85]\n"
      "v_pk_mul_f32 v[60:61], v[86:87], v[86:87]\n"
      "v_pk_fma_f32 v[54:55], v[54:55], v[50:51], v[48:49]\n"
      "v_pk_fma_f32 v[56:57], v[56:57], v[50:51], v[48:49]\n"
      "v_pk_fma_f32 v[58:59], v[58:59], v[50:51], v[48:49]\n"
      "v_pk_fma_f32 v[60:61], v[60:61], v[50:51], v[48:49]\n"
      "v_pk_mul_f32 v[54:55], v[80:81], v[54:55]\n"
      "v_pk_mul_f32 v[56:57], v[82:83], v[56:57]\n"
      "v_pk_mul_f32 v[58:59], v[84:85], v[58:59]\n"
      "v_pk_mul_f32 v[60:61], v[86:87], v[60:61]\n"
      "v_exp_f32 v54, v54\n"
      "v_exp_f32 v55, v55\n"
      "v_exp_f32 v56, v56\n"
      "v_exp_f32 v57, v57\n"
      "v_exp_f32 v58, v58\n"
      "v_exp_f32 v59, v59\n"
      "v_exp_f32 v60, v60\n"
      "v_exp_f32 v61, v61\n"
      "v_pk_fma_f32 v[54:55], v[54:55], v[52:53], v[52:53]\n"
      "v_pk_fma_f32 v[56:57], v[56:57], v[52:53], v[52:53]\n"
      "v_pk_fma_f32 v[58:59], v[58:59], v[52:53], v[52:53]\n"
      "v_pk_fma_f32 v[60:61], v[60:61], v[52:53], v[52:53]\n"
      "v_rcp_f32 v54, v54\n"
      "v_rcp_f32 v55, v55\n"
      "v_rcp_f32 v56, v56\n"
      "v_rcp_f32 v57, v57\n"
      "v_rcp_f32 v58, v58\n"
      "v_rcp_f32 v59, v59\n"
      "v_rcp_f32 v60, v60\n"
      "v_rcp_f32 v61, v61\n"
      "v_pk_mul_f32 v[54:55], v[80:81], v[54:55]\n"
      "v_pk_mul_f32 v[56:57], v[82:83], v[56:57]\n"
      "v_pk_mul_f32 v[58:59], v[84:85], v[58:59]\n"
      "v_pk_mul_f32 v[60:61], v[86:87], v[60:61]\n"
      "v_cvt_pk_bf16_f32 v16, v54, v55\n"
      "v_cvt_pk_bf16_f32 v17, v56, v57\n"
      "v_cvt_pk_bf16_f32 v18, v58, v59\n"
      "v_cvt_pk_bf16_f32 v19, v60, v61\n"
      "buffer_store_dwordx4 v[16:19], v29, s[20:23], s74 offen offset:0 sc1\n"
      "v_pk_mul_f32 v[54:55], v[88:89], v[88:89]\n"
      "v_pk_mul_f32 v[56:57], v[90:91], v[90:91]\n"
      "v_pk_mul_f32 v[58:59], v[92:93], v[92:93]\n"
      "v_pk_mul_f32 v[60:61], v[94:95], v[94:95]\n"
      "v_pk_fma_f32 v[54:55], v[54:55], v[50:51], v[48:49]\n"
      "v_pk_fma_f32 v[56:57], v[56:57], v[50:51], v[48:49]\n"
      "v_pk_fma_f32 v[58:59], v[58:59], v[50:51], v[48:49]\n"
      "v_pk_fma_f32 v[60:61], v[60:61], v[50:51], v[48:49]\n"
      "v_pk_mul_f32 v[54:55], v[88:89], v[54:55]\n"
      "v_pk_mul_f32 v[56:57], v[90:91], v[56:57]\n"
      "v_pk_mul_f32 v[58:59], v[92:93], v[58:59]\n"
      "v_pk_mul_f32 v[60:61], v[94:95], v[60:61]\n"
      "v_exp_f32 v54, v54\n"
      "v_exp_f32 v55, v55\n"
      "v_exp_f32 v56, v56\n"
      "v_exp_f32 v57, v57\n"
      "v_exp_f32 v58, v58\n"
      "v_exp_f32 v59, v59\n"
      "v_exp_f32 v60, v60\n"
      "v_exp_f32 v61, v61\n"
      "v_pk_fma_f32 v[54:55], v[54:55], v[52:53], v[52:53]\n"
      "v_pk_fma_f32 v[56:57], v[56:57], v[52:53], v[52:53]\n"
      "v_pk_fma_f32 v[58:59], v[58:59], v[52:53], v[52:53]\n"
      "v_pk_fma_f32 v[60:61], v[60:61], v[52:53], v[52:53]\n"
      "v_rcp_f32 v54, v54\n"
      "v_rcp_f32 v55, v55\n"
      "v_rcp_f32 v56, v56\n"
      "v_rcp_f32 v57, v57\n"
      "v_rcp_f32 v58, v58\n"
      "v_rcp_f32 v59, v59\n"
      "v_rcp_f32 v60, v60\n"
      "v_rcp_f32 v61, v61\n"
      "v_pk_mul_f32 v[54:55], v[88:89], v[54:55]\n"
      "v_pk_mul_f32 v[56:57], v[90:91], v[56:57]\n"
      "v_pk_mul_f32 v[58:59], v[92:93], v[58:59]\n"
      "v_pk_mul_f32 v[60:61], v[94:95], v[60:61]\n"
      "v_cvt_pk_bf16_f32 v20, v54, v55\n"
      "v_cvt_pk_bf16_f32 v21, v56, v57\n"
      "v_cvt_pk_bf16_f32 v22, v58, v59\n"
      "v_cvt_pk_bf16_f32 v23, v60, v61\n"
      "buffer_store_dwordx4 v[20:23], v29, s[20:23], s74 offen offset:256 sc1\n"
      "ds_read_b128 v[48:51], v4 offset:4096\n"
      "ds_read_b128 v[52:55], v5 offset:4096\n"
      "ds_read_b128 v[56:59], v6 offset:4096\n"
      "ds_read_b128 v[60:63], v7 offset:4096\n"
      "s_waitcnt lgkmcnt(8)\n"
      "v_mfma_f32_32x32x16_bf16 v[80:95], v[8:11], v[12:15], 0\n"
      "s_cbranch_vccnz .Lg_123\n"
      ".Lg_124:\n"
      "ds_read_b64 v[12:13], v30 offset:3088\n"
      "s_nop 0\n"
      "s_waitcnt lgkmcnt(5)\n"
      "v_mfma_f32_32x32x16_bf16 v[80:95], v[96:99], v[32:35], v[80:95]\n"
      "ds_read_b128 v[32:35], v4 offset:8192\n"
      "v_mul_f32 v20, v64, v64\n"
      "v_fma_f32 v20, v20, v25, v24\n"
      "v_mul_f32 v20, v64, v20\n"
      "v_exp_f32 v20, v20\n"
      "v_mfma_f32_32x32x16_bf16 v[80:95], v[100:103], v[36:39], v[80:95]\n"
      "ds_read_b128 v[36:39], v5 offset:8192\n"
      "v_mul_f32 v21, v65, v65\n"
      "v_fma_f32 v20, v20, v26, v26\n"
      "v_fma_f32 v21, v21, v25, v24\n"
      "v_mfma_f32_32x32x16_bf16 v[80:95], v[104:107], v[40:43], v[80:95]\n"
      "ds_read_b128 v[40:43], v6 offset:8192\n"
      "v_rcp_f32 v20, v20\n"
      "v_mul_f32 v21, v65, v21\n"
      "v_mul_f32 v20, v64, v20\n"
      "v_exp_f32 v21, v21\n"
      "v_mfma_f32_32x32x16_bf16 v[80:95], v[108:111], v[44:47], v[80:95]\n"
      "ds_read_b128 v[44:47], v7 offset:8192\n"
      "v_mul_f32 v22, v66, v66\n"
      "v_fma_f32 v21, v21, v26, v26\n"
      "v_fma_f32 v22, v22, v25, v24\n"
      "s_waitcnt lgkmcnt(4)\n"
      "v_mfma_f32_32x32x16_bf16 v[80:95], v[112:115], v[48:51], v[80:95]\n"
      "ds_read_b128 v[48:51], v4 offset:12288\n"
      "v_rcp_f32 v21, v21\n"
      "v_mul_f32 v22, v66, v22\n"
      "v_mul_f32 v21, v65, v21\n"
      "v_cvt_pk_bf16_f32 v16, v20, v21\n"
      "v_mfma_f32_32x32x16_bf16 v[80:95], v[116:119], v[52:55], v[80:95]\n"
      "ds_read_b128 v[52:55], v5 offset:12288\n"
      "v_exp_f32 v22, v22\n"
      "v_mul_f32 v23, v67, v67\n"
      "v_fma_f32 v22, v22, v26, v26\n"
      "v_mfma_f32_32x32x16_bf16 v[80:95], v[120:123], v[56:59], v[80:95]\n"
      "ds_read_b128 v[56:59], v6 offset:12288\n"
      "v_fma_f32 v23, v23, v25, v24\n"
      "v_rcp_f32 v22, v22\n"
      "v_mul_f32 v23, v67, v23\n"
      "v_mul_f32 v22, v66, v22\n"
      "v_mfma_f32_32x32x16_bf16 v[80:95], v[124:127], v[60:63], v[80:95]\n"
      "ds_read_b128 v[60:63], v7 offset:12288\n"
      "v_exp_f32 v23, v23\n"
      "v_mul_f32 v20, v68, v68\n"
      "s_waitcnt lgkmcnt(4)\n"
      "v_mfma_f32_32x32x16_bf16 v[80:95], v[128:131], v[32:35], v[80:95]\n"
      "ds_read_b128 v[32:35], v4 offset:16384\n"
      "v_fma_f32 v23, v23, v26, v26\n"
      "v_fma_f32 v20, v20, v25, v24\n"
      "v_rcp_f32 v23, v23\n"
      "ds_read_b32 v31, v2 offset:2072\n"
      "v_mfma_f32_32x32x16_bf16 v[80:95], v[132:135], v[36:39], v[80:95]\n"
      "ds_read_b128 v[36:39], v5 offset:16384\n"
      "v_mul_f32 v20, v68, v20\n"
      "v_mul_f32 v23, v67, v23\n"
      "v_cvt_pk_bf16_f32 v17, v22, v23\n"
      "v_exp_f32 v20, v20\n"
      "v_mfma_f32_32x32x16_bf16 v[80:95], v[136:139], v[40:43], v[80:95]\n"
      "ds_read_b128 v[40:43], v6 offset:16384\n"
      "v_mul_f32 v21, v69, v69\n"
      "v_fma_f32 v20, v20, v26, v26\n"
      "v_fma_f32 v21, v21, v25, v24\n"
      "v_mfma_f32_32x32x16_bf16 v[80:95], v[140:143], v[44:47], v[80:95]\n"
      "ds_read_b128 v[44:47], v7 offset:16384\n"
      "v_rcp_f32 v20, v20\n"
      "v_mul_f32 v21, v69, v21\n"
      "v_mul_f32 v20, v68, v20\n"
      "s_waitcnt lgkmcnt(3)\n"
      "v_mfma_f32_32x32x16_bf16 v[80:95], v[144:147], v[48:51], v[80:95]\n"
      "v_readfirstlane_b32 s2, v31\n"
      "s_cmp_lt_u32 s2, 4\n"
      "s_cbranch_scc1 .Lw_125\n"
      ".Lw_126:\n"
      "ds_read_b128 v[48:51], v4 offset:20480\n"
      "v_exp_f32 v21, v21\n"
      "v_mfma_f32_32x32x16_bf16 v[80:95], v[148:151], v[52:55], v[80:95]\n"
      "ds_read_b128 v[52:55], v5 offset:20480\n"
      "v_mul_f32 v22, v70, v70\n"
      "v_fma_f32 v21, v21, v26, v26\n"
      "v_fma_f32 v22, v22, v25, v24\n"
      "v_mfma_f32_32x32x16_bf16 v[80:95], v[152:155], v[56:59], v[80:95]\n"
      "ds_read_b128 v[56:59], v6 offset:20480\n"
      "v_rcp_f32 v21, v21\n"
      "v_mul_f32 v22, v70, v22\n"
      "v_mul_f32 v21, v69, v21\n"
      "v_cvt_pk_bf16_f32 v18, v20, v21\n"
      "v_mfma_f32_32x32x16_bf16 v[80:95], v[156:159], v[60:63], v[80:95]\n"
      "ds_read_b128 v[60:63], v7 offset:20480\n"
      "v_exp_f32 v22, v22\n"
      "v_mul_f32 v23, v71, v71\n"
      "v_fma_f32 v22, v22, v26, v26\n"
      "s_waitcnt lgkmcnt(4)\n"
      "v_mfma_f32_32x32x16_bf16 v[80:95], v[160:163], v[32:35], v[80:95]\n"
      "ds_read_b128 v[32:35], v4 offset:24576\n"
      "v_fma_f32 v23, v23, v25, v24\n"
      "v_rcp_f32 v22, v22\n"
      "v_mul_f32 v23, v71, v23\n"
      "v_mfma_f32_32x32x16_bf16 v[80:95], v[164:167], v[36:39], v[80:95]\n"
      "ds_read_b128 v[36:39], v5 offset:24576\n"
      "v_mul_f32 v22, v70, v22\n"
      "v_exp_f32 v23, v23\n"
      "v_mul_f32 v20, v72, v72\n"
      "v_fma_f32 v23, v23, v26, v26\n"
      "v_mfma_f32_32x32x16_bf16 v[80:95], v[168:171], v[40:43], v[80:95]\n"
      "ds_read_b128 v[40:43], v6 offset:24576\n"
      "v_fma_f32 v20, v20, v25, v24\n"
      "v_rcp_f32 v23, v23\n"
      "v_mul_f32 v20, v72, v20\n"
      "v_mfma_f32_32x32x16_bf16 v[80:95], v[172:175], v[44:47], v[80:95]\n"
      "ds_read_b128 v[44:47], v7 offset:24576\n"
      "v_mul_f32 v23, v71, v23\n"
      "v_cvt_pk_bf16_f32 v19, v22, v23\n"
      "buffer_store_dwordx4 v[16:19], v29, s[20:23], s75 offen offset:0 sc1\n"
      "s_waitcnt lgkmcnt(4)\n"
      "v_mfma_f32_32x32x16_bf16 v[80:95], v[176:179], v[48:51], v[80:95]\n"
      "ds_read_b128 v[48:51], v4 offset:28672\n"
      "v_exp_f32 v20, v20\n"
      "v_mul_f32 v21, v73, v73\n"
      "v_mfma_f32_32x32x16_bf16 v[80:95], v[180:183], v[52:55], v[80:95]\n"
      "ds_read_b128 v[52:55], v5 offset:28672\n"
      "v_fma_f32 v20, v20, v26, v26\n"
      "v_fma_f32 v21, v21, v25, v24\n"
      "v_rcp_f32 v20, v20\n"
      "v_mul_f32 v21, v73, v21\n"
      "v_mfma_f32_32x32x16_bf16 v[80:95], v[184:187], v[56:59], v[80:95]\n"
      "ds_read_b128 v[56:59], v6 offset:28672\n"
      "v_mul_f32 v20, v72, v20\n"
      "v_exp_f32 v21, v21\n"
      "v_mul_f32 v22, v74, v74\n"
      "v_mfma_f32_32x32x16_bf16 v[80:95], v[188:191], v[60:63], v[80:95]\n"
      "ds_read_b128 v[60:63], v7 offset:28672\n"
      "v_fma_f32 v21, v21, v26, v26\n"
      "v_fma_f32 v22, v22, v25, v24\n"
      "v_rcp_f32 v21, v21\n"
      "s_waitcnt lgkmcnt(4)\n"
      "v_mfma_f32_32x32x16_bf16 v[80:95], v[192:195], v[32:35], v[80:95]\n"
      "ds_read_b128 v[32:35], v4 offset:32768\n"
      "v_mul_f32 v22, v74, v22\n"
      "v_mul_f32 v21, v73, v21\n"
      "v_cvt_pk_bf16_f32 v16, v20, v21\n"
      "v_exp_f32 v22, v22\n"
      "v_mfma_f32_32x32x16_bf16 v[80:95], v[196:199], v[36:39], v[80:95]\n"
      "ds_read_b128 v[36:39], v5 offset:32768\n"
      "v_mul_f32 v23, v75, v75\n"
      "v_fma_f32 v22, v22, v26, v26\n"
      "v_fma_f32 v23, v23, v25, v24\n"
      "v_mfma_f32_32x32x16_bf16 v[80:95], v[200:203], v[40:43], v[80:95]\n"
      "ds_read_b128 v[40:43], v6 offset:32768\n"
      "v_rcp_f32 v22, v22\n"
      "v_mul_f32 v23, v75, v23\n"
      "v_mul_f32 v22, v74, v22\n"
      "v_exp_f32 v23, v23\n"
      "v_mfma_f32_32x32x16_bf16 v[80:95], v[204:207], v[44:47], v[80:95]\n"
      "ds_read_b128 v[44:47], v7 offset:32768\n"
      "v_mul_f32 v20, v76, v76\n"
      "v_fma_f32 v23, v23, v26, v26\n"
      "v_fma_f32 v20, v20, v25, v24\n"
      "s_waitcnt lgkmcnt(4)\n"
      "v_mfma_f32_32x32x16_bf16 v[80:95], v[208:211], v[48:51], v[80:95]\n"
      "ds_read_b128 v[48:51], v4 offset:36864\n"
      "v_rcp_f32 v23, v23\n"
      "v_mul_f32 v20, v76, v20\n"
      "ds_read_b32 v31, v2 offset:3096\n"
      "v_mfma_f32_32x32x16_bf16 v[80:95], v[212:215], v[52:55], v[80:95]\n"
      "ds_read_b128 v[52:55], v5 offset:36864\n"
      "v_mul_f32 v23, v75, v23\n"
      "v_cvt_pk_bf16_f32 v17, v22, v23\n"
      "v_exp_f32 v20, v20\n"
      "v_mul_f32 v21, v77, v77\n"
      "v_mfma_f32_32x32x16_bf16 v[80:95], v[216:219], v[56:59], v[80:95]\n"
      "ds_read_b128 v[56:59], v6 offset:36864\n"
      "v_fma_f32 v20, v20, v26, v26\n"
      "v_fma_f32 v21, v21, v25, v24\n"
      "v_rcp_f32 v20, v20\n"
      "v_mfma_f32_32x32x16_bf16 v[80:95], v[220:223], v[60:63], v[80:95]\n"
      "ds_read_b128 v[60:63], v7 offset:36864\n"
      "v_mul_f32 v21, v77, v21\n"
      "v_mul_f32 v20, v76, v20\n"
      "v_exp_f32 v21, v21\n"
      "s_waitcnt lgkmcnt(3)\n"
      "v_mfma_f32_32x32x16_bf16 v[80:95], v[224:227], v[32:35], v[80:95]\n"
      "v_readfirstlane_b32 s2, v31\n"
      "s_cmp_lt_u32 s2, 4\n"
      "s_cbranch_scc1 .Lw_128\n"
      ".Lw_129:\n"
      "ds_read_b128 v[32:35], v4 offset:49152\n"
      "v_mul_f32 v22, v78, v78\n"
      "v_mfma_f32_32x32x16_bf16 v[80:95], v[228:231], v[36:39], v[80:95]\n"
      "ds_read_b128 v[36:39], v5 offset:49152\n"
      "v_fma_f32 v21, v21, v26, v26\n"
      "v_fma_f32 v22, v22, v25, v24\n"
      "v_rcp_f32 v21, v21\n"
      "v_mfma_f32_32x32x16_bf16 v[80:95], v[232:235], v[40:43], v[80:95]\n"
      "ds_read_b128 v[40:43], v6 offset:49152\n"
      "v_mul_f32 v22, v78, v22\n"
      "v_mul_f32 v21, v77, v21\n"
      "v_cvt_pk_bf16_f32 v18, v20, v21\n"
      "v_exp_f32 v22, v22\n"
      "v_mfma_f32_32x32x16_bf16 v[80:95], v[236:239], v[44:47], v[80:95]\n"
      "ds_read_b128 v[44:47], v7 offset:49152\n"
      "v_mul_f32 v23, v79, v79\n"
      "v_fma_f32 v22, v22, v26, v26\n"
      "v_fma_f32 v23, v23, v25, v24\n"
      "s_waitcnt lgkmcnt(4)\n"
      "v_mfma_f32_32x32x16_bf16 v[80:95], v[240:243], v[48:51], v[80:95]\n"
      "ds_read_b128 v[48:51], v4 offset:53248\n"
      "v_rcp_f32 v22, v22\n"
      "v_mul_f32 v23, v79, v23\n"
      "v_mul_f32 v22, v78, v22\n"
      "v_exp_f32 v23, v23\n"
      "v_mfma_f32_32x32x16_bf16 v[80:95], v[244:247], v[52:55], v[80:95]\n"
      "ds_read_b128 v[52:55], v5 offset:53248\n"
      "v_fma_f32 v23, v23, v26, v26\n"
      "v_rcp_f32 v23, v23\n"
      "s_nop 0\n"
      "v_mul_f32 v23, v79, v23\n"
      "v_mfma_f32_32x32x16_bf16 v[80:95], v[248:251], v[56:59], v[80:95]\n"
      "ds_read_b128 v[56:59], v6 offset:53248\n"
      "v_cvt_pk_bf16_f32 v19, v22, v23\n"
      "buffer_store_dwordx4 v[16:19], v29, s[20:23], s75 offen offset:256 sc1\n"
      "ds_read_b128 v[24:27], v30 offset:2048\n"
      "v_mfma_f32_32x32x16_bf16 v[64:79], v[252:255], v[60:63], v[80:95]\n"
      "ds_read_b128 v[60:63], v7 offset:53248\n"
      "s_nop 3\n"
      "v_mfma_f32_32x32x16_bf16 v[80:95], v[8:11], v[12:15], 0\n"
      "s_cbranch_vccnz .Lg_131\n"
      ".Lg_132:\n"
      "s_nop 1\n"
      "s_waitcnt lgkmcnt(1)\n"
      "v_mfma_f32_32x32x16_bf16 v[80:95], v[96:99], v[32:35], v[80:95]\n"
      "ds_read_b128 v[32:35], v4 offset:57344\n"
      "v_mul_f32 v20, v64, v64\n"
      "v_fma_f32 v20, v20, v25, v24\n"
      "v_mul_f32 v20, v64, v20\n"
      "v_exp_f32 v20, v20\n"
      "v_mfma_f32_32x32x16_bf16 v[80:95], v[100:103], v[36:39], v[80:95]\n"
      "ds_read_b128 v[36:39], v5 offset:57344\n"
      "v_mul_f32 v21, v65, v65\n"
      "v_fma_f32 v20, v20, v26, v26\n"
      "v_fma_f32 v21, v21, v25, v24\n"
      "v_mov_b64 v[12:13], s[58:59]\n"
      "v_mfma_f32_32x32x16_bf16 v[80:95], v[104:107], v[40:43], v[80:95]\n"
      "ds_read_b128 v[40:43], v6 offset:57344\n"
      "v_rcp_f32 v20, v20\n"
      "v_mul_f32 v21, v65, v21\n"
      "v_mul_f32 v20, v64, v20\n"
      "v_exp_f32 v21, v21\n"
      "v_mfma_f32_32x32x16_bf16 v[80:95], v[108:111], v[44:47], v[80:95]\n"
      "ds_read_b128 v[44:47], v7 offset:57344\n"
      "v_mul_f32 v22, v66, v66\n"
      "v_fma_f32 v21, v21, v26, v26\n"
      "v_fma_f32 v22, v22, v25, v24\n"
      "s_waitcnt lgkmcnt(4)\n"
      "v_mfma_f32_32x32x16_bf16 v[80:95], v[112:115], v[48:51], v[80:95]\n"
      "ds_read_b128 v[48:51], v4 offset:61440\n"
      "v_rcp_f32 v21, v21\n"
      "v_mul_f32 v22, v66, v22\n"
      "v_mul_f32 v21, v65, v21\n"
      "v_cvt_pk_bf16_f32 v16, v20, v21\n"
      "v_mfma_f32_32x32x16_bf16 v[80:95], v[116:119], v[52:55], v[80:95]\n"
      "ds_read_b128 v[52:55], v5 offset:61440\n"
      "v_exp_f32 v22, v22\n"
      "v_mul_f32 v23, v67, v67\n"
      "v_fma_f32 v22, v22, v26, v26\n"
      "v_add_u32 v4, 49152, v4\n"
      "v_mfma_f32_32x32x16_bf16 v[80:95], v[120:123], v[56:59], v[80:95]\n"
      "ds_read_b128 v[56:59], v6 offset:61440\n"
      "v_fma_f32 v23, v23, v25, v24\n"
      "v_rcp_f32 v22, v22\n"
      "v_mul_f32 v23, v67, v23\n"
      "v_add_u32 v5, 49152, v5\n"
      "v_mfma_f32_32x32x16_bf16 v[80:95], v[124:127], v[60:63], v[80:95]\n"
      "ds_read_b128 v[60:63], v7 offset:61440\n"
      "v_mul_f32 v22, v66, v22\n"
      "v_exp_f32 v23, v23\n"
      "v_add_u32 v6, 49152, v6\n"
      "s_waitcnt lgkmcnt(4)\n"
      "v_mfma_f32_32x32x16_bf16 v[80:95], v[128:131], v[32:35], v[80:95]\n"
      "ds_read_b128 v[32:35], v4 offset:16384\n"
      "v_mul_f32 v20, v68, v68\n"
      "v_fma_f32 v23, v23, v26, v26\n"
      "v_fma_f32 v20, v20, v25, v24\n"
      "v_add_u32 v7, 49152, v7\n"
      "v_mfma_f32_32x32x16_bf16 v[80:95], v[132:135], v[36:39], v[80:95]\n"
      "ds_read_b128 v[36:39], v5 offset:16384\n"
      "v_rcp_f32 v23, v23\n"
      "v_mul_f32 v20, v68, v20\n"
      "v_mul_f32 v23, v67, v23\n"
      "v_cvt_pk_bf16_f32 v17, v22, v23\n"
      "v_mfma_f32_32x32x16_bf16 v[80:95], v[136:139], v[40:43], v[80:95]\n"
      "ds_read_b128 v[40:43], v6 offset:16384\n"
      "v_exp_f32 v20, v20\n"
      "v_mul_f32 v21, v69, v69\n"
      "v_fma_f32 v20, v20, v26, v26\n"
      "v_fma_f32 v21, v21, v25, v24\n"
      "v_mfma_f32_32x32x16_bf16 v[80:95], v[140:143], v[44:47], v[80:95]\n"
      "ds_read_b128 v[44:47], v7 offset:16384\n"
      "v_rcp_f32 v20, v20\n"
      "v_mul_f32 v21, v69, v21\n"
      "v_mul_f32 v20, v68, v20\n"
      "s_waitcnt lgkmcnt(4)\n"
      "v_mfma_f32_32x32x16_bf16 v[80:95], v[144:147], v[48:51], v[80:95]\n"
      "ds_read_b128 v[48:51], v4 offset:20480\n"
      "v_exp_f32 v21, v21\n"
      "v_mul_f32 v22, v70, v70\n"
      "v_fma_f32 v21, v21, v26, v26\n"
      "v_fma_f32 v22, v22, v25, v24\n"
      "v_mfma_f32_32x32x16_bf16 v[80:95], v[148:151], v[52:55], v[80:95]\n"
      "ds_read_b128 v[52:55], v5 offset:20480\n"
      "s_getreg_b32 s2, hwreg(HW_REG_IB_STS, 0, 4)\n"
      "s_add_u32 s2, s2, s84\n"
      "s_cmp_le_u32 s2, 0\n"
      "s_cbranch_scc1 .Lh_133\n"
      ".Lh_134:\n"
      "v_mfma_f32_32x32x16_bf16 v[80:95], v[152:155], v[56:59], v[80:95]\n"
      "ds_read_b128 v[56:59], v6 offset:20480\n"
      "v_rcp_f32 v21, v21\n"
      "v_mul_f32 v22, v70, v22\n"
      "v_mul_f32 v21, v69, v21\n"
      "v_cvt_pk_bf16_f32 v18, v20, v21\n"
      "v_mfma_f32_32x32x16_bf16 v[80:95], v[156:159], v[60:63], v[80:95]\n"
      "ds_read_b128 v[60:63], v7 offset:20480\n"
      "v_exp_f32 v22, v22\n"
      "v_mul_f32 v23, v71, v71\n"
      "v_fma_f32 v22, v22, v26, v26\n"
      "s_waitcnt lgkmcnt(4)\n"
      "v_mfma_f32_32x32x16_bf16 v[80:95], v[160:163], v[32:35], v[80:95]\n"
      "ds_read_b128 v[32:35], v4 offset:24576\n"
      "v_fma_f32 v23, v23, v25, v24\n"
      "v_rcp_f32 v22, v22\n"
      "v_mul_f32 v23, v71, v23\n"
      "v_mul_f32 v22, v70, v22\n"
      "v_mfma_f32_32x32x16_bf16 v[80:95], v[164:167], v[36:39], v[80:95]\n"
      "ds_read_b128 v[36:39], v5 offset:24576\n"
      "v_exp_f32 v23, v23\n"
      "v_mul_f32 v20, v72, v72\n"
      "v_fma_f32 v23, v23, v26, v26\n"
      "v_fma_f32 v20, v20, v25, v24\n"
      "v_mfma_f32_32x32x16_bf16 v[80:95], v[168:171], v[40:43], v[80:95]\n"
      "ds_read_b128 v[40:43], v6 offset:24576\n"
      "v_rcp_f32 v23, v23\n"
      "v_mul_f32 v20, v72, v20\n"
      "v_mul_f32 v23, v71, v23\n"
      "v_cvt_pk_bf16_f32 v19, v22, v23\n"
      "v_mfma_f32_32x32x16_bf16 v[80:95], v[172:175], v[44:47], v[80:95]\n"
      "ds_read_b128 v[44:47], v7 offset:24576\n"
      "buffer_store_dwordx4 v[16:19], v29, s[20:23], s76 offen offset:0 sc1\n"
      "s_waitcnt lgkmcnt(4)\n"
      "v_mfma_f32_32x32x16_bf16 v[80:95], v[176:179], v[48:51], v[80:95]\n"
      "ds_read_b128 v[48:51], v4 offset:28672\n"
      "v_exp_f32 v20, v20\n"
      "v_mul_f32 v21, v73, v73\n"
      "v_fma_f32 v20, v20, v26, v26\n"
      "v_fma_f32 v21, v21, v25, v24\n"
      "v_mfma_f32_32x32x16_bf16 v[80:95], v[180:183], v[52:55], v[80:95]\n"
      "ds_read_b128 v[52:55], v5 offset:28672\n"
      "s_getreg_b32 s2, hwreg(HW_REG_IB_STS, 0, 4)\n"
      "s_add_u32 s2, s2, s84\n"
      "s_cmp_le_u32 s2, 1\n"
      "s_cbranch_scc1 .Lh_135\n"
      ".Lh_136:\n"
      "v_mfma_f32_32x32x16_bf16 v[80:95], v[184:187], v[56:59], v[80:95]\n"
      "ds_read_b128 v[56:59], v6 offset:28672\n"
      "v_rcp_f32 v20, v20\n"
      "v_mul_f32 v21, v73, v21\n"
      "v_mul_f32 v20, v72, v20\n"
      "v_exp_f32 v21, v21\n"
      "v_mfma_f32_32x32x16_bf16 v[80:95], v[188:191], v[60:63], v[80:95]\n"
      "ds_read_b128 v[60:63], v7 offset:28672\n"
      "v_mul_f32 v22, v74, v74\n"
      "v_fma_f32 v21, v21, v26, v26\n"
      "v_fma_f32 v22, v22, v25, v24\n"
      "s_waitcnt lgkmcnt(4)\n"
      "v_mfma_f32_32x32x16_bf16 v[80:95], v[192:195], v[32:35], v[80:95]\n"
      "ds_read_b128 v[32:35], v4 offset:32768\n"
      "v_rcp_f32 v21, v21\n"
      "v_mul_f32 v22, v74, v22\n"
      "v_mul_f32 v21, v73, v21\n"
      "v_cvt_pk_bf16_f32 v16, v20, v21\n"
      "v_mfma_f32_32x32x16_bf16 v[80:95], v[196:199], v[36:39], v[80:95]\n"
      "ds_read_b128 v[36:39], v5 offset:32768\n"
      "v_exp_f32 v22, v22\n"
      "v_mul_f32 v23, v75, v75\n"
      "v_fma_f32 v22, v22, v26, v26\n"
      "v_fma_f32 v23, v23, v25, v24\n"
      "v_mfma_f32_32x32x16_bf16 v[80:95], v[200:203], v[40:43], v[80:95]\n"
      "ds_read_b128 v[40:43], v6 offset:32768\n"
      "v_rcp_f32 v22, v22\n"
      "v_mul_f32 v23, v75, v23\n"
      "v_mul_f32 v22, v74, v22\n"
      "v_mfma_f32_32x32x16_bf16 v[80:95], v[204:207], v[44:47], v[80:95]\n"
      "ds_read_b128 v[44:47], v7 offset:32768\n"
      "v_exp_f32 v23, v23\n"
      "v_mul_f32 v20, v76, v76\n"
      "v_fma_f32 v23, v23, v26, v26\n"
      "s_waitcnt lgkmcnt(4)\n"
      "v_mfma_f32_32x32x16_bf16 v[80:95], v[208:211], v[48:51], v[80:95]\n"
      "ds_read_b128 v[48:51], v4 offset:36864\n"
      "v_fma_f32 v20, v20, v25, v24\n"
      "v_rcp_f32 v23, v23\n"
      "v_mul_f32 v20, v76, v20\n"
      "v_mul_f32 v23, v75, v23\n"
      "v_mfma_f32_32x32x16_bf16 v[80:95], v[212:215], v[52:55], v[80:95]\n"
      "ds_read_b128 v[52:55], v5 offset:36864\n"
      "s_getreg_b32 s2, hwreg(HW_REG_IB_STS, 0, 4)\n"
      "s_add_u32 s2, s2, s84\n"
      "s_cmp_le_u32 s2, 1\n"
      "s_cbranch_scc1 .Lh_137\n"
      ".Lh_138:\n"
      "v_mfma_f32_32x32x16_bf16 v[80:95], v[216:219], v[56:59], v[80:95]\n"
      "ds_read_b128 v[56:59], v6 offset:36864\n"
      "v_cvt_pk_bf16_f32 v17, v22, v23\n"
      "v_exp_f32 v20, v20\n"
      "v_mul_f32 v21, v77, v77\n"
      "v_fma_f32 v20, v20, v26, v26\n"
      "v_mfma_f32_32x32x16_bf16 v[80:95], v[220:223], v[60:63], v[80:95]\n"
      "ds_read_b128 v[60:63], v7 offset:36864\n"
      "v_fma_f32 v21, v21, v25, v24\n"
      "v_rcp_f32 v20, v20\n"
      "v_mul_f32 v21, v77, v21\n"
      "s_waitcnt lgkmcnt(4)\n"
      "v_mfma_f32_32x32x16_bf16 v[80:95], v[224:227], v[32:35], v[80:95]\n"
      "v_mul_f32 v20, v76, v20\n"
      "v_exp_f32 v21, v21\n"
      "v_mul_f32 v22, v78, v78\n"
      "v_fma_f32 v21, v21, v26, v26\n"
      "v_fma_f32 v22, v22, v25, v24\n"
      "v_mfma_f32_32x32x16_bf16 v[80:95], v[228:231], v[36:39], v[80:95]\n"
      "v_rcp_f32 v21, v21\n"
      "v_mul_f32 v22, v78, v22\n"
      "v_mul_f32 v21, v77, v21\n"
      "v_xor_b32 v31, s86, v28\n"
      "ds_read_b64 v[218:219], v31\n"
      "v_mfma_f32_32x32x16_bf16 v[80:95], v[232:235], v[40:43], v[80:95]\n"
      "v_cvt_pk_bf16_f32 v18, v20, v21\n"
      "v_exp_f32 v22, v22\n"
      "v_mul_f32 v23, v79, v79\n"
      "v_xor_b32 v31, s87, v28\n"
      "ds_read_b64 v[220:221], v31\n"
      "v_mfma_f32_32x32x16_bf16 v[80:95], v[236:239], v[44:47], v[80:95]\n"
      "v_fma_f32 v22, v22, v26, v26\n"
      "v_fma_f32 v23, v23, v25, v24\n"
      "v_xor_b32 v31, s88, v28\n"
      "ds_read_b64 v[222:223], v31\n"
      "s_waitcnt lgkmcnt(3)\n"
      "v_mfma_f32_32x32x16_bf16 v[80:95], v[240:243], v[48:51], v[80:95]\n"
      "v_rcp_f32 v22, v22\n"
      "v_mul_f32 v23, v79, v23\n"
      "v_mul_f32 v22, v78, v22\n"
      "v_xor_b32 v31, s89, v28\n"
      "ds_read_b64 v[224:225], v31\n"
      "v_mfma_f32_32x32x16_bf16 v[80:95], v[244:247], v[52:55], v[80:95]\n"
      "v_exp_f32 v23, v23\n"
      "s_nop 0\n"
      "v_fma_f32 v23, v23, v26, v26\n"
      "v_rcp_f32 v23, v23\n"
      "v_xor_b32 v31, s90, v28\n"
      "ds_read_b64 v[226:227], v31\n"
      "v_mfma_f32_32x32x16_bf16 v[80:95], v[248:251], v[56:59], v[80:95]\n"
      "v_mul_f32 v23, v79, v23\n"
      "v_cvt_pk_bf16_f32 v19, v22, v23\n"
      "buffer_store_dwordx4 v[16:19], v29, s[20:23], s76 offen offset:256 sc1\n"
      "ds_read_b128 v[24:27], v30 offset:3072\n"
      "v_mfma_f32_32x32x16_bf16 v[64:79], v[252:255], v[60:63], v[80:95]\n"
      "ds_add_u32 v2, v3 offset:4160\n"
      "s_getreg_b32 s2, hwreg(HW_REG_IB_STS, 0, 4)\n"
      "s_add_u32 s2, s2, s84\n"
      "s_cmp_le_u32 s2, 2\n"
      "s_cbranch_scc1 .Lh_139\n"
      ".Lh_140:\n"
      "s_waitcnt lgkmcnt(0)\n"
      "s_cbranch_vccnz .Lg_141\n"
      ".Lg_142:\n"
      "v_mov_b32 v32, v24\n"
      "v_mov_b32 v33, v24\n"
      "v_mov_b32 v34, v25\n"
      "v_mov_b32 v35, v25\n"
      "v_mov_b32 v36, v26\n"
      "v_mov_b32 v37, v26\n"
      "v_pk_mul_f32 v[38:39], v[64:65], v[64:65]\n"
      "v_pk_mul_f32 v[40:41], v[66:67], v[66:67]\n"
      "v_pk_mul_f32 v[42:43], v[68:69], v[68:69]\n"
      "v_pk_mul_f32 v[44:45], v[70:71], v[70:71]\n"
      "v_pk_fma_f32 v[38:39], v[38:39], v[34:35], v[32:33]\n"
      "v_pk_fma_f32 v[40:41], v[40:41], v[34:35], v[32:33]\n"
      "v_pk_fma_f32 v[42:43], v[42:43], v[34:35], v[32:33]\n"
      "v_pk_fma_f32 v[44:45], v[44:45], v[34:35], v[32:33]\n"
      "v_pk_mul_f32 v[38:39], v[64:65], v[38:39]\n"
      "v_pk_mul_f32 v[40:41], v[66:67], v[40:41]\n"
      "v_pk_mul_f32 v[42:43], v[68:69], v[42:43]\n"
      "v_pk_mul_f32 v[44:45], v[70:71], v[44:45]\n"
      "v_exp_f32 v38, v38\n"
      "v_exp_f32 v39, v39\n"
      "v_exp_f32 v40, v40\n"
      "v_exp_f32 v41, v41\n"
      "v_exp_f32 v42, v42\n"
      "v_exp_f32 v43, v43\n"
      "v_exp_f32 v44, v44\n"
      "v_exp_f32 v45, v45\n"
      "v_pk_fma_f32 v[38:39], v[38:39], v[36:37], v[36:37]\n"
      "v_pk_fma_f32 v[40:41], v[40:41], v[36:37], v[36:37]\n"
      "v_pk_fma_f32 v[42:43], v[42:43], v[36:37], v[36:37]\n"
      "v_pk_fma_f32 v[44:45], v[44:45], v[36:37], v[36:37]\n"
      "v_rcp_f32 v38, v38\n"
      "v_rcp_f32 v39, v39\n"
      "v_rcp_f32 v40, v40\n"
      "v_rcp_f32 v41, v41\n"
      "v_rcp_f32 v42, v42\n"
      "v_rcp_f32 v43, v43\n"
      "v_rcp_f32 v44, v44\n"
      "v_rcp_f32 v45, v45\n"
      "v_pk_mul_f32 v[38:39], v[64:65], v[38:39]\n"
      "v_pk_mul_f32 v[40:41], v[66:67], v[40:41]\n"
      "v_pk_mul_f32 v[42:43], v[68:69], v[42:43]\n"
      "v_pk_mul_f32 v[44:45], v[70:71], v[44:45]\n"
      "v_cvt_pk_bf16_f32 v16, v38, v39\n"
      "v_cvt_pk_bf16_f32 v17, v40, v41\n"
      "v_cvt_pk_bf16_f32 v18, v42, v43\n"
      "v_cvt_pk_bf16_f32 v19, v44, v45\n"
      "buffer_store_dwordx4 v[16:19], v29, s[20:23], s77 offen offset:0 sc1\n"
      "v_pk_mul_f32 v[38:39], v[72:73], v[72:73]\n"
      "v_pk_mul_f32 v[40:41], v[74:75], v[74:75]\n"
      "v_pk_mul_f32 v[42:43], v[76:77], v[76:77]\n"
      "v_pk_mul_f32 v[44:45], v[78:79], v[78:79]\n"
      "v_pk_fma_f32 v[38:39], v[38:39], v[34:35], v[32:33]\n"
      "v_pk_fma_f32 v[40:41], v[40:41], v[34:35], v[32:33]\n"
      "v_pk_fma_f32 v[42:43], v[42:43], v[34:35], v[32:33]\n"
      "v_pk_fma_f32 v[44:45], v[44:45], v[34:35], v[32:33]\n"
      "v_pk_mul_f32 v[38:39], v[72:73], v[38:39]\n"
      "v_pk_mul_f32 v[40:41], v[74:75], v[40:41]\n"
      "v_pk_mul_f32 v[42:43], v[76:77], v[42:43]\n"
      "v_pk_mul_f32 v[44:45], v[78:79], v[44:45]\n"
      "v_exp_f32 v38, v38\n"
      "v_exp_f32 v39, v39\n"
      "v_exp_f32 v40, v40\n"
      "v_exp_f32 v41, v41\n"
      "v_exp_f32 v42, v42\n"
      "v_exp_f32 v43, v43\n"
      "v_exp_f32 v44, v44\n"
      "v_exp_f32 v45, v45\n"
      "v_pk_fma_f32 v[38:39], v[38:39], v[36:37], v[36:37]\n"
      "v_pk_fma_f32 v[40:41], v[40:41], v[36:37], v[36:37]\n"
      "v_pk_fma_f32 v[42:43], v[42:43], v[36:37], v[36:37]\n"
      "v_pk_fma_f32 v[44:45], v[44:45], v[36:37], v[36:37]\n"
      "ds_read_b32 v31, v2 offset:4128\n"
      "v_rcp_f32 v38, v38\n"
      "v_rcp_f32 v39, v39\n"
      "v_rcp_f32 v40, v40\n"
      "v_rcp_f32 v41, v41\n"
      "v_rcp_f32 v42, v42\n"
      "v_rcp_f32 v43, v43\n"
      "v_rcp_f32 v44, v44\n"
      "v_rcp_f32 v45, v45\n"
      "v_pk_mul_f32 v[38:39], v[72:73], v[38:39]\n"
      "v_pk_mul_f32 v[40:41], v[74:75], v[40:41]\n"
      "v_pk_mul_f32 v[42:43], v[76:77], v[42:43]\n"
      "v_pk_mul_f32 v[44:45], v[78:79], v[44:45]\n"
      "v_cvt_pk_bf16_f32 v20, v38, v39\n"
      "v_cvt_pk_bf16_f32 v21, v40, v41\n"
      "v_cvt_pk_bf16_f32 v22, v42, v43\n"
      "v_cvt_pk_bf16_f32 v23, v44, v45\n"
      "buffer_store_dwordx4 v[20:23], v29, s[20:23], s77 offen offset:256 sc1\n"
      "s_getreg_b32 s2, hwreg(HW_REG_IB_STS, 0, 4)\n"
      "s_add_u32 s2, s2, s84\n"
      "s_cmp_le_u32 s2, 4\n"
      "s_cbranch_scc1 .Lh_143\n"
      ".Lh_144:\n"
      ".LCONV:\n"
      "v_and_b32 v10, 1, v1\n"
      "v_xor_b32 v10, 1, v10\n"
      "v_mul_u32_u24 v10, 4160, v10\n"
      "v_and_b32 v11, 14, v1\n"
      "v_lshl_add_u32 v10, v11, 1, v10\n"
      "v_and_b32 v11, 48, v1\n"
      "v_sub_u32 v10, v10, v11\n"
      "v_and_b32 v21, 15, v1\n"
      "v_lshl_add_u32 v11, v21, 5, v11\n"
      "s_lshl_b32 s62, s53, 7\n"
      "s_add_u32 s63, s62, 98388\n"
      "v_add_u32 v208, s63, v10\n"
      "s_lshl_b32 s63, s54, 9\n"
      "s_sub_u32 s63, s63, s62\n"
      "s_add_u32 s63, s63, 149152\n"
      "v_add_u32 v216, s63, v11\n"
      "v_add_u32 v209, 1024, v208\n"
      "s_waitcnt lgkmcnt(0)\n"
      "v_readfirstlane_b32 s2, v31\n"
      "s_cmp_lt_u32 s2, 4\n"
      "s_cbranch_scc1 .Lw_145\n"
      ".Lw_146:\n"
      "ds_read2_b32 v[144:145], v208 offset0:3 offset1:2\n"
      "ds_read2_b32 v[146:147], v208 offset0:1 offset1:0\n"
      "ds_read_b128 v[204:207], v216 offset:3648\n"
      "ds_read_b128 v[196:199], v216 offset:2624\n"
      "ds_read_b128 v[188:191], v216 offset:1600\n"
      "ds_read_b128 v[180:183], v216 offset:576\n"
      "ds_read2_b32 v[148:149], v208 offset0:131 offset1:130\n"
      "ds_read2_b32 v[150:151], v208 offset0:129 offset1:128\n"
      "ds_read_b128 v[200:203], v216 offset:3136\n"
      "ds_read_b128 v[192:195], v216 offset:2112\n"
      "ds_read_b128 v[184:187], v216 offset:1088\n"
      "ds_read_b128 v[176:179], v216 offset:64\n"
      "ds_read2_b32 v[152:153], v209 offset0:3 offset1:2\n"
      "ds_read2_b32 v[154:155], v209 offset0:1 offset1:0\n"
      "v_add_u32 v210, 2048, v208\n"
      "v_add_u32 v211, 3072, v208\n"
      "s_lshr_b32 s64, s53, 1\n"
      "s_and_b32 s65, s53, 1\n"
      "s_lshl_b32 s66, s65, 8\n"
      "s_mul_i32 s62, s64, 8320\n"
      "s_add_u32 s62, s62, s66\n"
      "s_add_u32 s62, s62, 122964\n"
      "v_add_u32 v212, s62, v10\n"
      "s_mul_i32 s62, s64, 4608\n"
      "s_sub_u32 s63, s62, s66\n"
      "s_add_u32 s63, s63, 139808\n"
      "v_add_u32 v217, s63, v11\n"
      "v_add_u32 v213, 1024, v212\n"
      "v_add_u32 v214, 2048, v212\n"
      "v_add_u32 v215, 3072, v212\n"
      "s_waitcnt lgkmcnt(11)\n"
      "v_mfma_f32_16x16x32_bf16 v[140:143], v[144:147], v[204:207], 0\n"
      "ds_read2_b32 v[156:157], v209 offset0:131 offset1:130\n"
      "ds_read2_b32 v[158:159], v209 offset0:129 offset1:128\n"
      "s_waitcnt lgkmcnt(12)\n"
      "v_mfma_f32_16x16x32_bf16 v[136:139], v[144:147], v[196:199], 0\n"
      "ds_read2_b32 v[160:161], v210 offset0:3 offset1:2\n"
      "s_waitcnt lgkmcnt(12)\n"
      "v_mfma_f32_16x16x32_bf16 v[132:135], v[144:147], v[188:191], 0\n"
      "ds_read2_b32 v[162:163], v210 offset0:1 offset1:0\n"
      "s_waitcnt lgkmcnt(12)\n"
      "v_mfma_f32_16x16x32_bf16 v[128:131], v[144:147], v[180:183], 0\n"
      "ds_read2_b32 v[164:165], v210 offset0:131 offset1:130\n"
      "s_waitcnt lgkmcnt(10)\n"
      "v_mfma_f32_16x16x32_bf16 v[140:143], v[148:151], v[200:203], v[140:143]\n"
      "ds_read2_b32 v[166:167], v210 offset0:129 offset1:128\n"
      "v_lshlrev_b32 v9, 4, v1\n"
      "s_waitcnt lgkmcnt(10)\n"
      "v_mfma_f32_16x16x32_bf16 v[136:139], v[148:151], v[192:195], v[136:139]\n"
      "ds_read2_b32 v[168:169], v211 offset0:3 offset1:2\n"
      "s_lshl_b32 s62, s53, 12\n"
      "s_waitcnt lgkmcnt(7)\n"
      "v_mfma_f32_16x16x32_bf16 v[132:135], v[148:151], v[184:187], v[132:135]\n"
      "ds_read2_b32 v[170:171], v211 offset0:1 offset1:0\n"
      "v_add_u32 v14, s62, v9\n"
      "v_mfma_f32_16x16x32_bf16 v[128:131], v[148:151], v[176:179], v[128:131]\n"
      "ds_read2_b32 v[172:173], v211 offset0:131 offset1:130\n"
      "s_xor_b32 s62, s62, 4096\n"
      "v_mfma_f32_16x16x32_bf16 v[140:143], v[152:155], v[196:199], v[140:143]\n"
      "ds_read2_b32 v[174:175], v211 offset0:129 offset1:128\n"
      "v_add_u32 v15, s62, v9\n"
      "v_mfma_f32_16x16x32_bf16 v[136:139], v[152:155], v[188:191], v[136:139]\n"
      "ds_read2_b32 v[144:145], v208 offset0:19 offset1:18\n"
      "ds_read2_b32 v[146:147], v208 offset0:17 offset1:16\n"
      "v_mfma_f32_16x16x32_bf16 v[132:135], v[152:155], v[180:183], v[132:135]\n"
      "ds_read_b128 v[204:207], v216 offset:3584\n"
      "ds_read_b128 v[196:199], v216 offset:2560\n"
      "s_waitcnt lgkmcnt(9)\n"
      "v_mfma_f32_16x16x32_bf16 v[140:143], v[156:159], v[192:195], v[140:143]\n"
      "ds_read2_b32 v[148:149], v208 offset0:147 offset1:146\n"
      "s_lshl_b32 s62, s53, 10\n"
      "v_mfma_f32_16x16x32_bf16 v[136:139], v[156:159], v[184:187], v[136:139]\n"
      "ds_read2_b32 v[150:151], v208 offset0:145 offset1:144\n"
      "v_add_u32 v18, s62, v9\n"
      "v_mfma_f32_16x16x32_bf16 v[132:135], v[156:159], v[176:179], v[132:135]\n"
      "ds_read_b128 v[200:203], v216 offset:3072\n"
      "s_lshl_b32 s62, s53, 2\n"
      "v_mfma_f32_16x16x32_bf16 v[140:143], v[160:163], v[188:191], v[140:143]\n"
      "ds_read_b128 v[188:191], v216 offset:1536\n"
      "ds_read_b128 v[192:195], v216 offset:2048\n"
      "v_mfma_f32_16x16x32_bf16 v[136:139], v[160:163], v[180:183], v[136:139]\n"
      "ds_read2_b32 v[152:153], v209 offset0:19 offset1:18\n"
      "v_add_u32 v19, s62, v2\n"
      "s_waitcnt lgkmcnt(10)\n"
      "v_mfma_f32_16x16x32_bf16 v[140:143], v[164:167], v[184:187], v[140:143]\n"
      "ds_read_b128 v[184:187], v216 offset:1024\n"
      "s_xor_b32 s62, s62, 4\n"
      "v_mfma_f32_16x16x32_bf16 v[136:139], v[164:167], v[176:179], v[136:139]\n"
      "ds_read2_b32 v[154:155], v209 offset0:17 offset1:16\n"
      "v_add_u32 v20, s62, v2\n"
      "v_mfma_f32_16x16x32_bf16 v[140:143], v[168:171], v[180:183], v[140:143]\n"
      "ds_read_b128 v[180:183], v216 offset:512\n"
      "v_and_b32 v16, 15, v1\n"
      "v_mfma_f32_16x16x32_bf16 v[140:143], v[172:175], v[176:179], v[140:143]\n"
      "ds_read_b128 v[176:179], v216 offset:0\n"
      "v_lshrrev_b32 v17, 4, v1\n"
      "s_waitcnt lgkmcnt(8)\n"
      "v_mfma_f32_16x16x32_bf16 v[140:143], v[144:147], v[204:207], v[140:143]\n"
      "ds_read2_b32 v[156:157], v209 offset0:147 offset1:146\n"
      "ds_read2_b32 v[158:159], v209 offset0:145 offset1:144\n"
      "v_mfma_f32_16x16x32_bf16 v[136:139], v[144:147], v[196:199], v[136:139]\n"
      "ds_read2_b32 v[160:161], v210 offset0:19 offset1:18\n"
      "v_lshrrev_b32 v21, 5, v1\n"
      "s_waitcnt lgkmcnt(8)\n"
      "v_mfma_f32_16x16x32_bf16 v[132:135], v[144:147], v[188:191], v[132:135]\n"
      "ds_read2_b32 v[162:163], v210 offset0:17 offset1:16\n"
      "v_lshlrev_b32 v21, 4, v21\n"
      "s_waitcnt lgkmcnt(5)\n"
      "v_mfma_f32_16x16x32_bf16 v[128:131], v[144:147], v[180:183], v[128:131]\n"
      "ds_read2_b32 v[164:165], v210 offset0:147 offset1:146\n"
      "v_lshl_add_u32 v21, v16, 5, v21\n"
      "v_mfma_f32_16x16x32_bf16 v[140:143], v[148:151], v[200:203], v[140:143]\n"
      "ds_read2_b32 v[166:167], v210 offset0:145 offset1:144\n"
      "v_and_b32 v22, 1, v17\n"
      "v_mfma_f32_16x16x32_bf16 v[136:139], v[148:151], v[192:195], v[136:139]\n"
      "ds_read2_b32 v[168:169], v211 offset0:19 offset1:18\n"
      "v_lshl_add_u32 v5, v22, 9, v21\n"
      "v_mfma_f32_16x16x32_bf16 v[132:135], v[148:151], v[184:187], v[132:135]\n"
      "ds_read2_b32 v[170:171], v211 offset0:17 offset1:16\n"
      "v_lshl_add_u32 v24, v22, 30, v21\n"
      "s_waitcnt lgkmcnt(8)\n"
      "v_mfma_f32_16x16x32_bf16 v[128:131], v[148:151], v[176:179], v[128:131]\n"
      "ds_read2_b32 v[172:173], v211 offset0:147 offset1:146\n"
      "s_lshr_b32 s62, s60, 1\n"
      "v_mfma_f32_16x16x32_bf16 v[140:143], v[152:155], v[196:199], v[140:143]\n"
      "ds_read2_b32 v[174:175], v211 offset0:145 offset1:144\n"
      "s_mul_i32 s62, s62, 5\n"
      "v_mfma_f32_16x16x32_bf16 v[136:139], v[152:155], v[188:191], v[136:139]\n"
      "ds_read2_b32 v[144:145], v212 offset0:3 offset1:2\n"
      "ds_read2_b32 v[146:147], v212 offset0:1 offset1:0\n"
      "v_mfma_f32_16x16x32_bf16 v[132:135], v[152:155], v[180:183], v[132:135]\n"
      "ds_read_b128 v[204:207], v217 offset:3776\n"
      "ds_read_b128 v[200:203], v217 offset:3264\n"
      "s_waitcnt lgkmcnt(9)\n"
      "v_mfma_f32_16x16x32_bf16 v[140:143], v[156:159], v[192:195], v[140:143]\n"
      "ds_read_b128 v[196:199], v217 offset:2752\n"
      "s_mul_i32 s63, s54, 3\n"
      "v_mfma_f32_16x16x32_bf16 v[136:139], v[156:159], v[184:187], v[136:139]\n"
      "ds_read_b128 v[192:195], v217 offset:2240\n"
      "s_add_u32 s63, s63, s64\n"
      "v_mfma_f32_16x16x32_bf16 v[132:135], v[156:159], v[176:179], v[132:135]\n"
      "ds_read2_b32 v[148:149], v212 offset0:131 offset1:130\n"
      "s_add_u32 s63, s63, s62\n"
      "v_mfma_f32_16x16x32_bf16 v[140:143], v[160:163], v[188:191], v[140:143]\n"
      "ds_read_b128 v[188:191], v217 offset:1728\n"
      "s_mul_i32 s63, s63, 4224\n"
      "v_mfma_f32_16x16x32_bf16 v[136:139], v[160:163], v[180:183], v[136:139]\n"
      "ds_read2_b32 v[150:151], v212 offset0:129 offset1:128\n"
      "s_lshl_b32 s67, s65, 11\n"
      "s_waitcnt lgkmcnt(9)\n"
      "v_mfma_f32_16x16x32_bf16 v[140:143], v[164:167], v[184:187], v[140:143]\n"
      "ds_read_b128 v[184:187], v217 offset:1216\n"
      "s_add_u32 s63, s63, s67\n"
      "v_mfma_f32_16x16x32_bf16 v[136:139], v[164:167], v[176:179], v[136:139]\n"
      "ds_read2_b32 v[152:153], v213 offset0:3 offset1:2\n"
      "s_add_u32 s80, s63, 2818048\n"
      "v_mfma_f32_16x16x32_bf16 v[140:143], v[168:171], v[180:183], v[140:143]\n"
      "ds_read_b128 v[180:183], v217 offset:704\n"
      "s_mul_i32 s62, s62, 4224\n"
      "v_mfma_f32_16x16x32_bf16 v[140:143], v[172:175], v[176:179], v[140:143]\n"
      "ds_read_b128 v[176:179], v217 offset:192\n"
      "s_lshl1_add_u32 s67, s53, s54\n"
      "s_waitcnt lgkmcnt(7)\n"
      "v_mfma_f32_16x16x32_bf16 v[124:127], v[144:147], v[204:207], 0\n"
      "ds_read2_b32 v[154:155], v213 offset0:1 offset1:0\n"
      "s_lshl_b32 s67, s67, 9\n"
      "v_mfma_f32_16x16x32_bf16 v[120:123], v[144:147], v[200:203], 0\n"
      "ds_read2_b32 v[156:157], v213 offset0:131 offset1:130\n"
      "s_add_u32 s62, s62, s67\n"
      "v_mfma_f32_16x16x32_bf16 v[116:119], v[144:147], v[196:199], 0\n"
      "ds_read2_b32 v[158:159], v213 offset0:129 offset1:128\n"
      "s_add_u32 s81, s62, 2826496\n"
      "v_mfma_f32_16x16x32_bf16 v[112:115], v[144:147], v[192:195], 0\n"
      "ds_read2_b32 v[160:161], v214 offset0:3 offset1:2\n"
      "s_waitcnt lgkmcnt(7)\n"
      "v_mfma_f32_16x16x32_bf16 v[108:111], v[144:147], v[188:191], 0\n"
      "ds_read2_b32 v[162:163], v214 offset0:1 offset1:0\n"
      "v_mfma_f32_16x16x32_bf16 v[104:107], v[144:147], v[184:187], 0\n"
      "s_getreg_b32 s2, hwreg(HW_REG_IB_STS, 0, 4)\n"
      "s_add_u32 s2, s2, s84\n"
      "s_cmp_le_u32 s2, 4\n"
      "s_cbranch_scc1 .Lh_148\n"
      ".Lh_149:\n"
      "s_waitcnt lgkmcnt(6)\n"
      "v_mfma_f32_16x16x32_bf16 v[100:103], v[144:147], v[180:183], 0\n"
      "ds_read2_b32 v[164:165], v214 offset0:131 offset1:130\n"
      "s_waitcnt lgkmcnt(6)\n"
      "v_mfma_f32_16x16x32_bf16 v[96:99], v[144:147], v[176:179], 0\n"
      "ds_read2_b32 v[166:167], v214 offset0:129 offset1:128\n"
      "v_mfma_f32_16x16x32_bf16 v[124:127], v[148:151], v[200:203], v[124:127]\n"
      "s_getreg_b32 s2, hwreg(HW_REG_IB_STS, 0, 4)\n"
      "s_add_u32 s2, s2, s85\n"
      "s_cmp_le_u32 s2, 0\n"
      "s_cbranch_scc1 .Lh_150\n"
      ".Lh_151:\n"
      "v_mfma_f32_16x16x32_bf16 v[120:123], v[148:151], v[196:199], v[120:123]\n"
      "ds_read2_b32 v[168:169], v215 offset0:3 offset1:2\n"
      "ds_read_b32 v31, v2 offset:4160\n"
      "v_mfma_f32_16x16x32_bf16 v[116:119], v[148:151], v[192:195], v[116:119]\n"
      "ds_read2_b32 v[170:171], v215 offset0:1 offset1:0\n"
      "v_mfma_f32_16x16x32_bf16 v[112:115], v[148:151], v[188:191], v[112:115]\n"
      "ds_read2_b32 v[172:173], v215 offset0:131 offset1:130\n"
      "v_mfma_f32_16x16x32_bf16 v[108:111], v[148:151], v[184:187], v[108:111]\n"
      "ds_read2_b32 v[174:175], v215 offset0:129 offset1:128\n"
      "v_mfma_f32_16x16x32_bf16 v[104:107], v[148:151], v[180:183], v[104:107]\n"
      "ds_read2_b32 v[144:145], v212 offset0:19 offset1:18\n"
      "v_mfma_f32_16x16x32_bf16 v[100:103], v[148:151], v[176:179], v[100:103]\n"
      "ds_read2_b32 v[146:147], v212 offset0:17 offset1:16\n"
      "s_waitcnt lgkmcnt(7)\n"
      "v_mfma_f32_16x16x32_bf16 v[124:127], v[152:155], v[196:199], v[124:127]\n"
      "ds_read_b128 v[204:207], v217 offset:3712\n"
      "v_mfma_f32_16x16x32_bf16 v[120:123], v[152:155], v[192:195], v[120:123]\n"
      "ds_read_b128 v[200:203], v217 offset:3200\n"
      "v_mfma_f32_16x16x32_bf16 v[116:119], v[152:155], v[188:191], v[116:119]\n"
      "ds_read_b128 v[196:199], v217 offset:2688\n"
      "v_mfma_f32_16x16x32_bf16 v[112:115], v[152:155], v[184:187], v[112:115]\n"
      "s_cmp_eq_u32 s84, 0\n"
      "s_cbranch_scc1 .Lh_152\n"
      ".Lh_153:\n"
      "v_mfma_f32_16x16x32_bf16 v[108:111], v[152:155], v[180:183], v[108:111]\n"
      "s_getreg_b32 s2, hwreg(HW_REG_IB_STS, 0, 4)\n"
      "s_add_u32 s2, s2, s85\n"
      "s_cmp_le_u32 s2, 0\n"
      "s_cbranch_scc1 .Lh_154\n"
      ".Lh_155:\n"
      "v_mfma_f32_16x16x32_bf16 v[104:107], v[152:155], v[176:179], v[104:107]\n"
      "ds_read2_b32 v[148:149], v212 offset0:147 offset1:146\n"
      "v_mfma_f32_16x16x32_bf16 v[124:127], v[156:159], v[192:195], v[124:127]\n"
      "ds_read_b128 v[192:195], v217 offset:2176\n"
      "v_mfma_f32_16x16x32_bf16 v[120:123], v[156:159], v[188:191], v[120:123]\n"
      "ds_read2_b32 v[150:151], v212 offset0:145 offset1:144\n"
      "v_mfma_f32_16x16x32_bf16 v[116:119], v[156:159], v[184:187], v[116:119]\n"
      "s_waitcnt lgkmcnt(4)\n"
      "v_readfirstlane_b32 s2, v31\n"
      "s_cmp_lt_u32 s2, 4\n"
      "s_cbranch_scc1 .Lw_156\n"
      ".Lw_157:\n"
      "v_mfma_f32_16x16x32_bf16 v[112:115], v[156:159], v[180:183], v[112:115]\n"
      "ds_write_b128 v14, v[128:131] offset:16384\n"
      "v_mfma_f32_16x16x32_bf16 v[108:111], v[156:159], v[176:179], v[108:111]\n"
      "ds_write_b128 v14, v[132:135] offset:17408\n"
      "v_mfma_f32_16x16x32_bf16 v[124:127], v[160:163], v[188:191], v[124:127]\n"
      "ds_write_b128 v14, v[136:139] offset:18432\n"
      "v_mfma_f32_16x16x32_bf16 v[120:123], v[160:163], v[184:187], v[120:123]\n"
      "ds_write_b128 v14, v[140:143] offset:19456\n"
      "v_mfma_f32_16x16x32_bf16 v[116:119], v[160:163], v[180:183], v[116:119]\n"
      "ds_add_u32 v2, v3 offset:4132\n"
      "s_sub_u32 s82, 3, s53\n"
      "v_mfma_f32_16x16x32_bf16 v[112:115], v[160:163], v[176:179], v[112:115]\n"
      "s_lshr_b32 s63, s82, 1\n"
      "s_lshl1_add_u32 s63, s54, s63\n"
      "v_mfma_f32_16x16x32_bf16 v[124:127], v[164:167], v[184:187], v[124:127]\n"
      "ds_read_b128 v[188:191], v217 offset:1664\n"
      "ds_read_b128 v[184:187], v217 offset:1152\n"
      "v_mfma_f32_16x16x32_bf16 v[120:123], v[164:167], v[180:183], v[120:123]\n"
      "ds_read2_b32 v[152:153], v213 offset0:19 offset1:18\n"
      "s_xor_b32 s63, s63, s99\n"
      "v_mfma_f32_16x16x32_bf16 v[116:119], v[164:167], v[176:179], v[116:119]\n"
      "ds_read2_b32 v[154:155], v213 offset0:17 offset1:16\n"
      "s_lshl_b32 s63, s63, 10\n"
      "v_mfma_f32_16x16x32_bf16 v[124:127], v[168:171], v[180:183], v[124:127]\n"
      "ds_read_b128 v[180:183], v217 offset:640\n"
      "s_and_b32 s67, s82, 1\n"
      "v_mfma_f32_16x16x32_bf16 v[120:123], v[168:171], v[176:179], v[120:123]\n"
      "ds_read2_b32 v[156:157], v213 offset0:147 offset1:146\n"
      "s_lshl_b32 s67, s67, 9\n"
      "v_mfma_f32_16x16x32_bf16 v[124:127], v[172:175], v[176:179], v[124:127]\n"
      "ds_read_b128 v[176:179], v217 offset:128\n"
      "s_add_u32 s63, s63, s67\n"
      "v_mfma_f32_16x16x32_bf16 v[124:127], v[144:147], v[204:207], v[124:127]\n"
      "ds_read2_b32 v[158:159], v213 offset0:145 offset1:144\n"
      "s_add_u32 s63, s63, 157716\n"
      "v_mfma_f32_16x16x32_bf16 v[120:123], v[144:147], v[200:203], v[120:123]\n"
      "v_lshl_add_u32 v21, v16, 5, s63\n"
      "ds_read_b32 v23, v21\n"
      "s_waitcnt lgkmcnt(7)\n"
      "v_mfma_f32_16x16x32_bf16 v[116:119], v[144:147], v[196:199], v[116:119]\n"
      "ds_read2_b32 v[160:161], v214 offset0:19 offset1:18\n"
      "s_lshl1_add_u32 s62, s56, s54\n"
      "v_mfma_f32_16x16x32_bf16 v[112:115], v[144:147], v[192:195], v[112:115]\n"
      "ds_read2_b32 v[162:163], v214 offset0:17 offset1:16\n"
      "s_lshl2_add_u32 s63, s62, s82\n"
      "v_mfma_f32_16x16x32_bf16 v[108:111], v[144:147], v[188:191], v[108:111]\n"
      "s_mul_i32 s70, s63, 20480\n"
      "s_mul_i32 s63, s55, 160\n"
      "v_mfma_f32_16x16x32_bf16 v[104:107], v[144:147], v[184:187], v[104:107]\n"
      "s_add_u32 s70, s70, s63\n"
      "v_mul_u32_u24 v4, 1280, v16\n"
      "s_waitcnt lgkmcnt(5)\n"
      "v_mfma_f32_16x16x32_bf16 v[100:103], v[144:147], v[180:183], v[100:103]\n"
      "ds_read2_b32 v[164:165], v214 offset0:147 offset1:146\n"
      "v_lshl_add_u32 v4, v17, 3, v4\n"
      "s_waitcnt lgkmcnt(5)\n"
      "v_mfma_f32_16x16x32_bf16 v[96:99], v[144:147], v[176:179], v[96:99]\n"
      "ds_read2_b32 v[166:167], v214 offset0:145 offset1:144\n"
      "s_mul_i32 s63, s53, 5120\n"
      "v_mfma_f32_16x16x32_bf16 v[124:127], v[148:151], v[200:203], v[124:127]\n"
      "s_lshl2_add_u32 s72, s60, s53\n"
      "s_mul_i32 s72, s72, 3072\n"
      "v_mfma_f32_16x16x32_bf16 v[120:123], v[148:151], v[196:199], v[120:123]\n"
      "ds_read2_b32 v[168:169], v215 offset0:19 offset1:18\n"
      "s_add_u32 s72, s72, 15237120\n"
      "v_mfma_f32_16x16x32_bf16 v[116:119], v[148:151], v[192:195], v[116:119]\n"
      "ds_read2_b32 v[170:171], v215 offset0:17 offset1:16\n"
      "s_add_u32 s63, s63, 53248\n"
      "v_mfma_f32_16x16x32_bf16 v[112:115], v[148:151], v[188:191], v[112:115]\n"
      "ds_read2_b32 v[172:173], v215 offset0:147 offset1:146\n"
      "v_add_u32 v8, s63, v9\n"
      "v_mfma_f32_16x16x32_bf16 v[108:111], v[148:151], v[184:187], v[108:111]\n"
      "ds_read2_b32 v[174:175], v215 offset0:145 offset1:144\n"
      "s_lshl_b32 s74, s93, 16\n"
      "v_mfma_f32_16x16x32_bf16 v[104:107], v[148:151], v[180:183], v[104:107]\n"
      "ds_read2_b32 v[144:145], v212 offset0:35 offset1:34\n"
      "s_lshl2_add_u32 s63, s55, s53\n"
      "v_mfma_f32_16x16x32_bf16 v[100:103], v[148:151], v[176:179], v[100:103]\n"
      "ds_read2_b32 v[146:147], v212 offset0:33 offset1:32\n"
      "s_mul_i32 s73, s63, 84480\n"
      "v_mfma_f32_16x16x32_bf16 v[124:127], v[152:155], v[196:199], v[124:127]\n"
      "ds_read_b128 v[204:207], v217 offset:3648\n"
      "s_lshl_b32 s62, s62, 7\n"
      "v_mfma_f32_16x16x32_bf16 v[120:123], v[152:155], v[192:195], v[120:123]\n"
      "ds_read_b128 v[200:203], v217 offset:3136\n"
      "s_add_u32 s73, s73, s62\n"
      "v_mfma_f32_16x16x32_bf16 v[116:119], v[152:155], v[188:191], v[116:119]\n"
      "ds_read_b128 v[196:199], v217 offset:2624\n"
      "s_add_u32 s73, s73, 2818048\n"
      "v_mfma_f32_16x16x32_bf16 v[112:115], v[152:155], v[184:187], v[112:115]\n"
      "s_lshl_b32 s63, s63, 10\n"
      "s_add_u32 s63, s63, 18864128\n"
      "v_mfma_f32_16x16x32_bf16 v[108:111], v[152:155], v[180:183], v[108:111]\n"
      "v_and_b32 v21, 7, v1\n"
      "v_lshl_add_u32 v25, v21, 7, s63\n"
      "v_mfma_f32_16x16x32_bf16 v[104:107], v[152:155], v[176:179], v[104:107]\n"
      "ds_read2_b32 v[148:149], v212 offset0:163 offset1:162\n"
      "v_lshrrev_b32 v22, 3, v1\n"
      "s_waitcnt lgkmcnt(5)\n"
      "v_mfma_f32_16x16x32_bf16 v[124:127], v[156:159], v[192:195], v[124:127]\n"
      "ds_read_b128 v[192:195], v217 offset:2112\n"
      "v_mul_u32_u24 v6, 4224, v22\n"
      "v_mfma_f32_16x16x32_bf16 v[120:123], v[156:159], v[188:191], v[120:123]\n"
      "ds_read2_b32 v[150:151], v212 offset0:161 offset1:160\n"
      "v_lshl_add_u32 v6, v21, 4, v6\n"
      "v_mfma_f32_16x16x32_bf16 v[116:119], v[156:159], v[184:187], v[116:119]\n"
      "s_lshl_b32 s62, s82, 5\n"
      "s_add_u32 s62, s62, 139520\n"
      "v_mfma_f32_16x16x32_bf16 v[112:115], v[156:159], v[180:183], v[112:115]\n"
      "v_lshrrev_b32 v21, 2, v1\n"
      "v_lshl_add_u32 v21, v21, 7, s62\n"
      "v_mfma_f32_16x16x32_bf16 v[108:111], v[156:159], v[176:179], v[108:111]\n"
      "v_and_b32 v22, 3, v1\n"
      "v_lshl_add_u32 v7, v22, 3, v21\n"
      "v_mfma_f32_16x16x32_bf16 v[124:127], v[160:163], v[188:191], v[124:127]\n"
      "ds_read_b128 v[188:191], v217 offset:1600\n"
      "s_mov_b32 s76, 0xbfb8aa3b\n"
      "v_mfma_f32_16x16x32_bf16 v[120:123], v[160:163], v[184:187], v[120:123]\n"
      "ds_read2_b32 v[152:153], v213 offset0:35 offset1:34\n"
      "s_mov_b32 s77, 0xbf317218\n"
      "v_mfma_f32_16x16x32_bf16 v[116:119], v[160:163], v[180:183], v[116:119]\n"
      "ds_read2_b32 v[154:155], v213 offset0:33 offset1:32\n"
      "v_and_b32 v21, s92, v23\n"
      "v_mfma_f32_16x16x32_bf16 v[112:115], v[160:163], v[176:179], v[112:115]\n"
      "v_rcp_f32 v21, v21\n"
      "v_mfma_f32_16x16x32_bf16 v[124:127], v[164:167], v[184:187], v[124:127]\n"
      "ds_read_b128 v[184:187], v217 offset:1088\n"
      "v_lshlrev_b32 v22, 16, v23\n"
      "v_mfma_f32_16x16x32_bf16 v[120:123], v[164:167], v[180:183], v[120:123]\n"
      "v_mul_f32 v21, s77, v21\n"
      "v_mul_f32 v22, v22, v21\n"
      "v_mfma_f32_16x16x32_bf16 v[116:119], v[164:167], v[176:179], v[116:119]\n"
      "v_lshlrev_b32 v32, 16, v218\n"
      "v_and_b32 v33, s92, v218\n"
      "v_mfma_f32_16x16x32_bf16 v[124:127], v[168:171], v[180:183], v[124:127]\n"
      "ds_read_b128 v[180:183], v217 offset:576\n"
      "v_lshlrev_b32 v34, 16, v219\n"
      "v_mfma_f32_16x16x32_bf16 v[120:123], v[168:171], v[176:179], v[120:123]\n"
      "ds_read2_b32 v[156:157], v213 offset0:163 offset1:162\n"
      "v_and_b32 v35, s92, v219\n"
      "v_mfma_f32_16x16x32_bf16 v[124:127], v[172:175], v[176:179], v[124:127]\n"
      "ds_read_b128 v[176:179], v217 offset:64\n"
      "v_lshlrev_b32 v36, 16, v220\n"
      "s_waitcnt lgkmcnt(4)\n"
      "v_mfma_f32_16x16x32_bf16 v[124:127], v[144:147], v[204:207], v[124:127]\n"
      "ds_read2_b32 v[158:159], v213 offset0:161 offset1:160\n"
      "v_and_b32 v37, s92, v220\n"
      "v_mfma_f32_16x16x32_bf16 v[120:123], v[144:147], v[200:203], v[120:123]\n"
      "v_lshlrev_b32 v38, 16, v221\n"
      "v_and_b32 v39, s92, v221\n"
      "v_mfma_f32_16x16x32_bf16 v[116:119], v[144:147], v[196:199], v[116:119]\n"
      "ds_read2_b32 v[160:161], v214 offset0:35 offset1:34\n"
      "v_lshlrev_b32 v40, 16, v222\n"
      "v_mfma_f32_16x16x32_bf16 v[112:115], v[144:147], v[192:195], v[112:115]\n"
      "ds_read2_b32 v[162:163], v214 offset0:33 offset1:32\n"
      "v_and_b32 v41, s92, v222\n"
      "v_mfma_f32_16x16x32_bf16 v[108:111], v[144:147], v[188:191], v[108:111]\n"
      "s_getreg_b32 s2, hwreg(HW_REG_IB_STS, 0, 4)\n"
      "s_add_u32 s2, s2, s85\n"
      "s_cmp_le_u32 s2, 0\n"
      "s_cbranch_scc1 .Lh_159\n"
      ".Lh_160:\n"
      "s_waitcnt lgkmcnt(4)\n"
      "v_mfma_f32_16x16x32_bf16 v[104:107], v[144:147], v[184:187], v[104:107]\n"
      "s_getreg_b32 s2, hwreg(HW_REG_IB_STS, 0, 4)\n"
      "s_add_u32 s2, s2, s85\n"
      "s_cmp_le_u32 s2, 0\n"
      "s_cbranch_scc1 .Lh_161\n"
      ".Lh_162:\n"
      "v_mfma_f32_16x16x32_bf16 v[100:103], v[144:147], v[180:183], v[100:103]\n"
      "ds_read2_b32 v[164:165], v214 offset0:163 offset1:162\n"
      "s_waitcnt lgkmcnt(3)\n"
      "v_mfma_f32_16x16x32_bf16 v[96:99], v[144:147], v[176:179], v[96:99]\n"
      "ds_read2_b32 v[166:167], v214 offset0:161 offset1:160\n"
      "v_mfma_f32_16x16x32_bf16 v[124:127], v[148:151], v[200:203], v[124:127]\n"
      "s_cmp_eq_u32 s85, 0\n"
      "s_cbranch_scc1 .Lh_163\n"
      ".Lh_164:\n"
      "v_mfma_f32_16x16x32_bf16 v[120:123], v[148:151], v[196:199], v[120:123]\n"
      "ds_read2_b32 v[168:169], v215 offset0:35 offset1:34\n"
      "ds_read_b32 v31, v2 offset:4132\n"
      "v_mfma_f32_16x16x32_bf16 v[116:119], v[148:151], v[192:195], v[116:119]\n"
      "ds_read2_b32 v[170:171], v215 offset0:33 offset1:32\n"
      "v_mfma_f32_16x16x32_bf16 v[112:115], v[148:151], v[188:191], v[112:115]\n"
      "ds_read2_b32 v[172:173], v215 offset0:163 offset1:162\n"
      "v_mfma_f32_16x16x32_bf16 v[108:111], v[148:151], v[184:187], v[108:111]\n"
      "ds_read2_b32 v[174:175], v215 offset0:161 offset1:160\n"
      "v_mfma_f32_16x16x32_bf16 v[104:107], v[148:151], v[180:183], v[104:107]\n"
      "ds_read2_b32 v[144:145], v212 offset0:51 offset1:50\n"
      "v_mfma_f32_16x16x32_bf16 v[100:103], v[148:151], v[176:179], v[100:103]\n"
      "ds_read2_b32 v[146:147], v212 offset0:49 offset1:48\n"
      "v_mfma_f32_16x16x32_bf16 v[124:127], v[152:155], v[196:199], v[124:127]\n"
      "ds_read_b128 v[204:207], v217 offset:3584\n"
      "v_mfma_f32_16x16x32_bf16 v[120:123], v[152:155], v[192:195], v[120:123]\n"
      "ds_read_b128 v[200:203], v217 offset:3072\n"
      "v_mfma_f32_16x16x32_bf16 v[116:119], v[152:155], v[188:191], v[116:119]\n"
      "ds_read_b128 v[196:199], v217 offset:2560\n"
      "v_mfma_f32_16x16x32_bf16 v[112:115], v[152:155], v[184:187], v[112:115]\n"
      "s_waitcnt lgkmcnt(6)\n"
      "v_readfirstlane_b32 s2, v31\n"
      "s_cmp_lt_u32 s2, 4\n"
      "s_cbranch_scc1 .Lw_165\n"
      ".Lw_166:\n"
      "v_mfma_f32_16x16x32_bf16 v[108:111], v[152:155], v[180:183], v[108:111]\n"
      "ds_read_b128 v[128:131], v18 offset:16384\n"
      "ds_read_b128 v[132:135], v18 offset:20480\n"
      "v_mfma_f32_16x16x32_bf16 v[104:107], v[152:155], v[176:179], v[104:107]\n"
      "ds_read2_b32 v[148:149], v212 offset0:179 offset1:178\n"
      "ds_read_b128 v[136:139], v18 offset:24576\n"
      "v_mfma_f32_16x16x32_bf16 v[124:127], v[156:159], v[192:195], v[124:127]\n"
      "ds_read_b128 v[192:195], v217 offset:2048\n"
      "ds_read_b128 v[140:143], v18 offset:28672\n"
      "v_mfma_f32_16x16x32_bf16 v[120:123], v[156:159], v[188:191], v[120:123]\n"
      "ds_read2_b32 v[150:151], v212 offset0:177 offset1:176\n"
      "v_lshlrev_b32 v42, 16, v223\n"
      "v_mfma_f32_16x16x32_bf16 v[116:119], v[156:159], v[184:187], v[116:119]\n"
      "v_and_b32 v43, s92, v223\n"
      "v_lshlrev_b32 v44, 16, v224\n"
      "v_mfma_f32_16x16x32_bf16 v[112:115], v[156:159], v[180:183], v[112:115]\n"
      "v_and_b32 v45, s92, v224\n"
      "v_lshlrev_b32 v46, 16, v225\n"
      "v_mfma_f32_16x16x32_bf16 v[108:111], v[156:159], v[176:179], v[108:111]\n"
      "v_and_b32 v47, s92, v225\n"
      "v_lshlrev_b32 v48, 16, v226\n"
      "v_mfma_f32_16x16x32_bf16 v[124:127], v[160:163], v[188:191], v[124:127]\n"
      "ds_read_b128 v[188:191], v217 offset:1536\n"
      "v_and_b32 v49, s92, v226\n"
      "v_mfma_f32_16x16x32_bf16 v[120:123], v[160:163], v[184:187], v[120:123]\n"
      "ds_read2_b32 v[152:153], v213 offset0:51 offset1:50\n"
      "v_lshlrev_b32 v50, 16, v227\n"
      "v_mfma_f32_16x16x32_bf16 v[116:119], v[160:163], v[180:183], v[116:119]\n"
      "ds_read2_b32 v[154:155], v213 offset0:49 offset1:48\n"
      "v_and_b32 v51, s92, v227\n"
      "v_mfma_f32_16x16x32_bf16 v[112:115], v[160:163], v[176:179], v[112:115]\n"
      "v_fma_f32 v52, v32, v21, v22\n"
      "v_fma_f32 v53, v33, v21, v22\n"
      "v_mfma_f32_16x16x32_bf16 v[124:127], v[164:167], v[184:187], v[124:127]\n"
      "ds_read_b128 v[184:187], v217 offset:1024\n"
      "v_fma_f32 v54, v34, v21, v22\n"
      "v_mfma_f32_16x16x32_bf16 v[120:123], v[164:167], v[180:183], v[120:123]\n"
      "v_fma_f32 v55, v35, v21, v22\n"
      "v_fma_f32 v56, v36, v21, v22\n"
      "v_mfma_f32_16x16x32_bf16 v[116:119], v[164:167], v[176:179], v[116:119]\n"
      "v_fma_f32 v57, v37, v21, v22\n"
      "s_waitcnt lgkmcnt(4)\n"
      "v_add_f32 v136, v136, v140\n"
      "v_mfma_f32_16x16x32_bf16 v[124:127], v[168:171], v[180:183], v[124:127]\n"
      "ds_read_b128 v[180:183], v217 offset:512\n"
      "v_add_f32 v137, v137, v141\n"
      "v_mfma_f32_16x16x32_bf16 v[120:123], v[168:171], v[176:179], v[120:123]\n"
      "ds_read2_b32 v[156:157], v213 offset0:179 offset1:178\n"
      "v_add_f32 v138, v138, v142\n"
      "v_mfma_f32_16x16x32_bf16 v[124:127], v[172:175], v[176:179], v[124:127]\n"
      "ds_read_b128 v[176:179], v217 offset:0\n"
      "v_add_f32 v139, v139, v143\n"
      "v_mfma_f32_16x16x32_bf16 v[124:127], v[144:147], v[204:207], v[124:127]\n"
      "ds_read2_b32 v[158:159], v213 offset0:177 offset1:176\n"
      "v_add_f32 v128, v128, v132\n"
      "v_mfma_f32_16x16x32_bf16 v[120:123], v[144:147], v[200:203], v[120:123]\n"
      "v_add_f32 v129, v129, v133\n"
      "v_add_f32 v130, v130, v134\n"
      "v_mfma_f32_16x16x32_bf16 v[116:119], v[144:147], v[196:199], v[116:119]\n"
      "ds_read2_b32 v[160:161], v214 offset0:51 offset1:50\n"
      "v_add_f32 v131, v131, v135\n"
      "v_mfma_f32_16x16x32_bf16 v[112:115], v[144:147], v[192:195], v[112:115]\n"
      "ds_read2_b32 v[162:163], v214 offset0:49 offset1:48\n"
      "v_add_f32 v128, v128, v136\n"
      "s_waitcnt lgkmcnt(6)\n"
      "v_mfma_f32_16x16x32_bf16 v[108:111], v[144:147], v[188:191], v[108:111]\n"
      "v_add_f32 v129, v129, v137\n"
      "v_add_f32 v130, v130, v138\n"
      "v_mfma_f32_16x16x32_bf16 v[104:107], v[144:147], v[184:187], v[104:107]\n"
      "v_add_f32 v131, v131, v139\n"
      "v_cvt_pk_bf16_f32 v88, v128, v129\n"
      "s_waitcnt lgkmcnt(4)\n"
      "v_mfma_f32_16x16x32_bf16 v[100:103], v[144:147], v[180:183], v[100:103]\n"
      "ds_read2_b32 v[164:165], v214 offset0:179 offset1:178\n"
      "v_cvt_pk_bf16_f32 v90, v128, v129\n"
      "s_waitcnt lgkmcnt(4)\n"
      "v_mfma_f32_16x16x32_bf16 v[96:99], v[144:147], v[176:179], v[96:99]\n"
      "ds_read2_b32 v[166:167], v214 offset0:177 offset1:176\n"
      "v_cvt_pk_bf16_f32 v89, v130, v131\n"
      "v_mfma_f32_16x16x32_bf16 v[124:127], v[148:151], v[200:203], v[124:127]\n"
      "v_cvt_pk_bf16_f32 v91, v130, v131\n"
      "v_permlane16_swap_b32 v88, v90\n"
      "v_mfma_f32_16x16x32_bf16 v[120:123], v[148:151], v[196:199], v[120:123]\n"
      "ds_read2_b32 v[168:169], v215 offset0:51 offset1:50\n"
      "v_permlane16_swap_b32 v89, v91\n"
      "v_mfma_f32_16x16x32_bf16 v[116:119], v[148:151], v[192:195], v[116:119]\n"
      "ds_read2_b32 v[170:171], v215 offset0:49 offset1:48\n"
      "v_mfma_f32_16x16x32_bf16 v[112:115], v[148:151], v[188:191], v[112:115]\n"
      "ds_read2_b32 v[172:173], v215 offset0:179 offset1:178\n"
      "v_mfma_f32_16x16x32_bf16 v[108:111], v[148:151], v[184:187], v[108:111]\n"
      "ds_read2_b32 v[174:175], v215 offset0:177 offset1:176\n"
      "v_mfma_f32_16x16x32_bf16 v[104:107], v[148:151], v[180:183], v[104:107]\n"
      "buffer_store_dwordx4 v[88:91], v24, s[20:23], s81 offen sc1\n"
      "v_mfma_f32_16x16x32_bf16 v[100:103], v[148:151], v[176:179], v[100:103]\n"
      "v_fma_f32 v58, v38, v21, v22\n"
      "v_fma_f32 v59, v39, v21, v22\n"
      "v_mfma_f32_16x16x32_bf16 v[124:127], v[152:155], v[196:199], v[124:127]\n"
      "v_fma_f32 v60, v40, v21, v22\n"
      "v_fma_f32 v61, v41, v21, v22\n"
      "v_mfma_f32_16x16x32_bf16 v[120:123], v[152:155], v[192:195], v[120:123]\n"
      "v_fma_f32 v62, v42, v21, v22\n"
      "v_fma_f32 v63, v43, v21, v22\n"
      "v_mfma_f32_16x16x32_bf16 v[116:119], v[152:155], v[188:191], v[116:119]\n"
      "v_fma_f32 v64, v44, v21, v22\n"
      "v_fma_f32 v65, v45, v21, v22\n"
      "v_mfma_f32_16x16x32_bf16 v[112:115], v[152:155], v[184:187], v[112:115]\n"
      "v_fma_f32 v66, v46, v21, v22\n"
      "v_fma_f32 v67, v47, v21, v22\n"
      "v_mfma_f32_16x16x32_bf16 v[108:111], v[152:155], v[180:183], v[108:111]\n"
      "v_fma_f32 v68, v48, v21, v22\n"
      "v_fma_f32 v69, v49, v21, v22\n"
      "v_mfma_f32_16x16x32_bf16 v[104:107], v[152:155], v[176:179], v[104:107]\n"
      "v_fma_f32 v70, v50, v21, v22\n"
      "v_fma_f32 v71, v51, v21, v22\n"
      "s_waitcnt lgkmcnt(0)\n"
      "v_mfma_f32_16x16x32_bf16 v[124:127], v[156:159], v[192:195], v[124:127]\n"
      "v_mfma_f32_16x16x32_bf16 v[120:123], v[156:159], v[188:191], v[120:123]\n"
      "v_mfma_f32_16x16x32_bf16 v[116:119], v[156:159], v[184:187], v[116:119]\n"
      "v_mfma_f32_16x16x32_bf16 v[112:115], v[156:159], v[180:183], v[112:115]\n"
      "v_mfma_f32_16x16x32_bf16 v[108:111], v[156:159], v[176:179], v[108:111]\n"
      "v_mfma_f32_16x16x32_bf16 v[124:127], v[160:163], v[188:191], v[124:127]\n"
      "v_mfma_f32_16x16x32_bf16 v[120:123], v[160:163], v[184:187], v[120:123]\n"
      "v_mfma_f32_16x16x32_bf16 v[116:119], v[160:163], v[180:183], v[116:119]\n"
      "v_mfma_f32_16x16x32_bf16 v[112:115], v[160:163], v[176:179], v[112:115]\n"
      "v_mfma_f32_16x16x32_bf16 v[124:127], v[164:167], v[184:187], v[124:127]\n"
      "v_mfma_f32_16x16x32_bf16 v[120:123], v[164:167], v[180:183], v[120:123]\n"
      "v_mfma_f32_16x16x32_bf16 v[116:119], v[164:167], v[176:179], v[116:119]\n"
      "v_mfma_f32_16x16x32_bf16 v[124:127], v[168:171], v[180:183], v[124:127]\n"
      "v_mfma_f32_16x16x32_bf16 v[120:123], v[168:171], v[176:179], v[120:123]\n"
      "v_mfma_f32_16x16x32_bf16 v[124:127], v[172:175], v[176:179], v[124:127]\n"
      "s_setprio 3\n"
      "s_cmp_eq_u32 s65, 0\n"
      "s_cbranch_scc1 .Lx_168\n"
      "ds_write_b128 v14, v[96:99] offset:0\n"
      "ds_write_b128 v14, v[100:103] offset:1024\n"
      "ds_write_b128 v14, v[104:107] offset:2048\n"
      "ds_write_b128 v14, v[108:111] offset:3072\n"
      "ds_add_u32 v19, v3 offset:4176\n"
      "v_mov_b64 v[96:97], v[112:113]\n"
      "v_mov_b64 v[98:99], v[114:115]\n"
      "v_mov_b64 v[100:101], v[116:117]\n"
      "v_mov_b64 v[102:103], v[118:119]\n"
      "v_mov_b64 v[104:105], v[120:121]\n"
      "v_mov_b64 v[106:107], v[122:123]\n"
      "v_mov_b64 v[108:109], v[124:125]\n"
      "v_mov_b64 v[110:111], v[126:127]\n"
      "s_branch .Lx_169\n"
      ".Lx_168:\n"
      "ds_write_b128 v14, v[112:115] offset:0\n"
      "s_nop 0\n"
      "ds_write_b128 v14, v[116:119] offset:1024\n"
      "s_nop 0\n"
      "ds_write_b128 v14, v[120:123] offset:2048\n"
      "ds_write_b128 v14, v[124:127] offset:3072\n"
      "ds_add_u32 v19, v3 offset:4176\n"
      ".Lx_169:\n"
      "s_mov_b32 s3, 0x80000\n"
      ".Lp_170:\n"
      "ds_read_b32 v31, v20 offset:4176\n"
      "s_sub_u32 s3, s3, 1\n"
      "s_waitcnt lgkmcnt(0)\n"
      "v_readlane_b32 s2, v31, 0\n"
      "s_cselect_b32 s2, -1, s2\n"
      "s_cmp_lt_u32 s2, 1\n"
      "s_cbranch_scc1 .Lp_170\n"
      "ds_read_b128 v[72:75], v15 offset:0\n"
      "ds_read_b128 v[76:79], v15 offset:1024\n"
      "ds_read_b128 v[80:83], v15 offset:2048\n"
      "ds_read_b128 v[84:87], v15 offset:3072\n"
      "ds_add_u32 v2, v3 offset:4136\n"
      "s_waitcnt lgkmcnt(3)\n"
      "v_add_f32 v96, v96, v72\n"
      "v_add_f32 v97, v97, v73\n"
      "v_add_f32 v98, v98, v74\n"
      "v_add_f32 v99, v99, v75\n"
      "v_add_f32 v100, v100, v76\n"
      "v_add_f32 v101, v101, v77\n"
      "v_add_f32 v102, v102, v78\n"
      "v_add_f32 v103, v103, v79\n"
      "v_cvt_pk_bf16_f32 v88, v96, v97\n"
      "v_cvt_pk_bf16_f32 v90, v100, v101\n"
      "v_cvt_pk_bf16_f32 v89, v98, v99\n"
      "v_cvt_pk_bf16_f32 v91, v102, v103\n"
      "v_permlane16_swap_b32 v88, v90\n"
      "s_nop 0\n"
      "v_permlane16_swap_b32 v89, v91\n"
      "buffer_store_dwordx4 v[88:91], v5, s[20:23], s80 offen offset:0 sc1\n"
      "s_waitcnt lgkmcnt(1)\n"
      "v_add_f32 v104, v104, v80\n"
      "v_add_f32 v105, v105, v81\n"
      "v_add_f32 v106, v106, v82\n"
      "v_add_f32 v107, v107, v83\n"
      "v_add_f32 v108, v108, v84\n"
      "v_add_f32 v109, v109, v85\n"
      "v_add_f32 v110, v110, v86\n"
      "v_add_f32 v111, v111, v87\n"
      "v_cvt_pk_bf16_f32 v92, v104, v105\n"
      "v_cvt_pk_bf16_f32 v94, v108, v109\n"
      "v_cvt_pk_bf16_f32 v93, v106, v107\n"
      "v_cvt_pk_bf16_f32 v95, v110, v111\n"
      "v_permlane16_swap_b32 v92, v94\n"
      "s_nop 0\n"
      "v_permlane16_swap_b32 v93, v95\n"
      "buffer_store_dwordx4 v[92:95], v5, s[20:23], s80 offen offset:1024 sc1\n"
      "s_lshl_b32 s62, s60, 7\n"
      "s_add_u32 s62, s62, 18864128\n"
      "s_waitcnt vmcnt(0)\n"
      "ds_add_rtn_u32 v31, v2, v3 offset:4172\n"
      "s_waitcnt lgkmcnt(0)\n"
      "v_readfirstlane_b32 s2, v31\n"
      "s_cmp_eq_u32 s2, 3\n"
      "s_cselect_b32 s62, s62, 1073741824\n"
      "s_mov_b64 exec, 1\n"
      "buffer_store_dwordx2 v[12:13], off, s[20:23], s62 sc1\n"
      "s_mov_b64 exec, -1\n"
      "ds_read_b32 v31, v2 offset:4136\n"
      "s_waitcnt lgkmcnt(0)\n"
      "v_readfirstlane_b32 s2, v31\n"
      "s_cmp_lt_u32 s2, 4\n"
      "s_cbranch_scc1 .Lw_171\n"
      ".Lw_172:\n"
      "v_and_b32 v24, 1, v17\n"
      "v_mul_u32_u24 v24, 24, v24\n"
      "v_add_u32 v24, v24, v4\n"
      "s_mul_i32 s62, s53, 2560\n"
      "s_add_u32 s63, s73, 32768\n"
      "s_add_u32 s64, s73, 65536\n"
      "s_add_u32 m0, s62, 139520\n"
      "s_mov_b32 s3, 0x80000\n"
      ".Lp_174:\n"
      "s_sleep 1\n"
      "ds_read_b32 v31, v2 offset:4192\n"
      "s_sub_u32 s3, s3, 1\n"
      "s_waitcnt lgkmcnt(0)\n"
      "v_readlane_b32 s2, v31, s53\n"
      "s_cselect_b32 s2, -1, s2\n"
      "s_cmp_lt_u32 s2, 15\n"
      "s_cbranch_scc1 .Lp_174\n"
      "buffer_load_dwordx2 v[26:27], v25, s[20:23], 0 offen sc1\n"
      "s_waitcnt vmcnt(0)\n"
      "v_cmp_ne_u64 vcc, s[58:59], v[26:27]\n"
      "s_cbranch_vccnz .Lf_175\n"
      ".Lf_176:\n"
      "buffer_load_dwordx4 v6, s[20:23], s73 offen sc1 lds\n"
      "buffer_load_dwordx4 v6, s[20:23], s63 offen offset:1024 sc1 lds\n"
      "s_mov_b32 exec_hi, 0\n"
      "buffer_load_dwordx4 v6, s[20:23], s64 offen offset:2048 sc1 lds\n"
      "s_mov_b32 exec_hi, -1\n"
      "s_mov_b32 s3, 0x80000\n"
      ".Lp_178:\n"
      "s_sleep 1\n"
      "ds_read_b32 v31, v2 offset:4192\n"
      "s_sub_u32 s3, s3, 1\n"
      "s_waitcnt lgkmcnt(0)\n"
      "v_readlane_b32 s2, v31, s53\n"
      "s_cselect_b32 s2, -1, s2\n"
      "s_cmp_lt_u32 s2, 20\n"
      "s_cbranch_scc1 .Lp_178\n"
      "buffer_load_dwordx4 v[164:167], v9, s[20:23], s72 offen offset:0 sc1\n"
      "buffer_load_dwordx4 v[168:171], v9, s[20:23], s72 offen offset:1024 sc1\n"
      "buffer_load_dwordx4 v[172:175], v9, s[20:23], s72 offen offset:2048 sc1\n"
      "s_waitcnt vmcnt(3)\n"
      "ds_add_u32 v2, v3 offset:4140\n"
      "s_mov_b32 s3, 0x80000\n"
      ".Lp_179:\n"
      "ds_read_b32 v31, v2 offset:4140\n"
      "s_sub_u32 s3, s3, 1\n"
      "s_waitcnt lgkmcnt(0)\n"
      "v_readlane_b32 s2, v31, 0\n"
      "s_cselect_b32 s2, -1, s2\n"
      "s_cmp_lt_u32 s2, 4\n"
      "s_cbranch_scc1 .Lp_179\n"
      "ds_read_b64_tr_b16 v[218:219], v7 offset:0\n"
      "ds_read_b64_tr_b16 v[220:221], v7 offset:2048\n"
      "ds_read_b64_tr_b16 v[222:223], v7 offset:4096\n"
      "ds_read_b64_tr_b16 v[224:225], v7 offset:6144\n"
      "ds_read_b64_tr_b16 v[226:227], v7 offset:8192\n"
      "s_waitcnt lgkmcnt(3)\n"
      "v_lshlrev_b32 v21, 16, v218\n"
      "v_and_b32 v22, s92, v218\n"
      "v_mul_f32 v52, v52, v21\n"
      "v_mul_f32 v53, v53, v22\n"
      "v_lshlrev_b32 v21, 16, v219\n"
      "v_and_b32 v22, s92, v219\n"
      "v_mul_f32 v54, v54, v21\n"
      "v_mul_f32 v55, v55, v22\n"
      "v_lshlrev_b32 v21, 16, v220\n"
      "v_and_b32 v22, s92, v220\n"
      "v_mul_f32 v56, v56, v21\n"
      "v_mul_f32 v57, v57, v22\n"
      "v_lshlrev_b32 v21, 16, v221\n"
      "v_and_b32 v22, s92, v221\n"
      "v_mul_f32 v58, v58, v21\n"
      "v_mul_f32 v59, v59, v22\n"
      "s_waitcnt lgkmcnt(0)\n"
      "v_lshlrev_b32 v21, 16, v222\n"
      "v_and_b32 v22, s92, v222\n"
      "v_mul_f32 v60, v60, v21\n"
      "v_mul_f32 v61, v61, v22\n"
      "v_lshlrev_b32 v21, 16, v223\n"
      "v_and_b32 v22, s92, v223\n"
      "v_mul_f32 v62, v62, v21\n"
      "v_mul_f32 v63, v63, v22\n"
      "v_lshlrev_b32 v21, 16, v224\n"
      "v_and_b32 v22, s92, v224\n"
      "v_mul_f32 v64, v64, v21\n"
      "v_mul_f32 v65, v65, v22\n"
      "v_lshlrev_b32 v21, 16, v225\n"
      "v_and_b32 v22, s92, v225\n"
      "v_mul_f32 v66, v66, v21\n"
      "v_mul_f32 v67, v67, v22\n"
      "v_lshlrev_b32 v21, 16, v226\n"
      "v_and_b32 v22, s92, v226\n"
      "v_mul_f32 v68, v68, v21\n"
      "v_mul_f32 v69, v69, v22\n"
      "v_lshlrev_b32 v21, 16, v227\n"
      "v_and_b32 v22, s92, v227\n"
      "v_mul_f32 v70, v70, v21\n"
      "v_mul_f32 v71, v71, v22\n"
      "s_waitcnt vmcnt(0)\n"
      "s_waitcnt vmcnt(63)\n"
      "v_xor_b32 v21, s74, v167\n"
      "v_bitop3_b32 v21, v21, v171, s74 bitop3:0xf6\n"
      "v_bitop3_b32 v21, v21, v175, s74 bitop3:0xf6\n"
      "v_cmp_lt_u32 vcc, 0xffff, v21\n"
      "s_cbranch_vccnz .Ly_180\n"
      ".Ly_181:\n"
      "v_lshlrev_b32 v144, 16, v164\n"
      "v_and_b32 v145, s92, v164\n"
      "v_lshlrev_b32 v146, 16, v165\n"
      "v_and_b32 v147, s92, v165\n"
      "v_lshlrev_b32 v148, 16, v166\n"
      "v_and_b32 v149, s92, v166\n"
      "v_lshlrev_b32 v150, 16, v167\n"
      "v_lshlrev_b32 v151, 16, v168\n"
      "v_and_b32 v152, s92, v168\n"
      "v_lshlrev_b32 v153, 16, v169\n"
      "v_and_b32 v154, s92, v169\n"
      "v_lshlrev_b32 v155, 16, v170\n"
      "v_and_b32 v156, s92, v170\n"
      "v_lshlrev_b32 v157, 16, v171\n"
      "v_lshlrev_b32 v158, 16, v172\n"
      "v_and_b32 v159, s92, v172\n"
      "v_lshlrev_b32 v160, 16, v173\n"
      "v_and_b32 v161, s92, v173\n"
      "v_lshlrev_b32 v162, 16, v174\n"
      "v_and_b32 v163, s92, v174\n"
      "s_mov_b32 s77, s76\n"
      "v_pk_mul_f32 v[144:145], v[144:145], s[76:77]\n"
      "v_pk_mul_f32 v[146:147], v[146:147], s[76:77]\n"
      "v_pk_mul_f32 v[148:149], v[148:149], s[76:77]\n"
      "v_pk_mul_f32 v[150:151], v[150:151], s[76:77]\n"
      "v_pk_mul_f32 v[152:153], v[152:153], s[76:77]\n"
      "v_pk_mul_f32 v[154:155], v[154:155], s[76:77]\n"
      "v_pk_mul_f32 v[156:157], v[156:157], s[76:77]\n"
      "v_pk_mul_f32 v[158:159], v[158:159], s[76:77]\n"
      "v_pk_mul_f32 v[160:161], v[160:161], s[76:77]\n"
      "v_pk_mul_f32 v[162:163], v[162:163], s[76:77]\n"
      "v_mov_b32 v22, 1.0\n"
      "v_mov_b32 v23, 1.0\n"
      "s_mov_b32 s3, 0x80000\n"
      ".Lp_183:\n"
      "ds_read_b32 v31, v2 offset:4192\n"
      "s_sub_u32 s3, s3, 1\n"
      "s_waitcnt lgkmcnt(0)\n"
      "v_readlane_b32 s2, v31, s53\n"
      "s_cselect_b32 s2, -1, s2\n"
      "s_cmp_lt_u32 s2, 32\n"
      "s_cbranch_scc1 .Lp_183\n"
      "ds_read_b128 v[176:179], v8 offset:0\n"
      "ds_read_b128 v[180:183], v8 offset:1024\n"
      "s_waitcnt lgkmcnt(1)\n"
      "v_pk_fma_f32 v[176:177], v[176:177], s[76:77], v[144:145]\n"
      "v_pk_fma_f32 v[178:179], v[178:179], s[76:77], v[146:147]\n"
      "s_waitcnt lgkmcnt(0)\n"
      "v_pk_fma_f32 v[180:181], v[180:181], s[76:77], v[148:149]\n"
      "v_pk_fma_f32 v[182:183], v[182:183], s[76:77], v[150:151]\n"
      "v_exp_f32 v196, v176\n"
      "v_exp_f32 v197, v177\n"
      "v_exp_f32 v198, v178\n"
      "v_exp_f32 v199, v179\n"
      "v_exp_f32 v200, v180\n"
      "v_exp_f32 v201, v181\n"
      "v_exp_f32 v202, v182\n"
      "v_exp_f32 v203, v183\n"
      "v_pk_mul_f32 v[52:53], v[52:53], v[176:177]\n"
      "v_pk_mul_f32 v[54:55], v[54:55], v[178:179]\n"
      "v_pk_mul_f32 v[56:57], v[56:57], v[180:181]\n"
      "v_pk_mul_f32 v[58:59], v[58:59], v[182:183]\n"
      "v_pk_add_f32 v[196:197], v[196:197], v[22:23]\n"
      "v_pk_add_f32 v[198:199], v[198:199], v[22:23]\n"
      "v_pk_add_f32 v[200:201], v[200:201], v[22:23]\n"
      "v_pk_add_f32 v[202:203], v[202:203], v[22:23]\n"
      "v_rcp_f32 v196, v196\n"
      "v_rcp_f32 v197, v197\n"
      "v_rcp_f32 v198, v198\n"
      "v_rcp_f32 v199, v199\n"
      "v_rcp_f32 v200, v200\n"
      "v_rcp_f32 v201, v201\n"
      "v_rcp_f32 v202, v202\n"
      "v_rcp_f32 v203, v203\n"
      "v_pk_fma_f32 v[52:53], v[52:53], v[196:197], v[32:33]\n"
      "v_pk_fma_f32 v[54:55], v[54:55], v[198:199], v[34:35]\n"
      "v_pk_fma_f32 v[56:57], v[56:57], v[200:201], v[36:37]\n"
      "v_pk_fma_f32 v[58:59], v[58:59], v[202:203], v[38:39]\n"
      "v_cvt_pk_bf16_f32 v218, v52, v53\n"
      "v_cvt_pk_bf16_f32 v219, v54, v55\n"
      "v_cvt_pk_bf16_f32 v220, v56, v57\n"
      "v_cvt_pk_bf16_f32 v221, v58, v59\n"
      "s_nop 0\n"
      "v_permlane16_swap_b32 v218, v220\n"
      "v_permlane16_swap_b32 v219, v221\n"
      "buffer_store_dwordx4 v[218:221], v24, s[48:51], s70 offen offset:0\n"
      "s_mov_b32 s3, 0x80000\n"
      ".Lp_184:\n"
      "ds_read_b32 v31, v2 offset:4192\n"
      "s_sub_u32 s3, s3, 1\n"
      "s_waitcnt lgkmcnt(0)\n"
      "v_readlane_b32 s2, v31, s53\n"
      "s_cselect_b32 s2, -1, s2\n"
      "s_cmp_lt_u32 s2, 34\n"
      "s_cbranch_scc1 .Lp_184\n"
      "ds_read_b128 v[184:187], v8 offset:2048\n"
      "ds_read_b128 v[188:191], v8 offset:3072\n"
      "s_waitcnt lgkmcnt(1)\n"
      "v_pk_fma_f32 v[184:185], v[184:185], s[76:77], v[152:153]\n"
      "v_pk_fma_f32 v[186:187], v[186:187], s[76:77], v[154:155]\n"
      "s_waitcnt lgkmcnt(0)\n"
      "v_pk_fma_f32 v[188:189], v[188:189], s[76:77], v[156:157]\n"
      "v_pk_fma_f32 v[190:191], v[190:191], s[76:77], v[158:159]\n"
      "v_exp_f32 v204, v184\n"
      "v_exp_f32 v205, v185\n"
      "v_exp_f32 v206, v186\n"
      "v_exp_f32 v207, v187\n"
      "v_exp_f32 v208, v188\n"
      "v_exp_f32 v209, v189\n"
      "v_exp_f32 v210, v190\n"
      "v_exp_f32 v211, v191\n"
      "v_pk_mul_f32 v[60:61], v[60:61], v[184:185]\n"
      "v_pk_mul_f32 v[62:63], v[62:63], v[186:187]\n"
      "v_pk_mul_f32 v[64:65], v[64:65], v[188:189]\n"
      "v_pk_mul_f32 v[66:67], v[66:67], v[190:191]\n"
      "v_pk_add_f32 v[204:205], v[204:205], v[22:23]\n"
      "v_pk_add_f32 v[206:207], v[206:207], v[22:23]\n"
      "v_pk_add_f32 v[208:209], v[208:209], v[22:23]\n"
      "v_pk_add_f32 v[210:211], v[210:211], v[22:23]\n"
      "v_rcp_f32 v204, v204\n"
      "v_rcp_f32 v205, v205\n"
      "v_rcp_f32 v206, v206\n"
      "v_rcp_f32 v207, v207\n"
      "v_rcp_f32 v208, v208\n"
      "v_rcp_f32 v209, v209\n"
      "v_rcp_f32 v210, v210\n"
      "v_rcp_f32 v211, v211\n"
      "v_pk_fma_f32 v[60:61], v[60:61], v[204:205], v[40:41]\n"
      "v_pk_fma_f32 v[62:63], v[62:63], v[206:207], v[42:43]\n"
      "v_pk_fma_f32 v[64:65], v[64:65], v[208:209], v[44:45]\n"
      "v_pk_fma_f32 v[66:67], v[66:67], v[210:211], v[46:47]\n"
      "v_cvt_pk_bf16_f32 v222, v60, v61\n"
      "v_cvt_pk_bf16_f32 v223, v62, v63\n"
      "v_cvt_pk_bf16_f32 v224, v64, v65\n"
      "v_cvt_pk_bf16_f32 v225, v66, v67\n"
      "s_nop 0\n"
      "v_permlane16_swap_b32 v222, v224\n"
      "v_permlane16_swap_b32 v223, v225\n"
      "buffer_store_dwordx4 v[222:225], v24, s[48:51], s70 offen offset:64\n"
      "s_mov_b32 s3, 0x80000\n"
      ".Lp_185:\n"
      "ds_read_b32 v31, v2 offset:4192\n"
      "s_sub_u32 s3, s3, 1\n"
      "s_waitcnt lgkmcnt(0)\n"
      "v_readlane_b32 s2, v31, s53\n"
      "s_cselect_b32 s2, -1, s2\n"
      "s_cmp_lt_u32 s2, 35\n"
      "s_cbranch_scc1 .Lp_185\n"
      "ds_read_b128 v[192:195], v8 offset:4096\n"
      "s_waitcnt lgkmcnt(0)\n"
      "v_pk_fma_f32 v[192:193], v[192:193], s[76:77], v[160:161]\n"
      "v_pk_fma_f32 v[194:195], v[194:195], s[76:77], v[162:163]\n"
      "v_exp_f32 v212, v192\n"
      "v_exp_f32 v213, v193\n"
      "v_exp_f32 v214, v194\n"
      "v_exp_f32 v215, v195\n"
      "v_pk_mul_f32 v[68:69], v[68:69], v[192:193]\n"
      "v_pk_mul_f32 v[70:71], v[70:71], v[194:195]\n"
      "v_pk_add_f32 v[212:213], v[212:213], v[22:23]\n"
      "v_pk_add_f32 v[214:215], v[214:215], v[22:23]\n"
      "v_rcp_f32 v212, v212\n"
      "v_rcp_f32 v213, v213\n"
      "v_rcp_f32 v214, v214\n"
      "v_rcp_f32 v215, v215\n"
      "v_pk_fma_f32 v[68:69], v[68:69], v[212:213], v[48:49]\n"
      "v_pk_fma_f32 v[70:71], v[70:71], v[214:215], v[50:51]\n"
      "v_cvt_pk_bf16_f32 v226, v68, v69\n"
      "v_cvt_pk_bf16_f32 v227, v70, v71\n"
      "buffer_store_dwordx2 v[226:227], v4, s[48:51], s70 offen offset:128\n"
      "s_endpgm\n"
      ".LSPARE:\n"
      "s_and_b32 s70, s60, 7\n"
      "s_lshl2_add_u32 s70, s70, s61\n"
      "s_sub_u32 s70, s70, 28\n"
      "s_xor_b32 s71, s52, 4\n"
      "s_lshl_b32 s71, s71, 5\n"
      "s_add_u32 s71, s71, s70\n"
      "s_add_u32 s71, s71, 224\n"
      "v_lshrrev_b32 v20, 4, v1\n"
      "v_mul_u32_u24 v20, 1280, v20\n"
      "v_and_b32 v21, 15, v1\n"
      "v_lshl_add_u32 v20, v21, 4, v20\n"
      "v_add_u32 v21, 4080, v20\n"
      "v_add_u32 v22, 8160, v20\n"
      "v_add_u32 v23, 12240, v20\n"
      "s_mul_i32 s72, s71, 205\n"
      "s_lshr_b32 s72, s72, 10\n"
      "s_mul_i32 s73, s72, 5\n"
      "s_sub_u32 s73, s71, s73\n"
      "s_mul_i32 s74, s53, 8320\n"
      "s_add_u32 s74, s74, 53248\n"
      "s_cmp_lt_u32 s52, 4\n"
      "s_cbranch_scc1 .Ls_186\n"
      "s_waitcnt lgkmcnt(0)\n"
      "s_barrier\n"
      "buffer_load_dwordx2 v[42:43], off, s[20:23], 0 sc1\n"
      "s_cmp_lt_u32 s71, 270\n"
      "s_cbranch_scc0 .Ls_186\n"
      "s_mul_i32 s62, s72, 40960\n"
      "s_lshl_b32 s63, s73, 8\n"
      "s_add_u32 s62, s62, s63\n"
      "s_mov_b32 m0, s74\n"
      "s_add_u32 s63, s62, 20480\n"
      "buffer_load_dwordx4 v20, s[40:43], s62 offen offset:0 nt lds\n"
      "buffer_load_dwordx4 v21, s[40:43], s62 offen offset:1040 nt lds\n"
      "buffer_load_dwordx4 v22, s[40:43], s62 offen offset:2080 nt lds\n"
      "buffer_load_dwordx4 v23, s[40:43], s62 offen offset:3120 nt lds\n"
      "s_add_u32 m0, s74, 4160\n"
      "s_nop 0\n"
      "buffer_load_dwordx4 v20, s[40:43], s63 offen offset:0 nt lds\n"
      "buffer_load_dwordx4 v21, s[40:43], s63 offen offset:1040 nt lds\n"
      "buffer_load_dwordx4 v22, s[40:43], s63 offen offset:2080 nt lds\n"
      "buffer_load_dwordx4 v23, s[40:43], s63 offen offset:3120 nt lds\n"
      ".Ls_186:\n"
      "v_lshrrev_b32 v24, 3, v1\n"
      "v_mul_u32_u24 v25, 1280, v24\n"
      "v_and_b32 v26, 7, v1\n"
      "v_lshl_add_u32 v25, v26, 4, v25\n"
      "v_cmp_gt_u32 vcc, 40, v1\n"
      "v_mov_b32 v26, 1073741824\n"
      "v_lshlrev_b32 v24, 4, v24\n"
      "v_cndmask_b32 v26, v25, v26, vcc\n"
      "s_mul_i32 s62, s70, 3\n"
      "s_add_u32 s62, s62, 224\n"
      "s_mul_i32 s64, s52, 1040\n"
      "s_mul_i32 s65, s52, 10240\n"
      "s_mul_i32 s80, s62, 205\n"
      "s_lshr_b32 s80, s80, 11\n"
      "s_mul_i32 s81, s80, 10\n"
      "s_sub_u32 s81, s62, s81\n"
      "s_mul_i32 s86, s80, 81920\n"
      "s_lshl_b32 s63, s81, 7\n"
      "s_add_u32 s86, s86, s63\n"
      "s_add_u32 m0, s64, 1040\n"
      "s_add_u32 s63, s86, s65\n"
      "buffer_load_dwordx4 v25, s[12:15], s63 offen nt lds\n"
      "s_add_u32 m0, s64, 9360\n"
      "s_add_u32 s62, s62, 1\n"
      "buffer_load_dwordx4 v25, s[16:19], s63 offen nt lds\n"
      "s_mul_i32 s82, s62, 205\n"
      "s_lshr_b32 s82, s82, 11\n"
      "s_mul_i32 s83, s82, 10\n"
      "s_sub_u32 s83, s62, s83\n"
      "s_mul_i32 s87, s82, 81920\n"
      "s_lshl_b32 s63, s83, 7\n"
      "s_add_u32 s87, s87, s63\n"
      "s_add_u32 m0, s64, 18720\n"
      "s_add_u32 s63, s87, s65\n"
      "buffer_load_dwordx4 v25, s[12:15], s63 offen nt lds\n"
      "s_add_u32 m0, s64, 27040\n"
      "s_add_u32 s62, s62, 1\n"
      "buffer_load_dwordx4 v25, s[16:19], s63 offen nt lds\n"
      "s_mul_i32 s84, s62, 205\n"
      "s_lshr_b32 s84, s84, 11\n"
      "s_mul_i32 s85, s84, 10\n"
      "s_sub_u32 s85, s62, s85\n"
      "s_mul_i32 s88, s84, 81920\n"
      "s_lshl_b32 s63, s85, 7\n"
      "s_add_u32 s88, s88, s63\n"
      "s_add_u32 m0, s64, 36400\n"
      "s_add_u32 s63, s88, s65\n"
      "buffer_load_dwordx4 v25, s[12:15], s63 offen nt lds\n"
      "s_add_u32 m0, s64, 44720\n"
      "s_add_u32 s62, s62, 1\n"
      "buffer_load_dwordx4 v25, s[16:19], s63 offen nt lds\n"
      "s_cmp_eq_u32 s52, 0\n"
      "s_cselect_b32 s62, s86, s88\n"
      "s_cselect_b32 s63, 0, 35360\n"
      "s_cmp_eq_u32 s52, 1\n"
      "s_cselect_b32 s62, s87, s62\n"
      "s_cselect_b32 m0, 17680, s63\n"
      "s_sub_u32 s62, s62, 10240\n"
      "buffer_load_dwordx4 v26, s[12:15], s62 offen nt lds\n"
      "s_cmp_lt_u32 s52, 4\n"
      "s_cselect_b32 s80, s80, s82\n"
      "s_cselect_b32 s81, s81, s83\n"
      "s_cselect_b32 s89, 0, 17680\n"
      "s_lshl2_add_u32 s81, s81, s53\n"
      "s_lshl2_add_u32 s85, s85, s53\n"
      "s_lshl_b32 s62, s81, 7\n"
      "s_lshl_b32 s63, s85, 7\n"
      "buffer_load_dwordx4 v[176:179], v24, s[24:27], s62 offen\n"
      "buffer_load_dwordx4 v[180:183], v24, s[24:27], s63 offen\n"
      "v_mov_b64 v[36:37], 0\n"
      "v_mov_b64 v[38:39], 0\n"
      "s_lshl_b32 s63, s53, 10\n"
      "s_add_u32 s63, s63, 157696\n"
      "v_lshl_add_u32 v40, v1, 4, s63\n"
      "ds_write_b128 v40, v[36:39]\n"
      "v_lshlrev_b32 v40, 2, v1\n"
      "v_add_u32 v40, 161792, v40\n"
      "ds_write_b32 v40, v36\n"
      "v_lshrrev_b32 v20, 4, v1\n"
      "v_and_b32 v21, 1, v20\n"
      "v_lshrrev_b32 v20, 1, v20\n"
      "v_lshl_add_u32 v20, v21, 2, v20\n"
      "v_mul_u32_u24 v20, 1040, v20\n"
      "v_bfe_u32 v21, v1, 2, 2\n"
      "v_lshl_add_u32 v20, v21, 8, v20\n"
      "v_and_b32 v21, 3, v1\n"
      "v_lshl_add_u32 v20, v21, 3, v20\n"
      "v_lshlrev_b32 v22, 4, v1\n"
      "s_lshl3_add_u32 s62, s72, s73\n"
      "s_lshl_b32 s62, s62, 7\n"
      "s_add_u32 s62, s62, 18808832\n"
      "s_cmp_lt_u32 s71, 270\n"
      "s_cselect_b32 s62, s62, 1073741824\n"
      "v_mov_b32 v23, 1073741824\n"
      "v_writelane_b32 v23, s62, 0\n"
      "v_add_u32 v21, s74, v20\n"
      "s_lshr_b32 s63, s72, 2\n"
      "s_and_b32 s62, s72, 3\n"
      "s_mul_i32 s62, s62, 7\n"
      "s_add_u32 s62, s62, s63\n"
      "s_cmp_ge_u32 s63, 7\n"
      "s_cselect_b32 s63, 21, 0\n"
      "s_add_u32 s62, s62, s63\n"
      "s_lshl_b32 s62, s62, 10\n"
      "s_mul_i32 s63, s73, 458752\n"
      "s_add_u32 s62, s62, s63\n"
      "s_add_u32 s62, s62, 5603328\n"
      "s_waitcnt vmcnt(9)\n"
      "v_cmp_eq_u32 vcc, 0, v1\n"
      "v_lshlrev_b32 v2, 2, v1\n"
      "v_add_u32 v2, 157696, v2\n"
      "v_cndmask_b32 v3, 0, 1, vcc\n"
      "v_readfirstlane_b32 s58, v42\n"
      "v_readfirstlane_b32 s59, v43\n"
      "s_add_u32 s58, s58, 1\n"
      "s_addc_u32 s59, s59, 0\n"
      "v_mov_b64 v[12:13], s[58:59]\n"
      "s_cmp_lt_u32 s71, 270\n"
      "s_cbranch_scc0 .Ls_187\n"
      "ds_read_b64_tr_b16 v[96:97], v21 offset:0\n"
      "ds_read_b64_tr_b16 v[98:99], v21 offset:2080\n"
      "ds_read_b64_tr_b16 v[100:101], v21 offset:32\n"
      "ds_read_b64_tr_b16 v[102:103], v21 offset:2112\n"
      "ds_read_b64_tr_b16 v[104:105], v21 offset:64\n"
      "ds_read_b64_tr_b16 v[106:107], v21 offset:2144\n"
      "ds_read_b64_tr_b16 v[108:109], v21 offset:96\n"
      "ds_read_b64_tr_b16 v[110:111], v21 offset:2176\n"
      "s_waitcnt lgkmcnt(6)\n"
      "ds_read_b64_tr_b16 v[112:113], v21 offset:128\n"
      "ds_read_b64_tr_b16 v[114:115], v21 offset:2208\n"
      "ds_read_b64_tr_b16 v[116:117], v21 offset:160\n"
      "ds_read_b64_tr_b16 v[118:119], v21 offset:2240\n"
      "ds_read_b64_tr_b16 v[120:121], v21 offset:192\n"
      "ds_read_b64_tr_b16 v[122:123], v21 offset:2272\n"
      "ds_read_b64_tr_b16 v[124:125], v21 offset:224\n"
      "ds_read_b64_tr_b16 v[126:127], v21 offset:2304\n"
      "s_waitcnt lgkmcnt(14)\n"
      "buffer_store_dwordx4 v[96:99], v22, s[20:23], s62 offen sc1\n"
      "s_add_u32 s62, s62, 57344\n"
      "s_waitcnt lgkmcnt(12)\n"
      "buffer_store_dwordx4 v[100:103], v22, s[20:23], s62 offen sc1\n"
      "s_add_u32 s62, s62, 57344\n"
      "s_waitcnt lgkmcnt(10)\n"
      "buffer_store_dwordx4 v[104:107], v22, s[20:23], s62 offen sc1\n"
      "s_add_u32 s62, s62, 57344\n"
      "s_waitcnt lgkmcnt(8)\n"
      "buffer_store_dwordx4 v[108:111], v22, s[20:23], s62 offen sc1\n"
      "s_add_u32 s62, s62, 57344\n"
      "s_waitcnt lgkmcnt(6)\n"
      "buffer_store_dwordx4 v[112:115], v22, s[20:23], s62 offen sc1\n"
      "s_add_u32 s62, s62, 57344\n"
      "s_waitcnt lgkmcnt(4)\n"
      "buffer_store_dwordx4 v[116:119], v22, s[20:23], s62 offen sc1\n"
      "s_add_u32 s62, s62, 57344\n"
      "s_waitcnt lgkmcnt(2)\n"
      "buffer_store_dwordx4 v[120:123], v22, s[20:23], s62 offen sc1\n"
      "s_add_u32 s62, s62, 57344\n"
      "s_waitcnt lgkmcnt(0)\n"
      "buffer_store_dwordx4 v[124:127], v22, s[20:23], s62 offen sc1\n"
      "s_waitcnt vmcnt(8)\n"
      "s_branch .Ls_188\n"
      ".Ls_187:\n"
      "s_waitcnt vmcnt(0)\n"
      ".Ls_188:\n"
      "s_lshl_b32 s62, s53, 5\n"
      "s_add_u32 s62, s62, s89\n"
      "s_add_u32 s62, s62, 640\n"
      "v_and_b32 v53, 7, v1\n"
      "v_lshrrev_b32 v54, 3, v1\n"
      "v_mul_u32_u24 v192, 1040, v53\n"
      "v_lshl_add_u32 v192, v54, 2, v192\n"
      "v_add_u32 v192, s62, v192\n"
      "v_add_u32 v193, 8720, v192\n"
      "v_mul_u32_u24 v8, 8448, v54\n"
      "v_lshl_add_u32 v8, v53, 4, v8\n"
      "v_lshlrev_b32 v184, 16, v176\n"
      "v_lshlrev_b32 v185, 16, v178\n"
      "v_and_b32 v186, s92, v176\n"
      "v_and_b32 v187, s92, v178\n"
      "v_lshlrev_b32 v188, 16, v177\n"
      "v_lshlrev_b32 v189, 16, v179\n"
      "v_and_b32 v190, s92, v177\n"
      "v_and_b32 v191, s92, v179\n"
      "s_mul_i32 s62, s81, 67584\n"
      "s_lshl_b32 s63, s80, 7\n"
      "s_add_u32 s62, s62, s63\n"
      "s_add_u32 s62, s62, 32768\n"
      "s_add_u32 s63, s62, 4224\n"
      "s_mov_b32 s64, 0\n"
      "s_waitcnt lgkmcnt(0)\n"
      "s_barrier\n"
      ".LSKV:\n"
      "ds_read2_b32 v[196:197], v192 offset0:0 offset1:32\n"
      "ds_read2_b32 v[198:199], v192 offset0:64 offset1:100\n"
      "ds_read2_b32 v[200:201], v192 offset0:132 offset1:164\n"
      "ds_read2_b32 v[202:203], v192 offset0:196 offset1:228\n"
      "ds_read_b32 v204, v192 offset:1040\n"
      "ds_read_b32 v205, v192 offset:1168\n"
      "ds_read_b32 v206, v192 offset:1296\n"
      "ds_read2_b32 v[208:209], v193 offset0:0 offset1:32\n"
      "ds_read2_b32 v[210:211], v193 offset0:64 offset1:96\n"
      "ds_read2_b32 v[212:213], v193 offset0:128 offset1:160\n"
      "ds_read2_b32 v[214:215], v193 offset0:192 offset1:224\n"
      "s_waitcnt lgkmcnt(9)\n"
      "v_lshlrev_b32 v216, 16, v196\n"
      "v_and_b32 v217, s92, v196\n"
      "v_lshlrev_b32 v218, 16, v197\n"
      "v_and_b32 v219, s92, v197\n"
      "v_lshlrev_b32 v220, 16, v198\n"
      "v_and_b32 v221, s92, v198\n"
      "v_lshlrev_b32 v222, 16, v199\n"
      "v_and_b32 v223, s92, v199\n"
      "s_waitcnt lgkmcnt(7)\n"
      "v_lshlrev_b32 v224, 16, v200\n"
      "v_and_b32 v225, s92, v200\n"
      "v_lshlrev_b32 v226, 16, v201\n"
      "v_and_b32 v227, s92, v201\n"
      "v_lshlrev_b32 v228, 16, v202\n"
      "v_and_b32 v229, s92, v202\n"
      "v_lshlrev_b32 v230, 16, v203\n"
      "v_and_b32 v231, s92, v203\n"
      "s_waitcnt lgkmcnt(4)\n"
      "v_lshlrev_b32 v232, 16, v204\n"
      "v_and_b32 v233, s92, v204\n"
      "v_lshlrev_b32 v234, 16, v205\n"
      "v_and_b32 v235, s92, v205\n"
      "v_lshlrev_b32 v236, 16, v206\n"
      "v_and_b32 v237, s92, v206\n"
      "v_pk_mul_f32 v[240:241], v[216:217], v[190:191]\n"
      "v_pk_mul_f32 v[242:243], v[218:219], v[190:191]\n"
      "v_pk_mul_f32 v[244:245], v[220:221], v[190:191]\n"
      "v_pk_mul_f32 v[246:247], v[222:223], v[190:191]\n"
      "v_pk_mul_f32 v[248:249], v[224:225], v[190:191]\n"
      "v_pk_mul_f32 v[250:251], v[226:227], v[190:191]\n"
      "v_pk_mul_f32 v[252:253], v[228:229], v[190:191]\n"
      "v_pk_mul_f32 v[254:255], v[230:231], v[190:191]\n"
      "v_pk_fma_f32 v[240:241], v[218:219], v[188:189], v[240:241]\n"
      "v_pk_fma_f32 v[242:243], v[220:221], v[188:189], v[242:243]\n"
      "v_pk_fma_f32 v[244:245], v[222:223], v[188:189], v[244:245]\n"
      "v_pk_fma_f32 v[246:247], v[224:225], v[188:189], v[246:247]\n"
      "v_pk_fma_f32 v[248:249], v[226:227], v[188:189], v[248:249]\n"
      "v_pk_fma_f32 v[250:251], v[228:229], v[188:189], v[250:251]\n"
      "v_pk_fma_f32 v[252:253], v[230:231], v[188:189], v[252:253]\n"
      "v_pk_fma_f32 v[254:255], v[232:233], v[188:189], v[254:255]\n"
      "v_pk_fma_f32 v[240:241], v[220:221], v[186:187], v[240:241]\n"
      "v_pk_fma_f32 v[242:243], v[222:223], v[186:187], v[242:243]\n"
      "v_pk_fma_f32 v[244:245], v[224:225], v[186:187], v[244:245]\n"
      "v_pk_fma_f32 v[246:247], v[226:227], v[186:187], v[246:247]\n"
      "v_pk_fma_f32 v[248:249], v[228:229], v[186:187], v[248:249]\n"
      "v_pk_fma_f32 v[250:251], v[230:231], v[186:187], v[250:251]\n"
      "v_pk_fma_f32 v[252:253], v[232:233], v[186:187], v[252:253]\n"
      "v_pk_fma_f32 v[254:255], v[234:235], v[186:187], v[254:255]\n"
      "v_pk_fma_f32 v[240:241], v[222:223], v[184:185], v[240:241]\n"
      "v_pk_fma_f32 v[242:243], v[224:225], v[184:185], v[242:243]\n"
      "v_pk_fma_f32 v[244:245], v[226:227], v[184:185], v[244:245]\n"
      "v_pk_fma_f32 v[246:247], v[228:229], v[184:185], v[246:247]\n"
      "v_pk_fma_f32 v[248:249], v[230:231], v[184:185], v[248:249]\n"
      "v_pk_fma_f32 v[250:251], v[232:233], v[184:185], v[250:251]\n"
      "v_pk_fma_f32 v[252:253], v[234:235], v[184:185], v[252:253]\n"
      "v_pk_fma_f32 v[254:255], v[236:237], v[184:185], v[254:255]\n"
      "s_waitcnt lgkmcnt(0)\n"
      "v_lshlrev_b32 v216, 16, v208\n"
      "v_and_b32 v217, s92, v208\n"
      "v_lshlrev_b32 v218, 16, v209\n"
      "v_and_b32 v219, s92, v209\n"
      "v_lshlrev_b32 v220, 16, v210\n"
      "v_and_b32 v221, s92, v210\n"
      "v_lshlrev_b32 v222, 16, v211\n"
      "v_and_b32 v223, s92, v211\n"
      "v_lshlrev_b32 v224, 16, v212\n"
      "v_and_b32 v225, s92, v212\n"
      "v_lshlrev_b32 v226, 16, v213\n"
      "v_and_b32 v227, s92, v213\n"
      "v_lshlrev_b32 v228, 16, v214\n"
      "v_and_b32 v229, s92, v214\n"
      "v_lshlrev_b32 v230, 16, v215\n"
      "v_and_b32 v231, s92, v215\n"
      "v_pk_mul_f32 v[240:241], v[240:241], v[216:217]\n"
      "v_pk_mul_f32 v[242:243], v[242:243], v[218:219]\n"
      "v_pk_mul_f32 v[244:245], v[244:245], v[220:221]\n"
      "v_pk_mul_f32 v[246:247], v[246:247], v[222:223]\n"
      "v_pk_mul_f32 v[248:249], v[248:249], v[224:225]\n"
      "v_pk_mul_f32 v[250:251], v[250:251], v[226:227]\n"
      "v_pk_mul_f32 v[252:253], v[252:253], v[228:229]\n"
      "v_pk_mul_f32 v[254:255], v[254:255], v[230:231]\n"
      "v_cvt_pk_bf16_f32 v196, v242, v240\n"
      "v_cvt_pk_bf16_f32 v197, v246, v244\n"
      "v_cvt_pk_bf16_f32 v198, v250, v248\n"
      "v_cvt_pk_bf16_f32 v199, v254, v252\n"
      "buffer_store_dwordx4 v[196:199], v8, s[20:23], s62 offen sc1\n"
      "v_cvt_pk_bf16_f32 v200, v243, v241\n"
      "v_cvt_pk_bf16_f32 v201, v247, v245\n"
      "v_cvt_pk_bf16_f32 v202, v251, v249\n"
      "v_cvt_pk_bf16_f32 v203, v255, v253\n"
      "buffer_store_dwordx4 v[200:203], v8, s[20:23], s63 offen sc1\n"
      "s_waitcnt vmcnt(2)\n"
      "buffer_store_dwordx2 v[12:13], v23, s[20:23], 0 offen sc1\n"
      "s_lshl_b32 s62, s81, 5\n"
      "s_add_u32 s62, s62, s80\n"
      "s_lshl_b32 s62, s62, 7\n"
      "s_add_u32 s62, s62, 18644992\n"
      "v_mov_b32 v23, 1073741824\n"
      "v_writelane_b32 v23, s62, 0\n"
      "s_mov_b32 s80, s84\n"
      "s_mov_b32 s81, s85\n"
      "v_lshlrev_b32 v184, 16, v180\n"
      "v_lshlrev_b32 v185, 16, v182\n"
      "v_and_b32 v186, s92, v180\n"
      "v_and_b32 v187, s92, v182\n"
      "v_lshlrev_b32 v188, 16, v181\n"
      "v_lshlrev_b32 v189, 16, v183\n"
      "v_and_b32 v190, s92, v181\n"
      "v_and_b32 v191, s92, v183\n"
      "v_add_u32 v192, 35360, v192\n"
      "v_add_u32 v193, 35360, v193\n"
      "s_mul_i32 s62, s81, 67584\n"
      "s_lshl_b32 s63, s80, 7\n"
      "s_add_u32 s62, s62, s63\n"
      "s_add_u32 s62, s62, 32768\n"
      "s_add_u32 s63, s62, 4224\n"
      "s_add_u32 s64, s64, 4\n"
      "s_add_u32 s65, s64, s52\n"
      "s_cmp_lt_u32 s65, 8\n"
      "s_cbranch_scc1 .LSKV\n"
      "s_lshl3_add_u32 s65, s60, 4096\n"
      "s_cmp_eq_u32 s52, 0\n"
      "s_cselect_b32 s65, s65, 1073741824\n"
      "v_writelane_b32 v23, s65, 1\n"
      "s_waitcnt vmcnt(1) lgkmcnt(0)\n"
      "buffer_store_dwordx2 v[12:13], v23, s[20:23], 0 offen sc1\n"
      "s_barrier\n"
      "s_lshl_b32 s94, s53, 12\n"
      "s_lshl_b32 s95, s53, 10\n"
      "s_cmp_lt_u32 s52, 4\n"
      "s_cbranch_scc1 .Ls_189\n"
      "v_mov_b64 v[46:47], v[12:13]\n"
      "v_lshlrev_b32 v5, 4, v1\n"
      "v_add_u32 v6, 4096, v5\n"
      "s_lshr_b32 s62, s60, 1\n"
      "s_mul_i32 s62, s62, 5\n"
      "s_mul_i32 s63, s54, 3\n"
      "s_add_u32 s63, s63, s62\n"
      "s_lshl_b32 s24, s63, 12\n"
      "s_add_u32 s24, s24, s95\n"
      "s_add_u32 s25, s24, 4096\n"
      "s_add_u32 s63, s62, 2\n"
      "s_lshl_b32 s26, s63, 12\n"
      "s_add_u32 s26, s26, s95\n"
      "v_mov_b32 v32, s26\n"
      "v_writelane_b32 v32, s24, 0\n"
      "v_writelane_b32 v32, s25, 1\n"
      "s_cmp_eq_u32 s53, 0\n"
      "s_cselect_b32 s63, 1073741824, -4\n"
      "v_add_u32 v32, s63, v32\n"
      "v_lshrrev_b32 v33, 11, v32\n"
      "v_xor_b32 v33, 524288, v33\n"
      "s_lshr_b32 s91, s62, 4\n"
      "s_add_u32 s63, s62, 4\n"
      "s_lshr_b32 s63, s63, 4\n"
      "s_sub_u32 s63, s63, s91\n"
      "s_lshl_b32 s63, s63, 9\n"
      "v_and_b32 v72, 8, v1\n"
      "v_mul_u32_u24 v72, s63, v72\n"
      "v_and_b32 v73, 7, v1\n"
      "v_lshl_add_u32 v30, v73, 7, v72\n"
      "s_lshl_b32 s91, s91, 12\n"
      "s_add_u32 s91, s91, s95\n"
      "s_add_u32 s91, s91, 18644992\n"
      "v_add_u32 v30, s91, v30\n"
      "s_mul_i32 s63, s53, 4608\n"
      "s_cmp_eq_u32 s53, 3\n"
      "s_cselect_b32 s63, 9728, s63\n"
      "s_add_u32 s63, s63, 139520\n"
      "v_lshl_add_u32 v34, v1, 3, s63\n"
      "v_lshrrev_b32 v72, 1, v1\n"
      "v_min_u32 v72, 6, v72\n"
      "s_mul_i32 s62, s54, 28\n"
      "s_add_u32 s62, s62, s53\n"
      "v_lshl_add_u32 v72, v72, 2, s62\n"
      "v_min_u32 v72, 53, v72\n"
      "s_lshl_b32 s63, s56, 14\n"
      "s_add_u32 s63, s63, 18382848\n"
      "v_lshl_add_u32 v13, v72, 8, s63\n"
      "s_mul_i32 s63, s55, 80\n"
      "s_add_u32 s91, s63, 79\n"
      "s_lshr_b32 s63, s63, 7\n"
      "s_lshr_b32 s91, s91, 7\n"
      "s_sub_u32 s91, s91, s63\n"
      "v_and_b32 v73, s91, v1\n"
      "v_lshl_add_u32 v72, v72, 3, v73\n"
      "s_lshl_b32 s63, s63, 7\n"
      "s_add_u32 s63, s63, 18808832\n"
      "v_lshl_add_u32 v31, v72, 7, s63\n"
      "v_lshrrev_b32 v72, 3, v1\n"
      "v_min_u32 v72, 5, v72\n"
      "v_mul_u32_u24 v73, 4160, v72\n"
      "v_lshrrev_b32 v72, 2, v72\n"
      "v_mul_u32_u24 v72, 41216, v72\n"
      "v_sub_u32 v73, v73, v72\n"
      "v_add_u32 v73, 122880, v73\n"
      "v_and_b32 v72, 7, v1\n"
      "v_lshl_add_u32 v35, v72, 3, v73\n"
      "s_add_u32 s62, s95, 122944\n"
      "v_add_u32 v40, s62, v5\n"
      "v_subrev_u32 v41, 24576, v40\n"
      "v_and_b32 v72, 31, v1\n"
      "v_lshlrev_b32 v42, 3, v72\n"
      "s_add_u32 s62, s95, 157696\n"
      "v_lshl_add_u32 v43, v72, 5, s62\n"
      "s_xor_b32 s31, s99, s53\n"
      "s_lshl2_add_u32 s31, s56, s31\n"
      "s_lshl_b32 s31, s31, 8\n"
      "s_add_u32 s31, s31, 8192\n"
      "s_lshl2_add_u32 s62, s54, s53\n"
      "s_cmp_ge_u32 s62, 6\n"
      "s_cselect_b32 s63, 1073741824, 6144\n"
      "v_add_u32 v7, s63, v5\n"
      "s_mul_i32 s62, s62, 7168\n"
      "s_mul_i32 s63, s55, 286720\n"
      "s_add_u32 s63, s63, s62\n"
      "s_add_u32 s27, s63, 5603328\n"
      "s_mul_i32 s63, s56, 458752\n"
      "s_add_u32 s63, s63, s62\n"
      "s_add_u32 s63, s63, 7897088\n"
      "s_mul_i32 s62, s54, 229376\n"
      "s_add_u32 s91, s63, s62\n"
      "s_sub_u32 s62, 229376, s62\n"
      "s_add_u32 s93, s63, s62\n"
      "s_sub_u32 s62, 4, s53\n"
      "s_and_b32 s62, s62, 3\n"
      "s_mul_i32 s62, s62, 57344\n"
      "s_add_u32 s44, s93, s62\n"
      "s_add_u32 s48, s91, s62\n"
      "s_sub_u32 s62, 5, s53\n"
      "s_and_b32 s62, s62, 3\n"
      "s_mul_i32 s62, s62, 57344\n"
      "s_add_u32 s45, s93, s62\n"
      "s_add_u32 s49, s91, s62\n"
      "s_sub_u32 s62, 6, s53\n"
      "s_and_b32 s62, s62, 3\n"
      "s_mul_i32 s62, s62, 57344\n"
      "s_add_u32 s46, s93, s62\n"
      "s_add_u32 s50, s91, s62\n"
      "s_sub_u32 s62, 7, s53\n"
      "s_and_b32 s62, s62, 3\n"
      "s_mul_i32 s62, s62, 57344\n"
      "s_add_u32 s47, s93, s62\n"
      "s_add_u32 s51, s91, s62\n"
      "s_add_u32 s91, s53, 3\n"
      "s_and_b32 s91, s91, 3\n"
      "s_lshl2_add_u32 s62, s91, 161888\n"
      "v_mov_b32 v15, s62\n"
      "s_mul_i32 s62, s53, 5120\n"
      "s_add_u32 s63, s62, 32768\n"
      "v_add_u32 v8, s63, v5\n"
      "s_mul_i32 s63, s91, 5120\n"
      "s_add_u32 s63, s63, 32768\n"
      "v_add_u32 v9, s63, v5\n"
      "s_xor_b32 s63, s60, 1\n"
      "s_lshl2_add_u32 s63, s63, s53\n"
      "s_mul_i32 s28, s63, 3072\n"
      "s_add_u32 s28, s28, 15237120\n"
      "v_cmp_eq_u32 vcc, s53, v1\n"
      "v_and_b32 v14, 48, v1\n"
      "v_add_u32 v14, 163072, v14\n"
      "v_cndmask_b32 v55, 0, 1, vcc\n"
      "buffer_load_dwordx2 v[76:77], v42, s[20:23], s31 offen sc1\n"
      "v_readfirstlane_b32 s58, v46\n"
      "v_readfirstlane_b32 s59, v47\n"
      "v_mov_b32 v1, v55\n"
      "buffer_load_dwordx2 v[50:51], v31, s[20:23], 0 offen sc1\n"
      "buffer_load_dwordx2 v[10:11], v30, s[20:23], 0 offen sc1\n"
      "s_waitcnt vmcnt(0)\n"
      "v_cmp_ne_u32 vcc, s58, v77\n"
      "s_cbranch_vccnz .Lt_190\n"
      ".Lt_191:\n"
      "v_and_b32 v82, s92, v76\n"
      "v_rcp_f32 v77, v82\n"
      "v_xor_b32 v85, 0x8000, v76\n"
      "s_mov_b32 s62, 0x01000100\n"
      "v_mul_f32 v80, 0xc0135761, v77\n"
      "v_mul_f32 v81, v77, v77\n"
      "v_cmp_nle_f32 vcc, 0x3089705f, v77\n"
      "v_mul_f32 v81, 0x3d372713, v81\n"
      "v_perm_b32 v84, v85, v85, s62\n"
      "v_mul_f32 v81, v81, v80\n"
      "v_mov_b32 v83, 1.0\n"
      "ds_write_b64 v43, v[84:85] offset:16\n"
      "s_cbranch_vccnz .Lb_193\n"
      ".Lb_194:\n"
      "ds_write_b128 v43, v[80:83]\n"
      "s_branch .LS2\n"
      ".Ls_189:\n"
      "ds_read_b32 v26, v2 offset:5120\n"
      "v_lshlrev_b32 v31, 3, v1\n"
      "v_add_u32 v31, 163072, v31\n"
      "s_waitcnt lgkmcnt(0)\n"
      "v_and_b32 v27, s92, v26\n"
      "v_lshlrev_b32 v26, 16, v26\n"
      "ds_write_b64 v31, v[26:27]\n"
      "ds_add_u32 v2, v3 offset:4168\n"
      "ds_add_u32 v2, v3 offset:4160\n"
      "s_lshl3_add_u32 s64, s60, s52\n"
      "s_cmp_eq_u32 s64, 2040\n"
      "s_cbranch_scc0 .Le_195\n"
      "v_lshlrev_b32 v25, 3, v1\n"
      "s_mov_b32 s64, 4096\n"
      "buffer_load_dwordx2 v[26:27], v25, s[20:23], s64 offen sc1\n"
      "s_waitcnt vmcnt(0)\n"
      "v_cmp_ne_u64 vcc, s[58:59], v[26:27]\n"
      "s_cbranch_vccnz .Lf_196\n"
      ".Lf_197:\n"
      "s_mov_b32 s64, 4608\n"
      "buffer_load_dwordx2 v[26:27], v25, s[20:23], s64 offen sc1\n"
      "s_waitcnt vmcnt(0)\n"
      "v_cmp_ne_u64 vcc, s[58:59], v[26:27]\n"
      "s_cbranch_vccnz .Lf_199\n"
      ".Lf_200:\n"
      "s_mov_b32 s64, 5120\n"
      "buffer_load_dwordx2 v[26:27], v25, s[20:23], s64 offen sc1\n"
      "s_waitcnt vmcnt(0)\n"
      "v_cmp_ne_u64 vcc, s[58:59], v[26:27]\n"
      "s_cbranch_vccnz .Lf_202\n"
      ".Lf_203:\n"
      "s_mov_b32 s64, 5632\n"
      "buffer_load_dwordx2 v[26:27], v25, s[20:23], s64 offen sc1\n"
      "s_waitcnt vmcnt(0)\n"
      "v_cmp_ne_u64 vcc, s[58:59], v[26:27]\n"
      "s_cbranch_vccnz .Lf_205\n"
      ".Lf_206:\n"
      "s_mov_b64 exec, 1\n"
      "buffer_store_dwordx2 v[12:13], off, s[20:23], 0 sc1\n"
      "s_mov_b64 exec, -1\n"
      ".Le_195:\n"
      "s_sub_u32 s66, 3, s53\n"
      "s_lshl1_add_u32 s62, s56, s54\n"
      "s_lshl2_add_u32 s63, s62, s66\n"
      "s_mul_i32 s62, s63, 20480\n"
      "s_mul_i32 s63, s55, 160\n"
      "s_add_u32 s62, s62, s63\n"
      "v_and_b32 v21, 15, v1\n"
      "v_mul_u32_u24 v21, 1280, v21\n"
      "v_lshrrev_b32 v22, 4, v1\n"
      "v_lshl_add_u32 v21, v22, 3, v21\n"
      "buffer_load_dwordx2 v[218:219], v21, s[4:7], s62 offen offset:0\n"
      "buffer_load_dwordx2 v[220:221], v21, s[4:7], s62 offen offset:32\n"
      "buffer_load_dwordx2 v[222:223], v21, s[4:7], s62 offen offset:64\n"
      "buffer_load_dwordx2 v[224:225], v21, s[4:7], s62 offen offset:96\n"
      "buffer_load_dwordx2 v[226:227], v21, s[4:7], s62 offen offset:128\n"
      "s_mov_b32 s79, 1073741824\n"
      "s_mov_b32 s84, 64\n"
      "s_mov_b32 s85, 64\n"
      "s_waitcnt vmcnt(0)\n"
      "ds_read_b32 v31, v2 offset:4128\n"
      "s_setprio 3\n"
      "s_branch .LCONV\n"
      ".Lt_1:\n"
      "s_mov_b32 s3, 0x10000\n"
      ".Lt_3:\n"
      "s_sleep 4\n"
      "buffer_load_dwordx2 v[76:77], v42, s[20:23], s31 offen sc1\n"
      "s_waitcnt vmcnt(0)\n"
      "v_cmp_ne_u32 vcc, s58, v77\n"
      "s_sub_u32 s3, s3, 1\n"
      "s_cselect_b64 vcc, 0, vcc\n"
      "s_cbranch_vccnz .Lt_3\n"
      "s_branch .Lt_2\n"
      ".Lb_4:\n"
      "s_mov_b64 exec, vcc\n"
      "v_mov_b32 v80, 0xc0135761\n"
      "v_mov_b32 v81, 0xbdd2d3e8\n"
      "v_mov_b32 v82, 1.0\n"
      "v_mov_b32 v83, v77\n"
      "s_mov_b64 exec, -1\n"
      "ds_add_u32 v2, v3 offset:4144\n"
      "s_branch .Lb_5\n"
      ".Lw_6:\n"
      "s_mov_b32 s3, 0x80000\n"
      ".Lw_8:\n"
      "ds_read_b32 v4, v2 offset:4152\n"
      "s_waitcnt lgkmcnt(0)\n"
      "v_readfirstlane_b32 s2, v4\n"
      "s_sub_u32 s3, s3, 1\n"
      "s_cselect_b32 s2, -1, s2\n"
      "s_cmp_lt_u32 s2, 4\n"
      "s_cbranch_scc1 .Lw_8\n"
      "s_branch .Lw_7\n"
      ".Lf_9:\n"
      "s_mov_b32 s3, 0x10000\n"
      ".Lf_11:\n"
      "s_sleep 4\n"
      "buffer_load_dwordx2 v[50:51], v31, s[20:23], 0 offen sc1\n"
      "s_waitcnt vmcnt(0)\n"
      "v_cmp_ne_u64 vcc, s[58:59], v[50:51]\n"
      "s_sub_u32 s3, s3, 1\n"
      "s_cselect_b64 vcc, 0, vcc\n"
      "s_cbranch_vccnz .Lf_11\n"
      "s_branch .Lf_10\n"
      ".Lf_12:\n"
      "s_mov_b32 s3, 0x10000\n"
      ".Lf_14:\n"
      "s_sleep 4\n"
      "buffer_load_dwordx2 v[48:49], v30, s[20:23], 0 offen sc1\n"
      "s_waitcnt vmcnt(0)\n"
      "v_cmp_ne_u64 vcc, s[58:59], v[48:49]\n"
      "s_sub_u32 s3, s3, 1\n"
      "s_cselect_b64 vcc, 0, vcc\n"
      "s_cbranch_vccnz .Lf_14\n"
      "s_branch .Lf_13\n"
      ".Lw_16:\n"
      "s_mov_b32 s3, 0x80000\n"
      ".Lw_18:\n"
      "ds_read_b32 v4, v2 offset:4168\n"
      "s_waitcnt lgkmcnt(0)\n"
      "v_readfirstlane_b32 s2, v4\n"
      "s_sub_u32 s3, s3, 1\n"
      "s_cselect_b32 s2, -1, s2\n"
      "s_cmp_lt_u32 s2, 4\n"
      "s_cbranch_scc1 .Lw_18\n"
      "s_branch .Lw_17\n"
      ".Lf_19:\n"
      "s_mov_b32 s3, 0x10000\n"
      ".Lf_21:\n"
      "s_sleep 4\n"
      "buffer_load_dwordx2 v[10:11], v13, s[20:23], 0 offen sc1\n"
      "s_waitcnt vmcnt(0)\n"
      "v_cmp_ne_u64 vcc, s[58:59], v[10:11]\n"
      "s_sub_u32 s3, s3, 1\n"
      "s_cselect_b64 vcc, 0, vcc\n"
      "s_cbranch_vccnz .Lf_21\n"
      "s_branch .Lf_20\n"
      ".Lw_22:\n"
      "s_mov_b32 s3, 0x80000\n"
      ".Lw_24:\n"
      "ds_read_b32 v4, v2 offset:4160\n"
      "s_waitcnt lgkmcnt(0)\n"
      "v_readfirstlane_b32 s2, v4\n"
      "s_sub_u32 s3, s3, 1\n"
      "s_cselect_b32 s2, -1, s2\n"
      "s_cmp_lt_u32 s2, 4\n"
      "s_cbranch_scc1 .Lw_24\n"
      "s_branch .Lw_23\n"
      ".Lf_39:\n"
      "s_mov_b32 s3, 0x10000\n"
      ".Lf_41:\n"
      "s_sleep 4\n"
      "buffer_load_dwordx2 v[10:11], v13, s[20:23], s74 offen sc1\n"
      "s_waitcnt vmcnt(0)\n"
      "v_cmp_ne_u64 vcc, s[58:59], v[10:11]\n"
      "s_sub_u32 s3, s3, 1\n"
      "s_cselect_b64 vcc, 0, vcc\n"
      "s_cbranch_vccnz .Lf_41\n"
      "s_branch .Lf_40\n"
      ".Lw_58:\n"
      "s_mov_b32 s3, 0x80000\n"
      ".Lw_60:\n"
      "ds_read_b32 v4, v2 offset:4136\n"
      "s_waitcnt lgkmcnt(0)\n"
      "v_readfirstlane_b32 s2, v4\n"
      "s_sub_u32 s3, s3, 1\n"
      "s_cselect_b32 s2, -1, s2\n"
      "s_cmp_lt_u32 s2, 4\n"
      "s_cbranch_scc1 .Lw_60\n"
      "s_branch .Lw_59\n"
      ".Lg_121:\n"
      "s_waitcnt lgkmcnt(0)\n"
      "s_nop 11\n"
      "v_mul_f32 v80, v80, v27\n"
      "v_mul_f32 v81, v81, v27\n"
      "v_mul_f32 v82, v82, v27\n"
      "v_mul_f32 v83, v83, v27\n"
      "v_mul_f32 v84, v84, v27\n"
      "v_mul_f32 v85, v85, v27\n"
      "v_mul_f32 v86, v86, v27\n"
      "v_mul_f32 v87, v87, v27\n"
      "v_mul_f32 v88, v88, v27\n"
      "v_mul_f32 v89, v89, v27\n"
      "v_mul_f32 v90, v90, v27\n"
      "v_mul_f32 v91, v91, v27\n"
      "v_mul_f32 v92, v92, v27\n"
      "v_mul_f32 v93, v93, v27\n"
      "v_mul_f32 v94, v94, v27\n"
      "v_mul_f32 v95, v95, v27\n"
      "s_branch .Lg_122\n"
      ".Lg_123:\n"
      "s_waitcnt lgkmcnt(0)\n"
      "s_nop 11\n"
      "v_mul_f32 v64, v64, v27\n"
      "v_mul_f32 v65, v65, v27\n"
      "v_mul_f32 v66, v66, v27\n"
      "v_mul_f32 v67, v67, v27\n"
      "v_mul_f32 v68, v68, v27\n"
      "v_mul_f32 v69, v69, v27\n"
      "v_mul_f32 v70, v70, v27\n"
      "v_mul_f32 v71, v71, v27\n"
      "v_mul_f32 v72, v72, v27\n"
      "v_mul_f32 v73, v73, v27\n"
      "v_mul_f32 v74, v74, v27\n"
      "v_mul_f32 v75, v75, v27\n"
      "v_mul_f32 v76, v76, v27\n"
      "v_mul_f32 v77, v77, v27\n"
      "v_mul_f32 v78, v78, v27\n"
      "v_mul_f32 v79, v79, v27\n"
      "s_branch .Lg_124\n"
      ".Lw_125:\n"
      "s_mov_b32 s3, 0x80000\n"
      ".Lw_127:\n"
      "ds_read_b32 v31, v2 offset:2072\n"
      "s_waitcnt lgkmcnt(0)\n"
      "v_readfirstlane_b32 s2, v31\n"
      "s_sub_u32 s3, s3, 1\n"
      "s_cselect_b32 s2, -1, s2\n"
      "s_cmp_lt_u32 s2, 4\n"
      "s_cbranch_scc1 .Lw_127\n"
      "s_branch .Lw_126\n"
      ".Lw_128:\n"
      "s_mov_b32 s3, 0x80000\n"
      ".Lw_130:\n"
      "ds_read_b32 v31, v2 offset:3096\n"
      "s_waitcnt lgkmcnt(0)\n"
      "v_readfirstlane_b32 s2, v31\n"
      "s_sub_u32 s3, s3, 1\n"
      "s_cselect_b32 s2, -1, s2\n"
      "s_cmp_lt_u32 s2, 4\n"
      "s_cbranch_scc1 .Lw_130\n"
      "s_branch .Lw_129\n"
      ".Lg_131:\n"
      "s_waitcnt lgkmcnt(0)\n"
      "s_nop 11\n"
      "v_mul_f32 v64, v64, v27\n"
      "v_mul_f32 v65, v65, v27\n"
      "v_mul_f32 v66, v66, v27\n"
      "v_mul_f32 v67, v67, v27\n"
      "v_mul_f32 v68, v68, v27\n"
      "v_mul_f32 v69, v69, v27\n"
      "v_mul_f32 v70, v70, v27\n"
      "v_mul_f32 v71, v71, v27\n"
      "v_mul_f32 v72, v72, v27\n"
      "v_mul_f32 v73, v73, v27\n"
      "v_mul_f32 v74, v74, v27\n"
      "v_mul_f32 v75, v75, v27\n"
      "v_mul_f32 v76, v76, v27\n"
      "v_mul_f32 v77, v77, v27\n"
      "v_mul_f32 v78, v78, v27\n"
      "v_mul_f32 v79, v79, v27\n"
      "s_branch .Lg_132\n"
      ".Lh_133:\n"
      "s_waitcnt vmcnt(0)\n"
      "s_mov_b64 exec, 1\n"
      "buffer_store_dwordx2 v[12:13], off, s[20:23], s79 offset:0 sc1\n"
      "s_mov_b64 exec, -1\n"
      "s_mov_b32 s84, 64\n"
      "ds_add_u32 v2, v3 offset:4168\n"
      "s_branch .Lh_134\n"
      ".Lh_135:\n"
      "s_waitcnt vmcnt(1)\n"
      "s_mov_b64 exec, 1\n"
      "buffer_store_dwordx2 v[12:13], off, s[20:23], s79 offset:0 sc1\n"
      "s_mov_b64 exec, -1\n"
      "s_mov_b32 s84, 64\n"
      "ds_add_u32 v2, v3 offset:4168\n"
      "s_branch .Lh_136\n"
      ".Lh_137:\n"
      "s_waitcnt vmcnt(1)\n"
      "s_mov_b64 exec, 1\n"
      "buffer_store_dwordx2 v[12:13], off, s[20:23], s79 offset:0 sc1\n"
      "s_mov_b64 exec, -1\n"
      "s_mov_b32 s84, 64\n"
      "ds_add_u32 v2, v3 offset:4168\n"
      "s_branch .Lh_138\n"
      ".Lh_139:\n"
      "s_waitcnt vmcnt(2)\n"
      "s_mov_b64 exec, 1\n"
      "buffer_store_dwordx2 v[12:13], off, s[20:23], s79 offset:0 sc1\n"
      "s_mov_b64 exec, -1\n"
      "s_mov_b32 s84, 64\n"
      "ds_add_u32 v2, v3 offset:4168\n"
      "s_branch .Lh_140\n"
      ".Lg_141:\n"
      "s_waitcnt lgkmcnt(0)\n"
      "s_nop 11\n"
      "v_mul_f32 v64, v64, v27\n"
      "v_mul_f32 v65, v65, v27\n"
      "v_mul_f32 v66, v66, v27\n"
      "v_mul_f32 v67, v67, v27\n"
      "v_mul_f32 v68, v68, v27\n"
      "v_mul_f32 v69, v69, v27\n"
      "v_mul_f32 v70, v70, v27\n"
      "v_mul_f32 v71, v71, v27\n"
      "v_mul_f32 v72, v72, v27\n"
      "v_mul_f32 v73, v73, v27\n"
      "v_mul_f32 v74, v74, v27\n"
      "v_mul_f32 v75, v75, v27\n"
      "v_mul_f32 v76, v76, v27\n"
      "v_mul_f32 v77, v77, v27\n"
      "v_mul_f32 v78, v78, v27\n"
      "v_mul_f32 v79, v79, v27\n"
      "s_branch .Lg_142\n"
      ".Lh_143:\n"
      "s_waitcnt vmcnt(4)\n"
      "s_mov_b64 exec, 1\n"
      "buffer_store_dwordx2 v[12:13], off, s[20:23], s79 offset:0 sc1\n"
      "s_mov_b64 exec, -1\n"
      "s_mov_b32 s84, 64\n"
      "ds_add_u32 v2, v3 offset:4168\n"
      "s_branch .Lh_144\n"
      ".Lw_145:\n"
      "s_mov_b32 s3, 0x80000\n"
      ".Lw_147:\n"
      "ds_read_b32 v31, v2 offset:4128\n"
      "s_waitcnt lgkmcnt(0)\n"
      "v_readfirstlane_b32 s2, v31\n"
      "s_sub_u32 s3, s3, 1\n"
      "s_cselect_b32 s2, -1, s2\n"
      "s_cmp_lt_u32 s2, 4\n"
      "s_cbranch_scc1 .Lw_147\n"
      "s_branch .Lw_146\n"
      ".Lh_148:\n"
      "s_waitcnt vmcnt(4)\n"
      "s_mov_b64 exec, 1\n"
      "buffer_store_dwordx2 v[12:13], off, s[20:23], s79 offset:0 sc1\n"
      "s_mov_b64 exec, -1\n"
      "s_mov_b32 s84, 64\n"
      "ds_add_u32 v2, v3 offset:4168\n"
      "s_branch .Lh_149\n"
      ".Lh_150:\n"
      "s_waitcnt vmcnt(0)\n"
      "s_mov_b64 exec, 1\n"
      "buffer_store_dwordx2 v[12:13], off, s[20:23], s79 offset:128 sc1\n"
      "s_mov_b64 exec, -1\n"
      "s_mov_b32 s85, 64\n"
      "s_branch .Lh_151\n"
      ".Lh_152:\n"
      "s_waitcnt vmcnt(4)\n"
      "s_mov_b64 exec, 1\n"
      "buffer_store_dwordx2 v[12:13], off, s[20:23], s79 offset:0 sc1\n"
      "s_mov_b64 exec, -1\n"
      "s_mov_b32 s84, 64\n"
      "ds_add_u32 v2, v3 offset:4168\n"
      "s_branch .Lh_153\n"
      ".Lh_154:\n"
      "s_waitcnt vmcnt(0)\n"
      "s_mov_b64 exec, 1\n"
      "buffer_store_dwordx2 v[12:13], off, s[20:23], s79 offset:128 sc1\n"
      "s_mov_b64 exec, -1\n"
      "s_mov_b32 s85, 64\n"
      "s_branch .Lh_155\n"
      ".Lw_156:\n"
      "s_mov_b32 s3, 0x80000\n"
      ".Lw_158:\n"
      "ds_read_b32 v31, v2 offset:4160\n"
      "s_waitcnt lgkmcnt(0)\n"
      "v_readfirstlane_b32 s2, v31\n"
      "s_sub_u32 s3, s3, 1\n"
      "s_cselect_b32 s2, -1, s2\n"
      "s_cmp_lt_u32 s2, 4\n"
      "s_cbranch_scc1 .Lw_158\n"
      "s_branch .Lw_157\n"
      ".Lh_159:\n"
      "s_waitcnt vmcnt(0)\n"
      "s_mov_b64 exec, 1\n"
      "buffer_store_dwordx2 v[12:13], off, s[20:23], s79 offset:128 sc1\n"
      "s_mov_b64 exec, -1\n"
      "s_mov_b32 s85, 64\n"
      "s_branch .Lh_160\n"
      ".Lh_161:\n"
      "s_waitcnt vmcnt(0)\n"
      "s_mov_b64 exec, 1\n"
      "buffer_store_dwordx2 v[12:13], off, s[20:23], s79 offset:128 sc1\n"
      "s_mov_b64 exec, -1\n"
      "s_mov_b32 s85, 64\n"
      "s_branch .Lh_162\n"
      ".Lh_163:\n"
      "s_waitcnt vmcnt(0)\n"
      "s_mov_b64 exec, 1\n"
      "buffer_store_dwordx2 v[12:13], off, s[20:23], s79 offset:128 sc1\n"
      "s_mov_b64 exec, -1\n"
      "s_mov_b32 s85, 64\n"
      "s_branch .Lh_164\n"
      ".Lw_165:\n"
      "s_mov_b32 s3, 0x80000\n"
      ".Lw_167:\n"
      "ds_read_b32 v31, v2 offset:4132\n"
      "s_waitcnt lgkmcnt(0)\n"
      "v_readfirstlane_b32 s2, v31\n"
      "s_sub_u32 s3, s3, 1\n"
      "s_cselect_b32 s2, -1, s2\n"
      "s_cmp_lt_u32 s2, 4\n"
      "s_cbranch_scc1 .Lw_167\n"
      "s_branch .Lw_166\n"
      ".Lw_171:\n"
      "s_mov_b32 s3, 0x80000\n"
      ".Lw_173:\n"
      "ds_read_b32 v31, v2 offset:4136\n"
      "s_waitcnt lgkmcnt(0)\n"
      "v_readfirstlane_b32 s2, v31\n"
      "s_sub_u32 s3, s3, 1\n"
      "s_cselect_b32 s2, -1, s2\n"
      "s_cmp_lt_u32 s2, 4\n"
      "s_cbranch_scc1 .Lw_173\n"
      "s_branch .Lw_172\n"
      ".Lf_175:\n"
      "s_mov_b32 s3, 0x10000\n"
      ".Lf_177:\n"
      "s_sleep 4\n"
      "buffer_load_dwordx2 v[26:27], v25, s[20:23], 0 offen sc1\n"
      "s_waitcnt vmcnt(0)\n"
      "v_cmp_ne_u64 vcc, s[58:59], v[26:27]\n"
      "s_sub_u32 s3, s3, 1\n"
      "s_cselect_b64 vcc, 0, vcc\n"
      "s_cbranch_vccnz .Lf_177\n"
      "s_branch .Lf_176\n"
      ".Ly_180:\n"
      "s_mov_b32 s3, 0x10000\n"
      ".Ly_182:\n"
      "s_sleep 4\n"
      "buffer_load_dwordx4 v[164:167], v9, s[20:23], s72 offen offset:0 sc1\n"
      "buffer_load_dwordx4 v[168:171], v9, s[20:23], s72 offen offset:1024 sc1\n"
      "buffer_load_dwordx4 v[172:175], v9, s[20:23], s72 offen offset:2048 sc1\n"
      "s_waitcnt vmcnt(0)\n"
      "v_xor_b32 v21, s74, v167\n"
      "v_bitop3_b32 v21, v21, v171, s74 bitop3:0xf6\n"
      "v_bitop3_b32 v21, v21, v175, s74 bitop3:0xf6\n"
      "v_cmp_lt_u32 vcc, 0xffff, v21\n"
      "s_sub_u32 s3, s3, 1\n"
      "s_cselect_b64 vcc, 0, vcc\n"
      "s_cbranch_vccnz .Ly_182\n"
      "s_branch .Ly_181\n"
      ".Lt_190:\n"
      "s_mov_b32 s3, 0x10000\n"
      ".Lt_192:\n"
      "s_sleep 4\n"
      "buffer_load_dwordx2 v[76:77], v42, s[20:23], s31 offen sc1\n"
      "s_waitcnt vmcnt(0)\n"
      "v_cmp_ne_u32 vcc, s58, v77\n"
      "s_sub_u32 s3, s3, 1\n"
      "s_cselect_b64 vcc, 0, vcc\n"
      "s_cbranch_vccnz .Lt_192\n"
      "s_branch .Lt_191\n"
      ".Lb_193:\n"
      "s_mov_b64 exec, vcc\n"
      "v_mov_b32 v80, 0xc0135761\n"
      "v_mov_b32 v81, 0xbdd2d3e8\n"
      "v_mov_b32 v82, 1.0\n"
      "v_mov_b32 v83, v77\n"
      "s_mov_b64 exec, -1\n"
      "ds_add_u32 v2, v3 offset:4144\n"
      "s_branch .Lb_194\n"
      ".Lf_196:\n"
      "s_mov_b32 s3, 0x10000\n"
      ".Lf_198:\n"
      "s_sleep 4\n"
      "buffer_load_dwordx2 v[26:27], v25, s[20:23], s64 offen sc1\n"
      "s_waitcnt vmcnt(0)\n"
      "v_cmp_ne_u64 vcc, s[58:59], v[26:27]\n"
      "s_sub_u32 s3, s3, 1\n"
      "s_cselect_b64 vcc, 0, vcc\n"
      "s_cbranch_vccnz .Lf_198\n"
      "s_branch .Lf_197\n"
      ".Lf_199:\n"
      "s_mov_b32 s3, 0x10000\n"
      ".Lf_201:\n"
      "s_sleep 4\n"
      "buffer_load_dwordx2 v[26:27], v25, s[20:23], s64 offen sc1\n"
      "s_waitcnt vmcnt(0)\n"
      "v_cmp_ne_u64 vcc, s[58:59], v[26:27]\n"
      "s_sub_u32 s3, s3, 1\n"
      "s_cselect_b64 vcc, 0, vcc\n"
      "s_cbranch_vccnz .Lf_201\n"
      "s_branch .Lf_200\n"
      ".Lf_202:\n"
      "s_mov_b32 s3, 0x10000\n"
      ".Lf_204:\n"
      "s_sleep 4\n"
      "buffer_load_dwordx2 v[26:27], v25, s[20:23], s64 offen sc1\n"
      "s_waitcnt vmcnt(0)\n"
      "v_cmp_ne_u64 vcc, s[58:59], v[26:27]\n"
      "s_sub_u32 s3, s3, 1\n"
      "s_cselect_b64 vcc, 0, vcc\n"
      "s_cbranch_vccnz .Lf_204\n"
      "s_branch .Lf_203\n"
      ".Lf_205:\n"
      "s_mov_b32 s3, 0x10000\n"
      ".Lf_207:\n"
      "s_sleep 4\n"
      "buffer_load_dwordx2 v[26:27], v25, s[20:23], s64 offen sc1\n"
      "s_waitcnt vmcnt(0)\n"
      "v_cmp_ne_u64 vcc, s[58:59], v[26:27]\n"
      "s_sub_u32 s3, s3, 1\n"
      "s_cselect_b64 vcc, 0, vcc\n"
      "s_cbranch_vccnz .Lf_207\n"
      "s_branch .Lf_206\n"
      ".Lfz_end:\n"
      ".fill 320, 4, 0xBF800000\n"
      : "+{v0}"(tid) : "{s[0:1]}"(__builtin_amdgcn_kernarg_segment_ptr()), "{s2}"(blockIdx.x) : "v1", "v2", "v3", "v4", "v5", "v6", "v7", "v8", "v9", "v10", "v11", "v12", "v13", "v14", "v15", "v16", "v17", "v18", "v19", "v20", "v21", "v22", "v23", "v24", "v25", "v26", "v27", "v28", "v29", "v30", "v31", "v32", "v33", "v34", "v35", "v36", "v37", "v38", "v39", "v40", "v41", "v42", "v43", "v44", "v45", "v46", "v47", "v48", "v49", "v50", "v51", "v52", "v53", "v54", "v55", "v56", "v57", "v58", "v59", "v60", "v61", "v62", "v63", "v64", "v65", "v66", "v67", "v68", "v69", "v70", "v71", "v72", "v73", "v74", "v75", "v76", "v77", "v78", "v79", "v80", "v81", "v82", "v83", "v84", "v85", "v86", "v87", "v88", "v89", "v90", "v91", "v92", "v93", "v94", "v95", "v96", "v97", "v98", "v99", "v100", "v101", "v102", "v103", "v104", "v105", "v106", "v107", "v108", "v109", "v110", "v111", "v112", "v113", "v114", "v115", "v116", "v117", "v118", "v119", "v120", "v121", "v122", "v123", "v124", "v125", "v126", "v127", "v128", "v129", "v130", "v131", "v132", "v133", "v134", "v135", "v136", "v137", "v138", "v139", "v140", "v141", "v142", "v143", "v144", "v145", "v146", "v147", "v148", "v149", "v150", "v151", "v152", "v153", "v154", "v155", "v156", "v157", "v158", "v159", "v160", "v161", "v162", "v163", "v164", "v165", "v166", "v167", "v168", "v169", "v170", "v171", "v172", "v173", "v174", "v175", "v176", "v177", "v178", "v179", "v180", "v181", "v182", "v183", "v184", "v185", "v186", "v187", "v188", "v189", "v190", "v191", "v192", "v193", "v194", "v195", "v196", "v197", "v198", "v199", "v200", "v201", "v202", "v203", "v204", "v205", "v206", "v207", "v208", "v209", "v210", "v211", "v212", "v213", "v214", "v215", "v216", "v217", "v218", "v219", "v220", "v221", "v222", "v223", "v224", "v225", "v226", "v227", "v228", "v229", "v230", "v231", "v232", "v233", "v234", "v235", "v236", "v237", "v238", "v239", "v240", "v241", "v242", "v243", "v244", "v245", "v246", "v247", "v248", "v249", "v250", "v251", "v252", "v253", "v254", "v255", "s3", "s4", "s5", "s6", "s7", "s8", "s9", "s10", "s11", "s12", "s13", "s14", "s15", "s16", "s17", "s18", "s19", "s20", "s21", "s22", "s23", "s24", "s25", "s26", "s27", "s28", "s29", "s30", "s31", "s33", "s34", "s35", "s36", "s37", "s38", "s39", "s40", "s41", "s42", "s43", "s44", "s45", "s46", "s47", "s48", "s49", "s50", "s51", "s52", "s53", "s54", "s55", "s56", "s57", "s58", "s59", "s60", "s61", "s62", "s63", "s64", "s65", "s66", "s67", "s68", "s69", "s70", "s71", "s72", "s73", "s74", "s75", "s76", "s77", "s78", "s79", "s80", "s81", "s82", "s83", "s84", "s85", "s86", "s87", "s88", "s89", "s90", "s91", "s92", "s93", "s94", "s95", "s96", "s97", "s98", "s99", "vcc", "scc", "memory");
#endif
}
}

extern "C" void kernel_launch(void* const* d_in, const int* in_sizes, int n_in, void* d_out, int out_size, void* d_ws,
                              size_t ws_size, hipStream_t stream) {
  (void)hipFuncSetAttribute((const void*)fused_kernel, hipFuncAttributeMaxDynamicSharedMemorySize, 163840);
  fused_kernel<<<256, 512, 163840, stream>>>(d_in[0], 2621440u, 0x20000u, d_in[7], 2211840u, 0x20000u, d_in[1], 2621440u, 0x20000u, d_in[2], 2621440u, 0x20000u, d_ws, 18896896u, 0x20000u, d_in[3], 5120u, 0x20000u, d_in[8], 3456u, 0x20000u, d_in[5], 1280u, 0x20000u, d_in[4], 2621440u, 0x20000u, d_in[9], 2211840u, 0x20000u, d_in[10], 1280u, 0x20000u, d_out, 2621440u, 0x20000u);
}
```

```cpp
#include <hip/hip_runtime.h>
#include <stdint.h>
namespace {
constexpr unsigned WS_STATS = 8192, WS_KV = 32768, WS_Y2 = 2818048, WS_W2T = 5603328, WS_HT = 7897088, WS_EX = 15237120, WS_END = 18896896;
constexpr unsigned KVPI = 4224, Y2PI = 4224;
#define BUF const void*, unsigned, unsigned
__global__ __launch_bounds__(512, 2) void fused_kernel(BUF, BUF, BUF, BUF, BUF, BUF, BUF, BUF, BUF, BUF, BUF, BUF) {
#if defined(__HIP_DEVICE_COMPILE__)
  unsigned tid = threadIdx.x;
  asm volatile(
      "s_load_dwordx16 s[4:19], s[0:1], 0x0\n"
      "s_load_dwordx16 s[20:35], s[0:1], 0x40\n"
      "s_load_dwordx16 s[36:51], s[0:1], 0x80\n"
      "s_getpc_b64 s[64:65]\n"
      ".Lfz_pc:\n"
      "v_lshrrev_b32 v2, 6, v0\n"
      "v_and_b32 v1, 63, v0\n"
      "s_mov_b32 s60, s2\n"
      "v_readfirstlane_b32 s52, v2\n"
      "s_lshr_b32 s61, s2, 3\n"
      "s_mov_b32 s66, .Lfz_end-.Lfz_pc\n"
      "s_mov_b32 s67, 0x20000\n"
      "s_lshl3_add_u32 s62, s61, s52\n"
      "s_lshl_b32 s62, s62, 7\n"
      "v_and_b32 v2, 1, v0\n"
      "v_lshlrev_b32 v2, 15, v2\n"
      "buffer_load_dword v3, v2, s[64:67], s62 offen\n"
      "s_and_b32 s53, s52, 3\n"
      "s_and_b32 s54, s2, 1\n"
      "s_mul_i32 s62, s61, 37\n"
      "s_lshr_b32 s62, s62, 8\n"
      "s_mul_i32 s63, s62, 7\n"
      "s_sub_u32 s63, s61, s63\n"
      "s_cmp_lt_u32 s61, 28\n"
      "s_cselect_b32 s62, s62, s63\n"
      "s_cselect_b32 s55, s63, 7\n"
      "s_bfe_u32 s63, s2, 0x20001\n"
      "s_lshl2_add_u32 s56, s63, s62\n"
      "s_mul_i32 s63, s54, 7\n"
      "s_add_u32 s57, s63, s55\n"
      "s_mov_b32 s92, 0xffff0000\n"
      "s_mul_i32 s62, s2, 6\n"
      "s_sub_u32 s62, s62, 892\n"
      "s_lshl_b32 s63, s2, 1\n"
      "s_max_i32 s98, s62, s63\n"
      "s_and_b32 s62, s57, 1\n"
      "s_xor_b32 s63, s54, 1\n"
      "s_lshl1_add_u32 s99, s63, s62\n"
      "s_cmp_lt_u32 s52, 4\n"
      "s_cbranch_scc1 .LM\n"
      "s_cmp_lt_u32 s61, 28\n"
      "s_cbranch_scc0 .LSPARE\n"
      "v_lshrrev_b32 v72, 3, v1\n"
      "v_and_b32 v73, 7, v1\n"
      "v_mul_u32_u24 v74, 1280, v72\n"
      "v_lshl_add_u32 v24, v73, 4, v74\n"
      "v_lshrrev_b32 v75, 4, v1\n"
      "s_and_b32 s62, s53, 1\n"
      "s_lshl_b32 s62, s62, 2\n"
      "v_xor_b32 v73, v73, v75\n"
      "v_xor_b32 v73, s62, v73\n"
      "v_lshl_add_u32 v74, v73, 4, v74\n"
      "s_mul_i32 s62, s99, 40960\n"
      "v_add_u32 v20, s62, v74\n"
      "s_xor_b32 s62, s99, 1\n"
      "s_mul_i32 s62, s62, 40960\n"
      "v_add_u32 v21, s62, v74\n"
      "v_and_b32 v73, 15, v1\n"
      "v_lshlrev_b32 v76, 2, v75\n"
      "v_xor_b32 v73, v73, v76\n"
      "s_cmp_eq_u32 s57, 13\n"
      "s_cselect_b32 s63, 7, 15\n"
      "v_and_b32 v73, s63, v73\n"
      "v_mul_u32_u24 v75, 3456, v75\n"
      "v_lshl_add_u32 v16, v73, 4, v75\n"
      "v_add_u32 v17, 12800, v16\n"
      "v_add_u32 v18, 25600, v16\n"
      "v_add_u32 v19, 38400, v16\n"
      "s_mul_i32 s63, s56, 3\n"
      "s_add_u32 s63, s63, s57\n"
      "s_mul_i32 s64, s63, 205\n"
      "s_lshr_b32 s64, s64, 11\n"
      "s_mul_i32 s64, s64, 10\n"
      "s_sub_u32 s63, s63, s64\n"
      "s_lshl_b32 s64, s56, 4\n"
      "s_add_u32 s64, s64, s53\n"
      "s_mul_i32 s68, s64, 10240\n"
      "s_mul_i32 s69, s53, 55296\n"
      "s_lshl_b32 s64, s57, 8\n"
      "s_add_u32 s69, s69, s64\n"
      "s_lshl_b32 s64, s63, 7\n"
      "s_add_u32 s70, s68, s64\n"
      "s_mul_i32 s64, s63, 221184\n"
      "s_add_u32 s80, s69, s64\n"
      "s_add_u32 s64, s68, 1280\n"
      "s_and_b32 s91, s2, 7\n"
      "s_mul_i32 s91, s91, 28\n"
      "s_add_u32 s91, s91, s61\n"
      "s_mul_i32 s93, s91, 205\n"
      "s_lshr_b32 s90, s93, 10\n"
      "s_lshr_b32 s93, s93, 11\n"
      "s_mul_i32 s65, s93, 80640\n"
      "s_lshl_b32 s67, s91, 7\n"
      "s_add_u32 s65, s65, s67\n"
      "s_sub_u32 s66, s65, 10240\n"
      "s_cselect_b32 s66, 1073741824, s66\n"
      "s_mul_i32 s67, s53, 20480\n"
      "s_add_u32 s65, s65, s67\n"
      "s_lshr_b32 s67, s67, 1\n"
      "s_mul_i32 s90, s90, 39680\n"
      "s_add_u32 s90, s90, s67\n"
      "s_lshl_b32 s67, s91, 8\n"
      "s_add_u32 s90, s90, s67\n"
      "v_add_u32 v25, 9200, v24\n"
      "v_cmp_gt_u32 vcc, 40, v1\n"
      "v_mov_b32 v72, 1073741824\n"
      "s_mul_i32 s67, s53, 2080\n"
      "v_cndmask_b32 v26, v24, v72, vcc\n"
      "v_lshrrev_b32 v72, 4, v1\n"
      "v_mul_u32_u24 v72, 1280, v72\n"
      "v_and_b32 v73, 15, v1\n"
      "v_lshl_add_u32 v27, v73, 4, v72\n"
      "v_add_u32 v28, 4080, v27\n"
      "s_add_u32 s71, s70, 128\n"
      "s_add_u32 s81, s80, 221184\n"
      "s_cmp_eq_u32 s71, s64\n"
      "s_cselect_b32 s71, s68, s71\n"
      "s_cselect_b32 s81, s69, s81\n"
      "s_lshl_b32 s94, s53, 12\n"
      "s_lshl_b32 s95, s53, 10\n"
      "s_setprio 2\n"
      "s_waitcnt lgkmcnt(0)\n"
      "s_barrier\n"
      "s_add_u32 m0, s94, 0\n"
      "s_nop 0\n"
      "buffer_load_dwordx4 v16, s[8:11], s80 offen offset:0 lds\n"
      "buffer_load_dwordx4 v17, s[8:11], s80 offen offset:1024 lds\n"
      "buffer_load_dwordx4 v18, s[8:11], s80 offen offset:2048 lds\n"
      "buffer_load_dwordx4 v19, s[8:11], s80 offen offset:3072 lds\n"
      "s_add_u32 m0, s95, 16384\n"
      "s_nop 0\n"
      "buffer_load_dwordx4 v20, s[4:7], s70 offen lds\n"
      "s_add_u32 m0, s95, 20480\n"
      "s_nop 0\n"
      "buffer_load_dwordx4 v21, s[4:7], s70 offen lds\n"
      "s_mov_b32 m0, 139520\n"
      "s_nop 0\n"
      "buffer_load_dwordx4 v26, s[12:15], s66 offen nt lds\n"
      "s_add_u32 m0, s67, 140560\n"
      "s_nop 0\n"
      "buffer_load_dwordx4 v24, s[12:15], s65 offen nt lds\n"
      "buffer_load_dwordx4 v25, s[12:15], s65 offen offset:1040 nt lds\n"
      "s_add_u32 m0, s67, 148880\n"
      "s_nop 0\n"
      "buffer_load_dwordx4 v24, s[16:19], s65 offen nt lds\n"
      "buffer_load_dwordx4 v25, s[16:19], s65 offen offset:1040 nt lds\n"
      "s_add_u32 m0, s67, 122880\n"
      "s_nop 0\n"
      "buffer_load_dwordx4 v27, s[40:43], s90 offen nt lds\n"
      "buffer_load_dwordx4 v28, s[40:43], s90 offen offset:1040 nt lds\n"
      "s_add_u32 s72, s71, 128\n"
      "s_add_u32 s82, s81, 221184\n"
      "s_cmp_eq_u32 s72, s64\n"
      "s_cselect_b32 s72, s68, s72\n"
      "s_cselect_b32 s82, s69, s82\n"
      "s_add_u32 s73, s72, 128\n"
      "s_add_u32 s83, s82, 221184\n"
      "s_cmp_eq_u32 s73, s64\n"
      "s_cselect_b32 s73, s68, s73\n"
      "s_cselect_b32 s83, s69, s83\n"
      "s_add_u32 s74, s73, 128\n"
      "s_add_u32 s84, s83, 221184\n"
      "s_cmp_eq_u32 s74, s64\n"
      "s_cselect_b32 s74, s68, s74\n"
      "s_cselect_b32 s84, s69, s84\n"
      "s_add_u32 s75, s74, 128\n"
      "s_add_u32 s85, s84, 221184\n"
      "s_cmp_eq_u32 s75, s64\n"
      "s_cselect_b32 s75, s68, s75\n"
      "s_cselect_b32 s85, s69, s85\n"
      "s_add_u32 s76, s75, 128\n"
      "s_add_u32 s86, s85, 221184\n"
      "s_cmp_eq_u32 s76, s64\n"
      "s_cselect_b32 s76, s68, s76\n"
      "s_cselect_b32 s86, s69, s86\n"
      "s_add_u32 s77, s76, 128\n"
      "s_add_u32 s87, s86, 221184\n"
      "s_cmp_eq_u32 s77, s64\n"
      "s_cselect_b32 s77, s68, s77\n"
      "s_cselect_b32 s87, s69, s87\n"
      "s_add_u32 s78, s77, 128\n"
      "s_add_u32 s88, s87, 221184\n"
      "s_cmp_eq_u32 s78, s64\n"
      "s_cselect_b32 s78, s68, s78\n"
      "s_cselect_b32 s88, s69, s88\n"
      "s_add_u32 s79, s78, 128\n"
      "s_add_u32 s89, s88, 221184\n"
      "s_cmp_eq_u32 s79, s64\n"
      "s_cselect_b32 s79, s68, s79\n"
      "s_cselect_b32 s89, s69, s89\n"
      "s_xor_b32 s62, s99, 2\n"
      "s_mul_i32 s62, s62, 40960\n"
      "v_add_u32 v22, s62, v74\n"
      "s_xor_b32 s62, s99, 3\n"
      "s_mul_i32 s62, s62, 40960\n"
      "v_add_u32 v23, s62, v74\n"
      "s_mul_i32 s62, s93, 10\n"
      "s_sub_u32 s62, s91, s62\n"
      "s_lshl2_add_u32 s62, s62, s53\n"
      "s_lshl_b32 s62, s62, 5\n"
      "s_add_u32 s62, s62, s93\n"
      "s_lshl_b32 s62, s62, 7\n"
      "s_add_u32 s62, s62, 18644992\n"
      "v_mov_b32 v29, 1073741824\n"
      "s_mov_b32 exec_lo, 0xffff\n"
      "s_mov_b32 exec_hi, 0\n"
      "v_lshl_add_u32 v29, v1, 3, s62\n"
      "s_mov_b64 exec, -1\n"
      "s_mul_i32 s93, s91, 205\n"
      "s_lshr_b32 s93, s93, 10\n"
      "s_mul_i32 s62, s93, 5\n"
      "s_sub_u32 s91, s91, s62\n"
      "s_lshr_b32 s30, s93, 2\n"
      "s_and_b32 s29, s93, 3\n"
      "s_mul_i32 s29, s29, 7\n"
      "s_add_u32 s29, s29, s30\n"
      "s_cmp_ge_u32 s30, 7\n"
      "s_cselect_b32 s30, 21, 0\n"
      "s_add_u32 s29, s29, s30\n"
      "s_lshl_b32 s29, s29, 10\n"
      "s_mul_i32 s30, s91, 458752\n"
      "s_add_u32 s29, s29, s30\n"
      "s_add_u32 s29, s29, 5603328\n"
      "s_mul_i32 s30, s53, 114688\n"
      "s_add_u32 s29, s29, s30\n"
      "s_add_u32 s30, s29, 57344\n"
      "s_lshl3_add_u32 s62, s93, s91\n"
      "s_lshl_b32 s62, s62, 7\n"
      "s_add_u32 s62, s62, 18808832\n"
      "s_lshl3_add_u32 s63, s60, 4096\n"
      "s_cmp_eq_u32 s53, 0\n"
      "s_cselect_b32 s62, s62, 1073741824\n"
      "s_cselect_b32 s63, s63, 1073741824\n"
      "s_mov_b32 exec_lo, 0xffff0000\n"
      "s_mov_b32 exec_hi, 0\n"
      "s_sub_u32 s62, s62, 128\n"
      "v_lshl_add_u32 v29, v1, 3, s62\n"
      "s_mov_b64 exec, -1\n"
      "v_writelane_b32 v29, s63, 32\n"
      "v_lshrrev_b32 v44, 4, v1\n"
      "v_and_b32 v72, 1, v44\n"
      "v_lshrrev_b32 v44, 1, v44\n"
      "v_lshl_add_u32 v44, v72, 2, v44\n"
      "v_mul_u32_u24 v44, 1040, v44\n"
      "v_bfe_u32 v72, v1, 2, 2\n"
      "v_lshl_add_u32 v44, v72, 8, v44\n"
      "v_and_b32 v72, 3, v1\n"
      "v_lshl_add_u32 v44, v72, 3, v44\n"
      "s_lshl_b32 s64, s53, 6\n"
      "s_add_u32 s64, s64, 122880\n"
      "v_add_u32 v44, s64, v44\n"
      "v_mov_b64 v[36:37], 0\n"
      "v_mov_b64 v[38:39], 0\n"
      "s_lshl_b32 s63, s53, 10\n"
      "s_add_u32 s63, s63, 157696\n"
      "v_lshl_add_u32 v40, v1, 4, s63\n"
      "ds_write_b128 v40, v[36:39]\n"
      "v_lshlrev_b32 v40, 2, v1\n"
      "v_add_u32 v40, 161792, v40\n"
      "ds_write_b32 v40, v36\n"
      "v_lshlrev_b32 v5, 4, v1\n"
      "v_add_u32 v6, 4096, v5\n"
      "s_lshl_b32 s24, s98, 12\n"
      "s_add_u32 s24, s24, s95\n"
      "s_add_u32 s25, s24, 4096\n"
      "v_mov_b32 v32, s25\n"
      "v_writelane_b32 v32, s24, 0\n"
      "s_cmp_eq_u32 s53, 0\n"
      "s_cselect_b32 s63, 1073741824, -4\n"
      "v_add_u32 v32, s63, v32\n"
      "v_lshrrev_b32 v33, 11, v32\n"
      "v_xor_b32 v33, 524288, v33\n"
      "s_sub_u32 s91, s98, 0\n"
      "s_lshr_b32 s91, s91, 4\n"
      "s_lshr_b32 s63, s98, 4\n"
      "s_sub_u32 s63, s63, s91\n"
      "s_lshl_b32 s63, s63, 9\n"
      "v_and_b32 v72, 8, v1\n"
      "v_mul_u32_u24 v72, s63, v72\n"
      "v_and_b32 v73, 7, v1\n"
      "v_lshl_add_u32 v30, v73, 7, v72\n"
      "s_lshl_b32 s91, s91, 12\n"
      "s_add_u32 s91, s91, s95\n"
      "s_add_u32 s91, s91, 18644992\n"
      "v_add_u32 v30, s91, v30\n"
      "s_and_b32 s63, s53, 1\n"
      "s_mul_i32 s63, s63, 4608\n"
      "s_add_u32 s63, s63, 139520\n"
      "v_lshl_add_u32 v34, v1, 3, s63\n"
      "v_lshrrev_b32 v72, 1, v1\n"
      "v_min_u32 v72, 6, v72\n"
      "s_mul_i32 s62, s54, 28\n"
      "s_add_u32 s62, s62, s53\n"
      "v_lshl_add_u32 v72, v72, 2, s62\n"
      "v_min_u32 v72, 53, v72\n"
      "s_lshl_b32 s63, s56, 14\n"
      "s_add_u32 s63, s63, 18382848\n"
      "v_lshl_add_u32 v13, v72, 8, s63\n"
      "s_mul_i32 s63, s55, 80\n"
      "s_add_u32 s91, s63, 79\n"
      "s_lshr_b32 s63, s63, 7\n"
      "s_lshr_b32 s91, s91, 7\n"
      "s_sub_u32 s91, s91, s63\n"
      "v_and_b32 v73, s91, v1\n"
      "v_lshl_add_u32 v72, v72, 3, v73\n"
      "s_lshl_b32 s63, s63, 7\n"
      "s_add_u32 s63, s63, 18808832\n"
      "v_lshl_add_u32 v31, v72, 7, s63\n"
      "v_lshrrev_b32 v72, 3, v1\n"
      "v_and_b32 v72, 3, v72\n"
      "v_mul_u32_u24 v73, 4160, v72\n"
      "v_add_u32 v73, 122880, v73\n"
      "v_and_b32 v72, 7, v1\n"
      "v_lshl_add_u32 v35, v72, 3, v73\n"
      "s_add_u32 s62, s95, 122944\n"
      "v_add_u32 v40, s62, v5\n"
      "v_and_b32 v72, 31, v1\n"
      "v_lshlrev_b32 v42, 3, v72\n"
      "s_add_u32 s62, s95, 157696\n"
      "v_lshl_add_u32 v43, v72, 5, s62\n"
      "s_xor_b32 s31, s99, s53\n"
      "s_lshl2_add_u32 s31, s56, s31\n"
      "s_lshl_b32 s31, s31, 8\n"
      "s_add_u32 s31, s31, 8192\n"
      "s_lshl2_add_u32 s62, s54, s53\n"
      "s_cmp_ge_u32 s62, 6\n"
      "s_cselect_b32 s63, 1073741824, 6144\n"
      "v_add_u32 v7, s63, v5\n"
      "s_mul_i32 s62, s62, 7168\n"
      "s_mul_i32 s63, s55, 286720\n"
      "s_add_u32 s63, s63, s62\n"
      "s_add_u32 s27, s63, 5603328\n"
      "s_mul_i32 s63, s56, 458752\n"
      "s_add_u32 s63, s63, s62\n"
      "s_add_u32 s63, s63, 7897088\n"
      "s_mul_i32 s62, s54, 229376\n"
      "s_add_u32 s91, s63, s62\n"
      "s_sub_u32 s62, 229376, s62\n"
      "s_add_u32 s93, s63, s62\n"
      "s_sub_u32 s62, 4, s53\n"
      "s_and_b32 s62, s62, 3\n"
      "s_mul_i32 s62, s62, 57344\n"
      "s_add_u32 s44, s93, s62\n"
      "s_add_u32 s48, s91, s62\n"
      "s_sub_u32 s62, 5, s53\n"
      "s_and_b32 s62, s62, 3\n"
      "s_mul_i32 s62, s62, 57344\n"
      "s_add_u32 s45, s93, s62\n"
      "s_add_u32 s49, s91, s62\n"
      "s_sub_u32 s62, 6, s53\n"
      "s_and_b32 s62, s62, 3\n"
      "s_mul_i32 s62, s62, 57344\n"
      "s_add_u32 s46, s93, s62\n"
      "s_add_u32 s50, s91, s62\n"
      "s_sub_u32 s62, 7, s53\n"
      "s_and_b32 s62, s62, 3\n"
      "s_mul_i32 s62, s62, 57344\n"
      "s_add_u32 s47, s93, s62\n"
      "s_add_u32 s51, s91, s62\n"
      "s_add_u32 s91, s53, 3\n"
      "s_and_b32 s91, s91, 3\n"
      "s_lshl2_add_u32 s62, s91, 161888\n"
      "v_mov_b32 v15, s62\n"
      "s_mul_i32 s62, s53, 5120\n"
      "s_add_u32 s63, s62, 32768\n"
      "v_add_u32 v8, s63, v5\n"
      "s_mul_i32 s63, s91, 5120\n"
      "s_add_u32 s63, s63, 32768\n"
      "v_add_u32 v9, s63, v5\n"
      "s_xor_b32 s63, s60, 1\n"
      "s_lshl2_add_u32 s63, s63, s53\n"
      "s_mul_i32 s28, s63, 3072\n"
      "s_add_u32 s28, s28, 15237120\n"
      "v_cmp_eq_u32 vcc, s53, v1\n"
      "v_and_b32 v14, 48, v1\n"
      "v_add_u32 v14, 163072, v14\n"
      "v_cndmask_b32 v55, 0, 1, vcc\n"
      "v_mov_b32 v45, 161920\n"
      "s_add_u32 m0, s94, 24576\n"
      "s_nop 0\n"
      "buffer_load_dwordx4 v16, s[8:11], s81 offen offset:0 lds\n"
      "buffer_load_dwordx4 v17, s[8:11], s81 offen offset:1024 lds\n"
      "buffer_load_dwordx4 v18, s[8:11], s81 offen offset:2048 lds\n"
      "buffer_load_dwordx4 v19, s[8:11], s81 offen offset:3072 lds\n"
      "s_add_u32 m0, s95, 40960\n"
      "s_nop 0\n"
      "buffer_load_dwordx4 v20, s[4:7], s71 offen lds\n"
      "s_add_u32 m0, s95, 45056\n"
      "s_nop 0\n"
      "buffer_load_dwordx4 v21, s[4:7], s71 offen lds\n"
      "s_waitcnt vmcnt(13) lgkmcnt(0)\n"
      "v_cmp_eq_u32 vcc, 0, v1\n"
      "v_lshlrev_b32 v2, 2, v1\n"
      "v_add_u32 v2, 157696, v2\n"
      "v_cndmask_b32 v3, 0, 1, vcc\n"
      "s_barrier\n"
      "s_add_u32 m0, s94, 49152\n"
      "s_nop 0\n"
      "buffer_load_dwordx4 v16, s[8:11], s82 offen offset:0 lds\n"
      "buffer_load_dwordx4 v17, s[8:11], s82 offen offset:1024 lds\n"
      "buffer_load_dwordx4 v18, s[8:11], s82 offen offset:2048 lds\n"
      "buffer_load_dwordx4 v19, s[8:11], s82 offen offset:3072 lds\n"
      "s_add_u32 m0, s95, 65536\n"
      "s_nop 0\n"
      "buffer_load_dwordx4 v20, s[4:7], s72 offen lds\n"
      "s_add_u32 m0, s95, 69632\n"
      "s_nop 0\n"
      "buffer_load_dwordx4 v21, s[4:7], s72 offen lds\n"
      "s_waitcnt vmcnt(12)\n"
      "s_barrier\n"
      "ds_read_b64_tr_b16 v[88:89], v44 offset:0\n"
      "ds_read_b64_tr_b16 v[90:91], v44 offset:2080\n"
      "ds_read_b64_tr_b16 v[92:93], v44 offset:32\n"
      "ds_read_b64_tr_b16 v[94:95], v44 offset:2112\n"
      "ds_read_b64 v[46:47], v45\n"
      "s_waitcnt lgkmcnt(0)\n"
      "buffer_store_dwordx4 v[88:91], v5, s[20:23], s29 offen sc1\n"
      "buffer_store_dwordx4 v[92:95], v5, s[20:23], s30 offen sc1\n"
      "s_waitcnt lgkmcnt(0)\n"
      "v_readfirstlane_b32 s58, v46\n"
      "v_readfirstlane_b32 s59, v47\n"
      "v_mov_b32 v1, v55\n"
      "s_add_u32 m0, s94, 73728\n"
      "s_nop 0\n"
      "buffer_load_dwordx4 v16, s[8:11], s83 offen offset:0 lds\n"
      "buffer_load_dwordx4 v17, s[8:11], s83 offen offset:1024 lds\n"
      "buffer_load_dwordx4 v18, s[8:11], s83 offen offset:2048 lds\n"
      "buffer_load_dwordx4 v19, s[8:11], s83 offen offset:3072 lds\n"
      "s_add_u32 m0, s95, 90112\n"
      "s_nop 0\n"
      "buffer_load_dwordx4 v20, s[4:7], s73 offen lds\n"
      "s_add_u32 m0, s95, 94208\n"
      "s_nop 0\n"
      "buffer_load_dwordx4 v21, s[4:7], s73 offen lds\n"
      "s_waitcnt vmcnt(14)\n"
      "s_barrier\n"
      "s_add_u32 m0, s94, 98304\n"
      "s_nop 0\n"
      "buffer_load_dwordx4 v16, s[8:11], s84 offen offset:0 lds\n"
      "buffer_load_dwordx4 v17, s[8:11], s84 offen offset:1024 lds\n"
      "buffer_load_dwordx4 v18, s[8:11], s84 offen offset:2048 lds\n"
      "buffer_load_dwordx4 v19, s[8:11], s84 offen offset:3072 lds\n"
      "s_add_u32 m0, s95, 114688\n"
      "s_nop 0\n"
      "buffer_load_dwordx4 v20, s[4:7], s74 offen lds\n"
      "s_add_u32 m0, s95, 118784\n"
      "s_nop 0\n"
      "buffer_load_dwordx4 v21, s[4:7], s74 offen lds\n"
      "s_waitcnt vmcnt(14)\n"
      "s_barrier\n"
      "s_add_u32 m0, s94, 0\n"
      "s_nop 0\n"
      "buffer_load_dwordx4 v16, s[8:11], s85 offen offset:0 lds\n"
      "buffer_load_dwordx4 v17, s[8:11], s85 offen offset:1024 lds\n"
      "buffer_load_dwordx4 v18, s[8:11], s85 offen offset:2048 lds\n"
      "buffer_load_dwordx4 v19, s[8:11], s85 offen offset:3072 lds\n"
      "s_add_u32 m0, s95, 16384\n"
      "s_nop 0\n"
      "buffer_load_dwordx4 v20, s[4:7], s75 offen lds\n"
      "s_add_u32 m0, s95, 20480\n"
      "s_nop 0\n"
      "buffer_load_dwordx4 v21, s[4:7], s75 offen lds\n"
      "buffer_load_dwordx2 v[76:77], v42, s[20:23], s31 offen sc1\n"
      "s_waitcnt vmcnt(13)\n"
      "s_barrier\n"
      "s_add_u32 m0, s94, 24576\n"
      "s_nop 0\n"
      "buffer_load_dwordx4 v16, s[8:11], s86 offen offset:0 lds\n"
      "buffer_load_dwordx4 v17, s[8:11], s86 offen offset:1024 lds\n"
      "buffer_load_dwordx4 v18, s[8:11], s86 offen offset:2048 lds\n"
      "buffer_load_dwordx4 v19, s[8:11], s86 offen offset:3072 lds\n"
      "s_add_u32 m0, s95, 40960\n"
      "s_nop 0\n"
      "buffer_load_dwordx4 v20, s[4:7], s76 offen lds\n"
      "s_add_u32 m0, s95, 45056\n"
      "s_nop 0\n"
      "buffer_load_dwordx4 v21, s[4:7], s76 offen lds\n"
      "s_waitcnt vmcnt(13)\n"
      "s_barrier\n"
      "s_add_u32 m0, s94, 49152\n"
      "s_nop 0\n"
      "buffer_load_dwordx4 v16, s[8:11], s87 offen offset:0 lds\n"
      "buffer_load_dwordx4 v17, s[8:11], s87 offen offset:1024 lds\n"
      "buffer_load_dwordx4 v18, s[8:11], s87 offen offset:2048 lds\n"
      "buffer_load_dwordx4 v19, s[8:11], s87 offen offset:3072 lds\n"
      "s_add_u32 m0, s95, 65536\n"
      "s_nop 0\n"
      "buffer_load_dwordx4 v20, s[4:7], s77 offen lds\n"
      "s_add_u32 m0, s95, 69632\n"
      "s_nop 0\n"
      "buffer_load_dwordx4 v21, s[4:7], s77 offen lds\n"
      "s_waitcnt vmcnt(12)\n"
      "s_barrier\n"
      "buffer_store_dwordx2 v[46:47], v29, s[20:23], 0 offen sc1\n"
      "v_cmp_ne_u32 vcc, s58, v77\n"
      "s_cbranch_vccnz .Lt_1\n"
      ".Lt_2:\n"
      "v_and_b32 v82, s92, v76\n"
      "v_rcp_f32 v77, v82\n"
      "v_xor_b32 v85, 0x8000, v76\n"
      "s_mov_b32 s62, 0x01000100\n"
      "v_mul_f32 v80, 0xc0135761, v77\n"
      "v_mul_f32 v81, v77, v77\n"
      "v_cmp_nle_f32 vcc, 0x3089705f, v77\n"
      "v_mul_f32 v81, 0x3d372713, v81\n"
      "v_perm_b32 v84, v85, v85, s62\n"
      "v_mul_f32 v81, v81, v80\n"
      "v_mov_b32 v83, 1.0\n"
      "ds_write_b64 v43, v[84:85] offset:16\n"
      "s_cbranch_vccnz .Lb_4\n"
      ".Lb_5:\n"
      "ds_write_b128 v43, v[80:83]\n"
      "s_add_u32 m0, s94, 73728\n"
      "s_nop 0\n"
      "buffer_load_dwordx4 v16, s[8:11], s88 offen offset:0 lds\n"
      "buffer_load_dwordx4 v17, s[8:11], s88 offen offset:1024 lds\n"
      "buffer_load_dwordx4 v18, s[8:11], s88 offen offset:2048 lds\n"
      "buffer_load_dwordx4 v19, s[8:11], s88 offen offset:3072 lds\n"
      "s_add_u32 m0, s95, 90112\n"
      "s_nop 0\n"
      "buffer_load_dwordx4 v20, s[4:7], s78 offen lds\n"
      "s_add_u32 m0, s95, 94208\n"
      "s_nop 0\n"
      "buffer_load_dwordx4 v21, s[4:7], s78 offen lds\n"
      "s_waitcnt vmcnt(13)\n"
      "s_barrier\n"
      "s_add_u32 m0, s94, 98304\n"
      "s_nop 0\n"
      "buffer_load_dwordx4 v16, s[8:11], s89 offen offset:0 lds\n"
      "buffer_load_dwordx4 v17, s[8:11], s89 offen offset:1024 lds\n"
      "buffer_load_dwordx4 v18, s[8:11], s89 offen offset:2048 lds\n"
      "buffer_load_dwordx4 v19, s[8:11], s89 offen offset:3072 lds\n"
      "s_add_u32 m0, s95, 114688\n"
      "s_nop 0\n"
      "buffer_load_dwordx4 v20, s[4:7], s79 offen lds\n"
      "s_add_u32 m0, s95, 118784\n"
      "s_nop 0\n"
      "buffer_load_dwordx4 v21, s[4:7], s79 offen lds\n"
      "s_waitcnt vmcnt(13)\n"
      "s_barrier\n"
      "s_add_u32 m0, s95, 0\n"
      "s_nop 0\n"
      "buffer_load_dwordx4 v22, s[4:7], s70 offen lds\n"
      "s_add_u32 m0, s95, 4096\n"
      "s_nop 0\n"
      "buffer_load_dwordx4 v22, s[4:7], s71 offen lds\n"
      "s_add_u32 m0, s95, 8192\n"
      "s_nop 0\n"
      "buffer_load_dwordx4 v22, s[4:7], s72 offen lds\n"
      "s_add_u32 m0, s95, 12288\n"
      "s_nop 0\n"
      "buffer_load_dwordx4 v22, s[4:7], s73 offen lds\n"
      "s_add_u32 m0, s95, 16384\n"
      "s_nop 0\n"
      "buffer_load_dwordx4 v22, s[4:7], s74 offen lds\n"
      "s_waitcnt vmcnt(11)\n"
      "s_barrier\n"
      "s_add_u32 m0, s95, 20480\n"
      "s_nop 0\n"
      "buffer_load_dwordx4 v22, s[4:7], s75 offen lds\n"
      "s_add_u32 m0, s95, 24576\n"
      "s_nop 0\n"
      "buffer_load_dwordx4 v22, s[4:7], s76 offen lds\n"
      "s_add_u32 m0, s95, 28672\n"
      "s_nop 0\n"
      "buffer_load_dwordx4 v22, s[4:7], s77 offen lds\n"
      "s_add_u32 m0, s95, 32768\n"
      "s_nop 0\n"
      "buffer_load_dwordx4 v22, s[4:7], s78 offen lds\n"
      "s_add_u32 m0, s95, 36864\n"
      "s_nop 0\n"
      "buffer_load_dwordx4 v22, s[4:7], s79 offen lds\n"
      "s_waitcnt vmcnt(10) lgkmcnt(0)\n"
      "s_barrier\n"
      "s_add_u32 m0, s95, 49152\n"
      "s_nop 0\n"
      "buffer_load_dwordx4 v23, s[4:7], s70 offen lds\n"
      "s_add_u32 m0, s95, 53248\n"
      "s_nop 0\n"
      "buffer_load_dwordx4 v23, s[4:7], s71 offen lds\n"
      "s_add_u32 m0, s95, 57344\n"
      "s_nop 0\n"
      "buffer_load_dwordx4 v23, s[4:7], s72 offen lds\n"
      "s_add_u32 m0, s95, 61440\n"
      "s_nop 0\n"
      "buffer_load_dwordx4 v23, s[4:7], s73 offen lds\n"
      "s_add_u32 m0, s95, 65536\n"
      "s_nop 0\n"
      "buffer_load_dwordx4 v23, s[4:7], s74 offen lds\n"
      "buffer_load_dwordx2 v[50:51], v31, s[20:23], 0 offen sc1\n"
      "buffer_load_dwordx2 v[48:49], v30, s[20:23], 0 offen sc1\n"
      "s_waitcnt vmcnt(12)\n"
      "s_barrier\n"
      "s_add_u32 m0, s95, 69632\n"
      "s_nop 0\n"
      "buffer_load_dwordx4 v23, s[4:7], s75 offen lds\n"
      "s_add_u32 m0, s95, 73728\n"
      "s_nop 0\n"
      "buffer_load_dwordx4 v23, s[4:7], s76 offen lds\n"
      "s_add_u32 m0, s95, 77824\n"
      "s_nop 0\n"
      "buffer_load_dwordx4 v23, s[4:7], s77 offen lds\n"
      "s_add_u32 m0, s95, 81920\n"
      "s_nop 0\n"
      "buffer_load_dwordx4 v23, s[4:7], s78 offen lds\n"
      "s_add_u32 m0, s95, 86016\n"
      "s_nop 0\n"
      "buffer_load_dwordx4 v23, s[4:7], s79 offen lds\n"
      "ds_add_u32 v2, v3 offset:4152\n"
      "s_waitcnt vmcnt(12)\n"
      "ds_add_u32 v2, v3 offset:2072\n"
      "ds_read_b32 v4, v2 offset:4152\n"
      "s_waitcnt lgkmcnt(0)\n"
      "v_readfirstlane_b32 s2, v4\n"
      "s_cmp_lt_u32 s2, 4\n"
      "s_cbranch_scc1 .Lw_6\n"
      ".Lw_7:\n"
      ".LS2:\n"
      "buffer_load_dwordx4 v[56:59], v5, s[36:39], s24 offen nt\n"
      "buffer_load_dwordx4 v[60:63], v5, s[36:39], s25 offen nt\n"
      "buffer_load_dword v52, v32, s[36:39], 0 offen nt\n"
      "buffer_load_ushort v53, v33, s[32:35], 0 offen\n"
      ".LS3:\n"
      "ds_write_b64 v34, v[36:37]\n"
      "ds_write_b64 v35, v[36:37]\n"
      "s_waitcnt vmcnt(4)\n"
      "ds_add_u32 v2, v3 offset:3096\n"
      "s_waitcnt vmcnt(10)\n"
      "v_cmp_ne_u64 vcc, s[58:59], v[50:51]\n"
      "s_cbranch_vccnz .Lf_9\n"
      ".Lf_10:\n"
      "s_add_u32 s66, s27, 57344\n"
      "s_add_u32 s67, s27, 114688\n"
      "s_add_u32 s68, s27, 172032\n"
      "s_add_u32 s69, s27, 229376\n"
      "s_waitcnt vmcnt(9)\n"
      "v_cmp_ne_u64 vcc, s[58:59], v[48:49]\n"
      "s_cbranch_vccnz .Lf_12\n"
      ".Lf_13:\n"
      "s_mul_i32 s62, s98, 4224\n"
      "s_add_u32 s62, s62, s95\n"
      "s_add_u32 m0, s95, 140032\n"
      "s_add_u32 s63, s62, 32768\n"
      "buffer_load_dwordx4 v5, s[20:23], s63 offen sc1 lds\n"
      "s_add_u32 m0, s95, 144640\n"
      "s_add_u32 s63, s62, 36992\n"
      "buffer_load_dwordx4 v5, s[20:23], s63 offen sc1 lds\n"
      "s_waitcnt vmcnt(6)\n"
      "buffer_load_dwordx4 v[116:119], v5, s[20:23], s27 offen offset:0 sc1\n"
      "buffer_load_dwordx4 v[136:139], v5, s[20:23], s27 offen offset:1024 sc1\n"
      "s_waitcnt vmcnt(6)\n"
      "buffer_load_dwordx4 v[156:159], v5, s[20:23], s27 offen offset:2048 sc1\n"
      "buffer_load_dwordx4 v[176:179], v5, s[20:23], s27 offen offset:3072 sc1\n"
      "s_mov_b64 vcc, 0\n"
      "s_branch .LWARM\n"
      ".LWARME:\n"
      "s_waitcnt vmcnt(6)\n"
      "s_cmp_eq_u32 s53, 0\n"
      "s_cbranch_scc0 .Ld_15\n"
      "v_readlane_b32 s62, v53, 0\n"
      "v_readlane_b32 s63, v53, 1\n"
      "s_mov_b64 exec, 1\n"
      "v_lshlrev_b32 v72, 16, v56\n"
      "s_lshl_b32 s62, s62, 16\n"
      "v_and_b32 v73, s92, v56\n"
      "v_add_f32 v72, s62, v72\n"
      "v_cvt_pk_bf16_f32 v56, v72, v73\n"
      "v_lshlrev_b32 v72, 16, v60\n"
      "s_lshl_b32 s63, s63, 16\n"
      "v_and_b32 v73, s92, v60\n"
      "v_add_f32 v72, s63, v72\n"
      "v_cvt_pk_bf16_f32 v60, v72, v73\n"
      "s_mov_b64 exec, -1\n"
      ".Ld_15:\n"
      "v_readlane_b32 s62, v52, 1\n"
      "ds_write_b128 v40, v[60:63] offset:8320\n"
      "v_alignbyte_b32 v103, v63, v62, 2\n"
      "v_writelane_b32 v72, s62, 0\n"
      "v_alignbyte_b32 v101, v61, v60, 2\n"
      "v_alignbyte_b32 v102, v62, v61, 2\n"
      "v_mov_b32_dpp v72, v63 wave_shr:1 row_mask:0xf bank_mask:0xf\n"
      "v_alignbyte_b32 v100, v60, v72, 2\n"
      "ds_write_b128 v40, v[100:103] offset:12480\n"
      "s_waitcnt vmcnt(6)\n"
      "buffer_load_dwordx4 v[196:199], v6, s[20:23], s27 offen offset:0 sc1\n"
      "buffer_load_dwordx4 v[216:219], v6, s[20:23], s27 offen offset:1024 sc1\n"
      "ds_write_b128 v40, v[56:59] offset:0\n"
      "v_alignbyte_b32 v99, v59, v58, 2\n"
      "v_alignbyte_b32 v97, v57, v56, 2\n"
      "v_alignbyte_b32 v98, v58, v57, 2\n"
      "v_mov_b32_dpp v52, v59 wave_shr:1 row_mask:0xf bank_mask:0xf\n"
      "v_alignbyte_b32 v96, v56, v52, 2\n"
      "ds_write_b128 v40, v[96:99] offset:4160\n"
      "s_waitcnt vmcnt(6)\n"
      "buffer_load_dwordx4 v[236:239], v7, s[20:23], s27 offen offset:0 sc1\n"
      "buffer_load_dwordx4 v[120:123], v5, s[20:23], s66 offen offset:0 sc1\n"
      "s_waitcnt vmcnt(8)\n"
      "ds_add_u32 v2, v3 offset:4304\n"
      "s_waitcnt vmcnt(6)\n"
      "buffer_load_dwordx4 v[140:143], v5, s[20:23], s66 offen offset:1024 sc1\n"
      "buffer_load_dwordx4 v[160:163], v5, s[20:23], s66 offen offset:2048 sc1\n"
      "s_waitcnt vmcnt(6)\n"
      "buffer_load_dwordx4 v[180:183], v5, s[20:23], s66 offen offset:3072 sc1\n"
      "buffer_load_dwordx4 v[200:203], v6, s[20:23], s66 offen offset:0 sc1\n"
      "s_waitcnt vmcnt(6)\n"
      "buffer_load_dwordx4 v[220:223], v6, s[20:23], s66 offen offset:1024 sc1\n"
      "buffer_load_dwordx4 v[240:243], v7, s[20:23], s66 offen offset:0 sc1\n"
      "s_waitcnt vmcnt(6)\n"
      "buffer_load_dwordx4 v[124:127], v5, s[20:23], s67 offen offset:0 sc1\n"
      "buffer_load_dwordx4 v[144:147], v5, s[20:23], s67 offen offset:1024 sc1\n"
      "s_waitcnt vmcnt(6)\n"
      "buffer_load_dwordx4 v[164:167], v5, s[20:23], s67 offen offset:2048 sc1\n"
      "buffer_load_dwordx4 v[184:187], v5, s[20:23], s67 offen offset:3072 sc1\n"
      "s_waitcnt vmcnt(6)\n"
      "buffer_load_dwordx4 v[204:207], v6, s[20:23], s67 offen offset:0 sc1\n"
      "buffer_load_dwordx4 v[224:227], v6, s[20:23], s67 offen offset:1024 sc1\n"
      "s_waitcnt vmcnt(6)\n"
      "buffer_load_dwordx4 v[244:247], v7, s[20:23], s67 offen offset:0 sc1\n"
      "buffer_load_dwordx4 v[128:131], v5, s[20:23], s68 offen offset:0 sc1\n"
      "s_waitcnt vmcnt(6)\n"
      "buffer_load_dwordx4 v[148:151], v5, s[20:23], s68 offen offset:1024 sc1\n"
      "buffer_load_dwordx4 v[168:171], v5, s[20:23], s68 offen offset:2048 sc1\n"
      "s_waitcnt vmcnt(6)\n"
      "buffer_load_dwordx4 v[188:191], v5, s[20:23], s68 offen offset:3072 sc1\n"
      "buffer_load_dwordx4 v[208:211], v6, s[20:23], s68 offen offset:0 sc1\n"
      "s_waitcnt vmcnt(6)\n"
      "buffer_load_dwordx4 v[228:231], v6, s[20:23], s68 offen offset:1024 sc1\n"
      "buffer_load_dwordx4 v[248:251], v7, s[20:23], s68 offen offset:0 sc1\n"
      "s_waitcnt vmcnt(6)\n"
      "buffer_load_dwordx4 v[132:135], v5, s[20:23], s69 offen offset:0 sc1\n"
      "buffer_load_dwordx4 v[152:155], v5, s[20:23], s69 offen offset:1024 sc1\n"
      "s_waitcnt vmcnt(6)\n"
      "buffer_load_dwordx4 v[172:175], v5, s[20:23], s69 offen offset:2048 sc1\n"
      "buffer_load_dwordx4 v[192:195], v5, s[20:23], s69 offen offset:3072 sc1\n"
      "s_waitcnt vmcnt(6)\n"
      "buffer_load_dwordx4 v[212:215], v6, s[20:23], s69 offen offset:0 sc1\n"
      "buffer_load_dwordx4 v[232:235], v6, s[20:23], s69 offen offset:1024 sc1\n"
      "s_waitcnt vmcnt(6)\n"
      "buffer_load_dwordx4 v[252:255], v7, s[20:23], s69 offen offset:0 sc1\n"
      "s_and_b32 s75, s58, 0x1fff\n"
      "s_or_b32 s75, s75, 0x4000\n"
      "s_lshl_b32 s75, s75, 16\n"
      "s_mov_b32 s74, 128\n"
      "s_mov_b32 s73, 0x100000\n"
      "ds_read_b32 v4, v2 offset:4168\n"
      "s_waitcnt lgkmcnt(0)\n"
      "v_readfirstlane_b32 s2, v4\n"
      "s_cmp_lt_u32 s2, 4\n"
      "s_cbranch_scc1 .Lw_16\n"
      ".Lw_17:\n"
      "buffer_load_dwordx2 v[10:11], v13, s[20:23], 0 offen sc1\n"
      "s_waitcnt vmcnt(0)\n"
      "v_cmp_ne_u64 vcc, s[58:59], v[10:11]\n"
      "s_cbranch_vccnz .Lf_19\n"
      ".Lf_20:\n"
      "buffer_load_dwordx4 v[32:35], v5, s[20:23], s44 offen offset:0 sc1\n"
      "buffer_load_dwordx4 v[36:39], v5, s[20:23], s44 offen offset:1024 sc1\n"
      "buffer_load_dwordx4 v[40:43], v5, s[20:23], s44 offen offset:2048 sc1\n"
      "buffer_load_dwordx4 v[44:47], v5, s[20:23], s44 offen offset:3072 sc1\n"
      "buffer_load_dwordx4 v[48:51], v6, s[20:23], s44 offen offset:0 sc1\n"
      "buffer_load_dwordx4 v[52:55], v6, s[20:23], s44 offen offset:1024 sc1\n"
      "buffer_load_dwordx4 v[56:59], v7, s[20:23], s44 offen offset:0 sc1\n"
      "buffer_load_dwordx4 v[60:63], v5, s[20:23], s45 offen offset:0 sc1\n"
      "s_waitcnt vmcnt(6)\n"
      "buffer_load_dwordx4 v[64:67], v5, s[20:23], s45 offen offset:1024 sc1\n"
      "s_setprio 1\n"
      "s_waitcnt vmcnt(6)\n"
      "v_mfma_f32_16x16x32_bf16 v[16:19], v[116:119], v[32:35], 0\n"
      "v_mfma_f32_16x16x32_bf16 v[16:19], v[136:139], v[36:39], v[16:19]\n"
      "buffer_load_dwordx4 v[68:71], v5, s[20:23], s45 offen offset:2048 sc1\n"
      "v_mfma_f32_16x16x32_bf16 v[16:19], v[156:159], v[40:43], v[16:19]\n"
      "s_waitcnt vmcnt(3)\n"
      "v_mfma_f32_16x16x32_bf16 v[16:19], v[176:179], v[44:47], v[16:19]\n"
      "buffer_load_dwordx4 v[72:75], v5, s[20:23], s45 offen offset:3072 sc1\n"
      "ds_read_b32 v4, v2 offset:4276\n"
      "v_mfma_f32_16x16x32_bf16 v[16:19], v[196:199], v[48:51], v[16:19]\n"
      "v_mfma_f32_16x16x32_bf16 v[16:19], v[216:219], v[52:55], v[16:19]\n"
      "buffer_load_dwordx4 v[76:79], v6, s[20:23], s45 offen offset:0 sc1\n"
      "v_mfma_f32_16x16x32_bf16 v[16:19], v[236:239], v[56:59], v[16:19]\n"
      "v_mfma_f32_16x16x32_bf16 v[20:23], v[120:123], v[32:35], 0\n"
      "buffer_load_dwordx4 v[80:83], v6, s[20:23], s45 offen offset:1024 sc1\n"
      "v_mfma_f32_16x16x32_bf16 v[20:23], v[140:143], v[36:39], v[20:23]\n"
      "s_waitcnt lgkmcnt(0)\n"
      "v_readfirstlane_b32 s2, v4\n"
      "s_cmp_lt_u32 s2, 4\n"
      "s_cbranch_scc1 .Lw_22\n"
      ".Lw_23:\n"
      "v_mfma_f32_16x16x32_bf16 v[20:23], v[160:163], v[40:43], v[20:23]\n"
      "buffer_load_dwordx4 v[84:87], v7, s[20:23], s45 offen offset:0 sc1\n"
      "v_mfma_f32_16x16x32_bf16 v[20:23], v[180:183], v[44:47], v[20:23]\n"
      "v_mfma_f32_16x16x32_bf16 v[20:23], v[200:203], v[48:51], v[20:23]\n"
      "buffer_load_dwordx4 v[88:91], v5, s[20:23], s46 offen offset:0 sc1\n"
      "ds_write_b128 v8, v[16:19] offset:0\n"
      "v_mfma_f32_16x16x32_bf16 v[20:23], v[220:223], v[52:55], v[20:23]\n"
      "v_mfma_f32_16x16x32_bf16 v[20:23], v[240:243], v[56:59], v[20:23]\n"
      "buffer_load_dwordx4 v[92:95], v5, s[20:23], s46 offen offset:1024 sc1\n"
      "v_mfma_f32_16x16x32_bf16 v[24:27], v[124:127], v[32:35], 0\n"
      "v_mfma_f32_16x16x32_bf16 v[24:27], v[144:147], v[36:39], v[24:27]\n"
      "buffer_load_dwordx4 v[96:99], v5, s[20:23], s46 offen offset:2048 sc1\n"
      "ds_add_u32 v2, v1 offset:4192\n"
      "v_mfma_f32_16x16x32_bf16 v[24:27], v[164:167], v[40:43], v[24:27]\n"
      "v_mfma_f32_16x16x32_bf16 v[24:27], v[184:187], v[44:47], v[24:27]\n"
      "buffer_load_dwordx4 v[100:103], v5, s[20:23], s46 offen offset:3072 sc1\n"
      "v_mfma_f32_16x16x32_bf16 v[24:27], v[204:207], v[48:51], v[24:27]\n"
      "ds_write_b128 v8, v[20:23] offset:1024\n"
      "v_mfma_f32_16x16x32_bf16 v[24:27], v[224:227], v[52:55], v[24:27]\n"
      "buffer_load_dwordx4 v[104:107], v6, s[20:23], s46 offen offset:0 sc1\n"
      "v_mfma_f32_16x16x32_bf16 v[24:27], v[244:247], v[56:59], v[24:27]\n"
      "v_mfma_f32_16x16x32_bf16 v[28:31], v[128:131], v[32:35], 0\n"
      "buffer_load_dwordx4 v[108:111], v6, s[20:23], s46 offen offset:1024 sc1\n"
      "v_mfma_f32_16x16x32_bf16 v[28:31], v[148:151], v[36:39], v[28:31]\n"
      "ds_add_u32 v2, v1 offset:4192\n"
      "v_mfma_f32_16x16x32_bf16 v[28:31], v[168:171], v[40:43], v[28:31]\n"
      "buffer_load_dwordx4 v[112:115], v7, s[20:23], s46 offen offset:0 sc1\n"
      "v_mfma_f32_16x16x32_bf16 v[28:31], v[188:191], v[44:47], v[28:31]\n"
      "v_mfma_f32_16x16x32_bf16 v[28:31], v[208:211], v[48:51], v[28:31]\n"
      "ds_write_b128 v8, v[24:27] offset:2048\n"
      "v_mfma_f32_16x16x32_bf16 v[28:31], v[228:231], v[52:55], v[28:31]\n"
      "v_mfma_f32_16x16x32_bf16 v[28:31], v[248:251], v[56:59], v[28:31]\n"
      "buffer_load_dwordx2 v[10:11], v13, s[20:23], s74 offen sc1\n"
      "v_mfma_f32_16x16x32_bf16 v[16:19], v[132:135], v[32:35], 0\n"
      "buffer_load_dwordx4 v[32:35], v5, s[20:23], s47 offen offset:0 sc1\n"
      "ds_read_b32 v4, v15\n"
      "ds_read_b128 v[20:23], v9 offset:0\n"
      "v_mfma_f32_16x16x32_bf16 v[16:19], v[152:155], v[36:39], v[16:19]\n"
      "ds_add_u32 v2, v1 offset:4192\n"
      "v_mfma_f32_16x16x32_bf16 v[16:19], v[172:175], v[40:43], v[16:19]\n"
      "v_mfma_f32_16x16x32_bf16 v[16:19], v[192:195], v[44:47], v[16:19]\n"
      "s_waitcnt lgkmcnt(2)\n"
      "v_cmp_gt_u32 vcc, 1, v4\n"
      "v_mfma_f32_16x16x32_bf16 v[16:19], v[212:215], v[48:51], v[16:19]\n"
      "buffer_load_dwordx4 v[36:39], v5, s[20:23], s47 offen offset:1024 sc1\n"
      "ds_write_b128 v8, v[28:31] offset:3072\n"
      "v_mfma_f32_16x16x32_bf16 v[16:19], v[232:235], v[52:55], v[16:19]\n"
      "s_cbranch_vccnz .Lp_25\n"
      ".Lp_26:\n"
      "v_mfma_f32_16x16x32_bf16 v[16:19], v[252:255], v[56:59], v[16:19]\n"
      "s_waitcnt vmcnt(14)\n"
      "s_waitcnt lgkmcnt(2)\n"
      "v_mfma_f32_16x16x32_bf16 v[20:23], v[116:119], v[60:63], v[20:23]\n"
      "ds_read_b32 v4, v15\n"
      "ds_read_b128 v[24:27], v9 offset:1024\n"
      "v_mfma_f32_16x16x32_bf16 v[20:23], v[136:139], v[64:67], v[20:23]\n"
      "buffer_load_dwordx4 v[40:43], v5, s[20:23], s47 offen offset:2048 sc1\n"
      "ds_add_u32 v2, v1 offset:4192\n"
      "v_mfma_f32_16x16x32_bf16 v[20:23], v[156:159], v[68:71], v[20:23]\n"
      "s_waitcnt vmcnt(11)\n"
      "v_mfma_f32_16x16x32_bf16 v[20:23], v[176:179], v[72:75], v[20:23]\n"
      "s_waitcnt lgkmcnt(2)\n"
      "v_cmp_gt_u32 vcc, 2, v4\n"
      "v_mfma_f32_16x16x32_bf16 v[20:23], v[196:199], v[76:79], v[20:23]\n"
      "ds_write_b128 v8, v[16:19] offset:4096\n"
      "v_mfma_f32_16x16x32_bf16 v[20:23], v[216:219], v[80:83], v[20:23]\n"
      "buffer_load_dwordx4 v[44:47], v5, s[20:23], s47 offen offset:3072 sc1\n"
      "s_cbranch_vccnz .Lp_27\n"
      ".Lp_28:\n"
      "v_mfma_f32_16x16x32_bf16 v[20:23], v[236:239], v[84:87], v[20:23]\n"
      "s_waitcnt lgkmcnt(2)\n"
      "v_mfma_f32_16x16x32_bf16 v[24:27], v[120:123], v[60:63], v[24:27]\n"
      "ds_read_b32 v4, v15\n"
      "ds_read_b128 v[28:31], v9 offset:2048\n"
      "v_mfma_f32_16x16x32_bf16 v[24:27], v[140:143], v[64:67], v[24:27]\n"
      "ds_add_u32 v2, v1 offset:4192\n"
      "v_mfma_f32_16x16x32_bf16 v[24:27], v[160:163], v[68:71], v[24:27]\n"
      "buffer_load_dwordx4 v[48:51], v6, s[20:23], s47 offen offset:0 sc1\n"
      "v_mfma_f32_16x16x32_bf16 v[24:27], v[180:183], v[72:75], v[24:27]\n"
      "s_waitcnt lgkmcnt(2)\n"
      "v_cmp_gt_u32 vcc, 3, v4\n"
      "v_mfma_f32_16x16x32_bf16 v[24:27], v[200:203], v[76:79], v[24:27]\n"
      "ds_write_b128 v8, v[20:23] offset:20480\n"
      "v_mfma_f32_16x16x32_bf16 v[24:27], v[220:223], v[80:83], v[24:27]\n"
      "s_cbranch_vccnz .Lp_29\n"
      ".Lp_30:\n"
      "v_mfma_f32_16x16x32_bf16 v[24:27], v[240:243], v[84:87], v[24:27]\n"
      "buffer_load_dwordx4 v[52:55], v6, s[20:23], s47 offen offset:1024 sc1\n"
      "s_waitcnt lgkmcnt(2)\n"
      "v_mfma_f32_16x16x32_bf16 v[28:31], v[124:127], v[60:63], v[28:31]\n"
      "ds_read_b32 v4, v15\n"
      "ds_read_b128 v[16:19], v9 offset:3072\n"
      "v_mfma_f32_16x16x32_bf16 v[28:31], v[144:147], v[64:67], v[28:31]\n"
      "ds_add_u32 v2, v1 offset:4192\n"
      "v_mfma_f32_16x16x32_bf16 v[28:31], v[164:167], v[68:71], v[28:31]\n"
      "v_mfma_f32_16x16x32_bf16 v[28:31], v[184:187], v[72:75], v[28:31]\n"
      "buffer_load_dwordx4 v[56:59], v7, s[20:23], s47 offen offset:0 sc1\n"
      "s_waitcnt lgkmcnt(2)\n"
      "v_cmp_gt_u32 vcc, 4, v4\n"
      "v_mfma_f32_16x16x32_bf16 v[28:31], v[204:207], v[76:79], v[28:31]\n"
      "ds_write_b128 v8, v[24:27] offset:21504\n"
      "v_mfma_f32_16x16x32_bf16 v[28:31], v[224:227], v[80:83], v[28:31]\n"
      "s_cbranch_vccnz .Lp_31\n"
      ".Lp_32:\n"
      "v_mfma_f32_16x16x32_bf16 v[28:31], v[244:247], v[84:87], v[28:31]\n"
      "s_waitcnt lgkmcnt(2)\n"
      "v_mfma_f32_16x16x32_bf16 v[16:19], v[128:131], v[60:63], v[16:19]\n"
      "ds_read_b32 v4, v15\n"
      "ds_read_b128 v[20:23], v9 offset:4096\n"
      "v_mfma_f32_16x16x32_bf16 v[16:19], v[148:151], v[64:67], v[16:19]\n"
      "ds_add_u32 v2, v1 offset:4192\n"
      "v_mfma_f32_16x16x32_bf16 v[16:19], v[168:171], v[68:71], v[16:19]\n"
      "v_mfma_f32_16x16x32_bf16 v[16:19], v[188:191], v[72:75], v[16:19]\n"
      "s_waitcnt lgkmcnt(2)\n"
      "v_cmp_gt_u32 vcc, 5, v4\n"
      "v_mfma_f32_16x16x32_bf16 v[16:19], v[208:211], v[76:79], v[16:19]\n"
      "ds_write_b128 v8, v[28:31] offset:22528\n"
      "v_mfma_f32_16x16x32_bf16 v[16:19], v[228:231], v[80:83], v[16:19]\n"
      "s_cbranch_vccnz .Lp_33\n"
      ".Lp_34:\n"
      "v_mfma_f32_16x16x32_bf16 v[16:19], v[248:251], v[84:87], v[16:19]\n"
      "s_waitcnt lgkmcnt(2)\n"
      "v_mfma_f32_16x16x32_bf16 v[20:23], v[132:135], v[60:63], v[20:23]\n"
      "ds_read_b32 v4, v15\n"
      "ds_read_b128 v[24:27], v9 offset:20480\n"
      "v_mfma_f32_16x16x32_bf16 v[20:23], v[152:155], v[64:67], v[20:23]\n"
      "ds_add_u32 v2, v1 offset:4192\n"
      "v_mfma_f32_16x16x32_bf16 v[20:23], v[172:175], v[68:71], v[20:23]\n"
      "v_mfma_f32_16x16x32_bf16 v[20:23], v[192:195], v[72:75], v[20:23]\n"
      "s_waitcnt lgkmcnt(2)\n"
      "v_cmp_gt_u32 vcc, 6, v4\n"
      "v_mfma_f32_16x16x32_bf16 v[20:23], v[212:215], v[76:79], v[20:23]\n"
      "ds_write_b128 v8, v[16:19] offset:23552\n"
      "v_mfma_f32_16x16x32_bf16 v[20:23], v[232:235], v[80:83], v[20:23]\n"
      "s_cbranch_vccnz .Lp_35\n"
      ".Lp_36:\n"
      "v_mfma_f32_16x16x32_bf16 v[20:23], v[252:255], v[84:87], v[20:23]\n"
      "s_waitcnt vmcnt(12)\n"
      "s_waitcnt lgkmcnt(2)\n"
      "v_mfma_f32_16x16x32_bf16 v[24:27], v[116:119], v[88:91], v[24:27]\n"
      "ds_read_b32 v4, v15\n"
      "ds_read_b128 v[28:31], v9 offset:21504\n"
      "v_mfma_f32_16x16x32_bf16 v[24:27], v[136:139], v[92:95], v[24:27]\n"
      "ds_add_u32 v2, v1 offset:4192\n"
      "v_mfma_f32_16x16x32_bf16 v[24:27], v[156:159], v[96:99], v[24:27]\n"
      "s_waitcnt vmcnt(8)\n"
      "v_mfma_f32_16x16x32_bf16 v[24:27], v[176:179], v[100:103], v[24:27]\n"
      "s_waitcnt lgkmcnt(2)\n"
      "v_cmp_gt_u32 vcc, 7, v4\n"
      "v_mfma_f32_16x16x32_bf16 v[24:27], v[196:199], v[104:107], v[24:27]\n"
      "ds_write_b128 v8, v[20:23] offset:24576\n"
      "v_mfma_f32_16x16x32_bf16 v[24:27], v[216:219], v[108:111], v[24:27]\n"
      "s_cbranch_vccnz .Lp_37\n"
      ".Lp_38:\n"
      "v_mfma_f32_16x16x32_bf16 v[24:27], v[236:239], v[112:115], v[24:27]\n"
      "s_waitcnt lgkmcnt(2)\n"
      "v_mfma_f32_16x16x32_bf16 v[28:31], v[120:123], v[88:91], v[28:31]\n"
      "s_waitcnt vmcnt(7)\n"
      "v_cmp_ne_u64 vcc, s[58:59], v[10:11]\n"
      "s_cbranch_vccnz .Lf_39\n"
      ".Lf_40:\n"
      "buffer_load_dwordx4 v[60:63], v5, s[20:23], s48 offen offset:0 sc1\n"
      "ds_read_b32 v4, v15\n"
      "ds_read_b128 v[16:19], v9 offset:22528\n"
      "v_mfma_f32_16x16x32_bf16 v[28:31], v[140:143], v[92:95], v[28:31]\n"
      "ds_add_u32 v2, v1 offset:4192\n"
      "v_mfma_f32_16x16x32_bf16 v[28:31], v[160:163], v[96:99], v[28:31]\n"
      "buffer_load_dwordx4 v[64:67], v5, s[20:23], s48 offen offset:1024 sc1\n"
      "v_mfma_f32_16x16x32_bf16 v[28:31], v[180:183], v[100:103], v[28:31]\n"
      "s_waitcnt lgkmcnt(2)\n"
      "v_cmp_gt_u32 vcc, 8, v4\n"
      "v_mfma_f32_16x16x32_bf16 v[28:31], v[200:203], v[104:107], v[28:31]\n"
      "buffer_load_dwordx4 v[68:71], v5, s[20:23], s48 offen offset:2048 sc1\n"
      "ds_write_b128 v8, v[24:27] offset:40960\n"
      "v_mfma_f32_16x16x32_bf16 v[28:31], v[220:223], v[108:111], v[28:31]\n"
      "s_cbranch_vccnz .Lp_42\n"
      ".Lp_43:\n"
      "v_mfma_f32_16x16x32_bf16 v[28:31], v[240:243], v[112:115], v[28:31]\n"
      "buffer_load_dwordx4 v[72:75], v5, s[20:23], s48 offen offset:3072 sc1\n"
      "s_waitcnt lgkmcnt(2)\n"
      "v_mfma_f32_16x16x32_bf16 v[16:19], v[124:127], v[88:91], v[16:19]\n"
      "ds_read_b32 v4, v15\n"
      "ds_read_b128 v[20:23], v9 offset:23552\n"
      "v_mfma_f32_16x16x32_bf16 v[16:19], v[144:147], v[92:95], v[16:19]\n"
      "buffer_load_dwordx4 v[76:79], v6, s[20:23], s48 offen offset:0 sc1\n"
      "ds_add_u32 v2, v1 offset:4192\n"
      "v_mfma_f32_16x16x32_bf16 v[16:19], v[164:167], v[96:99], v[16:19]\n"
      "v_mfma_f32_16x16x32_bf16 v[16:19], v[184:187], v[100:103], v[16:19]\n"
      "buffer_load_dwordx4 v[80:83], v6, s[20:23], s48 offen offset:1024 sc1\n"
      "s_waitcnt lgkmcnt(2)\n"
      "v_cmp_gt_u32 vcc, 9, v4\n"
      "v_mfma_f32_16x16x32_bf16 v[16:19], v[204:207], v[104:107], v[16:19]\n"
      "ds_write_b128 v8, v[28:31] offset:41984\n"
      "v_mfma_f32_16x16x32_bf16 v[16:19], v[224:227], v[108:111], v[16:19]\n"
      "buffer_load_dwordx4 v[84:87], v7, s[20:23], s48 offen offset:0 sc1\n"
      "s_cbranch_vccnz .Lp_44\n"
      ".Lp_45:\n"
      "v_mfma_f32_16x16x32_bf16 v[16:19], v[244:247], v[112:115], v[16:19]\n"
      "s_waitcnt lgkmcnt(2)\n"
      "v_mfma_f32_16x16x32_bf16 v[20:23], v[128:131], v[88:91], v[20:23]\n"
      "ds_read_b32 v4, v15\n"
      "ds_read_b128 v[24:27], v9 offset:24576\n"
      "v_mfma_f32_16x16x32_bf16 v[20:23], v[148:151], v[92:95], v[20:23]\n"
      "ds_add_u32 v2, v1 offset:4192\n"
      "v_mfma_f32_16x16x32_bf16 v[20:23], v[168:171], v[96:99], v[20:23]\n"
      "v_mfma_f32_16x16x32_bf16 v[20:23], v[188:191], v[100:103], v[20:23]\n"
      "s_waitcnt lgkmcnt(2)\n"
      "v_cmp_gt_u32 vcc, 10, v4\n"
      "v_mfma_f32_16x16x32_bf16 v[20:23], v[208:211], v[104:107], v[20:23]\n"
      "ds_write_b128 v8, v[16:19] offset:43008\n"
      "v_mfma_f32_16x16x32_bf16 v[20:23], v[228:231], v[108:111], v[20:23]\n"
      "s_cbranch_vccnz .Lp_46\n"
      ".Lp_47:\n"
      "v_mfma_f32_16x16x32_bf16 v[20:23], v[248:251], v[112:115], v[20:23]\n"
      "s_waitcnt lgkmcnt(2)\n"
      "v_mfma_f32_16x16x32_bf16 v[24:27], v[132:135], v[88:91], v[24:27]\n"
      "buffer_load_dwordx4 v[88:91], v5, s[20:23], s49 offen offset:0 sc1\n"
      "ds_read_b32 v4, v15\n"
      "ds_read_b128 v[28:31], v9 offset:40960\n"
      "v_mfma_f32_16x16x32_bf16 v[24:27], v[152:155], v[92:95], v[24:27]\n"
      "ds_add_u32 v2, v1 offset:4192\n"
      "v_mfma_f32_16x16x32_bf16 v[24:27], v[172:175], v[96:99], v[24:27]\n"
      "v_mfma_f32_16x16x32_bf16 v[24:27], v[192:195], v[100:103], v[24:27]\n"
      "s_waitcnt lgkmcnt(2)\n"
      "v_cmp_gt_u32 vcc, 11, v4\n"
      "v_mfma_f32_16x16x32_bf16 v[24:27], v[212:215], v[104:107], v[24:27]\n"
      "buffer_load_dwordx4 v[92:95], v5, s[20:23], s49 offen offset:1024 sc1\n"
      "ds_write_b128 v8, v[20:23] offset:44032\n"
      "v_mfma_f32_16x16x32_bf16 v[24:27], v[232:235], v[108:111], v[24:27]\n"
      "s_cbranch_vccnz .Lp_48\n"
      ".Lp_49:\n"
      "v_mfma_f32_16x16x32_bf16 v[24:27], v[252:255], v[112:115], v[24:27]\n"
      "s_waitcnt vmcnt(13)\n"
      "s_waitcnt lgkmcnt(2)\n"
      "v_mfma_f32_16x16x32_bf16 v[28:31], v[116:119], v[32:35], v[28:31]\n"
      "ds_read_b32 v4, v15\n"
      "ds_read_b128 v[16:19], v9 offset:41984\n"
      "v_mfma_f32_16x16x32_bf16 v[28:31], v[136:139], v[36:39], v[28:31]\n"
      "buffer_load_dwordx4 v[96:99], v5, s[20:23], s49 offen offset:2048 sc1\n"
      "ds_add_u32 v2, v1 offset:4192\n"
      "v_mfma_f32_16x16x32_bf16 v[28:31], v[156:159], v[40:43], v[28:31]\n"
      "s_waitcnt vmcnt(10)\n"
      "v_mfma_f32_16x16x32_bf16 v[28:31], v[176:179], v[44:47], v[28:31]\n"
      "s_waitcnt lgkmcnt(2)\n"
      "v_cmp_gt_u32 vcc, 12, v4\n"
      "v_mfma_f32_16x16x32_bf16 v[28:31], v[196:199], v[48:51], v[28:31]\n"
      "ds_write_b128 v8, v[24:27] offset:45056\n"
      "v_mfma_f32_16x16x32_bf16 v[28:31], v[216:219], v[52:55], v[28:31]\n"
      "buffer_load_dwordx4 v[100:103], v5, s[20:23], s49 offen offset:3072 sc1\n"
      "s_cbranch_vccnz .Lp_50\n"
      ".Lp_51:\n"
      "v_mfma_f32_16x16x32_bf16 v[28:31], v[236:239], v[56:59], v[28:31]\n"
      "s_waitcnt lgkmcnt(2)\n"
      "v_mfma_f32_16x16x32_bf16 v[16:19], v[120:123], v[32:35], v[16:19]\n"
      "ds_read_b32 v4, v15\n"
      "ds_read_b128 v[20:23], v9 offset:43008\n"
      "v_mfma_f32_16x16x32_bf16 v[16:19], v[140:143], v[36:39], v[16:19]\n"
      "ds_add_u32 v2, v1 offset:4192\n"
      "v_mfma_f32_16x16x32_bf16 v[16:19], v[160:163], v[40:43], v[16:19]\n"
      "buffer_load_dwordx4 v[104:107], v6, s[20:23], s49 offen offset:0 sc1\n"
      "v_mfma_f32_16x16x32_bf16 v[16:19], v[180:183], v[44:47], v[16:19]\n"
      "s_waitcnt lgkmcnt(2)\n"
      "v_cmp_gt_u32 vcc, 13, v4\n"
      "v_mfma_f32_16x16x32_bf16 v[16:19], v[200:203], v[48:51], v[16:19]\n"
      "v_cvt_pk_bf16_f32 v10, v28, v29\n"
      "v_cvt_pk_bf16_f32 v11, v30, v31\n"
      "v_mfma_f32_16x16x32_bf16 v[16:19], v[220:223], v[52:55], v[16:19]\n"
      "s_cbranch_vccnz .Lp_52\n"
      ".Lp_53:\n"
      "v_mfma_f32_16x16x32_bf16 v[16:19], v[240:243], v[56:59], v[16:19]\n"
      "buffer_load_dwordx4 v[108:111], v6, s[20:23], s49 offen offset:1024 sc1\n"
      "s_waitcnt lgkmcnt(1)\n"
      "v_mfma_f32_16x16x32_bf16 v[20:23], v[124:127], v[32:35], v[20:23]\n"
      "ds_read_b32 v4, v15\n"
      "ds_read_b128 v[24:27], v9 offset:44032\n"
      "v_mfma_f32_16x16x32_bf16 v[20:23], v[144:147], v[36:39], v[20:23]\n"
      "v_mfma_f32_16x16x32_bf16 v[20:23], v[164:167], v[40:43], v[20:23]\n"
      "v_mfma_f32_16x16x32_bf16 v[20:23], v[184:187], v[44:47], v[20:23]\n"
      "buffer_load_dwordx4 v[112:115], v7, s[20:23], s49 offen offset:0 sc1\n"
      "s_waitcnt lgkmcnt(1)\n"
      "v_cmp_gt_u32 vcc, 14, v4\n"
      "v_mfma_f32_16x16x32_bf16 v[20:23], v[204:207], v[48:51], v[20:23]\n"
      "v_cvt_pk_bf16_f32 v12, v16, v17\n"
      "v_cvt_pk_bf16_f32 v13, v18, s75\n"
      "v_mfma_f32_16x16x32_bf16 v[20:23], v[224:227], v[52:55], v[20:23]\n"
      "s_cbranch_vccnz .Lp_54\n"
      ".Lp_55:\n"
      "v_mfma_f32_16x16x32_bf16 v[20:23], v[244:247], v[56:59], v[20:23]\n"
      "buffer_store_dwordx4 v[10:13], v5, s[20:23], s28 offen offset:0 sc1\n"
      "s_waitcnt lgkmcnt(0)\n"
      "v_mfma_f32_16x16x32_bf16 v[24:27], v[128:131], v[32:35], v[24:27]\n"
      "ds_read_b32 v4, v15\n"
      "ds_read_b128 v[28:31], v9 offset:45056\n"
      "v_mfma_f32_16x16x32_bf16 v[24:27], v[148:151], v[36:39], v[24:27]\n"
      "v_mfma_f32_16x16x32_bf16 v[24:27], v[168:171], v[40:43], v[24:27]\n"
      "v_mfma_f32_16x16x32_bf16 v[24:27], v[188:191], v[44:47], v[24:27]\n"
      "s_waitcnt lgkmcnt(1)\n"
      "v_cmp_gt_u32 vcc, 15, v4\n"
      "v_mfma_f32_16x16x32_bf16 v[24:27], v[208:211], v[48:51], v[24:27]\n"
      "v_cvt_pk_bf16_f32 v10, v19, v20\n"
      "v_cvt_pk_bf16_f32 v11, v21, v22\n"
      "v_mfma_f32_16x16x32_bf16 v[24:27], v[228:231], v[52:55], v[24:27]\n"
      "s_cbranch_vccnz .Lp_56\n"
      ".Lp_57:\n"
      "v_mfma_f32_16x16x32_bf16 v[24:27], v[248:251], v[56:59], v[24:27]\n"
      "s_waitcnt lgkmcnt(0)\n"
      "v_mfma_f32_16x16x32_bf16 v[28:31], v[132:135], v[32:35], v[28:31]\n"
      "buffer_load_dwordx4 v[32:35], v5, s[20:23], s50 offen offset:0 sc1\n"
      "ds_read_b128 v[16:19], v14 offset:0\n"
      "v_mfma_f32_16x16x32_bf16 v[28:31], v[152:155], v[36:39], v[28:31]\n"
      "v_mfma_f32_16x16x32_bf16 v[28:31], v[172:175], v[40:43], v[28:31]\n"
      "ds_read_b32 v4, v2 offset:4308\n"
      "v_mfma_f32_16x16x32_bf16 v[28:31], v[192:195], v[44:47], v[28:31]\n"
      "v_mfma_f32_16x16x32_bf16 v[28:31], v[212:215], v[48:51], v[28:31]\n"
      "buffer_load_dwordx4 v[36:39], v5, s[20:23], s50 offen offset:1024 sc1\n"
      "v_cvt_pk_bf16_f32 v12, v23, v24\n"
      "v_cvt_pk_bf16_f32 v13, v25, s75\n"
      "v_mfma_f32_16x16x32_bf16 v[28:31], v[232:235], v[52:55], v[28:31]\n"
      "v_mfma_f32_16x16x32_bf16 v[28:31], v[252:255], v[56:59], v[28:31]\n"
      "buffer_store_dwordx4 v[10:13], v5, s[20:23], s28 offen offset:1024 sc1\n"
      "s_waitcnt vmcnt(15)\n"
      "s_waitcnt lgkmcnt(1)\n"
      "v_mfma_f32_16x16x32_bf16 v[16:19], v[116:119], v[60:63], v[16:19]\n"
      "ds_read_b128 v[20:23], v14 offset:64\n"
      "v_mfma_f32_16x16x32_bf16 v[16:19], v[136:139], v[64:67], v[16:19]\n"
      "buffer_load_dwordx4 v[40:43], v5, s[20:23], s50 offen offset:2048 sc1\n"
      "v_mfma_f32_16x16x32_bf16 v[16:19], v[156:159], v[68:71], v[16:19]\n"
      "s_waitcnt vmcnt(12)\n"
      "v_mfma_f32_16x16x32_bf16 v[16:19], v[176:179], v[72:75], v[16:19]\n"
      "s_waitcnt lgkmcnt(1)\n"
      "v_readfirstlane_b32 s2, v4\n"
      "s_cmp_lt_u32 s2, 4\n"
      "s_cbranch_scc1 .Lw_58\n"
      ".Lw_59:\n"
      "v_mfma_f32_16x16x32_bf16 v[16:19], v[196:199], v[76:79], v[16:19]\n"
      "v_cvt_pk_bf16_f32 v10, v26, v27\n"
      "v_cvt_pk_bf16_f32 v11, v28, v29\n"
      "v_mfma_f32_16x16x32_bf16 v[16:19], v[216:219], v[80:83], v[16:19]\n"
      "buffer_load_dwordx4 v[44:47], v5, s[20:23], s50 offen offset:3072 sc1\n"
      "v_cvt_pk_bf16_f32 v12, v30, v31\n"
      "v_cvt_pk_bf16_f32 v13, v31, s75\n"
      "v_mfma_f32_16x16x32_bf16 v[16:19], v[236:239], v[84:87], v[16:19]\n"
      "buffer_store_dwordx4 v[10:13], v5, s[20:23], s28 offen offset:2048 sc1\n"
      "s_waitcnt lgkmcnt(0)\n"
      "v_mfma_f32_16x16x32_bf16 v[20:23], v[120:123], v[60:63], v[20:23]\n"
      "ds_read_b128 v[24:27], v14 offset:128\n"
      "v_mfma_f32_16x16x32_bf16 v[20:23], v[140:143], v[64:67], v[20:23]\n"
      "v_mfma_f32_16x16x32_bf16 v[20:23], v[160:163], v[68:71], v[20:23]\n"
      "buffer_load_dwordx4 v[48:51], v6, s[20:23], s50 offen offset:0 sc1\n"
      "v_mfma_f32_16x16x32_bf16 v[20:23], v[180:183], v[72:75], v[20:23]\n"
      "v_mfma_f32_16x16x32_bf16 v[20:23], v[200:203], v[76:79], v[20:23]\n"
      "v_add_u32 v8, 4294934528, v8\n"
      "ds_write_b128 v8, v[16:19] offset:0\n"
      "v_mfma_f32_16x16x32_bf16 v[20:23], v[220:223], v[80:83], v[20:23]\n"
      "v_mfma_f32_16x16x32_bf16 v[20:23], v[240:243], v[84:87], v[20:23]\n"
      "buffer_load_dwordx4 v[52:55], v6, s[20:23], s50 offen offset:1024 sc1\n"
      "s_waitcnt lgkmcnt(1)\n"
      "v_mfma_f32_16x16x32_bf16 v[24:27], v[124:127], v[60:63], v[24:27]\n"
      "ds_read_b128 v[28:31], v14 offset:192\n"
      "v_mfma_f32_16x16x32_bf16 v[24:27], v[144:147], v[64:67], v[24:27]\n"
      "ds_add_u32 v2, v1 offset:4192\n"
      "v_mfma_f32_16x16x32_bf16 v[24:27], v[164:167], v[68:71], v[24:27]\n"
      "v_mfma_f32_16x16x32_bf16 v[24:27], v[184:187], v[72:75], v[24:27]\n"
      "buffer_load_dwordx4 v[56:59], v7, s[20:23], s50 offen offset:0 sc1\n"
      "v_mfma_f32_16x16x32_bf16 v[24:27], v[204:207], v[76:79], v[24:27]\n"
      "ds_write_b128 v8, v[20:23] offset:1024\n"
      "v_mfma_f32_16x16x32_bf16 v[24:27], v[224:227], v[80:83], v[24:27]\n"
      "v_mfma_f32_16x16x32_bf16 v[24:27], v[244:247], v[84:87], v[24:27]\n"
      "s_waitcnt lgkmcnt(2)\n"
      "v_mfma_f32_16x16x32_bf16 v[28:31], v[128:131], v[60:63], v[28:31]\n"
      "ds_read_b128 v[16:19], v14 offset:256\n"
      "v_mfma_f32_16x16x32_bf16 v[28:31], v[148:151], v[64:67], v[28:31]\n"
      "ds_add_u32 v2, v1 offset:4192\n"
      "v_mfma_f32_16x16x32_bf16 v[28:31], v[168:171], v[68:71], v[28:31]\n"
      "v_mfma_f32_16x16x32_bf16 v[28:31], v[188:191], v[72:75], v[28:31]\n"
      "v_mfma_f32_16x16x32_bf16 v[28:31], v[208:211], v[76:79], v[28:31]\n"
      "ds_write_b128 v8, v[24:27] offset:2048\n"
      "v_mfma_f32_16x16x32_bf16 v[28:31], v[228:231], v[80:83], v[28:31]\n"
      "v_mfma_f32_16x16x32_bf16 v[28:31], v[248:251], v[84:87], v[28:31]\n"
      "s_waitcnt lgkmcnt(2)\n"
      "v_mfma_f32_16x16x32_bf16 v[16:19], v[132:135], v[60:63], v[16:19]\n"
      "buffer_load_dwordx4 v[60:63], v5, s[20:23], s51 offen offset:0 sc1\n"
      "v_add_u32 v9, 4294934528, v9\n"
      "ds_read_b32 v4, v15\n"
      "ds_read_b128 v[20:23], v9 offset:0\n"
      "v_mfma_f32_16x16x32_bf16 v[16:19], v[152:155], v[64:67], v[16:19]\n"
      "ds_add_u32 v2, v1 offset:4192\n"
      "v_mfma_f32_16x16x32_bf16 v[16:19], v[172:175], v[68:71], v[16:19]\n"
      "v_mfma_f32_16x16x32_bf16 v[16:19], v[192:195], v[72:75], v[16:19]\n"
      "s_waitcnt lgkmcnt(2)\n"
      "v_cmp_gt_u32 vcc, 16, v4\n"
      "v_mfma_f32_16x16x32_bf16 v[16:19], v[212:215], v[76:79], v[16:19]\n"
      "buffer_load_dwordx4 v[64:67], v5, s[20:23], s51 offen offset:1024 sc1\n"
      "ds_write_b128 v8, v[28:31] offset:3072\n"
      "v_mfma_f32_16x16x32_bf16 v[16:19], v[232:235], v[80:83], v[16:19]\n"
      "s_cbranch_vccnz .Lp_61\n"
      ".Lp_62:\n"
      "v_mfma_f32_16x16x32_bf16 v[16:19], v[252:255], v[84:87], v[16:19]\n"
      "s_waitcnt vmcnt(16)\n"
      "s_waitcnt lgkmcnt(2)\n"
      "v_mfma_f32_16x16x32_bf16 v[20:23], v[116:119], v[88:91], v[20:23]\n"
      "ds_read_b32 v4, v15\n"
      "ds_read_b128 v[24:27], v9 offset:1024\n"
      "v_mfma_f32_16x16x32_bf16 v[20:23], v[136:139], v[92:95], v[20:23]\n"
      "buffer_load_dwordx4 v[68:71], v5, s[20:23], s51 offen offset:2048 sc1\n"
      "ds_add_u32 v2, v1 offset:4192\n"
      "v_mfma_f32_16x16x32_bf16 v[20:23], v[156:159], v[96:99], v[20:23]\n"
      "s_waitcnt vmcnt(13)\n"
      "v_mfma_f32_16x16x32_bf16 v[20:23], v[176:179], v[100:103], v[20:23]\n"
      "s_waitcnt lgkmcnt(2)\n"
      "v_cmp_gt_u32 vcc, 17, v4\n"
      "v_mfma_f32_16x16x32_bf16 v[20:23], v[196:199], v[104:107], v[20:23]\n"
      "ds_write_b128 v8, v[16:19] offset:4096\n"
      "v_mfma_f32_16x16x32_bf16 v[20:23], v[216:219], v[108:111], v[20:23]\n"
      "buffer_load_dwordx4 v[72:75], v5, s[20:23], s51 offen offset:3072 sc1\n"
      "s_cbranch_vccnz .Lp_63\n"
      ".Lp_64:\n"
      "v_mfma_f32_16x16x32_bf16 v[20:23], v[236:239], v[112:115], v[20:23]\n"
      "s_waitcnt lgkmcnt(2)\n"
      "v_mfma_f32_16x16x32_bf16 v[24:27], v[120:123], v[88:91], v[24:27]\n"
      "ds_read_b32 v4, v15\n"
      "ds_read_b128 v[28:31], v9 offset:2048\n"
      "v_mfma_f32_16x16x32_bf16 v[24:27], v[140:143], v[92:95], v[24:27]\n"
      "ds_add_u32 v2, v1 offset:4192\n"
      "v_mfma_f32_16x16x32_bf16 v[24:27], v[160:163], v[96:99], v[24:27]\n"
      "buffer_load_dwordx4 v[76:79], v6, s[20:23], s51 offen offset:0 sc1\n"
      "v_mfma_f32_16x16x32_bf16 v[24:27], v[180:183], v[100:103], v[24:27]\n"
      "s_waitcnt lgkmcnt(2)\n"
      "v_cmp_gt_u32 vcc, 18, v4\n"
      "v_mfma_f32_16x16x32_bf16 v[24:27], v[200:203], v[104:107], v[24:27]\n"
      "v_add_u32 v8, 98304, v8\n"
      "ds_write_b128 v8, v[20:23] offset:0\n"
      "v_mfma_f32_16x16x32_bf16 v[24:27], v[220:223], v[108:111], v[24:27]\n"
      "s_cbranch_vccnz .Lp_65\n"
      ".Lp_66:\n"
      "v_mfma_f32_16x16x32_bf16 v[24:27], v[240:243], v[112:115], v[24:27]\n"
      "buffer_load_dwordx4 v[80:83], v6, s[20:23], s51 offen offset:1024 sc1\n"
      "s_waitcnt lgkmcnt(2)\n"
      "v_mfma_f32_16x16x32_bf16 v[28:31], v[124:127], v[88:91], v[28:31]\n"
      "ds_read_b32 v4, v15\n"
      "ds_read_b128 v[16:19], v9 offset:3072\n"
      "v_mfma_f32_16x16x32_bf16 v[28:31], v[144:147], v[92:95], v[28:31]\n"
      "ds_add_u32 v2, v1 offset:4192\n"
      "v_mfma_f32_16x16x32_bf16 v[28:31], v[164:167], v[96:99], v[28:31]\n"
      "v_mfma_f32_16x16x32_bf16 v[28:31], v[184:187], v[100:103], v[28:31]\n"
      "buffer_load_dwordx4 v[84:87], v7, s[20:23], s51 offen offset:0 sc1\n"
      "s_waitcnt lgkmcnt(2)\n"
      "v_cmp_gt_u32 vcc, 19, v4\n"
      "v_mfma_f32_16x16x32_bf16 v[28:31], v[204:207], v[104:107], v[28:31]\n"
      "ds_write_b128 v8, v[24:27] offset:1024\n"
      "v_mfma_f32_16x16x32_bf16 v[28:31], v[224:227], v[108:111], v[28:31]\n"
      "s_cbranch_vccnz .Lp_67\n"
      ".Lp_68:\n"
      "v_mfma_f32_16x16x32_bf16 v[28:31], v[244:247], v[112:115], v[28:31]\n"
      "s_waitcnt lgkmcnt(2)\n"
      "v_mfma_f32_16x16x32_bf16 v[16:19], v[128:131], v[88:91], v[16:19]\n"
      "ds_read_b32 v4, v15\n"
      "ds_read_b128 v[20:23], v9 offset:4096\n"
      "v_mfma_f32_16x16x32_bf16 v[16:19], v[148:151], v[92:95], v[16:19]\n"
      "ds_add_u32 v2, v1 offset:4192\n"
      "v_mfma_f32_16x16x32_bf16 v[16:19], v[168:171], v[96:99], v[16:19]\n"
      "v_mfma_f32_16x16x32_bf16 v[16:19], v[188:191], v[100:103], v[16:19]\n"
      "s_waitcnt lgkmcnt(2)\n"
      "v_cmp_gt_u32 vcc, 20, v4\n"
      "v_mfma_f32_16x16x32_bf16 v[16:19], v[208:211], v[104:107], v[16:19]\n"
      "ds_write_b128 v8, v[28:31] offset:2048\n"
      "v_mfma_f32_16x16x32_bf16 v[16:19], v[228:231], v[108:111], v[16:19]\n"
      "s_cbranch_vccnz .Lp_69\n"
      ".Lp_70:\n"
      "v_mfma_f32_16x16x32_bf16 v[16:19], v[248:251], v[112:115], v[16:19]\n"
      "s_waitcnt lgkmcnt(2)\n"
      "v_mfma_f32_16x16x32_bf16 v[20:23], v[132:135], v[88:91], v[20:23]\n"
      "v_add_u32 v9, 98304, v9\n"
      "ds_read_b32 v4, v15\n"
      "ds_read_b128 v[24:27], v9 offset:0\n"
      "v_mfma_f32_16x16x32_bf16 v[20:23], v[152:155], v[92:95], v[20:23]\n"
      "ds_add_u32 v2, v1 offset:4192\n"
      "v_mfma_f32_16x16x32_bf16 v[20:23], v[172:175], v[96:99], v[20:23]\n"
      "v_mfma_f32_16x16x32_bf16 v[20:23], v[192:195], v[100:103], v[20:23]\n"
      "s_waitcnt lgkmcnt(2)\n"
      "v_cmp_gt_u32 vcc, 21, v4\n"
      "v_mfma_f32_16x16x32_bf16 v[20:23], v[212:215], v[104:107], v[20:23]\n"
      "ds_write_b128 v8, v[16:19] offset:3072\n"
      "v_mfma_f32_16x16x32_bf16 v[20:23], v[232:235], v[108:111], v[20:23]\n"
      "s_cbranch_vccnz .Lp_71\n"
      ".Lp_72:\n"
      "v_mfma_f32_16x16x32_bf16 v[20:23], v[252:255], v[112:115], v[20:23]\n"
      "s_waitcnt vmcnt(12)\n"
      "s_waitcnt lgkmcnt(2)\n"
      "v_mfma_f32_16x16x32_bf16 v[24:27], v[116:119], v[32:35], v[24:27]\n"
      "ds_read_b32 v4, v15\n"
      "ds_read_b128 v[28:31], v9 offset:1024\n"
      "v_mfma_f32_16x16x32_bf16 v[24:27], v[136:139], v[36:39], v[24:27]\n"
      "ds_add_u32 v2, v1 offset:4192\n"
      "v_mfma_f32_16x16x32_bf16 v[24:27], v[156:159], v[40:43], v[24:27]\n"
      "s_waitcnt vmcnt(7)\n"
      "v_mfma_f32_16x16x32_bf16 v[24:27], v[176:179], v[44:47], v[24:27]\n"
      "s_waitcnt lgkmcnt(2)\n"
      "v_cmp_gt_u32 vcc, 22, v4\n"
      "v_mfma_f32_16x16x32_bf16 v[24:27], v[196:199], v[48:51], v[24:27]\n"
      "ds_write_b128 v8, v[20:23] offset:4096\n"
      "v_mfma_f32_16x16x32_bf16 v[24:27], v[216:219], v[52:55], v[24:27]\n"
      "s_cbranch_vccnz .Lp_73\n"
      ".Lp_74:\n"
      "v_mfma_f32_16x16x32_bf16 v[24:27], v[236:239], v[56:59], v[24:27]\n"
      "s_waitcnt lgkmcnt(2)\n"
      "v_mfma_f32_16x16x32_bf16 v[28:31], v[120:123], v[32:35], v[28:31]\n"
      "ds_read_b32 v4, v15\n"
      "ds_read_b128 v[16:19], v9 offset:2048\n"
      "v_mfma_f32_16x16x32_bf16 v[28:31], v[140:143], v[36:39], v[28:31]\n"
      "ds_add_u32 v2, v1 offset:4192\n"
      "v_mfma_f32_16x16x32_bf16 v[28:31], v[160:163], v[40:43], v[28:31]\n"
      "v_mfma_f32_16x16x32_bf16 v[28:31], v[180:183], v[44:47], v[28:31]\n"
      "s_waitcnt lgkmcnt(2)\n"
      "v_cmp_gt_u32 vcc, 23, v4\n"
      "v_mfma_f32_16x16x32_bf16 v[28:31], v[200:203], v[48:51], v[28:31]\n"
      "v_add_u32 v8, 4294901760, v8\n"
      "ds_write_b128 v8, v[24:27] offset:0\n"
      "v_mfma_f32_16x16x32_bf16 v[28:31], v[220:223], v[52:55], v[28:31]\n"
      "s_cbranch_vccnz .Lp_75\n"
      ".Lp_76:\n"
      "v_mfma_f32_16x16x32_bf16 v[28:31], v[240:243], v[56:59], v[28:31]\n"
      "s_waitcnt lgkmcnt(2)\n"
      "v_mfma_f32_16x16x32_bf16 v[16:19], v[124:127], v[32:35], v[16:19]\n"
      "ds_read_b32 v4, v15\n"
      "ds_read_b128 v[20:23], v9 offset:3072\n"
      "v_mfma_f32_16x16x32_bf16 v[16:19], v[144:147], v[36:39], v[16:19]\n"
      "ds_add_u32 v2, v1 offset:4192\n"
      "v_mfma_f32_16x16x32_bf16 v[16:19], v[164:167], v[40:43], v[16:19]\n"
      "v_mfma_f32_16x16x32_bf16 v[16:19], v[184:187], v[44:47], v[16:19]\n"
      "s_waitcnt lgkmcnt(2)\n"
      "v_cmp_gt_u32 vcc, 24, v4\n"
      "v_mfma_f32_16x16x32_bf16 v[16:19], v[204:207], v[48:51], v[16:19]\n"
      "ds_write_b128 v8, v[28:31] offset:1024\n"
      "v_mfma_f32_16x16x32_bf16 v[16:19], v[224:227], v[52:55], v[16:19]\n"
      "s_cbranch_vccnz .Lp_77\n"
      ".Lp_78:\n"
      "v_mfma_f32_16x16x32_bf16 v[16:19], v[244:247], v[56:59], v[16:19]\n"
      "s_waitcnt lgkmcnt(2)\n"
      "v_mfma_f32_16x16x32_bf16 v[20:23], v[128:131], v[32:35], v[20:23]\n"
      "ds_read_b32 v4, v15\n"
      "ds_read_b128 v[24:27], v9 offset:4096\n"
      "v_mfma_f32_16x16x32_bf16 v[20:23], v[148:151], v[36:39], v[20:23]\n"
      "ds_add_u32 v2, v1 offset:4192\n"
      "v_mfma_f32_16x16x32_bf16 v[20:23], v[168:171], v[40:43], v[20:23]\n"
      "v_mfma_f32_16x16x32_bf16 v[20:23], v[188:191], v[44:47], v[20:23]\n"
      "s_waitcnt lgkmcnt(2)\n"
      "v_cmp_gt_u32 vcc, 25, v4\n"
      "v_mfma_f32_16x16x32_bf16 v[20:23], v[208:211], v[48:51], v[20:23]\n"
      "ds_write_b128 v8, v[16:19] offset:2048\n"
      "v_mfma_f32_16x16x32_bf16 v[20:23], v[228:231], v[52:55], v[20:23]\n"
      "s_cbranch_vccnz .Lp_79\n"
      ".Lp_80:\n"
      "v_mfma_f32_16x16x32_bf16 v[20:23], v[248:251], v[56:59], v[20:23]\n"
      "s_waitcnt lgkmcnt(2)\n"
      "v_mfma_f32_16x16x32_bf16 v[24:27], v[132:135], v[32:35], v[24:27]\n"
      "v_add_u32 v9, 4294901760, v9\n"
      "ds_read_b32 v4, v15\n"
      "ds_read_b128 v[28:31], v9 offset:0\n"
      "v_mfma_f32_16x16x32_bf16 v[24:27], v[152:155], v[36:39], v[24:27]\n"
      "ds_add_u32 v2, v1 offset:4192\n"
      "v_mfma_f32_16x16x32_bf16 v[24:27], v[172:175], v[40:43], v[24:27]\n"
      "v_mfma_f32_16x16x32_bf16 v[24:27], v[192:195], v[44:47], v[24:27]\n"
      "s_waitcnt lgkmcnt(2)\n"
      "v_cmp_gt_u32 vcc, 26, v4\n"
      "v_mfma_f32_16x16x32_bf16 v[24:27], v[212:215], v[48:51], v[24:27]\n"
      "ds_write_b128 v8, v[20:23] offset:3072\n"
      "v_mfma_f32_16x16x32_bf16 v[24:27], v[232:235], v[52:55], v[24:27]\n"
      "s_cbranch_vccnz .Lp_81\n"
      ".Lp_82:\n"
      "v_mfma_f32_16x16x32_bf16 v[24:27], v[252:255], v[56:59], v[24:27]\n"
      "s_waitcnt vmcnt(4)\n"
      "s_waitcnt lgkmcnt(2)\n"
      "v_mfma_f32_16x16x32_bf16 v[28:31], v[116:119], v[60:63], v[28:31]\n"
      "ds_read_b32 v4, v15\n"
      "ds_read_b128 v[16:19], v9 offset:1024\n"
      "v_mfma_f32_16x16x32_bf16 v[28:31], v[136:139], v[64:67], v[28:31]\n"
      "ds_add_u32 v2, v1 offset:4192\n"
      "v_mfma_f32_16x16x32_bf16 v[28:31], v[156:159], v[68:71], v[28:31]\n"
      "s_waitcnt vmcnt(0)\n"
      "v_mfma_f32_16x16x32_bf16 v[28:31], v[176:179], v[72:75], v[28:31]\n"
      "s_waitcnt lgkmcnt(2)\n"
      "v_cmp_gt_u32 vcc, 27, v4\n"
      "v_mfma_f32_16x16x32_bf16 v[28:31], v[196:199], v[76:79], v[28:31]\n"
      "ds_write_b128 v8, v[24:27] offset:4096\n"
      "v_mfma_f32_16x16x32_bf16 v[28:31], v[216:219], v[80:83], v[28:31]\n"
      "s_cbranch_vccnz .Lp_83\n"
      ".Lp_84:\n"
      "v_mfma_f32_16x16x32_bf16 v[28:31], v[236:239], v[84:87], v[28:31]\n"
      "s_waitcnt lgkmcnt(2)\n"
      "v_mfma_f32_16x16x32_bf16 v[16:19], v[120:123], v[60:63], v[16:19]\n"
      "ds_read_b32 v4, v15\n"
      "ds_read_b128 v[20:23], v9 offset:2048\n"
      "v_mfma_f32_16x16x32_bf16 v[16:19], v[140:143], v[64:67], v[16:19]\n"
      "ds_add_u32 v2, v1 offset:4192\n"
      "v_mfma_f32_16x16x32_bf16 v[16:19], v[160:163], v[68:71], v[16:19]\n"
      "v_mfma_f32_16x16x32_bf16 v[16:19], v[180:183], v[72:75], v[16:19]\n"
      "s_waitcnt lgkmcnt(2)\n"
      "v_cmp_gt_u32 vcc, 28, v4\n"
      "v_mfma_f32_16x16x32_bf16 v[16:19], v[200:203], v[76:79], v[16:19]\n"
      "ds_write_b128 v8, v[28:31] offset:20480\n"
      "v_mfma_f32_16x16x32_bf16 v[16:19], v[220:223], v[80:83], v[16:19]\n"
      "s_cbranch_vccnz .Lp_85\n"
      ".Lp_86:\n"
      "v_mfma_f32_16x16x32_bf16 v[16:19], v[240:243], v[84:87], v[16:19]\n"
      "s_waitcnt lgkmcnt(2)\n"
      "v_mfma_f32_16x16x32_bf16 v[20:23], v[124:127], v[60:63], v[20:23]\n"
      "ds_read_b32 v4, v15\n"
      "ds_read_b128 v[24:27], v9 offset:3072\n"
      "v_mfma_f32_16x16x32_bf16 v[20:23], v[144:147], v[64:67], v[20:23]\n"
      "ds_add_u32 v2, v1 offset:4192\n"
      "v_mfma_f32_16x16x32_bf16 v[20:23], v[164:167], v[68:71], v[20:23]\n"
      "v_mfma_f32_16x16x32_bf16 v[20:23], v[184:187], v[72:75], v[20:23]\n"
      "s_waitcnt lgkmcnt(2)\n"
      "v_cmp_gt_u32 vcc, 29, v4\n"
      "v_mfma_f32_16x16x32_bf16 v[20:23], v[204:207], v[76:79], v[20:23]\n"
      "ds_write_b128 v8, v[16:19] offset:21504\n"
      "v_mfma_f32_16x16x32_bf16 v[20:23], v[224:227], v[80:83], v[20:23]\n"
      "s_cbranch_vccnz .Lp_87\n"
      ".Lp_88:\n"
      "v_mfma_f32_16x16x32_bf16 v[20:23], v[244:247], v[84:87], v[20:23]\n"
      "s_waitcnt lgkmcnt(2)\n"
      "v_mfma_f32_16x16x32_bf16 v[24:27], v[128:131], v[60:63], v[24:27]\n"
      "ds_read_b32 v4, v15\n"
      "ds_read_b128 v[28:31], v9 offset:4096\n"
      "v_mfma_f32_16x16x32_bf16 v[24:27], v[148:151], v[64:67], v[24:27]\n"
      "ds_add_u32 v2, v1 offset:4192\n"
      "v_mfma_f32_16x16x32_bf16 v[24:27], v[168:171], v[68:71], v[24:27]\n"
      "v_mfma_f32_16x16x32_bf16 v[24:27], v[188:191], v[72:75], v[24:27]\n"
      "s_waitcnt lgkmcnt(2)\n"
      "v_cmp_gt_u32 vcc, 30, v4\n"
      "v_mfma_f32_16x16x32_bf16 v[24:27], v[208:211], v[76:79], v[24:27]\n"
      "ds_write_b128 v8, v[20:23] offset:22528\n"
      "v_mfma_f32_16x16x32_bf16 v[24:27], v[228:231], v[80:83], v[24:27]\n"
      "s_cbranch_vccnz .Lp_89\n"
      ".Lp_90:\n"
      "v_mfma_f32_16x16x32_bf16 v[24:27], v[248:251], v[84:87], v[24:27]\n"
      "s_waitcnt lgkmcnt(2)\n"
      "v_mfma_f32_16x16x32_bf16 v[28:31], v[132:135], v[60:63], v[28:31]\n"
      "v_mfma_f32_16x16x32_bf16 v[28:31], v[152:155], v[64:67], v[28:31]\n"
      "ds_add_u32 v2, v1 offset:4192\n"
      "v_mfma_f32_16x16x32_bf16 v[28:31], v[172:175], v[68:71], v[28:31]\n"
      "v_mfma_f32_16x16x32_bf16 v[28:31], v[192:195], v[72:75], v[28:31]\n"
      "v_mfma_f32_16x16x32_bf16 v[28:31], v[212:215], v[76:79], v[28:31]\n"
      "s_nop 0\n"
      "ds_write_b128 v8, v[24:27] offset:23552\n"
      "v_mfma_f32_16x16x32_bf16 v[28:31], v[232:235], v[80:83], v[28:31]\n"
      "v_mfma_f32_16x16x32_bf16 v[28:31], v[252:255], v[84:87], v[28:31]\n"
      "ds_add_u32 v2, v1 offset:4192\n"
      "s_nop 6\n"
      "ds_write_b128 v8, v[28:31] offset:24576\n"
      "ds_add_u32 v2, v1 offset:4192\n"
      "s_endpgm\n"
      ".LWARM:\n"
      ".Lp_25:\n"
      "s_cbranch_vccz .Lp_27\n"
      ".Lp_91:\n"
      "ds_read_b32 v4, v15\n"
      "ds_read_b128 v[20:23], v9 offset:0\n"
      "s_sub_u32 s73, s73, 1\n"
      "s_cselect_b32 s73, 0, s73\n"
      "s_waitcnt lgkmcnt(0)\n"
      "v_cmp_gt_u32 vcc, 1, v4\n"
      "s_cselect_b64 vcc, 0, vcc\n"
      "s_cbranch_vccnz .Lp_91\n"
      "s_branch .Lp_26\n"
      ".Lp_27:\n"
      "s_cbranch_vccz .Lp_29\n"
      ".Lp_92:\n"
      "ds_read_b32 v4, v15\n"
      "ds_read_b128 v[24:27], v9 offset:1024\n"
      "s_sub_u32 s73, s73, 1\n"
      "s_cselect_b32 s73, 0, s73\n"
      "s_waitcnt lgkmcnt(0)\n"
      "v_cmp_gt_u32 vcc, 2, v4\n"
      "s_cselect_b64 vcc, 0, vcc\n"
      "s_cbranch_vccnz .Lp_92\n"
      "s_branch .Lp_28\n"
      ".Lp_29:\n"
      "s_cbranch_vccz .Lp_31\n"
      ".Lp_93:\n"
      "ds_read_b32 v4, v15\n"
      "ds_read_b128 v[28:31], v9 offset:2048\n"
      "s_sub_u32 s73, s73, 1\n"
      "s_cselect_b32 s73, 0, s73\n"
      "s_waitcnt lgkmcnt(0)\n"
      "v_cmp_gt_u32 vcc, 3, v4\n"
      "s_cselect_b64 vcc, 0, vcc\n"
      "s_cbranch_vccnz .Lp_93\n"
      "s_branch .Lp_30\n"
      ".Lp_31:\n"
      "s_cbranch_vccz .Lp_33\n"
      ".Lp_94:\n"
      "ds_read_b32 v4, v15\n"
      "ds_read_b128 v[16:19], v9 offset:3072\n"
      "s_sub_u32 s73, s73, 1\n"
      "s_cselect_b32 s73, 0, s73\n"
      "s_waitcnt lgkmcnt(0)\n"
      "v_cmp_gt_u32 vcc, 4, v4\n"
      "s_cselect_b64 vcc, 0, vcc\n"
      "s_cbranch_vccnz .Lp_94\n"
      "s_branch .Lp_32\n"
      ".Lp_33:\n"
      "s_cbranch_vccz .Lp_35\n"
      ".Lp_95:\n"
      "ds_read_b32 v4, v15\n"
      "ds_read_b128 v[20:23], v9 offset:4096\n"
      "s_sub_u32 s73, s73, 1\n"
      "s_cselect_b32 s73, 0, s73\n"
      "s_waitcnt lgkmcnt(0)\n"
      "v_cmp_gt_u32 vcc, 5, v4\n"
      "s_cselect_b64 vcc, 0, vcc\n"
      "s_cbranch_vccnz .Lp_95\n"
      "s_branch .Lp_34\n"
      ".Lp_35:\n"
      "s_cbranch_vccz .Lp_37\n"
      ".Lp_96:\n"
      "ds_read_b32 v4, v15\n"
      "ds_read_b128 v[24:27], v9 offset:20480\n"
      "s_sub_u32 s73, s73, 1\n"
      "s_cselect_b32 s73, 0, s73\n"
      "s_waitcnt lgkmcnt(0)\n"
      "v_cmp_gt_u32 vcc, 6, v4\n"
      "s_cselect_b64 vcc, 0, vcc\n"
      "s_cbranch_vccnz .Lp_96\n"
      "s_branch .Lp_36\n"
      ".Lp_37:\n"
      "s_cbranch_vccz .Lp_42\n"
      ".Lp_97:\n"
      "ds_read_b32 v4, v15\n"
      "ds_read_b128 v[28:31], v9 offset:21504\n"
      "s_sub_u32 s73, s73, 1\n"
      "s_cselect_b32 s73, 0, s73\n"
      "s_waitcnt lgkmcnt(0)\n"
      "v_cmp_gt_u32 vcc, 7, v4\n"
      "s_cselect_b64 vcc, 0, vcc\n"
      "s_cbranch_vccnz .Lp_97\n"
      "s_branch .Lp_38\n"
      ".Lp_42:\n"
      "s_cbranch_vccz .Lp_44\n"
      ".Lp_98:\n"
      "ds_read_b32 v4, v15\n"
      "ds_read_b128 v[16:19], v9 offset:22528\n"
      "s_sub_u32 s73, s73, 1\n"
      "s_cselect_b32 s73, 0, s73\n"
      "s_waitcnt lgkmcnt(0)\n"
      "v_cmp_gt_u32 vcc, 8, v4\n"
      "s_cselect_b64 vcc, 0, vcc\n"
      "s_cbranch_vccnz .Lp_98\n"
      "s_branch .Lp_43\n"
      ".Lp_44:\n"
      "s_cbranch_vccz .Lp_46\n"
      ".Lp_99:\n"
      "ds_read_b32 v4, v15\n"
      "ds_read_b128 v[20:23], v9 offset:23552\n"
      "s_sub_u32 s73, s73, 1\n"
      "s_cselect_b32 s73, 0, s73\n"
      "s_waitcnt lgkmcnt(0)\n"
      "v_cmp_gt_u32 vcc, 9, v4\n"
      "s_cselect_b64 vcc, 0, vcc\n"
      "s_cbranch_vccnz .Lp_99\n"
      "s_branch .Lp_45\n"
      ".Lp_46:\n"
      "s_cbranch_vccz .Lp_48\n"
      ".Lp_100:\n"
      "ds_read_b32 v4, v15\n"
      "ds_read_b128 v[24:27], v9 offset:24576\n"
      "s_sub_u32 s73, s73, 1\n"
      "s_cselect_b32 s73, 0, s73\n"
      "s_waitcnt lgkmcnt(0)\n"
      "v_cmp_gt_u32 vcc, 10, v4\n"
      "s_cselect_b64 vcc, 0, vcc\n"
      "s_cbranch_vccnz .Lp_100\n"
      "s_branch .Lp_47\n"
      ".Lp_48:\n"
      "s_cbranch_vccz .Lp_50\n"
      ".Lp_101:\n"
      "ds_read_b32 v4, v15\n"
      "ds_read_b128 v[28:31], v9 offset:40960\n"
      "s_sub_u32 s73, s73, 1\n"
      "s_cselect_b32 s73, 0, s73\n"
      "s_waitcnt lgkmcnt(0)\n"
      "v_cmp_gt_u32 vcc, 11, v4\n"
      "s_cselect_b64 vcc, 0, vcc\n"
      "s_cbranch_vccnz .Lp_101\n"
      "s_branch .Lp_49\n"
      ".Lp_50:\n"
      "s_cbranch_vccz .Lp_52\n"
      ".Lp_102:\n"
      "ds_read_b32 v4, v15\n"
      "ds_read_b128 v[16:19], v9 offset:41984\n"
      "s_sub_u32 s73, s73, 1\n"
      "s_cselect_b32 s73, 0, s73\n"
      "s_waitcnt lgkmcnt(0)\n"
      "v_cmp_gt_u32 vcc, 12, v4\n"
      "s_cselect_b64 vcc, 0, vcc\n"
      "s_cbranch_vccnz .Lp_102\n"
      "s_branch .Lp_51\n"
      ".Lp_52:\n"
      "s_cbranch_vccz .Lp_54\n"
      ".Lp_103:\n"
      "ds_read_b32 v4, v15\n"
      "ds_read_b128 v[20:23], v9 offset:43008\n"
      "s_sub_u32 s73, s73, 1\n"
      "s_cselect_b32 s73, 0, s73\n"
      "s_waitcnt lgkmcnt(0)\n"
      "v_cmp_gt_u32 vcc, 13, v4\n"
      "s_cselect_b64 vcc, 0, vcc\n"
      "s_cbranch_vccnz .Lp_103\n"
      "s_branch .Lp_53\n"
      ".Lp_54:\n"
      "s_cbranch_vccz .Lp_56\n"
      ".Lp_104:\n"
      "ds_read_b32 v4, v15\n"
      "ds_read_b128 v[24:27], v9 offset:44032\n"
      "s_sub_u32 s73, s73, 1\n"
      "s_cselect_b32 s73, 0, s73\n"
      "s_waitcnt lgkmcnt(0)\n"
      "v_cmp_gt_u32 vcc, 14, v4\n"
      "s_cselect_b64 vcc, 0, vcc\n"
      "s_cbranch_vccnz .Lp_104\n"
      "s_branch .Lp_55\n"
      ".Lp_56:\n"
      "s_cbranch_vccz .Lp_61\n"
      ".Lp_105:\n"
      "ds_read_b32 v4, v15\n"
      "ds_read_b128 v[28:31], v9 offset:45056\n"
      "s_sub_u32 s73, s73, 1\n"
      "s_cselect_b32 s73, 0, s73\n"
      "s_waitcnt lgkmcnt(0)\n"
      "v_cmp_gt_u32 vcc, 15, v4\n"
      "s_cselect_b64 vcc, 0, vcc\n"
      "s_cbranch_vccnz .Lp_105\n"
      "s_branch .Lp_57\n"
      ".Lp_61:\n"
      "s_cbranch_vccz .Lp_63\n"
      ".Lp_106:\n"
      "ds_read_b32 v4, v15\n"
      "ds_read_b128 v[20:23], v9 offset:0\n"
      "s_sub_u32 s73, s73, 1\n"
      "s_cselect_b32 s73, 0, s73\n"
      "s_waitcnt lgkmcnt(0)\n"
      "v_cmp_gt_u32 vcc, 16, v4\n"
      "s_cselect_b64 vcc, 0, vcc\n"
      "s_cbranch_vccnz .Lp_106\n"
      "s_branch .Lp_62\n"
      ".Lp_63:\n"
      "s_cbranch_vccz .Lp_65\n"
      ".Lp_107:\n"
      "ds_read_b32 v4, v15\n"
      "ds_read_b128 v[24:27], v9 offset:1024\n"
      "s_sub_u32 s73, s73, 1\n"
      "s_cselect_b32 s73, 0, s73\n"
      "s_waitcnt lgkmcnt(0)\n"
      "v_cmp_gt_u32 vcc, 17, v4\n"
      "s_cselect_b64 vcc, 0, vcc\n"
      "s_cbranch_vccnz .Lp_107\n"
      "s_branch .Lp_64\n"
      ".Lp_65:\n"
      "s_cbranch_vccz .Lp_67\n"
      ".Lp_108:\n"
      "ds_read_b32 v4, v15\n"
      "ds_read_b128 v[28:31], v9 offset:2048\n"
      "s_sub_u32 s73, s73, 1\n"
      "s_cselect_b32 s73, 0, s73\n"
      "s_waitcnt lgkmcnt(0)\n"
      "v_cmp_gt_u32 vcc, 18, v4\n"
      "s_cselect_b64 vcc, 0, vcc\n"
      "s_cbranch_vccnz .Lp_108\n"
      "s_branch .Lp_66\n"
      ".Lp_67:\n"
      "s_cbranch_vccz .Lp_69\n"
      ".Lp_109:\n"
      "ds_read_b32 v4, v15\n"
      "ds_read_b128 v[16:19], v9 offset:3072\n"
      "s_sub_u32 s73, s73, 1\n"
      "s_cselect_b32 s73, 0, s73\n"
      "s_waitcnt lgkmcnt(0)\n"
      "v_cmp_gt_u32 vcc, 19, v4\n"
      "s_cselect_b64 vcc, 0, vcc\n"
      "s_cbranch_vccnz .Lp_109\n"
      "s_branch .Lp_68\n"
      ".Lp_69:\n"
      "s_cbranch_vccz .Lp_71\n"
      ".Lp_110:\n"
      "ds_read_b32 v4, v15\n"
      "ds_read_b128 v[20:23], v9 offset:4096\n"
      "s_sub_u32 s73, s73, 1\n"
      "s_cselect_b32 s73, 0, s73\n"
      "s_waitcnt lgkmcnt(0)\n"
      "v_cmp_gt_u32 vcc, 20, v4\n"
      "s_cselect_b64 vcc, 0, vcc\n"
      "s_cbranch_vccnz .Lp_110\n"
      "s_branch .Lp_70\n"
      ".Lp_71:\n"
      "s_cbranch_vccz .Lp_73\n"
      ".Lp_111:\n"
      "ds_read_b32 v4, v15\n"
      "ds_read_b128 v[24:27], v9 offset:0\n"
      "s_sub_u32 s73, s73, 1\n"
      "s_cselect_b32 s73, 0, s73\n"
      "s_waitcnt lgkmcnt(0)\n"
      "v_cmp_gt_u32 vcc, 21, v4\n"
      "s_cselect_b64 vcc, 0, vcc\n"
      "s_cbranch_vccnz .Lp_111\n"
      "s_branch .Lp_72\n"
      ".Lp_73:\n"
      "s_cbranch_vccz .Lp_75\n"
      ".Lp_112:\n"
      "ds_read_b32 v4, v15\n"
      "ds_read_b128 v[28:31], v9 offset:1024\n"
      "s_sub_u32 s73, s73, 1\n"
      "s_cselect_b32 s73, 0, s73\n"
      "s_waitcnt lgkmcnt(0)\n"
      "v_cmp_gt_u32 vcc, 22, v4\n"
      "s_cselect_b64 vcc, 0, vcc\n"
      "s_cbranch_vccnz .Lp_112\n"
      "s_branch .Lp_74\n"
      ".Lp_75:\n"
      "s_cbranch_vccz .Lp_77\n"
      ".Lp_113:\n"
      "ds_read_b32 v4, v15\n"
      "ds_read_b128 v[16:19], v9 offset:2048\n"
      "s_sub_u32 s73, s73, 1\n"
      "s_cselect_b32 s73, 0, s73\n"
      "s_waitcnt lgkmcnt(0)\n"
      "v_cmp_gt_u32 vcc, 23, v4\n"
      "s_cselect_b64 vcc, 0, vcc\n"
      "s_cbranch_vccnz .Lp_113\n"
      "s_branch .Lp_76\n"
      ".Lp_77:\n"
      "s_cbranch_vccz .Lp_79\n"
      ".Lp_114:\n"
      "ds_read_b32 v4, v15\n"
      "ds_read_b128 v[20:23], v9 offset:3072\n"
      "s_sub_u32 s73, s73, 1\n"
      "s_cselect_b32 s73, 0, s73\n"
      "s_waitcnt lgkmcnt(0)\n"
      "v_cmp_gt_u32 vcc, 24, v4\n"
      "s_cselect_b64 vcc, 0, vcc\n"
      "s_cbranch_vccnz .Lp_114\n"
      "s_branch .Lp_78\n"
      ".Lp_79:\n"
      "s_cbranch_vccz .Lp_81\n"
      ".Lp_115:\n"
      "ds_read_b32 v4, v15\n"
      "ds_read_b128 v[24:27], v9 offset:4096\n"
      "s_sub_u32 s73, s73, 1\n"
      "s_cselect_b32 s73, 0, s73\n"
      "s_waitcnt lgkmcnt(0)\n"
      "v_cmp_gt_u32 vcc, 25, v4\n"
      "s_cselect_b64 vcc, 0, vcc\n"
      "s_cbranch_vccnz .Lp_115\n"
      "s_branch .Lp_80\n"
      ".Lp_81:\n"
      "s_cbranch_vccz .Lp_83\n"
      ".Lp_116:\n"
      "ds_read_b32 v4, v15\n"
      "ds_read_b128 v[28:31], v9 offset:0\n"
      "s_sub_u32 s73, s73, 1\n"
      "s_cselect_b32 s73, 0, s73\n"
      "s_waitcnt lgkmcnt(0)\n"
      "v_cmp_gt_u32 vcc, 26, v4\n"
      "s_cselect_b64 vcc, 0, vcc\n"
      "s_cbranch_vccnz .Lp_116\n"
      "s_branch .Lp_82\n"
      ".Lp_83:\n"
      "s_cbranch_vccz .Lp_85\n"
      ".Lp_117:\n"
      "ds_read_b32 v4, v15\n"
      "ds_read_b128 v[16:19], v9 offset:1024\n"
      "s_sub_u32 s73, s73, 1\n"
      "s_cselect_b32 s73, 0, s73\n"
      "s_waitcnt lgkmcnt(0)\n"
      "v_cmp_gt_u32 vcc, 27, v4\n"
      "s_cselect_b64 vcc, 0, vcc\n"
      "s_cbranch_vccnz .Lp_117\n"
      "s_branch .Lp_84\n"
      ".Lp_85:\n"
      "s_cbranch_vccz .Lp_87\n"
      ".Lp_118:\n"
      "ds_read_b32 v4, v15\n"
      "ds_read_b128 v[20:23], v9 offset:2048\n"
      "s_sub_u32 s73, s73, 1\n"
      "s_cselect_b32 s73, 0, s73\n"
      "s_waitcnt lgkmcnt(0)\n"
      "v_cmp_gt_u32 vcc, 28, v4\n"
      "s_cselect_b64 vcc, 0, vcc\n"
      "s_cbranch_vccnz .Lp_118\n"
      "s_branch .Lp_86\n"
      ".Lp_87:\n"
      "s_cbranch_vccz .Lp_89\n"
      ".Lp_119:\n"
      "ds_read_b32 v4, v15\n"
      "ds_read_b128 v[24:27], v9 offset:3072\n"
      "s_sub_u32 s73, s73, 1\n"
      "s_cselect_b32 s73, 0, s73\n"
      "s_waitcnt lgkmcnt(0)\n"
      "v_cmp_gt_u32 vcc, 29, v4\n"
      "s_cselect_b64 vcc, 0, vcc\n"
      "s_cbranch_vccnz .Lp_119\n"
      "s_branch .Lp_88\n"
      ".Lp_89:\n"
      "s_cbranch_vccz .LWARME\n"
      ".Lp_120:\n"
      "ds_read_b32 v4, v15\n"
      "ds_read_b128 v[28:31], v9 offset:4096\n"
      "s_sub_u32 s73, s73, 1\n"
      "s_cselect_b32 s73, 0, s73\n"
      "s_waitcnt lgkmcnt(0)\n"
      "v_cmp_gt_u32 vcc, 30, v4\n"
      "s_cselect_b64 vcc, 0, vcc\n"
      "s_cbranch_vccnz .Lp_120\n"
      "s_branch .Lp_90\n"
      ".LM:\n"
      "s_cmp_lt_u32 s61, 28\n"
      "s_cselect_b32 s62, s57, 14\n"
      "s_cselect_b32 s63, 0, s54\n"
      "s_add_u32 s62, s62, s63\n"
      "s_lshl4_add_u32 s62, s56, s62\n"
      "s_lshl2_add_u32 s62, s62, s53\n"
      "s_mul_i32 s63, s62, 2560\n"
      "s_lshl_b32 s69, s62, 4\n"
      "v_lshrrev_b32 v5, 5, v1\n"
      "v_and_b32 v6, 31, v1\n"
      "v_mul_u32_u24 v7, 1280, v5\n"
      "v_lshl_add_u32 v18, v6, 4, v7\n"
      "v_lshl_add_u32 v19, v6, 3, v7\n"
      "v_lshlrev_b32 v11, 2, v1\n"
      "s_mul_i32 s64, s55, 160\n"
      "s_lshl2_add_u32 s65, s57, s53\n"
      "s_lshl_b32 s65, s65, 6\n"
      "v_lshlrev_b32 v12, 1, v6\n"
      "s_and_b32 s66, s2, 7\n"
      "s_mul_i32 s66, s66, 28\n"
      "s_add_u32 s66, s66, s61\n"
      "s_mul_i32 s67, s66, 205\n"
      "s_lshr_b32 s67, s67, 11\n"
      "s_mul_i32 s68, s67, 10\n"
      "s_sub_u32 s68, s66, s68\n"
      "s_lshl2_add_u32 s66, s68, s53\n"
      "s_lshl_b32 s70, s66, 7\n"
      "v_lshrrev_b32 v13, 3, v1\n"
      "v_lshlrev_b32 v14, 4, v13\n"
      "s_mov_b32 m0, 162816\n"
      "s_waitcnt lgkmcnt(0)\n"
      "buffer_load_dwordx4 v[96:99], v18, s[4:7], s63 offen\n"
      "buffer_load_dwordx4 v[100:103], v18, s[4:7], s63 offen offset:512\n"
      "buffer_load_dwordx2 v[104:105], v19, s[4:7], s63 offen offset:1024\n"
      "buffer_load_dwordx2 v[42:43], off, s[20:23], 0 sc1\n"
      "buffer_load_dword v11, s[44:47], s64 offen lds\n"
      "buffer_load_dwordx4 v[44:47], v14, s[24:27], s70 offen\n"
      "buffer_load_ushort v9, v12, s[28:31], s65 offen\n"
      "s_barrier\n"
      "s_setprio 3\n"
      "s_lshl_b32 s62, s53, 5\n"
      "s_add_u32 s62, s62, 140160\n"
      "v_and_b32 v68, 7, v1\n"
      "v_lshrrev_b32 v69, 3, v1\n"
      "v_mul_u32_u24 v192, 1040, v68\n"
      "v_lshl_add_u32 v192, v69, 2, v192\n"
      "v_add_u32 v192, s62, v192\n"
      "v_add_u32 v193, 8720, v192\n"
      "v_mul_u32_u24 v8, 8448, v69\n"
      "v_lshl_add_u32 v8, v68, 4, v8\n"
      "s_mul_i32 s84, s66, 67584\n"
      "s_lshl_b32 s63, s67, 7\n"
      "s_add_u32 s84, s84, s63\n"
      "s_add_u32 s84, s84, 32768\n"
      "s_add_u32 s85, s84, 4224\n"
      "v_lshrrev_b32 v14, 5, v1\n"
      "v_bfe_u32 v15, v1, 2, 2\n"
      "v_lshl_add_u32 v16, v14, 3, v15\n"
      "v_lshlrev_b32 v16, 8, v16\n"
      "v_bfe_u32 v17, v1, 4, 1\n"
      "v_mul_u32_u24 v29, 57344, v17\n"
      "s_lshl_b32 s64, s53, 1\n"
      "v_or_b32 v17, s64, v17\n"
      "v_lshlrev_b32 v15, 1, v15\n"
      "v_xor_b32 v17, v17, v15\n"
      "v_lshl_add_u32 v16, v17, 5, v16\n"
      "v_and_b32 v17, 3, v1\n"
      "v_lshl_add_u32 v24, v17, 3, v16\n"
      "v_and_b32 v15, 0x2f, v1\n"
      "v_lshl_add_u32 v29, v15, 4, v29\n"
      "v_and_b32 v15, 31, v1\n"
      "v_bfe_u32 v16, v1, 1, 3\n"
      "v_xor_b32 v16, v16, v14\n"
      "v_lshlrev_b32 v16, 4, v16\n"
      "v_lshl_add_u32 v4, v15, 7, v16\n"
      "v_xor_b32 v5, 32, v4\n"
      "v_xor_b32 v6, 64, v4\n"
      "v_xor_b32 v7, 96, v4\n"
      "v_lshlrev_b32 v30, 5, v15\n"
      "v_add_u32 v30, 157696, v30\n"
      "v_lshrrev_b32 v16, 4, v1\n"
      "v_xor_b32 v16, v16, v1\n"
      "v_and_b32 v16, 1, v16\n"
      "v_cmp_eq_u32 vcc, 0, v16\n"
      "v_mov_b32 v17, 0x3f803f80\n"
      "v_mov_b64 v[10:11], 0\n"
      "v_cndmask_b32 v16, 0, v17, vcc\n"
      "v_mov_b32 v17, v16\n"
      "v_mov_b32 v18, v16\n"
      "v_mov_b32 v19, v16\n"
      "v_mov_b64 v[14:15], 0\n"
      "s_lshl2_add_u32 s64, s54, s53\n"
      "s_mul_i32 s64, s64, 7\n"
      "s_add_u32 s64, s64, s55\n"
      "s_lshl_b32 s65, s64, 10\n"
      "s_mul_i32 s78, s56, 458752\n"
      "s_add_u32 s78, s78, s65\n"
      "s_add_u32 s78, s78, 7897088\n"
      "s_lshl2_add_u32 s65, s57, s53\n"
      "s_cmp_ge_u32 s65, 54\n"
      "s_cselect_b32 s78, 1073741824, s78\n"
      "s_lshl_b32 s64, s56, 6\n"
      "s_add_u32 s64, s64, s65\n"
      "s_lshl_b32 s64, s64, 8\n"
      "s_add_u32 s79, s64, 18382848\n"
      "s_xor_b32 s74, s99, 0\n"
      "s_mul_i32 s74, s74, 114688\n"
      "s_add_u32 s74, s74, s78\n"
      "s_xor_b32 s75, s99, 1\n"
      "s_mul_i32 s75, s75, 114688\n"
      "s_add_u32 s75, s75, s78\n"
      "s_xor_b32 s76, s99, 2\n"
      "s_mul_i32 s76, s76, 114688\n"
      "s_add_u32 s76, s76, s78\n"
      "s_xor_b32 s77, s99, 3\n"
      "s_mul_i32 s77, s77, 114688\n"
      "s_add_u32 s77, s77, s78\n"
      "s_mul_i32 s63, s56, 3\n"
      "s_add_u32 s63, s63, s57\n"
      "s_mul_i32 s64, s63, 205\n"
      "s_lshr_b32 s64, s64, 11\n"
      "s_mul_i32 s64, s64, 10\n"
      "s_sub_u32 s63, s63, s64\n"
      "s_sub_u32 s64, 3, s53\n"
      "s_lshr_b32 s65, s64, 1\n"
      "s_xor_b32 s65, s65, s99\n"
      "s_and_b32 s65, s65, 1\n"
      "s_mul_i32 s65, s65, 49152\n"
      "s_and_b32 s64, s64, 1\n"
      "s_lshl_b32 s64, s64, 11\n"
      "s_add_u32 s65, s65, s64\n"
      "s_mul_i32 s64, s55, 5\n"
      "s_lshr_b32 s71, s64, 2\n"
      "s_sub_u32 s71, s71, s63\n"
      "s_cselect_b32 s72, 10, 0\n"
      "s_add_u32 s71, s71, s72\n"
      "s_lshl_b32 s71, s71, 12\n"
      "s_add_u32 s71, s71, s65\n"
      "s_and_b32 s72, s64, 3\n"
      "s_lshl_b32 s72, s72, 5\n"
      "s_or_b32 s86, s71, s72\n"
      "s_add_u32 s64, s64, 1\n"
      "s_lshr_b32 s71, s64, 2\n"
      "s_sub_u32 s71, s71, s63\n"
      "s_cselect_b32 s72, 10, 0\n"
      "s_add_u32 s71, s71, s72\n"
      "s_lshl_b32 s71, s71, 12\n"
      "s_add_u32 s71, s71, s65\n"
      "s_and_b32 s72, s64, 3\n"
      "s_lshl_b32 s72, s72, 5\n"
      "s_or_b32 s87, s71, s72\n"
      "s_add_u32 s64, s64, 1\n"
      "s_lshr_b32 s71, s64, 2\n"
      "s_sub_u32 s71, s71, s63\n"
      "s_cselect_b32 s72, 10, 0\n"
      "s_add_u32 s71, s71, s72\n"
      "s_lshl_b32 s71, s71, 12\n"
      "s_add_u32 s71, s71, s65\n"
      "s_and_b32 s72, s64, 3\n"
      "s_lshl_b32 s72, s72, 5\n"
      "s_or_b32 s88, s71, s72\n"
      "s_add_u32 s64, s64, 1\n"
      "s_lshr_b32 s71, s64, 2\n"
      "s_sub_u32 s71, s71, s63\n"
      "s_cselect_b32 s72, 10, 0\n"
      "s_add_u32 s71, s71, s72\n"
      "s_lshl_b32 s71, s71, 12\n"
      "s_add_u32 s71, s71, s65\n"
      "s_and_b32 s72, s64, 3\n"
      "s_lshl_b32 s72, s72, 5\n"
      "s_or_b32 s89, s71, s72\n"
      "s_add_u32 s64, s64, 1\n"
      "s_lshr_b32 s71, s64, 2\n"
      "s_sub_u32 s71, s71, s63\n"
      "s_cselect_b32 s72, 10, 0\n"
      "s_add_u32 s71, s71, s72\n"
      "s_lshl_b32 s71, s71, 12\n"
      "s_add_u32 s71, s71, s65\n"
      "s_and_b32 s72, s64, 3\n"
      "s_lshl_b32 s72, s72, 5\n"
      "s_or_b32 s90, s71, s72\n"
      "s_add_u32 s64, s64, 1\n"
      "v_and_b32 v50, 15, v1\n"
      "v_bfe_u32 v51, v1, 1, 3\n"
      "v_lshrrev_b32 v52, 5, v1\n"
      "v_xor_b32 v51, v51, v52\n"
      "v_lshlrev_b32 v51, 4, v51\n"
      "v_lshl_add_u32 v51, v50, 7, v51\n"
      "v_bfe_u32 v52, v1, 4, 1\n"
      "v_lshl_add_u32 v28, v52, 3, v51\n"
      "s_mov_b32 s80, s92\n"
      "s_mov_b32 s81, s92\n"
      "s_mov_b32 s82, -1\n"
      "s_mov_b32 s83, 0\n"
      "s_waitcnt vmcnt(3)\n"
      "ds_swizzle_b32 v106, v96 offset:0\n"
      "v_lshlrev_b32 v116, 16, v96\n"
      "v_and_b32 v117, s92, v96\n"
      "v_lshlrev_b32 v118, 16, v97\n"
      "v_and_b32 v119, s92, v97\n"
      "v_lshlrev_b32 v120, 16, v98\n"
      "v_and_b32 v121, s92, v98\n"
      "v_lshlrev_b32 v122, 16, v99\n"
      "v_and_b32 v123, s92, v99\n"
      "v_lshlrev_b32 v124, 16, v100\n"
      "v_and_b32 v125, s92, v100\n"
      "v_lshlrev_b32 v126, 16, v101\n"
      "v_and_b32 v127, s92, v101\n"
      "v_lshlrev_b32 v128, 16, v102\n"
      "v_and_b32 v129, s92, v102\n"
      "v_lshlrev_b32 v130, 16, v103\n"
      "v_and_b32 v131, s92, v103\n"
      "v_lshlrev_b32 v132, 16, v104\n"
      "v_and_b32 v133, s92, v104\n"
      "v_lshlrev_b32 v134, 16, v105\n"
      "v_and_b32 v135, s92, v105\n"
      "v_readfirstlane_b32 s58, v42\n"
      "v_readfirstlane_b32 s59, v43\n"
      "s_waitcnt lgkmcnt(0)\n"
      "v_lshlrev_b32 v106, 16, v106\n"
      "v_mov_b32 v107, v106\n"
      "s_add_u32 s58, s58, 1\n"
      "s_addc_u32 s59, s59, 0\n"
      "v_pk_add_f32 v[116:117], v[116:117], v[106:107] neg_lo:[0,1] neg_hi:[0,1]\n"
      "v_pk_add_f32 v[118:119], v[118:119], v[106:107] neg_lo:[0,1] neg_hi:[0,1]\n"
      "v_pk_add_f32 v[120:121], v[120:121], v[106:107] neg_lo:[0,1] neg_hi:[0,1]\n"
      "v_pk_add_f32 v[122:123], v[122:123], v[106:107] neg_lo:[0,1] neg_hi:[0,1]\n"
      "v_pk_add_f32 v[124:125], v[124:125], v[106:107] neg_lo:[0,1] neg_hi:[0,1]\n"
      "v_pk_add_f32 v[126:127], v[126:127], v[106:107] neg_lo:[0,1] neg_hi:[0,1]\n"
      "v_pk_add_f32 v[128:129], v[128:129], v[106:107] neg_lo:[0,1] neg_hi:[0,1]\n"
      "v_pk_add_f32 v[130:131], v[130:131], v[106:107] neg_lo:[0,1] neg_hi:[0,1]\n"
      "v_pk_add_f32 v[132:133], v[132:133], v[106:107] neg_lo:[0,1] neg_hi:[0,1]\n"
      "v_pk_add_f32 v[134:135], v[134:135], v[106:107] neg_lo:[0,1] neg_hi:[0,1]\n"
      "v_pk_mul_f32 v[108:109], v[116:117], v[116:117]\n"
      "v_pk_mul_f32 v[110:111], v[118:119], v[118:119]\n"
      "v_pk_fma_f32 v[108:109], v[120:121], v[120:121], v[108:109]\n"
      "v_pk_add_f32 v[116:117], v[116:117], v[120:121]\n"
      "v_pk_fma_f32 v[110:111], v[122:123], v[122:123], v[110:111]\n"
      "v_pk_add_f32 v[118:119], v[118:119], v[122:123]\n"
      "v_pk_fma_f32 v[108:109], v[124:125], v[124:125], v[108:109]\n"
      "v_pk_add_f32 v[116:117], v[116:117], v[124:125]\n"
      "v_pk_fma_f32 v[110:111], v[126:127], v[126:127], v[110:111]\n"
      "v_pk_add_f32 v[118:119], v[118:119], v[126:127]\n"
      "v_pk_fma_f32 v[108:109], v[128:129], v[128:129], v[108:109]\n"
      "v_pk_add_f32 v[116:117], v[116:117], v[128:129]\n"
      "v_pk_fma_f32 v[110:111], v[130:131], v[130:131], v[110:111]\n"
      "v_pk_add_f32 v[118:119], v[118:119], v[130:131]\n"
      "v_pk_fma_f32 v[108:109], v[132:133], v[132:133], v[108:109]\n"
      "v_pk_add_f32 v[116:117], v[116:117], v[132:133]\n"
      "v_pk_fma_f32 v[110:111], v[134:135], v[134:135], v[110:111]\n"
      "v_pk_add_f32 v[118:119], v[118:119], v[134:135]\n"
      "v_pk_add_f32 v[108:109], v[108:109], v[110:111]\n"
      "v_pk_add_f32 v[116:117], v[116:117], v[118:119]\n"
      "v_add_f32 v108, v108, v109\n"
      "v_add_f32 v109, v116, v117\n"
      "s_and_b32 s93, s58, 0x1fff\n"
      "v_add_f32_dpp v108, v108, v108 quad_perm:[1,0,3,2] row_mask:0xf bank_mask:0xf\n"
      "v_add_f32_dpp v109, v109, v109 quad_perm:[1,0,3,2] row_mask:0xf bank_mask:0xf\n"
      "s_or_b32 s93, s93, 0x4000\n"
      "v_add_f32_dpp v108, v108, v108 quad_perm:[2,3,0,1] row_mask:0xf bank_mask:0xf\n"
      "v_add_f32_dpp v109, v109, v109 quad_perm:[2,3,0,1] row_mask:0xf bank_mask:0xf\n"
      "v_mov_b32 v114, 1073741824\n"
      "v_add_f32_dpp v108, v108, v108 row_half_mirror row_mask:0xf bank_mask:0xf\n"
      "v_add_f32_dpp v109, v109, v109 row_half_mirror row_mask:0xf bank_mask:0xf\n"
      "v_writelane_b32 v114, 0, 16\n"
      "v_add_f32_dpp v108, v108, v108 row_mirror row_mask:0xf bank_mask:0xf\n"
      "v_add_f32_dpp v109, v109, v109 row_mirror row_mask:0xf bank_mask:0xf\n"
      "v_writelane_b32 v114, 8, 48\n"
      "v_add_f32_dpp v108, v108, v108 row_bcast:15 row_mask:0xa bank_mask:0xf\n"
      "v_add_f32_dpp v109, v109, v109 row_bcast:15 row_mask:0xa bank_mask:0xf\n"
      "v_mul_f32 v109, 0x3acccccd, v109\n"
      "v_mul_f32 v108, 0x3acccccd, v108\n"
      "v_fma_f32 v108, -v109, v109, v108\n"
      "v_max_f32 v108, 0, v108\n"
      "v_cvt_pk_bf16_f32 v108, 0, v108\n"
      "v_add_f32 v108, 0x3727c5ac, v108\n"
      "v_cvt_pk_bf16_f32 v108, 0, v108\n"
      "v_sqrt_f32 v108, v108\n"
      "v_add_f32 v109, v106, v109\n"
      "s_add_u32 s69, s69, 8192\n"
      "v_cvt_pk_bf16_f32 v112, v109, v108\n"
      "v_mov_b32 v113, s58\n"
      "buffer_store_dwordx2 v[112:113], v114, s[20:23], s69 offen sc1\n"
      "s_cmp_lt_u32 s61, 28\n"
      "s_cbranch_scc0 .LSPARE\n"
      "v_mov_b64 v[12:13], s[58:59]\n"
      "v_cmp_eq_u32 vcc, 0, v1\n"
      "v_lshlrev_b32 v2, 2, v1\n"
      "v_add_u32 v2, 157696, v2\n"
      "v_cndmask_b32 v3, 0, 1, vcc\n"
      "s_waitcnt vmcnt(1)\n"
      "v_lshlrev_b32 v184, 16, v44\n"
      "v_lshlrev_b32 v185, 16, v46\n"
      "v_and_b32 v186, s92, v44\n"
      "v_and_b32 v187, s92, v46\n"
      "v_lshlrev_b32 v188, 16, v45\n"
      "v_lshlrev_b32 v189, 16, v47\n"
      "v_and_b32 v190, s92, v45\n"
      "v_and_b32 v191, s92, v47\n"
      "v_lshlrev_b32 v9, 16, v9\n"
      "s_barrier\n"
      "v_mov_b32 v31, 161920\n"
      "ds_write_b64 v31, v[12:13]\n"
      "ds_read_b32 v26, v2 offset:5120\n"
      "v_lshlrev_b32 v31, 3, v1\n"
      "v_add_u32 v31, 163072, v31\n"
      "s_waitcnt lgkmcnt(0)\n"
      "v_and_b32 v27, s92, v26\n"
      "v_lshlrev_b32 v26, 16, v26\n"
      "ds_write_b64 v31, v[26:27]\n"
      "ds_read_b64_tr_b16 v[96:97], v24 offset:0\n"
      "ds_read_b64_tr_b16 v[98:99], v24 offset:1024\n"
      "ds_read_b128 v[32:35], v4 offset:16384\n"
      "ds_read_b128 v[36:39], v4 offset:20480\n"
      "ds_read_b64_tr_b16 v[100:101], v24 offset:4096\n"
      "ds_read_b64_tr_b16 v[102:103], v24 offset:5120\n"
      "ds_read_b128 v[40:43], v5 offset:16384\n"
      "ds_read_b128 v[44:47], v5 offset:20480\n"
      "ds_read_b64_tr_b16 v[104:105], v24 offset:8192\n"
      "ds_read_b64_tr_b16 v[106:107], v24 offset:9216\n"
      "ds_read_b128 v[48:51], v6 offset:16384\n"
      "ds_read_b128 v[52:55], v6 offset:20480\n"
      "ds_read_b64_tr_b16 v[108:109], v24 offset:12288\n"
      "ds_read_b64_tr_b16 v[110:111], v24 offset:13312\n"
      "ds_read_b128 v[56:59], v7 offset:16384\n"
      "ds_read_b128 v[60:63], v7 offset:20480\n"
      "s_waitcnt lgkmcnt(13)\n"
      "v_mfma_f32_32x32x16_bf16 v[80:95], v[96:99], v[32:35], 0\n"
      "s_waitcnt lgkmcnt(12)\n"
      "v_mfma_f32_32x32x16_bf16 v[64:79], v[96:99], v[36:39], 0\n"
      "v_mfma_f32_16x16x32_bf16 v[20:23], v[16:19], v[96:99], 0\n"
      "s_waitcnt lgkmcnt(10)\n"
      "v_mfma_f32_16x16x32_bf16 v[20:23], v[16:19], v[100:103], v[20:23]\n"
      "s_waitcnt lgkmcnt(9)\n"
      "v_mfma_f32_32x32x16_bf16 v[80:95], v[100:103], v[40:43], v[80:95]\n"
      "s_waitcnt lgkmcnt(0)\n"
      "v_mfma_f32_32x32x16_bf16 v[64:79], v[100:103], v[44:47], v[64:79]\n"
      "s_barrier\n"
      "ds_read2_b32 v[196:197], v192 offset0:0 offset1:32\n"
      "ds_read2_b32 v[198:199], v192 offset0:64 offset1:100\n"
      "ds_read2_b32 v[200:201], v192 offset0:132 offset1:164\n"
      "ds_read2_b32 v[202:203], v192 offset0:196 offset1:228\n"
      "ds_read_b32 v204, v192 offset:1040\n"
      "ds_read_b32 v205, v192 offset:1168\n"
      "ds_read_b32 v206, v192 offset:1296\n"
      "ds_read2_b32 v[208:209], v193 offset0:0 offset1:32\n"
      "ds_read2_b32 v[210:211], v193 offset0:64 offset1:96\n"
      "ds_read2_b32 v[212:213], v193 offset0:128 offset1:160\n"
      "ds_read2_b32 v[214:215], v193 offset0:192 offset1:224\n"
      "s_waitcnt lgkmcnt(11)\n"
      "v_mfma_f32_32x32x16_bf16 v[80:95], v[104:107], v[48:51], v[80:95]\n"
      "s_waitcnt lgkmcnt(9)\n"
      "v_lshlrev_b32 v216, 16, v196\n"
      "v_and_b32 v217, s92, v196\n"
      "v_lshlrev_b32 v218, 16, v197\n"
      "v_and_b32 v219, s92, v197\n"
      "v_mfma_f32_32x32x16_bf16 v[64:79], v[104:107], v[52:55], v[64:79]\n"
      "v_lshlrev_b32 v220, 16, v198\n"
      "v_and_b32 v221, s92, v198\n"
      "v_lshlrev_b32 v222, 16, v199\n"
      "v_and_b32 v223, s92, v199\n"
      "v_mfma_f32_32x32x16_bf16 v[80:95], v[108:111], v[56:59], v[80:95]\n"
      "s_waitcnt lgkmcnt(7)\n"
      "v_lshlrev_b32 v224, 16, v200\n"
      "v_and_b32 v225, s92, v200\n"
      "v_lshlrev_b32 v226, 16, v201\n"
      "v_and_b32 v227, s92, v201\n"
      "v_mfma_f32_32x32x16_bf16 v[64:79], v[108:111], v[60:63], v[64:79]\n"
      "v_lshlrev_b32 v228, 16, v202\n"
      "v_and_b32 v229, s92, v202\n"
      "v_lshlrev_b32 v230, 16, v203\n"
      "v_and_b32 v231, s92, v203\n"
      "v_mfma_f32_16x16x32_bf16 v[20:23], v[16:19], v[104:107], v[20:23]\n"
      "s_waitcnt lgkmcnt(4)\n"
      "v_lshlrev_b32 v232, 16, v204\n"
      "v_and_b32 v233, s92, v204\n"
      "v_lshlrev_b32 v234, 16, v205\n"
      "v_and_b32 v235, s92, v205\n"
      "v_mfma_f32_16x16x32_bf16 v[20:23], v[16:19], v[108:111], v[20:23]\n"
      "v_lshlrev_b32 v236, 16, v206\n"
      "v_and_b32 v237, s92, v206\n"
      "v_pk_mul_f32 v[240:241], v[216:217], v[190:191]\n"
      "v_pk_mul_f32 v[242:243], v[218:219], v[190:191]\n"
      "v_pk_mul_f32 v[244:245], v[220:221], v[190:191]\n"
      "v_pk_mul_f32 v[246:247], v[222:223], v[190:191]\n"
      "v_pk_mul_f32 v[248:249], v[224:225], v[190:191]\n"
      "v_pk_mul_f32 v[250:251], v[226:227], v[190:191]\n"
      "v_pk_mul_f32 v[252:253], v[228:229], v[190:191]\n"
      "v_pk_mul_f32 v[254:255], v[230:231], v[190:191]\n"
      "v_pk_fma_f32 v[240:241], v[218:219], v[188:189], v[240:241]\n"
      "v_pk_fma_f32 v[242:243], v[220:221], v[188:189], v[242:243]\n"
      "v_pk_fma_f32 v[244:245], v[222:223], v[188:189], v[244:245]\n"
      "v_pk_fma_f32 v[246:247], v[224:225], v[188:189], v[246:247]\n"
      "v_pk_fma_f32 v[248:249], v[226:227], v[188:189], v[248:249]\n"
      "v_pk_fma_f32 v[250:251], v[228:229], v[188:189], v[250:251]\n"
      "v_pk_fma_f32 v[252:253], v[230:231], v[188:189], v[252:253]\n"
      "v_pk_fma_f32 v[254:255], v[232:233], v[188:189], v[254:255]\n"
      "v_pk_fma_f32 v[240:241], v[220:221], v[186:187], v[240:241]\n"
      "v_pk_fma_f32 v[242:243], v[222:223], v[186:187], v[242:243]\n"
      "v_pk_fma_f32 v[244:245], v[224:225], v[186:187], v[244:245]\n"
      "v_pk_fma_f32 v[246:247], v[226:227], v[186:187], v[246:247]\n"
      "v_pk_fma_f32 v[248:249], v[228:229], v[186:187], v[248:249]\n"
      "v_pk_fma_f32 v[250:251], v[230:231], v[186:187], v[250:251]\n"
      "v_pk_fma_f32 v[252:253], v[232:233], v[186:187], v[252:253]\n"
      "v_pk_fma_f32 v[254:255], v[234:235], v[186:187], v[254:255]\n"
      "v_pk_fma_f32 v[240:241], v[222:223], v[184:185], v[240:241]\n"
      "v_pk_fma_f32 v[242:243], v[224:225], v[184:185], v[242:243]\n"
      "v_pk_fma_f32 v[244:245], v[226:227], v[184:185], v[244:245]\n"
      "v_pk_fma_f32 v[246:247], v[228:229], v[184:185], v[246:247]\n"
      "v_pk_fma_f32 v[248:249], v[230:231], v[184:185], v[248:249]\n"
      "v_pk_fma_f32 v[250:251], v[232:233], v[184:185], v[250:251]\n"
      "v_pk_fma_f32 v[252:253], v[234:235], v[184:185], v[252:253]\n"
      "v_pk_fma_f32 v[254:255], v[236:237], v[184:185], v[254:255]\n"
      "s_waitcnt lgkmcnt(0)\n"
      "v_lshlrev_b32 v216, 16, v208\n"
      "v_and_b32 v217, s92, v208\n"
      "v_lshlrev_b32 v218, 16, v209\n"
      "v_and_b32 v219, s92, v209\n"
      "v_lshlrev_b32 v220, 16, v210\n"
      "v_and_b32 v221, s92, v210\n"
      "v_lshlrev_b32 v222, 16, v211\n"
      "v_and_b32 v223, s92, v211\n"
      "v_lshlrev_b32 v224, 16, v212\n"
      "v_and_b32 v225, s92, v212\n"
      "v_lshlrev_b32 v226, 16, v213\n"
      "v_and_b32 v227, s92, v213\n"
      "v_lshlrev_b32 v228, 16, v214\n"
      "v_and_b32 v229, s92, v214\n"
      "v_lshlrev_b32 v230, 16, v215\n"
      "v_and_b32 v231, s92, v215\n"
      "v_pk_mul_f32 v[240:241], v[240:241], v[216:217]\n"
      "v_pk_mul_f32 v[242:243], v[242:243], v[218:219]\n"
      "v_pk_mul_f32 v[244:245], v[244:245], v[220:221]\n"
      "v_pk_mul_f32 v[246:247], v[246:247], v[222:223]\n"
      "v_pk_mul_f32 v[248:249], v[248:249], v[224:225]\n"
      "v_pk_mul_f32 v[250:251], v[250:251], v[226:227]\n"
      "v_pk_mul_f32 v[252:253], v[252:253], v[228:229]\n"
      "v_pk_mul_f32 v[254:255], v[254:255], v[230:231]\n"
      "v_cvt_pk_bf16_f32 v196, v242, v240\n"
      "v_cvt_pk_bf16_f32 v197, v246, v244\n"
      "v_cvt_pk_bf16_f32 v198, v250, v248\n"
      "v_cvt_pk_bf16_f32 v199, v254, v252\n"
      "buffer_store_dwordx4 v[196:199], v8, s[20:23], s84 offen sc1\n"
      "v_cvt_pk_bf16_f32 v200, v243, v241\n"
      "v_cvt_pk_bf16_f32 v201, v247, v245\n"
      "v_cvt_pk_bf16_f32 v202, v251, v249\n"
      "v_cvt_pk_bf16_f32 v203, v255, v253\n"
      "buffer_store_dwordx4 v[200:203], v8, s[20:23], s85 offen sc1\n"
      "s_barrier\n"
      "ds_read_b64_tr_b16 v[112:113], v24 offset:24576\n"
      "ds_read_b64_tr_b16 v[114:115], v24 offset:25600\n"
      "ds_read_b128 v[32:35], v4 offset:40960\n"
      "ds_read_b128 v[36:39], v4 offset:45056\n"
      "ds_read_b64_tr_b16 v[116:117], v24 offset:28672\n"
      "ds_read_b64_tr_b16 v[118:119], v24 offset:29696\n"
      "ds_read_b128 v[40:43], v5 offset:40960\n"
      "ds_read_b128 v[44:47], v5 offset:45056\n"
      "ds_read_b64_tr_b16 v[120:121], v24 offset:32768\n"
      "ds_read_b64_tr_b16 v[122:123], v24 offset:33792\n"
      "ds_read_b128 v[48:51], v6 offset:40960\n"
      "ds_read_b128 v[52:55], v6 offset:45056\n"
      "ds_read_b64_tr_b16 v[124:125], v24 offset:36864\n"
      "ds_read_b64_tr_b16 v[126:127], v24 offset:37888\n"
      "ds_read_b128 v[56:59], v7 offset:40960\n"
      "ds_read_b128 v[60:63], v7 offset:45056\n"
      "s_waitcnt lgkmcnt(13)\n"
      "v_mfma_f32_32x32x16_bf16 v[80:95], v[112:115], v[32:35], v[80:95]\n"
      "s_waitcnt lgkmcnt(12)\n"
      "v_mfma_f32_32x32x16_bf16 v[64:79], v[112:115], v[36:39], v[64:79]\n"
      "v_mfma_f32_16x16x32_bf16 v[20:23], v[16:19], v[112:115], v[20:23]\n"
      "s_waitcnt lgkmcnt(10)\n"
      "v_mfma_f32_16x16x32_bf16 v[20:23], v[16:19], v[116:119], v[20:23]\n"
      "s_waitcnt lgkmcnt(9)\n"
      "v_mfma_f32_32x32x16_bf16 v[80:95], v[116:119], v[40:43], v[80:95]\n"
      "s_waitcnt lgkmcnt(0)\n"
      "v_add_u32 v24, 49152, v24\n"
      "v_add_u32 v4, 49152, v4\n"
      "v_add_u32 v5, 49152, v5\n"
      "v_add_u32 v6, 49152, v6\n"
      "v_add_u32 v7, 49152, v7\n"
      "v_mfma_f32_32x32x16_bf16 v[64:79], v[116:119], v[44:47], v[64:79]\n"
      "s_barrier\n"
      "v_mfma_f32_32x32x16_bf16 v[80:95], v[120:123], v[48:51], v[80:95]\n"
      "ds_read_b64_tr_b16 v[128:129], v24 offset:0\n"
      "ds_read_b64_tr_b16 v[130:131], v24 offset:1024\n"
      "ds_read_b128 v[32:35], v4 offset:16384\n"
      "ds_read_b128 v[36:39], v4 offset:20480\n"
      "v_mfma_f32_32x32x16_bf16 v[64:79], v[120:123], v[52:55], v[64:79]\n"
      "ds_read_b64_tr_b16 v[132:133], v24 offset:4096\n"
      "ds_read_b64_tr_b16 v[134:135], v24 offset:5120\n"
      "ds_read_b128 v[40:43], v5 offset:16384\n"
      "ds_read_b128 v[44:47], v5 offset:20480\n"
      "v_mfma_f32_32x32x16_bf16 v[80:95], v[124:127], v[56:59], v[80:95]\n"
      "ds_read_b64_tr_b16 v[136:137], v24 offset:8192\n"
      "ds_read_b64_tr_b16 v[138:139], v24 offset:9216\n"
      "ds_read_b128 v[48:51], v6 offset:16384\n"
      "ds_read_b128 v[52:55], v6 offset:20480\n"
      "v_mfma_f32_32x32x16_bf16 v[64:79], v[124:127], v[60:63], v[64:79]\n"
      "ds_read_b64_tr_b16 v[140:141], v24 offset:12288\n"
      "ds_read_b64_tr_b16 v[142:143], v24 offset:13312\n"
      "ds_read_b128 v[56:59], v7 offset:16384\n"
      "ds_read_b128 v[60:63], v7 offset:20480\n"
      "v_mfma_f32_16x16x32_bf16 v[20:23], v[16:19], v[120:123], v[20:23]\n"
      "v_mfma_f32_16x16x32_bf16 v[20:23], v[16:19], v[124:127], v[20:23]\n"
      "s_waitcnt lgkmcnt(13)\n"
      "v_mfma_f32_32x32x16_bf16 v[80:95], v[128:131], v[32:35], v[80:95]\n"
      "s_waitcnt lgkmcnt(12)\n"
      "v_mfma_f32_32x32x16_bf16 v[64:79], v[128:131], v[36:39], v[64:79]\n"
      "v_mfma_f32_16x16x32_bf16 v[20:23], v[16:19], v[128:131], v[20:23]\n"
      "s_waitcnt lgkmcnt(8)\n"
      "v_mfma_f32_16x16x32_bf16 v[20:23], v[16:19], v[132:135], v[20:23]\n"
      "v_mfma_f32_32x32x16_bf16 v[80:95], v[132:135], v[40:43], v[80:95]\n"
      "s_waitcnt lgkmcnt(0)\n"
      "v_mfma_f32_32x32x16_bf16 v[64:79], v[132:135], v[44:47], v[64:79]\n"
      "s_barrier\n"
      "v_mfma_f32_32x32x16_bf16 v[80:95], v[136:139], v[48:51], v[80:95]\n"
      "ds_read_b64_tr_b16 v[144:145], v24 offset:24576\n"
      "ds_read_b64_tr_b16 v[146:147], v24 offset:25600\n"
      "ds_read_b128 v[32:35], v4 offset:40960\n"
      "ds_read_b128 v[36:39], v4 offset:45056\n"
      "v_mfma_f32_32x32x16_bf16 v[64:79], v[136:139], v[52:55], v[64:79]\n"
      "ds_read_b64_tr_b16 v[148:149], v24 offset:28672\n"
      "ds_read_b64_tr_b16 v[150:151], v24 offset:29696\n"
      "ds_read_b128 v[40:43], v5 offset:40960\n"
      "ds_read_b128 v[44:47], v5 offset:45056\n"
      "v_mfma_f32_32x32x16_bf16 v[80:95], v[140:143], v[56:59], v[80:95]\n"
      "ds_read_b64_tr_b16 v[152:153], v24 offset:32768\n"
      "ds_read_b64_tr_b16 v[154:155], v24 offset:33792\n"
      "ds_read_b128 v[48:51], v6 offset:40960\n"
      "ds_read_b128 v[52:55], v6 offset:45056\n"
      "v_mfma_f32_32x32x16_bf16 v[64:79], v[140:143], v[60:63], v[64:79]\n"
      "ds_read_b64_tr_b16 v[156:157], v24 offset:36864\n"
      "ds_read_b64_tr_b16 v[158:159], v24 offset:37888\n"
      "ds_read_b128 v[56:59], v7 offset:40960\n"
      "ds_read_b128 v[60:63], v7 offset:45056\n"
      "v_mfma_f32_16x16x32_bf16 v[20:23], v[16:19], v[136:139], v[20:23]\n"
      "v_mfma_f32_16x16x32_bf16 v[20:23], v[16:19], v[140:143], v[20:23]\n"
      "s_waitcnt lgkmcnt(13)\n"
      "v_mfma_f32_32x32x16_bf16 v[80:95], v[144:147], v[32:35], v[80:95]\n"
      "s_waitcnt lgkmcnt(12)\n"
      "v_mfma_f32_32x32x16_bf16 v[64:79], v[144:147], v[36:39], v[64:79]\n"
      "v_mfma_f32_16x16x32_bf16 v[20:23], v[16:19], v[144:147], v[20:23]\n"
      "s_waitcnt lgkmcnt(8)\n"
      "v_mfma_f32_16x16x32_bf16 v[20:23], v[16:19], v[148:151], v[20:23]\n"
      "v_mfma_f32_32x32x16_bf16 v[80:95], v[148:151], v[40:43], v[80:95]\n"
      "s_waitcnt lgkmcnt(0)\n"
      "v_add_u32 v24, 49152, v24\n"
      "v_add_u32 v4, 49152, v4\n"
      "v_add_u32 v5, 49152, v5\n"
      "v_add_u32 v6, 49152, v6\n"
      "v_add_u32 v7, 49152, v7\n"
      "v_mfma_f32_32x32x16_bf16 v[64:79], v[148:151], v[44:47], v[64:79]\n"
      "s_barrier\n"
      "v_mfma_f32_32x32x16_bf16 v[80:95], v[152:155], v[48:51], v[80:95]\n"
      "ds_read_b64_tr_b16 v[160:161], v24 offset:0\n"
      "ds_read_b64_tr_b16 v[162:163], v24 offset:1024\n"
      "ds_read_b128 v[32:35], v4 offset:16384\n"
      "ds_read_b128 v[36:39], v4 offset:20480\n"
      "v_mfma_f32_32x32x16_bf16 v[64:79], v[152:155], v[52:55], v[64:79]\n"
      "ds_read_b64_tr_b16 v[164:165], v24 offset:4096\n"
      "ds_read_b64_tr_b16 v[166:167], v24 offset:5120\n"
      "ds_read_b128 v[40:43], v5 offset:16384\n"
      "ds_read_b128 v[44:47], v5 offset:20480\n"
      "v_mfma_f32_32x32x16_bf16 v[80:95], v[156:159], v[56:59], v[80:95]\n"
      "ds_read_b64_tr_b16 v[168:169], v24 offset:8192\n"
      "ds_read_b64_tr_b16 v[170:171], v24 offset:9216\n"
      "ds_read_b128 v[48:51], v6 offset:16384\n"
      "ds_read_b128 v[52:55], v6 offset:20480\n"
      "v_mfma_f32_32x32x16_bf16 v[64:79], v[156:159], v[60:63], v[64:79]\n"
      "ds_read_b64_tr_b16 v[172:173], v24 offset:12288\n"
      "ds_read_b64_tr_b16 v[174:175], v24 offset:13312\n"
      "ds_read_b128 v[56:59], v7 offset:16384\n"
      "ds_read_b128 v[60:63], v7 offset:20480\n"
      "v_mfma_f32_16x16x32_bf16 v[20:23], v[16:19], v[152:155], v[20:23]\n"
      "v_mfma_f32_16x16x32_bf16 v[20:23], v[16:19], v[156:159], v[20:23]\n"
      "s_waitcnt lgkmcnt(13)\n"
      "v_mfma_f32_32x32x16_bf16 v[80:95], v[160:163], v[32:35], v[80:95]\n"
      "s_waitcnt lgkmcnt(12)\n"
      "v_mfma_f32_32x32x16_bf16 v[64:79], v[160:163], v[36:39], v[64:79]\n"
      "v_mfma_f32_16x16x32_bf16 v[20:23], v[16:19], v[160:163], v[20:23]\n"
      "s_waitcnt lgkmcnt(8)\n"
      "v_mfma_f32_16x16x32_bf16 v[20:23], v[16:19], v[164:167], v[20:23]\n"
      "v_mfma_f32_32x32x16_bf16 v[80:95], v[164:167], v[40:43], v[80:95]\n"
      "s_waitcnt lgkmcnt(0)\n"
      "v_add_u32 v24, 4294868992, v24\n"
      "v_add_u32 v4, 4294868992, v4\n"
      "v_add_u32 v5, 4294868992, v5\n"
      "v_add_u32 v6, 4294868992, v6\n"
      "v_add_u32 v7, 4294868992, v7\n"
      "s_waitcnt vmcnt(0)\n"
      "v_mfma_f32_32x32x16_bf16 v[64:79], v[164:167], v[44:47], v[64:79]\n"
      "s_barrier\n"
      "v_mfma_f32_32x32x16_bf16 v[80:95], v[168:171], v[48:51], v[80:95]\n"
      "ds_read_b64_tr_b16 v[176:177], v24 offset:0\n"
      "ds_read_b64_tr_b16 v[178:179], v24 offset:1024\n"
      "ds_read_b128 v[32:35], v4 offset:16384\n"
      "ds_read_b128 v[36:39], v4 offset:20480\n"
      "v_mfma_f32_32x32x16_bf16 v[64:79], v[168:171], v[52:55], v[64:79]\n"
      "ds_read_b64_tr_b16 v[180:181], v24 offset:4096\n"
      "ds_read_b64_tr_b16 v[182:183], v24 offset:5120\n"
      "ds_read_b128 v[40:43], v5 offset:16384\n"
      "ds_read_b128 v[44:47], v5 offset:20480\n"
      "v_mfma_f32_32x32x16_bf16 v[80:95], v[172:175], v[56:59], v[80:95]\n"
      "ds_read_b64_tr_b16 v[184:185], v24 offset:8192\n"
      "ds_read_b64_tr_b16 v[186:187], v24 offset:9216\n"
      "ds_read_b128 v[48:51], v6 offset:16384\n"
      "ds_read_b128 v[52:55], v6 offset:20480\n"
      "v_mfma_f32_32x32x16_bf16 v[64:79], v[172:175], v[60:63], v[64:79]\n"
      "ds_read_b64_tr_b16 v[188:189], v24 offset:12288\n"
      "ds_read_b64_tr_b16 v[190:191], v24 offset:13312\n"
      "ds_read_b128 v[56:59], v7 offset:16384\n"
      "ds_read_b128 v[60:63], v7 offset:20480\n"
      "v_mfma_f32_16x16x32_bf16 v[20:23], v[16:19], v[168:171], v[20:23]\n"
      "v_mfma_f32_16x16x32_bf16 v[20:23], v[16:19], v[172:175], v[20:23]\n"
      "s_waitcnt lgkmcnt(13)\n"
      "v_mfma_f32_32x32x16_bf16 v[80:95], v[176:179], v[32:35], v[80:95]\n"
      "s_waitcnt lgkmcnt(12)\n"
      "v_mfma_f32_32x32x16_bf16 v[64:79], v[176:179], v[36:39], v[64:79]\n"
      "v_mfma_f32_16x16x32_bf16 v[20:23], v[16:19], v[176:179], v[20:23]\n"
      "s_waitcnt lgkmcnt(8)\n"
      "v_mfma_f32_16x16x32_bf16 v[20:23], v[16:19], v[180:183], v[20:23]\n"
      "v_mfma_f32_32x32x16_bf16 v[80:95], v[180:183], v[40:43], v[80:95]\n"
      "s_waitcnt lgkmcnt(0)\n"
      "v_mfma_f32_32x32x16_bf16 v[64:79], v[180:183], v[44:47], v[64:79]\n"
      "s_barrier\n"
      "v_mfma_f32_32x32x16_bf16 v[80:95], v[184:187], v[48:51], v[80:95]\n"
      "ds_read_b64_tr_b16 v[192:193], v24 offset:24576\n"
      "ds_read_b64_tr_b16 v[194:195], v24 offset:25600\n"
      "ds_read_b128 v[32:35], v4 offset:40960\n"
      "ds_read_b128 v[36:39], v4 offset:45056\n"
      "v_mfma_f32_32x32x16_bf16 v[64:79], v[184:187], v[52:55], v[64:79]\n"
      "ds_read_b64_tr_b16 v[196:197], v24 offset:28672\n"
      "ds_read_b64_tr_b16 v[198:199], v24 offset:29696\n"
      "ds_read_b128 v[40:43], v5 offset:40960\n"
      "ds_read_b128 v[44:47], v5 offset:45056\n"
      "v_mfma_f32_32x32x16_bf16 v[80:95], v[188:191], v[56:59], v[80:95]\n"
      "ds_read_b64_tr_b16 v[200:201], v24 offset:32768\n"
      "ds_read_b64_tr_b16 v[202:203], v24 offset:33792\n"
      "ds_read_b128 v[48:51], v6 offset:40960\n"
      "ds_read_b128 v[52:55], v6 offset:45056\n"
      "v_mfma_f32_32x32x16_bf16 v[64:79], v[188:191], v[60:63], v[64:79]\n"
      "ds_read_b64_tr_b16 v[204:205], v24 offset:36864\n"
      "ds_read_b64_tr_b16 v[206:207], v24 offset:37888\n"
      "ds_read_b128 v[56:59], v7 offset:40960\n"
      "ds_read_b128 v[60:63], v7 offset:45056\n"
      "v_mfma_f32_16x16x32_bf16 v[20:23], v[16:19], v[184:187], v[20:23]\n"
      "v_mfma_f32_16x16x32_bf16 v[20:23], v[16:19], v[188:191], v[20:23]\n"
      "s_waitcnt lgkmcnt(13)\n"
      "v_mfma_f32_32x32x16_bf16 v[80:95], v[192:195], v[32:35], v[80:95]\n"
      "s_waitcnt lgkmcnt(12)\n"
      "v_mfma_f32_32x32x16_bf16 v[64:79], v[192:195], v[36:39], v[64:79]\n"
      "v_mfma_f32_16x16x32_bf16 v[20:23], v[16:19], v[192:195], v[20:23]\n"
      "s_waitcnt lgkmcnt(8)\n"
      "v_mfma_f32_16x16x32_bf16 v[20:23], v[16:19], v[196:199], v[20:23]\n"
      "v_mfma_f32_32x32x16_bf16 v[80:95], v[196:199], v[40:43], v[80:95]\n"
      "s_waitcnt lgkmcnt(0)\n"
      "v_add_u32 v24, 49152, v24\n"
      "v_add_u32 v4, 49152, v4\n"
      "v_add_u32 v5, 49152, v5\n"
      "v_add_u32 v6, 49152, v6\n"
      "v_add_u32 v7, 49152, v7\n"
      "v_mfma_f32_32x32x16_bf16 v[64:79], v[196:199], v[44:47], v[64:79]\n"
      "s_barrier\n"
      "v_mfma_f32_32x32x16_bf16 v[80:95], v[200:203], v[48:51], v[80:95]\n"
      "ds_read_b64_tr_b16 v[208:209], v24 offset:0\n"
      "ds_read_b64_tr_b16 v[210:211], v24 offset:1024\n"
      "ds_read_b128 v[32:35], v4 offset:16384\n"
      "ds_read_b128 v[36:39], v4 offset:20480\n"
      "v_mfma_f32_32x32x16_bf16 v[64:79], v[200:203], v[52:55], v[64:79]\n"
      "ds_read_b64_tr_b16 v[212:213], v24 offset:4096\n"
      "ds_read_b64_tr_b16 v[214:215], v24 offset:5120\n"
      "ds_read_b128 v[40:43], v5 offset:16384\n"
      "ds_read_b128 v[44:47], v5 offset:20480\n"
      "v_mfma_f32_32x32x16_bf16 v[80:95], v[204:207], v[56:59], v[80:95]\n"
      "ds_read_b64_tr_b16 v[216:217], v24 offset:8192\n"
      "ds_read_b64_tr_b16 v[218:219], v24 offset:9216\n"
      "ds_read_b128 v[48:51], v6 offset:16384\n"
      "ds_read_b128 v[52:55], v6 offset:20480\n"
      "v_mfma_f32_32x32x16_bf16 v[64:79], v[204:207], v[60:63], v[64:79]\n"
      "ds_read_b64_tr_b16 v[220:221], v24 offset:12288\n"
      "ds_read_b64_tr_b16 v[222:223], v24 offset:13312\n"
      "ds_read_b128 v[56:59], v7 offset:16384\n"
      "ds_read_b128 v[60:63], v7 offset:20480\n"
      "v_mfma_f32_16x16x32_bf16 v[20:23], v[16:19], v[200:203], v[20:23]\n"
      "v_mfma_f32_16x16x32_bf16 v[20:23], v[16:19], v[204:207], v[20:23]\n"
      "s_waitcnt lgkmcnt(13)\n"
      "v_mfma_f32_32x32x16_bf16 v[80:95], v[208:211], v[32:35], v[80:95]\n"
      "s_waitcnt lgkmcnt(12)\n"
      "v_mfma_f32_32x32x16_bf16 v[64:79], v[208:211], v[36:39], v[64:79]\n"
      "v_mfma_f32_16x16x32_bf16 v[20:23], v[16:19], v[208:211], v[20:23]\n"
      "s_waitcnt lgkmcnt(8)\n"
      "v_mfma_f32_16x16x32_bf16 v[20:23], v[16:19], v[212:215], v[20:23]\n"
      "v_mfma_f32_32x32x16_bf16 v[80:95], v[212:215], v[40:43], v[80:95]\n"
      "s_waitcnt lgkmcnt(0)\n"
      "v_mfma_f32_32x32x16_bf16 v[64:79], v[212:215], v[44:47], v[64:79]\n"
      "s_barrier\n"
      "v_mfma_f32_32x32x16_bf16 v[80:95], v[216:219], v[48:51], v[80:95]\n"
      "ds_read_b64_tr_b16 v[224:225], v24 offset:24576\n"
      "ds_read_b64_tr_b16 v[226:227], v24 offset:25600\n"
      "ds_read_b128 v[32:35], v4 offset:40960\n"
      "ds_read_b128 v[36:39], v4 offset:45056\n"
      "v_mfma_f32_32x32x16_bf16 v[64:79], v[216:219], v[52:55], v[64:79]\n"
      "ds_read_b64_tr_b16 v[228:229], v24 offset:28672\n"
      "ds_read_b64_tr_b16 v[230:231], v24 offset:29696\n"
      "ds_read_b128 v[40:43], v5 offset:40960\n"
      "ds_read_b128 v[44:47], v5 offset:45056\n"
      "v_mfma_f32_32x32x16_bf16 v[80:95], v[220:223], v[56:59], v[80:95]\n"
      "ds_read_b64_tr_b16 v[232:233], v24 offset:32768\n"
      "ds_read_b64_tr_b16 v[234:235], v24 offset:33792\n"
      "ds_read_b128 v[48:51], v6 offset:40960\n"
      "ds_read_b128 v[52:55], v6 offset:45056\n"
      "v_mfma_f32_32x32x16_bf16 v[64:79], v[220:223], v[60:63], v[64:79]\n"
      "ds_read_b64_tr_b16 v[236:237], v24 offset:36864\n"
      "ds_read_b64_tr_b16 v[238:239], v24 offset:37888\n"
      "ds_read_b128 v[56:59], v7 offset:40960\n"
      "ds_read_b128 v[60:63], v7 offset:45056\n"
      "v_mfma_f32_16x16x32_bf16 v[20:23], v[16:19], v[216:219], v[20:23]\n"
      "v_mfma_f32_16x16x32_bf16 v[20:23], v[16:19], v[220:223], v[20:23]\n"
      "s_waitcnt lgkmcnt(13)\n"
      "v_mfma_f32_32x32x16_bf16 v[80:95], v[224:227], v[32:35], v[80:95]\n"
      "s_waitcnt lgkmcnt(12)\n"
      "v_mfma_f32_32x32x16_bf16 v[64:79], v[224:227], v[36:39], v[64:79]\n"
      "v_mfma_f32_16x16x32_bf16 v[20:23], v[16:19], v[224:227], v[20:23]\n"
      "s_waitcnt lgkmcnt(8)\n"
      "v_mfma_f32_16x16x32_bf16 v[20:23], v[16:19], v[228:231], v[20:23]\n"
      "v_mfma_f32_32x32x16_bf16 v[80:95], v[228:231], v[40:43], v[80:95]\n"
      "s_waitcnt lgkmcnt(0)\n"
      "v_add_u32 v24, 49152, v24\n"
      "v_add_u32 v4, 49152, v4\n"
      "v_add_u32 v5, 49152, v5\n"
      "v_add_u32 v6, 49152, v6\n"
      "v_add_u32 v7, 49152, v7\n"
      "v_mfma_f32_32x32x16_bf16 v[64:79], v[228:231], v[44:47], v[64:79]\n"
      "s_barrier\n"
      "v_mfma_f32_32x32x16_bf16 v[80:95], v[232:235], v[48:51], v[80:95]\n"
      "ds_read_b64_tr_b16 v[240:241], v24 offset:0\n"
      "ds_read_b64_tr_b16 v[242:243], v24 offset:1024\n"
      "ds_read_b128 v[32:35], v4 offset:16384\n"
      "ds_read_b128 v[36:39], v4 offset:20480\n"
      "v_mfma_f32_32x32x16_bf16 v[64:79], v[232:235], v[52:55], v[64:79]\n"
      "ds_read_b64_tr_b16 v[244:245], v24 offset:4096\n"
      "ds_read_b64_tr_b16 v[246:247], v24 offset:5120\n"
      "ds_read_b128 v[40:43], v5 offset:16384\n"
      "ds_read_b128 v[44:47], v5 offset:20480\n"
      "v_mfma_f32_32x32x16_bf16 v[80:95], v[236:239], v[56:59], v[80:95]\n"
      "ds_read_b64_tr_b16 v[248:249], v24 offset:8192\n"
      "ds_read_b64_tr_b16 v[250:251], v24 offset:9216\n"
      "ds_read_b128 v[48:51], v6 offset:16384\n"
      "ds_read_b128 v[52:55], v6 offset:20480\n"
      "v_mfma_f32_32x32x16_bf16 v[64:79], v[236:239], v[60:63], v[64:79]\n"
      "ds_read_b64_tr_b16 v[252:253], v24 offset:12288\n"
      "ds_read_b64_tr_b16 v[254:255], v24 offset:13312\n"
      "ds_read_b128 v[56:59], v7 offset:16384\n"
      "ds_read_b128 v[60:63], v7 offset:20480\n"
      "v_mfma_f32_16x16x32_bf16 v[20:23], v[16:19], v[232:235], v[20:23]\n"
      "v_mfma_f32_16x16x32_bf16 v[20:23], v[16:19], v[236:239], v[20:23]\n"
      "ds_read_b32 v31, v2 offset:4144\n"
      "ds_read_b64 v[12:13], v30 offset:1040\n"
      "s_waitcnt lgkmcnt(14)\n"
      "v_mfma_f32_16x16x32_bf16 v[20:23], v[16:19], v[240:243], v[20:23]\n"
      "v_subrev_u32 v4, 98304, v4\n"
      "s_waitcnt lgkmcnt(12)\n"
      "v_mfma_f32_16x16x32_bf16 v[20:23], v[16:19], v[244:247], v[20:23]\n"
      "v_subrev_u32 v5, 98304, v5\n"
      "s_waitcnt lgkmcnt(8)\n"
      "v_mfma_f32_16x16x32_bf16 v[20:23], v[16:19], v[248:251], v[20:23]\n"
      "v_subrev_u32 v6, 98304, v6\n"
      "s_waitcnt lgkmcnt(4)\n"
      "v_mfma_f32_16x16x32_bf16 v[20:23], v[16:19], v[252:255], v[20:23]\n"
      "v_subrev_u32 v7, 98304, v7\n"
      "s_waitcnt lgkmcnt(0)\n"
      "v_mfma_f32_32x32x16_bf16 v[80:95], v[240:243], v[32:35], v[80:95]\n"
      "ds_read_b64 v[16:17], v30 offset:16\n"
      "v_readlane_b32 s64, v31, 0\n"
      "s_mov_b32 vcc_hi, 0\n"
      "s_mov_b32 vcc_lo, s64\n"
      "s_nop 0\n"
      "v_mfma_f32_32x32x16_bf16 v[80:95], v[244:247], v[40:43], v[80:95]\n"
      "v_cndmask_b32 v24, v20, v21, s[80:81]\n"
      "v_cvt_pk_bf16_f32 v25, 0, v24\n"
      "v_sub_f32 v24, v24, v25\n"
      "v_cvt_pk_bf16_f32 v26, 0, v24\n"
      "v_sub_f32 v24, v24, v26\n"
      "v_mfma_f32_32x32x16_bf16 v[80:95], v[248:251], v[48:51], v[80:95]\n"
      "v_cvt_pk_bf16_f32 v25, v25, v26\n"
      "v_cvt_pk_bf16_f32 v26, v24, v9\n"
      "v_cndmask_b32 v8, 0, v25, s[82:83]\n"
      "v_cndmask_b32 v9, 0, v26, s[82:83]\n"
      "ds_read_b128 v[24:27], v30\n"
      "v_mfma_f32_32x32x16_bf16 v[80:95], v[252:255], v[56:59], v[80:95]\n"
      "s_waitcnt lgkmcnt(1)\n"
      "v_mfma_f32_32x32x16_bf16 v[80:95], v[8:11], v[16:19], v[80:95]\n"
      "v_mfma_f32_32x32x16_bf16 v[64:79], v[240:243], v[36:39], v[64:79]\n"
      "v_mfma_f32_32x32x16_bf16 v[64:79], v[244:247], v[44:47], v[64:79]\n"
      "v_mfma_f32_32x32x16_bf16 v[64:79], v[248:251], v[52:55], v[64:79]\n"
      "v_mfma_f32_32x32x16_bf16 v[64:79], v[252:255], v[60:63], v[64:79]\n"
      "v_mfma_f32_32x32x16_bf16 v[64:79], v[8:11], v[12:15], v[64:79]\n"
      "ds_read_b64 v[12:13], v30 offset:2064\n"
      "s_barrier\n"
      "s_mov_b64 s[84:85], 0\n"
      "s_mov_b32 s81, 2\n"
      "s_waitcnt lgkmcnt(1)\n"
      "s_cbranch_vccnz .Lg_121\n"
      ".Lg_122:\n"
      "v_mov_b32 v48, v24\n"
      "v_mov_b32 v49, v24\n"
      "v_mov_b32 v50, v25\n"
      "v_mov_b32 v51, v25\n"
      "v_mov_b32 v52, v26\n"
      "v_mov_b32 v53, v26\n"
      "ds_read_b128 v[24:27], v30 offset:1024\n"
      "ds_read_b128 v[32:35], v4\n"
      "ds_read_b128 v[36:39], v5\n"
      "ds_read_b128 v[40:43], v6\n"
      "ds_read_b128 v[44:47], v7\n"
      "v_pk_mul_f32 v[54:55], v[80:81], v[80:81]\n"
      "v_pk_mul_f32 v[56:57], v[82:83], v[82:83]\n"
      "v_pk_mul_f32 v[58:59], v[84:85], v[84:85]\n"
      "v_pk_mul_f32 v[60:61], v[86:87], v[86:87]\n"
      "v_pk_fma_f32 v[54:55], v[54:55], v[50:51], v[48:49]\n"
      "v_pk_fma_f32 v[56:57], v[56:57], v[50:51], v[48:49]\n"
      "v_pk_fma_f32 v[58:59], v[58:59], v[50:51], v[48:49]\n"
      "v_pk_fma_f32 v[60:61], v[60:61], v[50:51], v[48:49]\n"
      "v_pk_mul_f32 v[54:55], v[80:81], v[54:55]\n"
      "v_pk_mul_f32 v[56:57], v[82:83], v[56:57]\n"
      "v_pk_mul_f32 v[58:59], v[84:85], v[58:59]\n"
      "v_pk_mul_f32 v[60:61], v[86:87], v[60:61]\n"
      "v_exp_f32 v54, v54\n"
      "v_exp_f32 v55, v55\n"
      "v_exp_f32 v56, v56\n"
      "v_exp_f32 v57, v57\n"
      "v_exp_f32 v58, v58\n"
      "v_exp_f32 v59, v59\n"
      "v_exp_f32 v60, v60\n"
      "v_exp_f32 v61, v61\n"
      "v_pk_fma_f32 v[54:55], v[54:55], v[52:53], v[52:53]\n"
      "v_pk_fma_f32 v[56:57], v[56:57], v[52:53], v[52:53]\n"
      "v_pk_fma_f32 v[58:59], v[58:59], v[52:53], v[52:53]\n"
      "v_pk_fma_f32 v[60:61], v[60:61], v[52:53], v[52:53]\n"
      "v_rcp_f32 v54, v54\n"
      "v_rcp_f32 v55, v55\n"
      "v_rcp_f32 v56, v56\n"
      "v_rcp_f32 v57, v57\n"
      "v_rcp_f32 v58, v58\n"
      "v_rcp_f32 v59, v59\n"
      "v_rcp_f32 v60, v60\n"
      "v_rcp_f32 v61, v61\n"
      "v_pk_mul_f32 v[54:55], v[80:81], v[54:55]\n"
      "v_pk_mul_f32 v[56:57], v[82:83], v[56:57]\n"
      "v_pk_mul_f32 v[58:59], v[84:85], v[58:59]\n"
      "v_pk_mul_f32 v[60:61], v[86:87], v[60:61]\n"
      "v_cvt_pk_bf16_f32 v16, v54, v55\n"
      "v_cvt_pk_bf16_f32 v17, v56, v57\n"
      "v_cvt_pk_bf16_f32 v18, v58, v59\n"
      "v_cvt_pk_bf16_f32 v19, v60, v61\n"
      "buffer_store_dwordx4 v[16:19], v29, s[20:23], s74 offen offset:0 sc1\n"
      "v_pk_mul_f32 v[54:55], v[88:89], v[88:89]\n"
      "v_pk_mul_f32 v[56:57], v[90:91], v[90:91]\n"
      "v_pk_mul_f32 v[58:59], v[92:93], v[92:93]\n"
      "v_pk_mul_f32 v[60:61], v[94:95], v[94:95]\n"
      "v_pk_fma_f32 v[54:55], v[54:55], v[50:51], v[48:49]\n"
      "v_pk_fma_f32 v[56:57], v[56:57], v[50:51], v[48:49]\n"
      "v_pk_fma_f32 v[58:59], v[58:59], v[50:51], v[48:49]\n"
      "v_pk_fma_f32 v[60:61], v[60:61], v[50:51], v[48:49]\n"
      "v_pk_mul_f32 v[54:55], v[88:89], v[54:55]\n"
      "v_pk_mul_f32 v[56:57], v[90:91], v[56:57]\n"
      "v_pk_mul_f32 v[58:59], v[92:93], v[58:59]\n"
      "v_pk_mul_f32 v[60:61], v[94:95], v[60:61]\n"
      "v_exp_f32 v54, v54\n"
      "v_exp_f32 v55, v55\n"
      "v_exp_f32 v56, v56\n"
      "v_exp_f32 v57, v57\n"
      "v_exp_f32 v58, v58\n"
      "v_exp_f32 v59, v59\n"
      "v_exp_f32 v60, v60\n"
      "v_exp_f32 v61, v61\n"
      "v_pk_fma_f32 v[54:55], v[54:55], v[52:53], v[52:53]\n"
      "v_pk_fma_f32 v[56:57], v[56:57], v[52:53], v[52:53]\n"
      "v_pk_fma_f32 v[58:59], v[58:59], v[52:53], v[52:53]\n"
      "v_pk_fma_f32 v[60:61], v[60:61], v[52:53], v[52:53]\n"
      "v_rcp_f32 v54, v54\n"
      "v_rcp_f32 v55, v55\n"
      "v_rcp_f32 v56, v56\n"
      "v_rcp_f32 v57, v57\n"
      "v_rcp_f32 v58, v58\n"
      "v_rcp_f32 v59, v59\n"
      "v_rcp_f32 v60, v60\n"
      "v_rcp_f32 v61, v61\n"
      "v_pk_mul_f32 v[54:55], v[88:89], v[54:55]\n"
      "v_pk_mul_f32 v[56:57], v[90:91], v[56:57]\n"
      "v_pk_mul_f32 v[58:59], v[92:93], v[58:59]\n"
      "v_pk_mul_f32 v[60:61], v[94:95], v[60:61]\n"
      "v_cvt_pk_bf16_f32 v20, v54, v55\n"
      "v_cvt_pk_bf16_f32 v21, v56, v57\n"
      "v_cvt_pk_bf16_f32 v22, v58, v59\n"
      "v_cvt_pk_bf16_f32 v23, v60, v61\n"
      "buffer_store_dwordx4 v[20:23], v29, s[20:23], s74 offen offset:256 sc1\n"
      "ds_read_b128 v[48:51], v4 offset:4096\n"
      "ds_read_b128 v[52:55], v5 offset:4096\n"
      "ds_read_b128 v[56:59], v6 offset:4096\n"
      "ds_read_b128 v[60:63], v7 offset:4096\n"
      "s_waitcnt lgkmcnt(8)\n"
      "v_mfma_f32_32x32x16_bf16 v[80:95], v[8:11], v[12:15], 0\n"
      "s_cbranch_vccnz .Lg_123\n"
      ".Lg_124:\n"
      "ds_read_b64 v[12:13], v30 offset:3088\n"
      "s_nop 0\n"
      "s_waitcnt lgkmcnt(5)\n"
      "v_mfma_f32_32x32x16_bf16 v[80:95], v[96:99], v[32:35], v[80:95]\n"
      "ds_read_b128 v[32:35], v4 offset:8192\n"
      "v_mul_f32 v20, v64, v64\n"
      "v_fma_f32 v20, v20, v25, v24\n"
      "v_mul_f32 v20, v64, v20\n"
      "v_exp_f32 v20, v20\n"
      "v_mfma_f32_32x32x16_bf16 v[80:95], v[100:103], v[36:39], v[80:95]\n"
      "ds_read_b128 v[36:39], v5 offset:8192\n"
      "v_mul_f32 v21, v65, v65\n"
      "v_fma_f32 v20, v20, v26, v26\n"
      "v_fma_f32 v21, v21, v25, v24\n"
      "v_mfma_f32_32x32x16_bf16 v[80:95], v[104:107], v[40:43], v[80:95]\n"
      "ds_read_b128 v[40:43], v6 offset:8192\n"
      "v_rcp_f32 v20, v20\n"
      "v_mul_f32 v21, v65, v21\n"
      "v_mul_f32 v20, v64, v20\n"
      "v_exp_f32 v21, v21\n"
      "v_mfma_f32_32x32x16_bf16 v[80:95], v[108:111], v[44:47], v[80:95]\n"
      "ds_read_b128 v[44:47], v7 offset:8192\n"
      "v_mul_f32 v22, v66, v66\n"
      "v_fma_f32 v21, v21, v26, v26\n"
      "v_fma_f32 v22, v22, v25, v24\n"
      "s_waitcnt lgkmcnt(4)\n"
      "v_mfma_f32_32x32x16_bf16 v[80:95], v[112:115], v[48:51], v[80:95]\n"
      "ds_read_b128 v[48:51], v4 offset:12288\n"
      "v_rcp_f32 v21, v21\n"
      "v_mul_f32 v22, v66, v22\n"
      "v_mul_f32 v21, v65, v21\n"
      "v_cvt_pk_bf16_f32 v16, v20, v21\n"
      "v_mfma_f32_32x32x16_bf16 v[80:95], v[116:119], v[52:55], v[80:95]\n"
      "ds_read_b128 v[52:55], v5 offset:12288\n"
      "v_exp_f32 v22, v22\n"
      "v_mul_f32 v23, v67, v67\n"
      "v_fma_f32 v22, v22, v26, v26\n"
      "v_mfma_f32_32x32x16_bf16 v[80:95], v[120:123], v[56:59], v[80:95]\n"
      "ds_read_b128 v[56:59], v6 offset:12288\n"
      "v_fma_f32 v23, v23, v25, v24\n"
      "v_rcp_f32 v22, v22\n"
      "v_mul_f32 v23, v67, v23\n"
      "v_mul_f32 v22, v66, v22\n"
      "v_mfma_f32_32x32x16_bf16 v[80:95], v[124:127], v[60:63], v[80:95]\n"
      "ds_read_b128 v[60:63], v7 offset:12288\n"
      "v_exp_f32 v23, v23\n"
      "v_mul_f32 v20, v68, v68\n"
      "s_waitcnt lgkmcnt(4)\n"
      "v_mfma_f32_32x32x16_bf16 v[80:95], v[128:131], v[32:35], v[80:95]\n"
      "ds_read_b128 v[32:35], v4 offset:16384\n"
      "v_fma_f32 v23, v23, v26, v26\n"
      "v_fma_f32 v20, v20, v25, v24\n"
      "v_rcp_f32 v23, v23\n"
      "ds_read_b32 v31, v2 offset:2072\n"
      "v_mfma_f32_32x32x16_bf16 v[80:95], v[132:135], v[36:39], v[80:95]\n"
      "ds_read_b128 v[36:39], v5 offset:16384\n"
      "v_mul_f32 v20, v68, v20\n"
      "v_mul_f32 v23, v67, v23\n"
      "v_cvt_pk_bf16_f32 v17, v22, v23\n"
      "v_exp_f32 v20, v20\n"
      "v_mfma_f32_32x32x16_bf16 v[80:95], v[136:139], v[40:43], v[80:95]\n"
      "ds_read_b128 v[40:43], v6 offset:16384\n"
      "v_mul_f32 v21, v69, v69\n"
      "v_fma_f32 v20, v20, v26, v26\n"
      "v_fma_f32 v21, v21, v25, v24\n"
      "v_mfma_f32_32x32x16_bf16 v[80:95], v[140:143], v[44:47], v[80:95]\n"
      "ds_read_b128 v[44:47], v7 offset:16384\n"
      "v_rcp_f32 v20, v20\n"
      "v_mul_f32 v21, v69, v21\n"
      "v_mul_f32 v20, v68, v20\n"
      "s_waitcnt lgkmcnt(3)\n"
      "v_mfma_f32_32x32x16_bf16 v[80:95], v[144:147], v[48:51], v[80:95]\n"
      "v_readfirstlane_b32 s2, v31\n"
      "s_cmp_lt_u32 s2, 4\n"
      "s_cbranch_scc1 .Lw_125\n"
      ".Lw_126:\n"
      "ds_read_b128 v[48:51], v4 offset:20480\n"
      "v_exp_f32 v21, v21\n"
      "v_mfma_f32_32x32x16_bf16 v[80:95], v[148:151], v[52:55], v[80:95]\n"
      "ds_read_b128 v[52:55], v5 offset:20480\n"
      "v_mul_f32 v22, v70, v70\n"
      "v_fma_f32 v21, v21, v26, v26\n"
      "v_fma_f32 v22, v22, v25, v24\n"
      "v_mfma_f32_32x32x16_bf16 v[80:95], v[152:155], v[56:59], v[80:95]\n"
      "ds_read_b128 v[56:59], v6 offset:20480\n"
      "v_rcp_f32 v21, v21\n"
      "v_mul_f32 v22, v70, v22\n"
      "v_mul_f32 v21, v69, v21\n"
      "v_cvt_pk_bf16_f32 v18, v20, v21\n"
      "v_mfma_f32_32x32x16_bf16 v[80:95], v[156:159], v[60:63], v[80:95]\n"
      "ds_read_b128 v[60:63], v7 offset:20480\n"
      "v_exp_f32 v22, v22\n"
      "v_mul_f32 v23, v71, v71\n"
      "v_fma_f32 v22, v22, v26, v26\n"
      "s_waitcnt lgkmcnt(4)\n"
      "v_mfma_f32_32x32x16_bf16 v[80:95], v[160:163], v[32:35], v[80:95]\n"
      "ds_read_b128 v[32:35], v4 offset:24576\n"
      "v_fma_f32 v23, v23, v25, v24\n"
      "v_rcp_f32 v22, v22\n"
      "v_mul_f32 v23, v71, v23\n"
      "v_mfma_f32_32x32x16_bf16 v[80:95], v[164:167], v[36:39], v[80:95]\n"
      "ds_read_b128 v[36:39], v5 offset:24576\n"
      "v_mul_f32 v22, v70, v22\n"
      "v_exp_f32 v23, v23\n"
      "v_mul_f32 v20, v72, v72\n"
      "v_fma_f32 v23, v23, v26, v26\n"
      "v_mfma_f32_32x32x16_bf16 v[80:95], v[168:171], v[40:43], v[80:95]\n"
      "ds_read_b128 v[40:43], v6 offset:24576\n"
      "v_fma_f32 v20, v20, v25, v24\n"
      "v_rcp_f32 v23, v23\n"
      "v_mul_f32 v20, v72, v20\n"
      "v_mfma_f32_32x32x16_bf16 v[80:95], v[172:175], v[44:47], v[80:95]\n"
      "ds_read_b128 v[44:47], v7 offset:24576\n"
      "v_mul_f32 v23, v71, v23\n"
      "v_cvt_pk_bf16_f32 v19, v22, v23\n"
      "buffer_store_dwordx4 v[16:19], v29, s[20:23], s75 offen offset:0 sc1\n"
      "s_waitcnt lgkmcnt(4)\n"
      "v_mfma_f32_32x32x16_bf16 v[80:95], v[176:179], v[48:51], v[80:95]\n"
      "ds_read_b128 v[48:51], v4 offset:28672\n"
      "v_exp_f32 v20, v20\n"
      "v_mul_f32 v21, v73, v73\n"
      "v_mfma_f32_32x32x16_bf16 v[80:95], v[180:183], v[52:55], v[80:95]\n"
      "ds_read_b128 v[52:55], v5 offset:28672\n"
      "v_fma_f32 v20, v20, v26, v26\n"
      "v_fma_f32 v21, v21, v25, v24\n"
      "v_rcp_f32 v20, v20\n"
      "v_mul_f32 v21, v73, v21\n"
      "v_mfma_f32_32x32x16_bf16 v[80:95], v[184:187], v[56:59], v[80:95]\n"
      "ds_read_b128 v[56:59], v6 offset:28672\n"
      "v_mul_f32 v20, v72, v20\n"
      "v_exp_f32 v21, v21\n"
      "v_mul_f32 v22, v74, v74\n"
      "v_mfma_f32_32x32x16_bf16 v[80:95], v[188:191], v[60:63], v[80:95]\n"
      "ds_read_b128 v[60:63], v7 offset:28672\n"
      "v_fma_f32 v21, v21, v26, v26\n"
      "v_fma_f32 v22, v22, v25, v24\n"
      "v_rcp_f32 v21, v21\n"
      "s_waitcnt lgkmcnt(4)\n"
      "v_mfma_f32_32x32x16_bf16 v[80:95], v[192:195], v[32:35], v[80:95]\n"
      "ds_read_b128 v[32:35], v4 offset:32768\n"
      "v_mul_f32 v22, v74, v22\n"
      "v_mul_f32 v21, v73, v21\n"
      "v_cvt_pk_bf16_f32 v16, v20, v21\n"
      "v_exp_f32 v22, v22\n"
      "v_mfma_f32_32x32x16_bf16 v[80:95], v[196:199], v[36:39], v[80:95]\n"
      "ds_read_b128 v[36:39], v5 offset:32768\n"
      "v_mul_f32 v23, v75, v75\n"
      "v_fma_f32 v22, v22, v26, v26\n"
      "v_fma_f32 v23, v23, v25, v24\n"
      "v_mfma_f32_32x32x16_bf16 v[80:95], v[200:203], v[40:43], v[80:95]\n"
      "ds_read_b128 v[40:43], v6 offset:32768\n"
      "v_rcp_f32 v22, v22\n"
      "v_mul_f32 v23, v75, v23\n"
      "v_mul_f32 v22, v74, v22\n"
      "v_exp_f32 v23, v23\n"
      "v_mfma_f32_32x32x16_bf16 v[80:95], v[204:207], v[44:47], v[80:95]\n"
      "ds_read_b128 v[44:47], v7 offset:32768\n"
      "v_mul_f32 v20, v76, v76\n"
      "v_fma_f32 v23, v23, v26, v26\n"
      "v_fma_f32 v20, v20, v25, v24\n"
      "s_waitcnt lgkmcnt(4)\n"
      "v_mfma_f32_32x32x16_bf16 v[80:95], v[208:211], v[48:51], v[80:95]\n"
      "ds_read_b128 v[48:51], v4 offset:36864\n"
      "v_rcp_f32 v23, v23\n"
      "v_mul_f32 v20, v76, v20\n"
      "ds_read_b32 v31, v2 offset:3096\n"
      "v_mfma_f32_32x32x16_bf16 v[80:95], v[212:215], v[52:55], v[80:95]\n"
      "ds_read_b128 v[52:55], v5 offset:36864\n"
      "v_mul_f32 v23, v75, v23\n"
      "v_cvt_pk_bf16_f32 v17, v22, v23\n"
      "v_exp_f32 v20, v20\n"
      "v_mul_f32 v21, v77, v77\n"
      "v_mfma_f32_32x32x16_bf16 v[80:95], v[216:219], v[56:59], v[80:95]\n"
      "ds_read_b128 v[56:59], v6 offset:36864\n"
      "v_fma_f32 v20, v20, v26, v26\n"
      "v_fma_f32 v21, v21, v25, v24\n"
      "v_rcp_f32 v20, v20\n"
      "v_mfma_f32_32x32x16_bf16 v[80:95], v[220:223], v[60:63], v[80:95]\n"
      "ds_read_b128 v[60:63], v7 offset:36864\n"
      "v_mul_f32 v21, v77, v21\n"
      "v_mul_f32 v20, v76, v20\n"
      "v_exp_f32 v21, v21\n"
      "s_waitcnt lgkmcnt(3)\n"
      "v_mfma_f32_32x32x16_bf16 v[80:95], v[224:227], v[32:35], v[80:95]\n"
      "v_readfirstlane_b32 s2, v31\n"
      "s_cmp_lt_u32 s2, 4\n"
      "s_cbranch_scc1 .Lw_128\n"
      ".Lw_129:\n"
      "ds_read_b128 v[32:35], v4 offset:49152\n"
      "v_mul_f32 v22, v78, v78\n"
      "v_mfma_f32_32x32x16_bf16 v[80:95], v[228:231], v[36:39], v[80:95]\n"
      "ds_read_b128 v[36:39], v5 offset:49152\n"
      "v_fma_f32 v21, v21, v26, v26\n"
      "v_fma_f32 v22, v22, v25, v24\n"
      "v_rcp_f32 v21, v21\n"
      "v_mfma_f32_32x32x16_bf16 v[80:95], v[232:235], v[40:43], v[80:95]\n"
      "ds_read_b128 v[40:43], v6 offset:49152\n"
      "v_mul_f32 v22, v78, v22\n"
      "v_mul_f32 v21, v77, v21\n"
      "v_cvt_pk_bf16_f32 v18, v20, v21\n"
      "v_exp_f32 v22, v22\n"
      "v_mfma_f32_32x32x16_bf16 v[80:95], v[236:239], v[44:47], v[80:95]\n"
      "ds_read_b128 v[44:47], v7 offset:49152\n"
      "v_mul_f32 v23, v79, v79\n"
      "v_fma_f32 v22, v22, v26, v26\n"
      "v_fma_f32 v23, v23, v25, v24\n"
      "s_waitcnt lgkmcnt(4)\n"
      "v_mfma_f32_32x32x16_bf16 v[80:95], v[240:243], v[48:51], v[80:95]\n"
      "ds_read_b128 v[48:51], v4 offset:53248\n"
      "v_rcp_f32 v22, v22\n"
      "v_mul_f32 v23, v79, v23\n"
      "v_mul_f32 v22, v78, v22\n"
      "v_exp_f32 v23, v23\n"
      "v_mfma_f32_32x32x16_bf16 v[80:95], v[244:247], v[52:55], v[80:95]\n"
      "ds_read_b128 v[52:55], v5 offset:53248\n"
      "v_fma_f32 v23, v23, v26, v26\n"
      "v_rcp_f32 v23, v23\n"
      "s_nop 0\n"
      "v_mul_f32 v23, v79, v23\n"
      "v_mfma_f32_32x32x16_bf16 v[80:95], v[248:251], v[56:59], v[80:95]\n"
      "ds_read_b128 v[56:59], v6 offset:53248\n"
      "v_cvt_pk_bf16_f32 v19, v22, v23\n"
      "buffer_store_dwordx4 v[16:19], v29, s[20:23], s75 offen offset:256 sc1\n"
      "ds_read_b128 v[24:27], v30 offset:2048\n"
      "v_mfma_f32_32x32x16_bf16 v[64:79], v[252:255], v[60:63], v[80:95]\n"
      "ds_read_b128 v[60:63], v7 offset:53248\n"
      "s_nop 3\n"
      "v_mfma_f32_32x32x16_bf16 v[80:95], v[8:11], v[12:15], 0\n"
      "s_cbranch_vccnz .Lg_131\n"
      ".Lg_132:\n"
      "s_nop 1\n"
      "s_waitcnt lgkmcnt(1)\n"
      "v_mfma_f32_32x32x16_bf16 v[80:95], v[96:99], v[32:35], v[80:95]\n"
      "ds_read_b128 v[32:35], v4 offset:57344\n"
      "v_mul_f32 v20, v64, v64\n"
      "v_fma_f32 v20, v20, v25, v24\n"
      "v_mul_f32 v20, v64, v20\n"
      "v_exp_f32 v20, v20\n"
      "v_mfma_f32_32x32x16_bf16 v[80:95], v[100:103], v[36:39], v[80:95]\n"
      "ds_read_b128 v[36:39], v5 offset:57344\n"
      "v_mul_f32 v21, v65, v65\n"
      "v_fma_f32 v20, v20, v26, v26\n"
      "v_fma_f32 v21, v21, v25, v24\n"
      "v_mov_b64 v[12:13], s[58:59]\n"
      "v_mfma_f32_32x32x16_bf16 v[80:95], v[104:107], v[40:43], v[80:95]\n"
      "ds_read_b128 v[40:43], v6 offset:57344\n"
      "v_rcp_f32 v20, v20\n"
      "v_mul_f32 v21, v65, v21\n"
      "v_mul_f32 v20, v64, v20\n"
      "v_exp_f32 v21, v21\n"
      "v_mfma_f32_32x32x16_bf16 v[80:95], v[108:111], v[44:47], v[80:95]\n"
      "ds_read_b128 v[44:47], v7 offset:57344\n"
      "v_mul_f32 v22, v66, v66\n"
      "v_fma_f32 v21, v21, v26, v26\n"
      "v_fma_f32 v22, v22, v25, v24\n"
      "s_waitcnt lgkmcnt(4)\n"
      "v_mfma_f32_32x32x16_bf16 v[80:95], v[112:115], v[48:51], v[80:95]\n"
      "ds_read_b128 v[48:51], v4 offset:61440\n"
      "v_rcp_f32 v21, v21\n"
      "v_mul_f32 v22, v66, v22\n"
      "v_mul_f32 v21, v65, v21\n"
      "v_cvt_pk_bf16_f32 v16, v20, v21\n"
      "v_mfma_f32_32x32x16_bf16 v[80:95], v[116:119], v[52:55], v[80:95]\n"
      "ds_read_b128 v[52:55], v5 offset:61440\n"
      "v_exp_f32 v22, v22\n"
      "v_mul_f32 v23, v67, v67\n"
      "v_fma_f32 v22, v22, v26, v26\n"
      "v_add_u32 v4, 49152, v4\n"
      "v_mfma_f32_32x32x16_bf16 v[80:95], v[120:123], v[56:59], v[80:95]\n"
      "ds_read_b128 v[56:59], v6 offset:61440\n"
      "v_fma_f32 v23, v23, v25, v24\n"
      "v_rcp_f32 v22, v22\n"
      "v_mul_f32 v23, v67, v23\n"
      "v_add_u32 v5, 49152, v5\n"
      "v_mfma_f32_32x32x16_bf16 v[80:95], v[124:127], v[60:63], v[80:95]\n"
      "ds_read_b128 v[60:63], v7 offset:61440\n"
      "v_mul_f32 v22, v66, v22\n"
      "v_exp_f32 v23, v23\n"
      "v_add_u32 v6, 49152, v6\n"
      "s_waitcnt lgkmcnt(4)\n"
      "v_mfma_f32_32x32x16_bf16 v[80:95], v[128:131], v[32:35], v[80:95]\n"
      "ds_read_b128 v[32:35], v4 offset:16384\n"
      "v_mul_f32 v20, v68, v68\n"
      "v_fma_f32 v23, v23, v26, v26\n"
      "v_fma_f32 v20, v20, v25, v24\n"
      "v_add_u32 v7, 49152, v7\n"
      "v_mfma_f32_32x32x16_bf16 v[80:95], v[132:135], v[36:39], v[80:95]\n"
      "ds_read_b128 v[36:39], v5 offset:16384\n"
      "v_rcp_f32 v23, v23\n"
      "v_mul_f32 v20, v68, v20\n"
      "v_mul_f32 v23, v67, v23\n"
      "v_cvt_pk_bf16_f32 v17, v22, v23\n"
      "v_mfma_f32_32x32x16_bf16 v[80:95], v[136:139], v[40:43], v[80:95]\n"
      "ds_read_b128 v[40:43], v6 offset:16384\n"
      "v_exp_f32 v20, v20\n"
      "v_mul_f32 v21, v69, v69\n"
      "v_fma_f32 v20, v20, v26, v26\n"
      "v_fma_f32 v21, v21, v25, v24\n"
      "v_mfma_f32_32x32x16_bf16 v[80:95], v[140:143], v[44:47], v[80:95]\n"
      "ds_read_b128 v[44:47], v7 offset:16384\n"
      "v_rcp_f32 v20, v20\n"
      "v_mul_f32 v21, v69, v21\n"
      "v_mul_f32 v20, v68, v20\n"
      "s_waitcnt lgkmcnt(4)\n"
      "v_mfma_f32_32x32x16_bf16 v[80:95], v[144:147], v[48:51], v[80:95]\n"
      "ds_read_b128 v[48:51], v4 offset:20480\n"
      "v_exp_f32 v21, v21\n"
      "v_mul_f32 v22, v70, v70\n"
      "v_fma_f32 v21, v21, v26, v26\n"
      "v_fma_f32 v22, v22, v25, v24\n"
      "v_mfma_f32_32x32x16_bf16 v[80:95], v[148:151], v[52:55], v[80:95]\n"
      "ds_read_b128 v[52:55], v5 offset:20480\n"
      "s_getreg_b32 s2, hwreg(HW_REG_IB_STS, 0, 4)\n"
      "s_add_u32 s2, s2, s84\n"
      "s_cmp_le_u32 s2, 0\n"
      "s_cbranch_scc1 .Lh_133\n"
      ".Lh_134:\n"
      "v_mfma_f32_32x32x16_bf16 v[80:95], v[152:155], v[56:59], v[80:95]\n"
      "ds_read_b128 v[56:59], v6 offset:20480\n"
      "v_rcp_f32 v21, v21\n"
      "v_mul_f32 v22, v70, v22\n"
      "v_mul_f32 v21, v69, v21\n"
      "v_cvt_pk_bf16_f32 v18, v20, v21\n"
      "v_mfma_f32_32x32x16_bf16 v[80:95], v[156:159], v[60:63], v[80:95]\n"
      "ds_read_b128 v[60:63], v7 offset:20480\n"
      "v_exp_f32 v22, v22\n"
      "v_mul_f32 v23, v71, v71\n"
      "v_fma_f32 v22, v22, v26, v26\n"
      "s_waitcnt lgkmcnt(4)\n"
      "v_mfma_f32_32x32x16_bf16 v[80:95], v[160:163], v[32:35], v[80:95]\n"
      "ds_read_b128 v[32:35], v4 offset:24576\n"
      "v_fma_f32 v23, v23, v25, v24\n"
      "v_rcp_f32 v22, v22\n"
      "v_mul_f32 v23, v71, v23\n"
      "v_mul_f32 v22, v70, v22\n"
      "v_mfma_f32_32x32x16_bf16 v[80:95], v[164:167], v[36:39], v[80:95]\n"
      "ds_read_b128 v[36:39], v5 offset:24576\n"
      "v_exp_f32 v23, v23\n"
      "v_mul_f32 v20, v72, v72\n"
      "v_fma_f32 v23, v23, v26, v26\n"
      "v_fma_f32 v20, v20, v25, v24\n"
      "v_mfma_f32_32x32x16_bf16 v[80:95], v[168:171], v[40:43], v[80:95]\n"
      "ds_read_b128 v[40:43], v6 offset:24576\n"
      "v_rcp_f32 v23, v23\n"
      "v_mul_f32 v20, v72, v20\n"
      "v_mul_f32 v23, v71, v23\n"
      "v_cvt_pk_bf16_f32 v19, v22, v23\n"
      "v_mfma_f32_32x32x16_bf16 v[80:95], v[172:175], v[44:47], v[80:95]\n"
      "ds_read_b128 v[44:47], v7 offset:24576\n"
      "buffer_store_dwordx4 v[16:19], v29, s[20:23], s76 offen offset:0 sc1\n"
      "s_waitcnt lgkmcnt(4)\n"
      "v_mfma_f32_32x32x16_bf16 v[80:95], v[176:179], v[48:51], v[80:95]\n"
      "ds_read_b128 v[48:51], v4 offset:28672\n"
      "v_exp_f32 v20, v20\n"
      "v_mul_f32 v21, v73, v73\n"
      "v_fma_f32 v20, v20, v26, v26\n"
      "v_fma_f32 v21, v21, v25, v24\n"
      "v_mfma_f32_32x32x16_bf16 v[80:95], v[180:183], v[52:55], v[80:95]\n"
      "ds_read_b128 v[52:55], v5 offset:28672\n"
      "s_getreg_b32 s2, hwreg(HW_REG_IB_STS, 0, 4)\n"
      "s_add_u32 s2, s2, s84\n"
      "s_cmp_le_u32 s2, 1\n"
      "s_cbranch_scc1 .Lh_135\n"
      ".Lh_136:\n"
      "v_mfma_f32_32x32x16_bf16 v[80:95], v[184:187], v[56:59], v[80:95]\n"
      "ds_read_b128 v[56:59], v6 offset:28672\n"
      "v_rcp_f32 v20, v20\n"
      "v_mul_f32 v21, v73, v21\n"
      "v_mul_f32 v20, v72, v20\n"
      "v_exp_f32 v21, v21\n"
      "v_mfma_f32_32x32x16_bf16 v[80:95], v[188:191], v[60:63], v[80:95]\n"
      "ds_read_b128 v[60:63], v7 offset:28672\n"
      "v_mul_f32 v22, v74, v74\n"
      "v_fma_f32 v21, v21, v26, v26\n"
      "v_fma_f32 v22, v22, v25, v24\n"
      "s_waitcnt lgkmcnt(4)\n"
      "v_mfma_f32_32x32x16_bf16 v[80:95], v[192:195], v[32:35], v[80:95]\n"
      "ds_read_b128 v[32:35], v4 offset:32768\n"
      "v_rcp_f32 v21, v21\n"
      "v_mul_f32 v22, v74, v22\n"
      "v_mul_f32 v21, v73, v21\n"
      "v_cvt_pk_bf16_f32 v16, v20, v21\n"
      "v_mfma_f32_32x32x16_bf16 v[80:95], v[196:199], v[36:39], v[80:95]\n"
      "ds_read_b128 v[36:39], v5 offset:32768\n"
      "v_exp_f32 v22, v22\n"
      "v_mul_f32 v23, v75, v75\n"
      "v_fma_f32 v22, v22, v26, v26\n"
      "v_fma_f32 v23, v23, v25, v24\n"
      "v_mfma_f32_32x32x16_bf16 v[80:95], v[200:203], v[40:43], v[80:95]\n"
      "ds_read_b128 v[40:43], v6 offset:32768\n"
      "v_rcp_f32 v22, v22\n"
      "v_mul_f32 v23, v75, v23\n"
      "v_mul_f32 v22, v74, v22\n"
      "v_mfma_f32_32x32x16_bf16 v[80:95], v[204:207], v[44:47], v[80:95]\n"
      "ds_read_b128 v[44:47], v7 offset:32768\n"
      "v_exp_f32 v23, v23\n"
      "v_mul_f32 v20, v76, v76\n"
      "v_fma_f32 v23, v23, v26, v26\n"
      "s_waitcnt lgkmcnt(4)\n"
      "v_mfma_f32_32x32x16_bf16 v[80:95], v[208:211], v[48:51], v[80:95]\n"
      "ds_read_b128 v[48:51], v4 offset:36864\n"
      "v_fma_f32 v20, v20, v25, v24\n"
      "v_rcp_f32 v23, v23\n"
      "v_mul_f32 v20, v76, v20\n"
      "v_mul_f32 v23, v75, v23\n"
      "v_mfma_f32_32x32x16_bf16 v[80:95], v[212:215], v[52:55], v[80:95]\n"
      "ds_read_b128 v[52:55], v5 offset:36864\n"
      "s_getreg_b32 s2, hwreg(HW_REG_IB_STS, 0, 4)\n"
      "s_add_u32 s2, s2, s84\n"
      "s_cmp_le_u32 s2, 1\n"
      "s_cbranch_scc1 .Lh_137\n"
      ".Lh_138:\n"
      "v_mfma_f32_32x32x16_bf16 v[80:95], v[216:219], v[56:59], v[80:95]\n"
      "ds_read_b128 v[56:59], v6 offset:36864\n"
      "v_cvt_pk_bf16_f32 v17, v22, v23\n"
      "v_exp_f32 v20, v20\n"
      "v_mul_f32 v21, v77, v77\n"
      "v_fma_f32 v20, v20, v26, v26\n"
      "v_mfma_f32_32x32x16_bf16 v[80:95], v[220:223], v[60:63], v[80:95]\n"
      "ds_read_b128 v[60:63], v7 offset:36864\n"
      "v_fma_f32 v21, v21, v25, v24\n"
      "v_rcp_f32 v20, v20\n"
      "v_mul_f32 v21, v77, v21\n"
      "s_waitcnt lgkmcnt(4)\n"
      "v_mfma_f32_32x32x16_bf16 v[80:95], v[224:227], v[32:35], v[80:95]\n"
      "v_mul_f32 v20, v76, v20\n"
      "v_exp_f32 v21, v21\n"
      "v_mul_f32 v22, v78, v78\n"
      "v_fma_f32 v21, v21, v26, v26\n"
      "v_fma_f32 v22, v22, v25, v24\n"
      "v_mfma_f32_32x32x16_bf16 v[80:95], v[228:231], v[36:39], v[80:95]\n"
      "v_rcp_f32 v21, v21\n"
      "v_mul_f32 v22, v78, v22\n"
      "v_mul_f32 v21, v77, v21\n"
      "v_xor_b32 v31, s86, v28\n"
      "ds_read_b64 v[218:219], v31\n"
      "v_mfma_f32_32x32x16_bf16 v[80:95], v[232:235], v[40:43], v[80:95]\n"
      "v_cvt_pk_bf16_f32 v18, v20, v21\n"
      "v_exp_f32 v22, v22\n"
      "v_mul_f32 v23, v79, v79\n"
      "v_xor_b32 v31, s87, v28\n"
      "ds_read_b64 v[220:221], v31\n"
      "v_mfma_f32_32x32x16_bf16 v[80:95], v[236:239], v[44:47], v[80:95]\n"
      "v_fma_f32 v22, v22, v26, v26\n"
      "v_fma_f32 v23, v23, v25, v24\n"
      "v_xor_b32 v31, s88, v28\n"
      "ds_read_b64 v[222:223], v31\n"
      "s_waitcnt lgkmcnt(3)\n"
      "v_mfma_f32_32x32x16_bf16 v[80:95], v[240:243], v[48:51], v[80:95]\n"
      "v_rcp_f32 v22, v22\n"
      "v_mul_f32 v23, v79, v23\n"
      "v_mul_f32 v22, v78, v22\n"
      "v_xor_b32 v31, s89, v28\n"
      "ds_read_b64 v[224:225], v31\n"
      "v_mfma_f32_32x32x16_bf16 v[80:95], v[244:247], v[52:55], v[80:95]\n"
      "v_exp_f32 v23, v23\n"
      "s_nop 0\n"
      "v_fma_f32 v23, v23, v26, v26\n"
      "v_rcp_f32 v23, v23\n"
      "v_xor_b32 v31, s90, v28\n"
      "ds_read_b64 v[226:227], v31\n"
      "v_mfma_f32_32x32x16_bf16 v[80:95], v[248:251], v[56:59], v[80:95]\n"
      "v_mul_f32 v23, v79, v23\n"
      "v_cvt_pk_bf16_f32 v19, v22, v23\n"
      "buffer_store_dwordx4 v[16:19], v29, s[20:23], s76 offen offset:256 sc1\n"
      "ds_read_b128 v[24:27], v30 offset:3072\n"
      "v_mfma_f32_32x32x16_bf16 v[64:79], v[252:255], v[60:63], v[80:95]\n"
      "ds_add_u32 v2, v3 offset:4276\n"
      "s_getreg_b32 s2, hwreg(HW_REG_IB_STS, 0, 4)\n"
      "s_add_u32 s2, s2, s84\n"
      "s_cmp_le_u32 s2, 2\n"
      "s_cbranch_scc1 .Lh_139\n"
      ".Lh_140:\n"
      "s_waitcnt lgkmcnt(0)\n"
      "s_cbranch_vccnz .Lg_141\n"
      ".Lg_142:\n"
      "v_mov_b32 v32, v24\n"
      "v_mov_b32 v33, v24\n"
      "v_mov_b32 v34, v25\n"
      "v_mov_b32 v35, v25\n"
      "v_mov_b32 v36, v26\n"
      "v_mov_b32 v37, v26\n"
      "v_pk_mul_f32 v[38:39], v[64:65], v[64:65]\n"
      "v_pk_mul_f32 v[40:41], v[66:67], v[66:67]\n"
      "v_pk_mul_f32 v[42:43], v[68:69], v[68:69]\n"
      "v_pk_mul_f32 v[44:45], v[70:71], v[70:71]\n"
      "v_pk_fma_f32 v[38:39], v[38:39], v[34:35], v[32:33]\n"
      "v_pk_fma_f32 v[40:41], v[40:41], v[34:35], v[32:33]\n"
      "v_pk_fma_f32 v[42:43], v[42:43], v[34:35], v[32:33]\n"
      "v_pk_fma_f32 v[44:45], v[44:45], v[34:35], v[32:33]\n"
      "v_pk_mul_f32 v[38:39], v[64:65], v[38:39]\n"
      "v_pk_mul_f32 v[40:41], v[66:67], v[40:41]\n"
      "v_pk_mul_f32 v[42:43], v[68:69], v[42:43]\n"
      "v_pk_mul_f32 v[44:45], v[70:71], v[44:45]\n"
      "v_exp_f32 v38, v38\n"
      "v_exp_f32 v39, v39\n"
      "v_exp_f32 v40, v40\n"
      "v_exp_f32 v41, v41\n"
      "v_exp_f32 v42, v42\n"
      "v_exp_f32 v43, v43\n"
      "v_exp_f32 v44, v44\n"
      "v_exp_f32 v45, v45\n"
      "v_pk_fma_f32 v[38:39], v[38:39], v[36:37], v[36:37]\n"
      "v_pk_fma_f32 v[40:41], v[40:41], v[36:37], v[36:37]\n"
      "v_pk_fma_f32 v[42:43], v[42:43], v[36:37], v[36:37]\n"
      "v_pk_fma_f32 v[44:45], v[44:45], v[36:37], v[36:37]\n"
      "v_rcp_f32 v38, v38\n"
      "v_rcp_f32 v39, v39\n"
      "v_rcp_f32 v40, v40\n"
      "v_rcp_f32 v41, v41\n"
      "v_rcp_f32 v42, v42\n"
      "v_rcp_f32 v43, v43\n"
      "v_rcp_f32 v44, v44\n"
      "v_rcp_f32 v45, v45\n"
      "v_pk_mul_f32 v[38:39], v[64:65], v[38:39]\n"
      "v_pk_mul_f32 v[40:41], v[66:67], v[40:41]\n"
      "v_pk_mul_f32 v[42:43], v[68:69], v[42:43]\n"
      "v_pk_mul_f32 v[44:45], v[70:71], v[44:45]\n"
      "v_cvt_pk_bf16_f32 v16, v38, v39\n"
      "v_cvt_pk_bf16_f32 v17, v40, v41\n"
      "v_cvt_pk_bf16_f32 v18, v42, v43\n"
      "v_cvt_pk_bf16_f32 v19, v44, v45\n"
      "buffer_store_dwordx4 v[16:19], v29, s[20:23], s77 offen offset:0 sc1\n"
      "v_pk_mul_f32 v[38:39], v[72:73], v[72:73]\n"
      "v_pk_mul_f32 v[40:41], v[74:75], v[74:75]\n"
      "v_pk_mul_f32 v[42:43], v[76:77], v[76:77]\n"
      "v_pk_mul_f32 v[44:45], v[78:79], v[78:79]\n"
      "v_pk_fma_f32 v[38:39], v[38:39], v[34:35], v[32:33]\n"
      "v_pk_fma_f32 v[40:41], v[40:41], v[34:35], v[32:33]\n"
      "v_pk_fma_f32 v[42:43], v[42:43], v[34:35], v[32:33]\n"
      "v_pk_fma_f32 v[44:45], v[44:45], v[34:35], v[32:33]\n"
      "v_pk_mul_f32 v[38:39], v[72:73], v[38:39]\n"
      "v_pk_mul_f32 v[40:41], v[74:75], v[40:41]\n"
      "v_pk_mul_f32 v[42:43], v[76:77], v[42:43]\n"
      "v_pk_mul_f32 v[44:45], v[78:79], v[44:45]\n"
      "v_exp_f32 v38, v38\n"
      "v_exp_f32 v39, v39\n"
      "v_exp_f32 v40, v40\n"
      "v_exp_f32 v41, v41\n"
      "v_exp_f32 v42, v42\n"
      "v_exp_f32 v43, v43\n"
      "v_exp_f32 v44, v44\n"
      "v_exp_f32 v45, v45\n"
      "v_pk_fma_f32 v[38:39], v[38:39], v[36:37], v[36:37]\n"
      "v_pk_fma_f32 v[40:41], v[40:41], v[36:37], v[36:37]\n"
      "v_pk_fma_f32 v[42:43], v[42:43], v[36:37], v[36:37]\n"
      "v_pk_fma_f32 v[44:45], v[44:45], v[36:37], v[36:37]\n"
      "ds_read_b32 v31, v2 offset:4304\n"
      "v_rcp_f32 v38, v38\n"
      "v_rcp_f32 v39, v39\n"
      "v_rcp_f32 v40, v40\n"
      "v_rcp_f32 v41, v41\n"
      "v_rcp_f32 v42, v42\n"
      "v_rcp_f32 v43, v43\n"
      "v_rcp_f32 v44, v44\n"
      "v_rcp_f32 v45, v45\n"
      "v_pk_mul_f32 v[38:39], v[72:73], v[38:39]\n"
      "v_pk_mul_f32 v[40:41], v[74:75], v[40:41]\n"
      "v_pk_mul_f32 v[42:43], v[76:77], v[42:43]\n"
      "v_pk_mul_f32 v[44:45], v[78:79], v[44:45]\n"
      "v_cvt_pk_bf16_f32 v20, v38, v39\n"
      "v_cvt_pk_bf16_f32 v21, v40, v41\n"
      "v_cvt_pk_bf16_f32 v22, v42, v43\n"
      "v_cvt_pk_bf16_f32 v23, v44, v45\n"
      "buffer_store_dwordx4 v[20:23], v29, s[20:23], s77 offen offset:256 sc1\n"
      "s_getreg_b32 s2, hwreg(HW_REG_IB_STS, 0, 4)\n"
      "s_add_u32 s2, s2, s84\n"
      "s_cmp_le_u32 s2, 4\n"
      "s_cbranch_scc1 .Lh_143\n"
      ".Lh_144:\n"
      ".LCONV:\n"
      "v_and_b32 v10, 1, v1\n"
      "v_xor_b32 v10, 1, v10\n"
      "v_mul_u32_u24 v10, 4160, v10\n"
      "v_and_b32 v11, 14, v1\n"
      "v_lshl_add_u32 v10, v11, 1, v10\n"
      "v_and_b32 v11, 48, v1\n"
      "v_sub_u32 v10, v10, v11\n"
      "v_and_b32 v21, 15, v1\n"
      "v_lshl_add_u32 v11, v21, 5, v11\n"
      "s_lshr_b32 s64, s53, 1\n"
      "s_and_b32 s65, s53, 1\n"
      "s_lshl_b32 s66, s65, 8\n"
      "s_mul_i32 s67, s81, 26112\n"
      "s_cmp_eq_u32 s81, 2\n"
      "s_cselect_b32 s67, 122880, s67\n"
      "s_mul_i32 s62, s64, 8320\n"
      "s_add_u32 s62, s62, s66\n"
      "s_add_u32 s62, s62, s67\n"
      "s_add_u32 s62, s62, 84\n"
      "v_add_u32 v212, s62, v10\n"
      "s_mul_i32 s62, s64, 4608\n"
      "s_sub_u32 s63, s62, s66\n"
      "s_add_u32 s63, s63, s67\n"
      "s_add_u32 s63, s63, 16928\n"
      "v_add_u32 v217, s63, v11\n"
      "s_lshl_b32 s62, s81, 5\n"
      "v_add_u32 v18, s62, v2\n"
      "s_waitcnt lgkmcnt(0)\n"
      "v_readfirstlane_b32 s2, v31\n"
      "s_cmp_lt_u32 s2, 4\n"
      "s_cbranch_scc1 .Lw_145\n"
      ".Lw_146:\n"
      "ds_read2_b32 v[144:145], v212 offset0:3 offset1:2\n"
      "ds_read2_b32 v[146:147], v212 offset0:1 offset1:0\n"
      "ds_read_b128 v[204:207], v217 offset:3776\n"
      "ds_read_b128 v[200:203], v217 offset:3264\n"
      "ds_read_b128 v[196:199], v217 offset:2752\n"
      "ds_read_b128 v[192:195], v217 offset:2240\n"
      "ds_read_b128 v[188:191], v217 offset:1728\n"
      "ds_read_b128 v[184:187], v217 offset:1216\n"
      "ds_read_b128 v[180:183], v217 offset:704\n"
      "ds_read_b128 v[176:179], v217 offset:192\n"
      "ds_read2_b32 v[148:149], v212 offset0:131 offset1:130\n"
      "ds_read2_b32 v[150:151], v212 offset0:129 offset1:128\n"
      "v_add_u32 v213, 1024, v212\n"
      "v_add_u32 v214, 2048, v212\n"
      "v_add_u32 v215, 3072, v212\n"
      "s_waitcnt lgkmcnt(9)\n"
      "v_mfma_f32_16x16x32_bf16 v[124:127], v[144:147], v[204:207], 0\n"
      "ds_read2_b32 v[152:153], v213 offset0:3 offset1:2\n"
      "s_waitcnt lgkmcnt(9)\n"
      "v_mfma_f32_16x16x32_bf16 v[120:123], v[144:147], v[200:203], 0\n"
      "ds_read2_b32 v[154:155], v213 offset0:1 offset1:0\n"
      "s_waitcnt lgkmcnt(9)\n"
      "v_mfma_f32_16x16x32_bf16 v[116:119], v[144:147], v[196:199], 0\n"
      "ds_read2_b32 v[156:157], v213 offset0:131 offset1:130\n"
      "v_lshlrev_b32 v9, 4, v1\n"
      "s_waitcnt lgkmcnt(9)\n"
      "v_mfma_f32_16x16x32_bf16 v[112:115], v[144:147], v[192:195], 0\n"
      "ds_read2_b32 v[158:159], v213 offset0:129 offset1:128\n"
      "s_and_b32 s63, s81, 1\n"
      "s_waitcnt lgkmcnt(9)\n"
      "v_mfma_f32_16x16x32_bf16 v[108:111], v[144:147], v[188:191], 0\n"
      "ds_read2_b32 v[160:161], v214 offset0:3 offset1:2\n"
      "s_mul_i32 s63, s63, 45056\n"
      "s_waitcnt lgkmcnt(9)\n"
      "v_mfma_f32_16x16x32_bf16 v[104:107], v[144:147], v[184:187], 0\n"
      "ds_read2_b32 v[162:163], v214 offset0:1 offset1:0\n"
      "s_sub_u32 s63, 98304, s63\n"
      "s_waitcnt lgkmcnt(7)\n"
      "v_mfma_f32_16x16x32_bf16 v[100:103], v[144:147], v[180:183], 0\n"
      "ds_read2_b32 v[164:165], v214 offset0:131 offset1:130\n"
      "s_lshl_b32 s62, s53, 12\n"
      "v_mfma_f32_16x16x32_bf16 v[96:99], v[144:147], v[176:179], 0\n"
      "ds_read2_b32 v[166:167], v214 offset0:129 offset1:128\n"
      "s_add_u32 s67, s62, s63\n"
      "s_waitcnt lgkmcnt(7)\n"
      "v_mfma_f32_16x16x32_bf16 v[124:127], v[148:151], v[200:203], v[124:127]\n"
      "v_add_u32 v14, s67, v9\n"
      "s_xor_b32 s62, s62, 4096\n"
      "v_mfma_f32_16x16x32_bf16 v[120:123], v[148:151], v[196:199], v[120:123]\n"
      "ds_read2_b32 v[168:169], v215 offset0:3 offset1:2\n"
      "s_add_u32 s67, s62, s63\n"
      "v_mfma_f32_16x16x32_bf16 v[116:119], v[148:151], v[192:195], v[116:119]\n"
      "ds_read2_b32 v[170:171], v215 offset0:1 offset1:0\n"
      "v_add_u32 v15, s67, v9\n"
      "v_mfma_f32_16x16x32_bf16 v[112:115], v[148:151], v[188:191], v[112:115]\n"
      "ds_read2_b32 v[172:173], v215 offset0:131 offset1:130\n"
      "s_lshl_b32 s62, s53, 2\n"
      "v_mfma_f32_16x16x32_bf16 v[108:111], v[148:151], v[184:187], v[108:111]\n"
      "ds_read2_b32 v[174:175], v215 offset0:129 offset1:128\n"
      "v_add_u32 v19, s62, v18\n"
      "v_mfma_f32_16x16x32_bf16 v[104:107], v[148:151], v[180:183], v[104:107]\n"
      "ds_read2_b32 v[144:145], v212 offset0:19 offset1:18\n"
      "s_xor_b32 s62, s62, 4\n"
      "v_mfma_f32_16x16x32_bf16 v[100:103], v[148:151], v[176:179], v[100:103]\n"
      "ds_read2_b32 v[146:147], v212 offset0:17 offset1:16\n"
      "v_add_u32 v20, s62, v18\n"
      "s_waitcnt lgkmcnt(6)\n"
      "v_mfma_f32_16x16x32_bf16 v[124:127], v[152:155], v[196:199], v[124:127]\n"
      "ds_read_b128 v[204:207], v217 offset:3712\n"
      "v_and_b32 v16, 15, v1\n"
      "v_mfma_f32_16x16x32_bf16 v[120:123], v[152:155], v[192:195], v[120:123]\n"
      "ds_read_b128 v[200:203], v217 offset:3200\n"
      "v_lshrrev_b32 v17, 4, v1\n"
      "v_mfma_f32_16x16x32_bf16 v[116:119], v[152:155], v[188:191], v[116:119]\n"
      "ds_read_b128 v[196:199], v217 offset:2688\n"
      "v_lshrrev_b32 v21, 5, v1\n"
      "v_mfma_f32_16x16x32_bf16 v[112:115], v[152:155], v[184:187], v[112:115]\n"
      "v_lshlrev_b32 v21, 4, v21\n"
      "v_lshl_add_u32 v21, v16, 5, v21\n"
      "v_mfma_f32_16x16x32_bf16 v[108:111], v[152:155], v[180:183], v[108:111]\n"
      "v_and_b32 v22, 1, v17\n"
      "v_lshl_add_u32 v5, v22, 9, v21\n"
      "v_mfma_f32_16x16x32_bf16 v[104:107], v[152:155], v[176:179], v[104:107]\n"
      "ds_read2_b32 v[148:149], v212 offset0:147 offset1:146\n"
      "s_lshl1_add_u32 s63, s81, s98\n"
      "v_mfma_f32_16x16x32_bf16 v[124:127], v[156:159], v[192:195], v[124:127]\n"
      "ds_read_b128 v[192:195], v217 offset:2176\n"
      "s_add_u32 s63, s63, s64\n"
      "v_mfma_f32_16x16x32_bf16 v[120:123], v[156:159], v[188:191], v[120:123]\n"
      "ds_read2_b32 v[150:151], v212 offset0:145 offset1:144\n"
      "s_mul_i32 s63, s63, 4224\n"
      "v_mfma_f32_16x16x32_bf16 v[116:119], v[156:159], v[184:187], v[116:119]\n"
      "s_getreg_b32 s2, hwreg(HW_REG_IB_STS, 0, 4)\n"
      "s_add_u32 s2, s2, s84\n"
      "s_cmp_le_u32 s2, 4\n"
      "s_cbranch_scc1 .Lh_148\n"
      ".Lh_149:\n"
      "v_mfma_f32_16x16x32_bf16 v[112:115], v[156:159], v[180:183], v[112:115]\n"
      "s_lshl_b32 s67, s65, 11\n"
      "s_add_u32 s63, s63, s67\n"
      "v_mfma_f32_16x16x32_bf16 v[108:111], v[156:159], v[176:179], v[108:111]\n"
      "s_add_u32 s80, s63, 2801152\n"
      "s_sub_u32 s82, 3, s53\n"
      "v_mfma_f32_16x16x32_bf16 v[124:127], v[160:163], v[188:191], v[124:127]\n"
      "ds_read_b128 v[188:191], v217 offset:1664\n"
      "s_lshr_b32 s63, s82, 1\n"
      "v_mfma_f32_16x16x32_bf16 v[120:123], v[160:163], v[184:187], v[120:123]\n"
      "ds_read2_b32 v[152:153], v213 offset0:19 offset1:18\n"
      "s_lshl1_add_u32 s63, s54, s63\n"
      "v_mfma_f32_16x16x32_bf16 v[116:119], v[160:163], v[180:183], v[116:119]\n"
      "ds_read2_b32 v[154:155], v213 offset0:17 offset1:16\n"
      "s_xor_b32 s63, s63, s99\n"
      "v_mfma_f32_16x16x32_bf16 v[112:115], v[160:163], v[176:179], v[112:115]\n"
      "s_lshl_b32 s63, s63, 10\n"
      "s_and_b32 s67, s82, 1\n"
      "v_mfma_f32_16x16x32_bf16 v[124:127], v[164:167], v[184:187], v[124:127]\n"
      "ds_read_b128 v[184:187], v217 offset:1152\n"
      "s_lshl_b32 s67, s67, 9\n"
      "v_mfma_f32_16x16x32_bf16 v[120:123], v[164:167], v[180:183], v[120:123]\n"
      "s_getreg_b32 s2, hwreg(HW_REG_IB_STS, 0, 4)\n"
      "s_add_u32 s2, s2, s85\n"
      "s_cmp_le_u32 s2, 0\n"
      "s_cbranch_scc1 .Lh_150\n"
      ".Lh_151:\n"
      "v_mfma_f32_16x16x32_bf16 v[116:119], v[164:167], v[176:179], v[116:119]\n"
      "s_add_u32 s63, s63, s67\n"
      "s_add_u32 s63, s63, 157716\n"
      "s_waitcnt lgkmcnt(3)\n"
      "v_mfma_f32_16x16x32_bf16 v[124:127], v[168:171], v[180:183], v[124:127]\n"
      "ds_read_b128 v[180:183], v217 offset:640\n"
      "v_lshl_add_u32 v21, v16, 5, s63\n"
      "v_mfma_f32_16x16x32_bf16 v[120:123], v[168:171], v[176:179], v[120:123]\n"
      "ds_read2_b32 v[156:157], v213 offset0:147 offset1:146\n"
      "ds_read_b32 v23, v21\n"
      "v_mfma_f32_16x16x32_bf16 v[124:127], v[172:175], v[176:179], v[124:127]\n"
      "ds_read_b128 v[176:179], v217 offset:128\n"
      "s_lshl1_add_u32 s62, s56, s54\n"
      "v_mfma_f32_16x16x32_bf16 v[124:127], v[144:147], v[204:207], v[124:127]\n"
      "ds_read2_b32 v[158:159], v213 offset0:145 offset1:144\n"
      "s_lshl2_add_u32 s63, s62, s82\n"
      "v_mfma_f32_16x16x32_bf16 v[120:123], v[144:147], v[200:203], v[120:123]\n"
      "s_mul_i32 s70, s63, 20480\n"
      "s_mul_i32 s63, s55, 160\n"
      "v_mfma_f32_16x16x32_bf16 v[116:119], v[144:147], v[196:199], v[116:119]\n"
      "ds_read2_b32 v[160:161], v214 offset0:19 offset1:18\n"
      "s_add_u32 s70, s70, s63\n"
      "v_mfma_f32_16x16x32_bf16 v[112:115], v[144:147], v[192:195], v[112:115]\n"
      "ds_read2_b32 v[162:163], v214 offset0:17 offset1:16\n"
      "v_mul_u32_u24 v4, 1280, v16\n"
      "v_mfma_f32_16x16x32_bf16 v[108:111], v[144:147], v[188:191], v[108:111]\n"
      "v_lshl_add_u32 v4, v17, 3, v4\n"
      "s_mul_i32 s63, s53, 5120\n"
      "s_waitcnt lgkmcnt(6)\n"
      "v_mfma_f32_16x16x32_bf16 v[104:107], v[144:147], v[184:187], v[104:107]\n"
      "s_lshl2_add_u32 s72, s60, s53\n"
      "s_mul_i32 s72, s72, 3072\n"
      "v_mfma_f32_16x16x32_bf16 v[100:103], v[144:147], v[180:183], v[100:103]\n"
      "ds_read2_b32 v[164:165], v214 offset0:147 offset1:146\n"
      "s_add_u32 s72, s72, 15237120\n"
      "s_waitcnt lgkmcnt(4)\n"
      "v_mfma_f32_16x16x32_bf16 v[96:99], v[144:147], v[176:179], v[96:99]\n"
      "ds_read2_b32 v[166:167], v214 offset0:145 offset1:144\n"
      "s_add_u32 s63, s63, 53248\n"
      "v_mfma_f32_16x16x32_bf16 v[124:127], v[148:151], v[200:203], v[124:127]\n"
      "s_cmp_eq_u32 s84, 0\n"
      "s_cbranch_scc1 .Lh_152\n"
      ".Lh_153:\n"
      "v_mfma_f32_16x16x32_bf16 v[120:123], v[148:151], v[196:199], v[120:123]\n"
      "ds_read2_b32 v[168:169], v215 offset0:19 offset1:18\n"
      "v_add_u32 v8, s63, v9\n"
      "v_mfma_f32_16x16x32_bf16 v[116:119], v[148:151], v[192:195], v[116:119]\n"
      "ds_read2_b32 v[170:171], v215 offset0:17 offset1:16\n"
      "s_lshl_b32 s74, s93, 16\n"
      "v_mfma_f32_16x16x32_bf16 v[112:115], v[148:151], v[188:191], v[112:115]\n"
      "ds_read2_b32 v[172:173], v215 offset0:147 offset1:146\n"
      "s_lshl2_add_u32 s63, s55, s53\n"
      "v_mfma_f32_16x16x32_bf16 v[108:111], v[148:151], v[184:187], v[108:111]\n"
      "ds_read2_b32 v[174:175], v215 offset0:145 offset1:144\n"
      "s_mul_i32 s73, s63, 84480\n"
      "v_mfma_f32_16x16x32_bf16 v[104:107], v[148:151], v[180:183], v[104:107]\n"
      "ds_read2_b32 v[144:145], v212 offset0:35 offset1:34\n"
      "s_lshl_b32 s62, s62, 7\n"
      "v_mfma_f32_16x16x32_bf16 v[100:103], v[148:151], v[176:179], v[100:103]\n"
      "ds_read2_b32 v[146:147], v212 offset0:33 offset1:32\n"
      "s_add_u32 s73, s73, s62\n"
      "v_mfma_f32_16x16x32_bf16 v[124:127], v[152:155], v[196:199], v[124:127]\n"
      "ds_read_b128 v[204:207], v217 offset:3648\n"
      "s_add_u32 s73, s73, 2818048\n"
      "v_mfma_f32_16x16x32_bf16 v[120:123], v[152:155], v[192:195], v[120:123]\n"
      "ds_read_b128 v[200:203], v217 offset:3136\n"
      "s_mul_i32 s63, s63, 10\n"
      "v_mfma_f32_16x16x32_bf16 v[116:119], v[152:155], v[188:191], v[116:119]\n"
      "ds_read_b128 v[196:199], v217 offset:2624\n"
      "v_min_u32 v21, 9, v1\n"
      "v_mfma_f32_16x16x32_bf16 v[112:115], v[152:155], v[184:187], v[112:115]\n"
      "s_getreg_b32 s2, hwreg(HW_REG_IB_STS, 0, 4)\n"
      "s_add_u32 s2, s2, s85\n"
      "s_cmp_le_u32 s2, 0\n"
      "s_cbranch_scc1 .Lh_154\n"
      ".Lh_155:\n"
      "v_mfma_f32_16x16x32_bf16 v[108:111], v[152:155], v[180:183], v[108:111]\n"
      "v_add_u32 v21, s63, v21\n"
      "v_add_u32 v22, 448, v21\n"
      "v_mfma_f32_16x16x32_bf16 v[104:107], v[152:155], v[176:179], v[104:107]\n"
      "ds_read2_b32 v[148:149], v212 offset0:163 offset1:162\n"
      "v_mul_u32_u24 v22, 21846, v22\n"
      "s_waitcnt lgkmcnt(4)\n"
      "v_mfma_f32_16x16x32_bf16 v[124:127], v[156:159], v[192:195], v[124:127]\n"
      "ds_read_b128 v[192:195], v217 offset:2112\n"
      "v_lshrrev_b32 v22, 16, v22\n"
      "v_mfma_f32_16x16x32_bf16 v[120:123], v[156:159], v[188:191], v[120:123]\n"
      "ds_read2_b32 v[150:151], v212 offset0:161 offset1:160\n"
      "v_min_u32 v21, v21, v22\n"
      "v_mfma_f32_16x16x32_bf16 v[116:119], v[156:159], v[184:187], v[116:119]\n"
      "s_mov_b32 s63, 18864128\n"
      "v_lshl_add_u32 v25, v21, 7, s63\n"
      "v_mfma_f32_16x16x32_bf16 v[112:115], v[156:159], v[180:183], v[112:115]\n"
      "v_and_b32 v21, 7, v1\n"
      "v_lshrrev_b32 v22, 3, v1\n"
      "v_mfma_f32_16x16x32_bf16 v[108:111], v[156:159], v[176:179], v[108:111]\n"
      "v_mul_u32_u24 v6, 4224, v22\n"
      "v_lshl_add_u32 v6, v21, 4, v6\n"
      "v_mfma_f32_16x16x32_bf16 v[124:127], v[160:163], v[188:191], v[124:127]\n"
      "ds_read_b128 v[188:191], v217 offset:1600\n"
      "s_lshl_b32 s62, s82, 5\n"
      "v_mfma_f32_16x16x32_bf16 v[120:123], v[160:163], v[184:187], v[120:123]\n"
      "ds_read2_b32 v[152:153], v213 offset0:35 offset1:34\n"
      "s_add_u32 s62, s62, 139520\n"
      "v_mfma_f32_16x16x32_bf16 v[116:119], v[160:163], v[180:183], v[116:119]\n"
      "ds_read2_b32 v[154:155], v213 offset0:33 offset1:32\n"
      "v_lshrrev_b32 v21, 2, v1\n"
      "v_mfma_f32_16x16x32_bf16 v[112:115], v[160:163], v[176:179], v[112:115]\n"
      "v_lshl_add_u32 v21, v21, 7, s62\n"
      "v_and_b32 v22, 3, v1\n"
      "v_mfma_f32_16x16x32_bf16 v[124:127], v[164:167], v[184:187], v[124:127]\n"
      "ds_read_b128 v[184:187], v217 offset:1088\n"
      "v_lshl_add_u32 v7, v22, 3, v21\n"
      "v_mfma_f32_16x16x32_bf16 v[120:123], v[164:167], v[180:183], v[120:123]\n"
      "s_getreg_b32 s2, hwreg(HW_REG_IB_STS, 0, 4)\n"
      "s_add_u32 s2, s2, s85\n"
      "s_cmp_le_u32 s2, 0\n"
      "s_cbranch_scc1 .Lh_156\n"
      ".Lh_157:\n"
      "v_mfma_f32_16x16x32_bf16 v[116:119], v[164:167], v[176:179], v[116:119]\n"
      "s_mov_b32 s76, 0xbfb8aa3b\n"
      "s_mov_b32 s77, 0xbf317218\n"
      "v_mfma_f32_16x16x32_bf16 v[124:127], v[168:171], v[180:183], v[124:127]\n"
      "ds_read_b128 v[180:183], v217 offset:576\n"
      "v_and_b32 v21, s92, v23\n"
      "v_mfma_f32_16x16x32_bf16 v[120:123], v[168:171], v[176:179], v[120:123]\n"
      "ds_read2_b32 v[156:157], v213 offset0:163 offset1:162\n"
      "v_mfma_f32_16x16x32_bf16 v[124:127], v[172:175], v[176:179], v[124:127]\n"
      "ds_read_b128 v[176:179], v217 offset:64\n"
      "s_waitcnt lgkmcnt(4)\n"
      "v_mfma_f32_16x16x32_bf16 v[124:127], v[144:147], v[204:207], v[124:127]\n"
      "ds_read2_b32 v[158:159], v213 offset0:161 offset1:160\n"
      "v_mfma_f32_16x16x32_bf16 v[120:123], v[144:147], v[200:203], v[120:123]\n"
      "v_rcp_f32 v21, v21\n"
      "v_mfma_f32_16x16x32_bf16 v[116:119], v[144:147], v[196:199], v[116:119]\n"
      "ds_read2_b32 v[160:161], v214 offset0:35 offset1:34\n"
      "v_lshlrev_b32 v22, 16, v23\n"
      "v_mfma_f32_16x16x32_bf16 v[112:115], v[144:147], v[192:195], v[112:115]\n"
      "ds_read2_b32 v[162:163], v214 offset0:33 offset1:32\n"
      "v_mul_f32 v21, s77, v21\n"
      "v_mfma_f32_16x16x32_bf16 v[108:111], v[144:147], v[188:191], v[108:111]\n"
      "v_mul_f32 v22, v22, v21\n"
      "v_lshlrev_b32 v32, 16, v218\n"
      "s_waitcnt lgkmcnt(5)\n"
      "v_mfma_f32_16x16x32_bf16 v[104:107], v[144:147], v[184:187], v[104:107]\n"
      "v_and_b32 v33, s92, v218\n"
      "v_lshlrev_b32 v34, 16, v219\n"
      "v_mfma_f32_16x16x32_bf16 v[100:103], v[144:147], v[180:183], v[100:103]\n"
      "ds_read2_b32 v[164:165], v214 offset0:163 offset1:162\n"
      "v_and_b32 v35, s92, v219\n"
      "s_waitcnt lgkmcnt(4)\n"
      "v_mfma_f32_16x16x32_bf16 v[96:99], v[144:147], v[176:179], v[96:99]\n"
      "ds_read2_b32 v[166:167], v214 offset0:161 offset1:160\n"
      "v_lshlrev_b32 v36, 16, v220\n"
      "v_mfma_f32_16x16x32_bf16 v[124:127], v[148:151], v[200:203], v[124:127]\n"
      "v_and_b32 v37, s92, v220\n"
      "v_lshlrev_b32 v38, 16, v221\n"
      "v_mfma_f32_16x16x32_bf16 v[120:123], v[148:151], v[196:199], v[120:123]\n"
      "ds_read2_b32 v[168:169], v215 offset0:35 offset1:34\n"
      "v_and_b32 v39, s92, v221\n"
      "v_mfma_f32_16x16x32_bf16 v[116:119], v[148:151], v[192:195], v[116:119]\n"
      "ds_read2_b32 v[170:171], v215 offset0:33 offset1:32\n"
      "v_lshlrev_b32 v40, 16, v222\n"
      "v_mfma_f32_16x16x32_bf16 v[112:115], v[148:151], v[188:191], v[112:115]\n"
      "ds_read2_b32 v[172:173], v215 offset0:163 offset1:162\n"
      "v_and_b32 v41, s92, v222\n"
      "v_mfma_f32_16x16x32_bf16 v[108:111], v[148:151], v[184:187], v[108:111]\n"
      "ds_read2_b32 v[174:175], v215 offset0:161 offset1:160\n"
      "v_lshlrev_b32 v42, 16, v223\n"
      "v_mfma_f32_16x16x32_bf16 v[104:107], v[148:151], v[180:183], v[104:107]\n"
      "ds_read2_b32 v[144:145], v212 offset0:51 offset1:50\n"
      "v_and_b32 v43, s92, v223\n"
      "v_mfma_f32_16x16x32_bf16 v[100:103], v[148:151], v[176:179], v[100:103]\n"
      "ds_read2_b32 v[146:147], v212 offset0:49 offset1:48\n"
      "v_lshlrev_b32 v44, 16, v224\n"
      "v_mfma_f32_16x16x32_bf16 v[124:127], v[152:155], v[196:199], v[124:127]\n"
      "ds_read_b128 v[204:207], v217 offset:3584\n"
      "v_and_b32 v45, s92, v224\n"
      "v_mfma_f32_16x16x32_bf16 v[120:123], v[152:155], v[192:195], v[120:123]\n"
      "ds_read_b128 v[200:203], v217 offset:3072\n"
      "v_lshlrev_b32 v46, 16, v225\n"
      "v_mfma_f32_16x16x32_bf16 v[116:119], v[152:155], v[188:191], v[116:119]\n"
      "ds_read_b128 v[196:199], v217 offset:2560\n"
      "v_and_b32 v47, s92, v225\n"
      "v_mfma_f32_16x16x32_bf16 v[112:115], v[152:155], v[184:187], v[112:115]\n"
      "s_getreg_b32 s2, hwreg(HW_REG_IB_STS, 0, 4)\n"
      "s_add_u32 s2, s2, s85\n"
      "s_cmp_le_u32 s2, 0\n"
      "s_cbranch_scc1 .Lh_158\n"
      ".Lh_159:\n"
      "v_mfma_f32_16x16x32_bf16 v[108:111], v[152:155], v[180:183], v[108:111]\n"
      "v_lshlrev_b32 v48, 16, v226\n"
      "v_and_b32 v49, s92, v226\n"
      "v_mfma_f32_16x16x32_bf16 v[104:107], v[152:155], v[176:179], v[104:107]\n"
      "ds_read2_b32 v[148:149], v212 offset0:179 offset1:178\n"
      "v_lshlrev_b32 v50, 16, v227\n"
      "s_waitcnt lgkmcnt(4)\n"
      "v_mfma_f32_16x16x32_bf16 v[124:127], v[156:159], v[192:195], v[124:127]\n"
      "ds_read_b128 v[192:195], v217 offset:2048\n"
      "v_and_b32 v51, s92, v227\n"
      "v_mfma_f32_16x16x32_bf16 v[120:123], v[156:159], v[188:191], v[120:123]\n"
      "ds_read2_b32 v[150:151], v212 offset0:177 offset1:176\n"
      "v_fma_f32 v52, v32, v21, v22\n"
      "v_mfma_f32_16x16x32_bf16 v[116:119], v[156:159], v[184:187], v[116:119]\n"
      "v_fma_f32 v53, v33, v21, v22\n"
      "v_fma_f32 v54, v34, v21, v22\n"
      "v_mfma_f32_16x16x32_bf16 v[112:115], v[156:159], v[180:183], v[112:115]\n"
      "v_fma_f32 v55, v35, v21, v22\n"
      "v_fma_f32 v56, v36, v21, v22\n"
      "v_mfma_f32_16x16x32_bf16 v[108:111], v[156:159], v[176:179], v[108:111]\n"
      "v_fma_f32 v57, v37, v21, v22\n"
      "v_fma_f32 v58, v38, v21, v22\n"
      "v_mfma_f32_16x16x32_bf16 v[124:127], v[160:163], v[188:191], v[124:127]\n"
      "ds_read_b128 v[188:191], v217 offset:1536\n"
      "v_fma_f32 v59, v39, v21, v22\n"
      "v_mfma_f32_16x16x32_bf16 v[120:123], v[160:163], v[184:187], v[120:123]\n"
      "ds_read2_b32 v[152:153], v213 offset0:51 offset1:50\n"
      "v_fma_f32 v60, v40, v21, v22\n"
      "v_mfma_f32_16x16x32_bf16 v[116:119], v[160:163], v[180:183], v[116:119]\n"
      "ds_read2_b32 v[154:155], v213 offset0:49 offset1:48\n"
      "v_fma_f32 v61, v41, v21, v22\n"
      "v_mfma_f32_16x16x32_bf16 v[112:115], v[160:163], v[176:179], v[112:115]\n"
      "v_fma_f32 v62, v42, v21, v22\n"
      "v_fma_f32 v63, v43, v21, v22\n"
      "v_mfma_f32_16x16x32_bf16 v[124:127], v[164:167], v[184:187], v[124:127]\n"
      "ds_read_b128 v[184:187], v217 offset:1024\n"
      "v_fma_f32 v64, v44, v21, v22\n"
      "v_mfma_f32_16x16x32_bf16 v[120:123], v[164:167], v[180:183], v[120:123]\n"
      "s_cmp_eq_u32 s85, 0\n"
      "s_cbranch_scc1 .Lh_160\n"
      ".Lh_161:\n"
      "v_mfma_f32_16x16x32_bf16 v[116:119], v[164:167], v[176:179], v[116:119]\n"
      "v_fma_f32 v65, v45, v21, v22\n"
      "v_fma_f32 v66, v46, v21, v22\n"
      "v_mfma_f32_16x16x32_bf16 v[124:127], v[168:171], v[180:183], v[124:127]\n"
      "ds_read_b128 v[180:183], v217 offset:512\n"
      "v_fma_f32 v67, v47, v21, v22\n"
      "v_mfma_f32_16x16x32_bf16 v[120:123], v[168:171], v[176:179], v[120:123]\n"
      "ds_read2_b32 v[156:157], v213 offset0:179 offset1:178\n"
      "v_fma_f32 v68, v48, v21, v22\n"
      "v_mfma_f32_16x16x32_bf16 v[124:127], v[172:175], v[176:179], v[124:127]\n"
      "ds_read_b128 v[176:179], v217 offset:0\n"
      "v_fma_f32 v69, v49, v21, v22\n"
      "s_waitcnt lgkmcnt(4)\n"
      "v_mfma_f32_16x16x32_bf16 v[124:127], v[144:147], v[204:207], v[124:127]\n"
      "ds_read2_b32 v[158:159], v213 offset0:177 offset1:176\n"
      "v_fma_f32 v70, v50, v21, v22\n"
      "v_mfma_f32_16x16x32_bf16 v[120:123], v[144:147], v[200:203], v[120:123]\n"
      "v_fma_f32 v71, v51, v21, v22\n"
      "v_mfma_f32_16x16x32_bf16 v[116:119], v[144:147], v[196:199], v[116:119]\n"
      "ds_read2_b32 v[160:161], v214 offset0:51 offset1:50\n"
      "v_mfma_f32_16x16x32_bf16 v[112:115], v[144:147], v[192:195], v[112:115]\n"
      "ds_read2_b32 v[162:163], v214 offset0:49 offset1:48\n"
      "v_mfma_f32_16x16x32_bf16 v[108:111], v[144:147], v[188:191], v[108:111]\n"
      "ds_read_b32 v31, v2 offset:4276\n"
      "s_waitcnt lgkmcnt(6)\n"
      "v_mfma_f32_16x16x32_bf16 v[104:107], v[144:147], v[184:187], v[104:107]\n"
      "v_mfma_f32_16x16x32_bf16 v[100:103], v[144:147], v[180:183], v[100:103]\n"
      "ds_read2_b32 v[164:165], v214 offset0:179 offset1:178\n"
      "s_waitcnt lgkmcnt(5)\n"
      "v_mfma_f32_16x16x32_bf16 v[96:99], v[144:147], v[176:179], v[96:99]\n"
      "ds_read2_b32 v[166:167], v214 offset0:177 offset1:176\n"
      "v_mfma_f32_16x16x32_bf16 v[124:127], v[148:151], v[200:203], v[124:127]\n"
      "v_mfma_f32_16x16x32_bf16 v[120:123], v[148:151], v[196:199], v[120:123]\n"
      "ds_read2_b32 v[168:169], v215 offset0:51 offset1:50\n"
      "v_mfma_f32_16x16x32_bf16 v[116:119], v[148:151], v[192:195], v[116:119]\n"
      "ds_read2_b32 v[170:171], v215 offset0:49 offset1:48\n"
      "v_mfma_f32_16x16x32_bf16 v[112:115], v[148:151], v[188:191], v[112:115]\n"
      "ds_read2_b32 v[172:173], v215 offset0:179 offset1:178\n"
      "v_mfma_f32_16x16x32_bf16 v[108:111], v[148:151], v[184:187], v[108:111]\n"
      "ds_read2_b32 v[174:175], v215 offset0:177 offset1:176\n"
      "v_mfma_f32_16x16x32_bf16 v[104:107], v[148:151], v[180:183], v[104:107]\n"
      "s_waitcnt lgkmcnt(5)\n"
      "v_readfirstlane_b32 s2, v31\n"
      "s_cmp_lt_u32 s2, 4\n"
      "s_cbranch_scc1 .Lw_162\n"
      ".Lw_163:\n"
      "v_mfma_f32_16x16x32_bf16 v[100:103], v[148:151], v[176:179], v[100:103]\n"
      "v_mfma_f32_16x16x32_bf16 v[124:127], v[152:155], v[196:199], v[124:127]\n"
      "v_mfma_f32_16x16x32_bf16 v[120:123], v[152:155], v[192:195], v[120:123]\n"
      "v_mfma_f32_16x16x32_bf16 v[116:119], v[152:155], v[188:191], v[116:119]\n"
      "v_mfma_f32_16x16x32_bf16 v[112:115], v[152:155], v[184:187], v[112:115]\n"
      "v_mfma_f32_16x16x32_bf16 v[108:111], v[152:155], v[180:183], v[108:111]\n"
      "v_mfma_f32_16x16x32_bf16 v[104:107], v[152:155], v[176:179], v[104:107]\n"
      "v_mfma_f32_16x16x32_bf16 v[124:127], v[156:159], v[192:195], v[124:127]\n"
      "v_mfma_f32_16x16x32_bf16 v[120:123], v[156:159], v[188:191], v[120:123]\n"
      "v_mfma_f32_16x16x32_bf16 v[116:119], v[156:159], v[184:187], v[116:119]\n"
      "v_mfma_f32_16x16x32_bf16 v[112:115], v[156:159], v[180:183], v[112:115]\n"
      "v_mfma_f32_16x16x32_bf16 v[108:111], v[156:159], v[176:179], v[108:111]\n"
      "v_mfma_f32_16x16x32_bf16 v[124:127], v[160:163], v[188:191], v[124:127]\n"
      "v_mfma_f32_16x16x32_bf16 v[120:123], v[160:163], v[184:187], v[120:123]\n"
      "v_mfma_f32_16x16x32_bf16 v[116:119], v[160:163], v[180:183], v[116:119]\n"
      "v_mfma_f32_16x16x32_bf16 v[112:115], v[160:163], v[176:179], v[112:115]\n"
      "s_waitcnt lgkmcnt(0)\n"
      "v_mfma_f32_16x16x32_bf16 v[124:127], v[164:167], v[184:187], v[124:127]\n"
      "v_mfma_f32_16x16x32_bf16 v[120:123], v[164:167], v[180:183], v[120:123]\n"
      "v_mfma_f32_16x16x32_bf16 v[116:119], v[164:167], v[176:179], v[116:119]\n"
      "v_mfma_f32_16x16x32_bf16 v[124:127], v[168:171], v[180:183], v[124:127]\n"
      "v_mfma_f32_16x16x32_bf16 v[120:123], v[168:171], v[176:179], v[120:123]\n"
      "v_mfma_f32_16x16x32_bf16 v[124:127], v[172:175], v[176:179], v[124:127]\n"
      "s_setprio 3\n"
      "s_cmp_eq_u32 s65, 0\n"
      "s_cbranch_scc1 .Lx_165\n"
      "ds_write_b128 v14, v[96:99] offset:0\n"
      "ds_write_b128 v14, v[100:103] offset:1024\n"
      "ds_write_b128 v14, v[104:107] offset:2048\n"
      "ds_write_b128 v14, v[108:111] offset:3072\n"
      "ds_add_u32 v19, v3 offset:4248\n"
      "v_mov_b64 v[96:97], v[112:113]\n"
      "v_mov_b64 v[98:99], v[114:115]\n"
      "v_mov_b64 v[100:101], v[116:117]\n"
      "v_mov_b64 v[102:103], v[118:119]\n"
      "v_mov_b64 v[104:105], v[120:121]\n"
      "v_mov_b64 v[106:107], v[122:123]\n"
      "v_mov_b64 v[108:109], v[124:125]\n"
      "v_mov_b64 v[110:111], v[126:127]\n"
      "s_branch .Lx_166\n"
      ".Lx_165:\n"
      "ds_write_b128 v14, v[112:115] offset:0\n"
      "s_nop 0\n"
      "ds_write_b128 v14, v[116:119] offset:1024\n"
      "s_nop 0\n"
      "ds_write_b128 v14, v[120:123] offset:2048\n"
      "ds_write_b128 v14, v[124:127] offset:3072\n"
      "ds_add_u32 v19, v3 offset:4248\n"
      ".Lx_166:\n"
      "s_mov_b32 s3, 0x80000\n"
      ".Lp_167:\n"
      "ds_read_b32 v31, v20 offset:4248\n"
      "s_sub_u32 s3, s3, 1\n"
      "s_waitcnt lgkmcnt(0)\n"
      "v_readlane_b32 s2, v31, 0\n"
      "s_cselect_b32 s2, -1, s2\n"
      "s_cmp_lt_u32 s2, 1\n"
      "s_cbranch_scc1 .Lp_167\n"
      "ds_read_b128 v[72:75], v15 offset:0\n"
      "ds_read_b128 v[76:79], v15 offset:1024\n"
      "ds_read_b128 v[80:83], v15 offset:2048\n"
      "ds_read_b128 v[84:87], v15 offset:3072\n"
      "ds_add_u32 v18, v3 offset:4244\n"
      "s_waitcnt lgkmcnt(3)\n"
      "v_add_f32 v96, v96, v72\n"
      "v_add_f32 v97, v97, v73\n"
      "v_add_f32 v98, v98, v74\n"
      "v_add_f32 v99, v99, v75\n"
      "v_add_f32 v100, v100, v76\n"
      "v_add_f32 v101, v101, v77\n"
      "v_add_f32 v102, v102, v78\n"
      "v_add_f32 v103, v103, v79\n"
      "v_cvt_pk_bf16_f32 v88, v96, v97\n"
      "v_cvt_pk_bf16_f32 v90, v100, v101\n"
      "v_cvt_pk_bf16_f32 v89, v98, v99\n"
      "v_cvt_pk_bf16_f32 v91, v102, v103\n"
      "v_permlane16_swap_b32 v88, v90\n"
      "s_nop 0\n"
      "v_permlane16_swap_b32 v89, v91\n"
      "buffer_store_dwordx4 v[88:91], v5, s[20:23], s80 offen offset:0 sc1\n"
      "s_waitcnt lgkmcnt(1)\n"
      "v_add_f32 v104, v104, v80\n"
      "v_add_f32 v105, v105, v81\n"
      "v_add_f32 v106, v106, v82\n"
      "v_add_f32 v107, v107, v83\n"
      "v_add_f32 v108, v108, v84\n"
      "v_add_f32 v109, v109, v85\n"
      "v_add_f32 v110, v110, v86\n"
      "v_add_f32 v111, v111, v87\n"
      "v_cvt_pk_bf16_f32 v92, v104, v105\n"
      "v_cvt_pk_bf16_f32 v94, v108, v109\n"
      "v_cvt_pk_bf16_f32 v93, v106, v107\n"
      "v_cvt_pk_bf16_f32 v95, v110, v111\n"
      "v_permlane16_swap_b32 v92, v94\n"
      "s_nop 0\n"
      "v_permlane16_swap_b32 v93, v95\n"
      "buffer_store_dwordx4 v[92:95], v5, s[20:23], s80 offen offset:1024 sc1\n"
      "s_lshl_b32 s62, s60, 7\n"
      "s_add_u32 s62, s62, 18864128\n"
      "s_waitcnt vmcnt(0)\n"
      "ds_add_rtn_u32 v31, v2, v3 offset:4172\n"
      "s_waitcnt lgkmcnt(0)\n"
      "v_readfirstlane_b32 s2, v31\n"
      "s_cmp_eq_u32 s2, 3\n"
      "s_cselect_b32 s62, s62, 1073741824\n"
      "s_mov_b64 exec, 255\n"
      "buffer_store_dwordx4 v[12:15], v9, s[20:23], s62 offen sc1\n"
      "s_mov_b64 exec, -1\n"
      "ds_read_b32 v31, v2 offset:4308\n"
      "s_waitcnt lgkmcnt(0)\n"
      "v_readfirstlane_b32 s2, v31\n"
      "s_cmp_lt_u32 s2, 4\n"
      "s_cbranch_scc1 .Lw_168\n"
      ".Lw_169:\n"
      "v_and_b32 v24, 1, v17\n"
      "v_mul_u32_u24 v24, 24, v24\n"
      "v_add_u32 v24, v24, v4\n"
      "s_mul_i32 s62, s53, 2560\n"
      "s_add_u32 s63, s73, 32768\n"
      "s_add_u32 s64, s73, 65536\n"
      "s_add_u32 m0, s62, 139520\n"
      "s_mov_b32 s3, 0x80000\n"
      ".Lp_171:\n"
      "s_sleep 1\n"
      "ds_read_b32 v31, v2 offset:4192\n"
      "s_sub_u32 s3, s3, 1\n"
      "s_waitcnt lgkmcnt(0)\n"
      "v_readlane_b32 s2, v31, s53\n"
      "s_cselect_b32 s2, -1, s2\n"
      "s_cmp_lt_u32 s2, 15\n"
      "s_cbranch_scc1 .Lp_171\n"
      "buffer_load_dwordx2 v[26:27], v25, s[20:23], 0 offen sc1\n"
      "s_waitcnt vmcnt(0)\n"
      "v_cmp_ne_u64 vcc, s[58:59], v[26:27]\n"
      "s_cbranch_vccnz .Lf_172\n"
      ".Lf_173:\n"
      "buffer_load_dwordx4 v6, s[20:23], s73 offen sc1 lds\n"
      "buffer_load_dwordx4 v6, s[20:23], s63 offen offset:1024 sc1 lds\n"
      "s_mov_b32 exec_hi, 0\n"
      "buffer_load_dwordx4 v6, s[20:23], s64 offen offset:2048 sc1 lds\n"
      "s_mov_b32 exec_hi, -1\n"
      "s_mov_b32 s3, 0x80000\n"
      ".Lp_175:\n"
      "s_sleep 1\n"
      "ds_read_b32 v31, v2 offset:4192\n"
      "s_sub_u32 s3, s3, 1\n"
      "s_waitcnt lgkmcnt(0)\n"
      "v_readlane_b32 s2, v31, s53\n"
      "s_cselect_b32 s2, -1, s2\n"
      "s_cmp_lt_u32 s2, 20\n"
      "s_cbranch_scc1 .Lp_175\n"
      "buffer_load_dwordx4 v[164:167], v9, s[20:23], s72 offen offset:0 sc1\n"
      "buffer_load_dwordx4 v[168:171], v9, s[20:23], s72 offen offset:1024 sc1\n"
      "buffer_load_dwordx4 v[172:175], v9, s[20:23], s72 offen offset:2048 sc1\n"
      "s_waitcnt vmcnt(3)\n"
      "ds_add_u32 v2, v3 offset:4140\n"
      "s_mov_b32 s3, 0x80000\n"
      ".Lp_176:\n"
      "ds_read_b32 v31, v2 offset:4140\n"
      "s_sub_u32 s3, s3, 1\n"
      "s_waitcnt lgkmcnt(0)\n"
      "v_readlane_b32 s2, v31, 0\n"
      "s_cselect_b32 s2, -1, s2\n"
      "s_cmp_lt_u32 s2, 4\n"
      "s_cbranch_scc1 .Lp_176\n"
      "ds_read_b64_tr_b16 v[218:219], v7 offset:0\n"
      "ds_read_b64_tr_b16 v[220:221], v7 offset:2048\n"
      "ds_read_b64_tr_b16 v[222:223], v7 offset:4096\n"
      "ds_read_b64_tr_b16 v[224:225], v7 offset:6144\n"
      "ds_read_b64_tr_b16 v[226:227], v7 offset:8192\n"
      "s_waitcnt lgkmcnt(3)\n"
      "v_lshlrev_b32 v21, 16, v218\n"
      "v_and_b32 v22, s92, v218\n"
      "v_mul_f32 v52, v52, v21\n"
      "v_mul_f32 v53, v53, v22\n"
      "v_lshlrev_b32 v21, 16, v219\n"
      "v_and_b32 v22, s92, v219\n"
      "v_mul_f32 v54, v54, v21\n"
      "v_mul_f32 v55, v55, v22\n"
      "v_lshlrev_b32 v21, 16, v220\n"
      "v_and_b32 v22, s92, v220\n"
      "v_mul_f32 v56, v56, v21\n"
      "v_mul_f32 v57, v57, v22\n"
      "v_lshlrev_b32 v21, 16, v221\n"
      "v_and_b32 v22, s92, v221\n"
      "v_mul_f32 v58, v58, v21\n"
      "v_mul_f32 v59, v59, v22\n"
      "s_waitcnt lgkmcnt(0)\n"
      "v_lshlrev_b32 v21, 16, v222\n"
      "v_and_b32 v22, s92, v222\n"
      "v_mul_f32 v60, v60, v21\n"
      "v_mul_f32 v61, v61, v22\n"
      "v_lshlrev_b32 v21, 16, v223\n"
      "v_and_b32 v22, s92, v223\n"
      "v_mul_f32 v62, v62, v21\n"
      "v_mul_f32 v63, v63, v22\n"
      "v_lshlrev_b32 v21, 16, v224\n"
      "v_and_b32 v22, s92, v224\n"
      "v_mul_f32 v64, v64, v21\n"
      "v_mul_f32 v65, v65, v22\n"
      "v_lshlrev_b32 v21, 16, v225\n"
      "v_and_b32 v22, s92, v225\n"
      "v_mul_f32 v66, v66, v21\n"
      "v_mul_f32 v67, v67, v22\n"
      "v_lshlrev_b32 v21, 16, v226\n"
      "v_and_b32 v22, s92, v226\n"
      "v_mul_f32 v68, v68, v21\n"
      "v_mul_f32 v69, v69, v22\n"
      "v_lshlrev_b32 v21, 16, v227\n"
      "v_and_b32 v22, s92, v227\n"
      "v_mul_f32 v70, v70, v21\n"
      "v_mul_f32 v71, v71, v22\n"
      "s_waitcnt vmcnt(0)\n"
      "s_waitcnt vmcnt(63)\n"
      "v_xor_b32 v21, s74, v167\n"
      "v_bitop3_b32 v21, v21, v171, s74 bitop3:0xf6\n"
      "v_bitop3_b32 v21, v21, v175, s74 bitop3:0xf6\n"
      "v_cmp_lt_u32 vcc, 0xffff, v21\n"
      "s_cbranch_vccnz .Ly_177\n"
      ".Ly_178:\n"
      "v_lshlrev_b32 v144, 16, v164\n"
      "v_and_b32 v145, s92, v164\n"
      "v_lshlrev_b32 v146, 16, v165\n"
      "v_and_b32 v147, s92, v165\n"
      "v_lshlrev_b32 v148, 16, v166\n"
      "v_and_b32 v149, s92, v166\n"
      "v_lshlrev_b32 v150, 16, v167\n"
      "v_lshlrev_b32 v151, 16, v168\n"
      "v_and_b32 v152, s92, v168\n"
      "v_lshlrev_b32 v153, 16, v169\n"
      "v_and_b32 v154, s92, v169\n"
      "v_lshlrev_b32 v155, 16, v170\n"
      "v_and_b32 v156, s92, v170\n"
      "v_lshlrev_b32 v157, 16, v171\n"
      "v_lshlrev_b32 v158, 16, v172\n"
      "v_and_b32 v159, s92, v172\n"
      "v_lshlrev_b32 v160, 16, v173\n"
      "v_and_b32 v161, s92, v173\n"
      "v_lshlrev_b32 v162, 16, v174\n"
      "v_and_b32 v163, s92, v174\n"
      "s_mov_b32 s77, s76\n"
      "v_pk_mul_f32 v[144:145], v[144:145], s[76:77]\n"
      "v_pk_mul_f32 v[146:147], v[146:147], s[76:77]\n"
      "v_pk_mul_f32 v[148:149], v[148:149], s[76:77]\n"
      "v_pk_mul_f32 v[150:151], v[150:151], s[76:77]\n"
      "v_pk_mul_f32 v[152:153], v[152:153], s[76:77]\n"
      "v_pk_mul_f32 v[154:155], v[154:155], s[76:77]\n"
      "v_pk_mul_f32 v[156:157], v[156:157], s[76:77]\n"
      "v_pk_mul_f32 v[158:159], v[158:159], s[76:77]\n"
      "v_pk_mul_f32 v[160:161], v[160:161], s[76:77]\n"
      "v_pk_mul_f32 v[162:163], v[162:163], s[76:77]\n"
      "v_mov_b32 v22, 1.0\n"
      "v_mov_b32 v23, 1.0\n"
      "s_mov_b32 s3, 0x80000\n"
      ".Lp_180:\n"
      "ds_read_b32 v31, v2 offset:4192\n"
      "s_sub_u32 s3, s3, 1\n"
      "s_waitcnt lgkmcnt(0)\n"
      "v_readlane_b32 s2, v31, s53\n"
      "s_cselect_b32 s2, -1, s2\n"
      "s_cmp_lt_u32 s2, 32\n"
      "s_cbranch_scc1 .Lp_180\n"
      "ds_read_b128 v[176:179], v8 offset:0\n"
      "ds_read_b128 v[180:183], v8 offset:1024\n"
      "s_waitcnt lgkmcnt(1)\n"
      "v_pk_fma_f32 v[176:177], v[176:177], s[76:77], v[144:145]\n"
      "v_pk_fma_f32 v[178:179], v[178:179], s[76:77], v[146:147]\n"
      "s_waitcnt lgkmcnt(0)\n"
      "v_pk_fma_f32 v[180:181], v[180:181], s[76:77], v[148:149]\n"
      "v_pk_fma_f32 v[182:183], v[182:183], s[76:77], v[150:151]\n"
      "v_exp_f32 v196, v176\n"
      "v_exp_f32 v197, v177\n"
      "v_exp_f32 v198, v178\n"
      "v_exp_f32 v199, v179\n"
      "v_exp_f32 v200, v180\n"
      "v_exp_f32 v201, v181\n"
      "v_exp_f32 v202, v182\n"
      "v_exp_f32 v203, v183\n"
      "v_pk_mul_f32 v[52:53], v[52:53], v[176:177]\n"
      "v_pk_mul_f32 v[54:55], v[54:55], v[178:179]\n"
      "v_pk_mul_f32 v[56:57], v[56:57], v[180:181]\n"
      "v_pk_mul_f32 v[58:59], v[58:59], v[182:183]\n"
      "v_pk_add_f32 v[196:197], v[196:197], v[22:23]\n"
      "v_pk_add_f32 v[198:199], v[198:199], v[22:23]\n"
      "v_pk_add_f32 v[200:201], v[200:201], v[22:23]\n"
      "v_pk_add_f32 v[202:203], v[202:203], v[22:23]\n"
      "v_rcp_f32 v196, v196\n"
      "v_rcp_f32 v197, v197\n"
      "v_rcp_f32 v198, v198\n"
      "v_rcp_f32 v199, v199\n"
      "v_rcp_f32 v200, v200\n"
      "v_rcp_f32 v201, v201\n"
      "v_rcp_f32 v202, v202\n"
      "v_rcp_f32 v203, v203\n"
      "v_pk_fma_f32 v[52:53], v[52:53], v[196:197], v[32:33]\n"
      "v_pk_fma_f32 v[54:55], v[54:55], v[198:199], v[34:35]\n"
      "v_pk_fma_f32 v[56:57], v[56:57], v[200:201], v[36:37]\n"
      "v_pk_fma_f32 v[58:59], v[58:59], v[202:203], v[38:39]\n"
      "v_cvt_pk_bf16_f32 v218, v52, v53\n"
      "v_cvt_pk_bf16_f32 v219, v54, v55\n"
      "v_cvt_pk_bf16_f32 v220, v56, v57\n"
      "v_cvt_pk_bf16_f32 v221, v58, v59\n"
      "s_nop 0\n"
      "v_permlane16_swap_b32 v218, v220\n"
      "v_permlane16_swap_b32 v219, v221\n"
      "buffer_store_dwordx4 v[218:221], v24, s[48:51], s70 offen offset:0\n"
      "s_mov_b32 s3, 0x80000\n"
      ".Lp_181:\n"
      "ds_read_b32 v31, v2 offset:4192\n"
      "s_sub_u32 s3, s3, 1\n"
      "s_waitcnt lgkmcnt(0)\n"
      "v_readlane_b32 s2, v31, s53\n"
      "s_cselect_b32 s2, -1, s2\n"
      "s_cmp_lt_u32 s2, 34\n"
      "s_cbranch_scc1 .Lp_181\n"
      "ds_read_b128 v[184:187], v8 offset:2048\n"
      "ds_read_b128 v[188:191], v8 offset:3072\n"
      "s_waitcnt lgkmcnt(1)\n"
      "v_pk_fma_f32 v[184:185], v[184:185], s[76:77], v[152:153]\n"
      "v_pk_fma_f32 v[186:187], v[186:187], s[76:77], v[154:155]\n"
      "s_waitcnt lgkmcnt(0)\n"
      "v_pk_fma_f32 v[188:189], v[188:189], s[76:77], v[156:157]\n"
      "v_pk_fma_f32 v[190:191], v[190:191], s[76:77], v[158:159]\n"
      "v_exp_f32 v204, v184\n"
      "v_exp_f32 v205, v185\n"
      "v_exp_f32 v206, v186\n"
      "v_exp_f32 v207, v187\n"
      "v_exp_f32 v208, v188\n"
      "v_exp_f32 v209, v189\n"
      "v_exp_f32 v210, v190\n"
      "v_exp_f32 v211, v191\n"
      "v_pk_mul_f32 v[60:61], v[60:61], v[184:185]\n"
      "v_pk_mul_f32 v[62:63], v[62:63], v[186:187]\n"
      "v_pk_mul_f32 v[64:65], v[64:65], v[188:189]\n"
      "v_pk_mul_f32 v[66:67], v[66:67], v[190:191]\n"
      "v_pk_add_f32 v[204:205], v[204:205], v[22:23]\n"
      "v_pk_add_f32 v[206:207], v[206:207], v[22:23]\n"
      "v_pk_add_f32 v[208:209], v[208:209], v[22:23]\n"
      "v_pk_add_f32 v[210:211], v[210:211], v[22:23]\n"
      "v_rcp_f32 v204, v204\n"
      "v_rcp_f32 v205, v205\n"
      "v_rcp_f32 v206, v206\n"
      "v_rcp_f32 v207, v207\n"
      "v_rcp_f32 v208, v208\n"
      "v_rcp_f32 v209, v209\n"
      "v_rcp_f32 v210, v210\n"
      "v_rcp_f32 v211, v211\n"
      "v_pk_fma_f32 v[60:61], v[60:61], v[204:205], v[40:41]\n"
      "v_pk_fma_f32 v[62:63], v[62:63], v[206:207], v[42:43]\n"
      "v_pk_fma_f32 v[64:65], v[64:65], v[208:209], v[44:45]\n"
      "v_pk_fma_f32 v[66:67], v[66:67], v[210:211], v[46:47]\n"
      "v_cvt_pk_bf16_f32 v222, v60, v61\n"
      "v_cvt_pk_bf16_f32 v223, v62, v63\n"
      "v_cvt_pk_bf16_f32 v224, v64, v65\n"
      "v_cvt_pk_bf16_f32 v225, v66, v67\n"
      "s_nop 0\n"
      "v_permlane16_swap_b32 v222, v224\n"
      "v_permlane16_swap_b32 v223, v225\n"
      "buffer_store_dwordx4 v[222:225], v24, s[48:51], s70 offen offset:64\n"
      "s_mov_b32 s3, 0x80000\n"
      ".Lp_182:\n"
      "ds_read_b32 v31, v2 offset:4192\n"
      "s_sub_u32 s3, s3, 1\n"
      "s_waitcnt lgkmcnt(0)\n"
      "v_readlane_b32 s2, v31, s53\n"
      "s_cselect_b32 s2, -1, s2\n"
      "s_cmp_lt_u32 s2, 35\n"
      "s_cbranch_scc1 .Lp_182\n"
      "ds_read_b128 v[192:195], v8 offset:4096\n"
      "s_waitcnt lgkmcnt(0)\n"
      "v_pk_fma_f32 v[192:193], v[192:193], s[76:77], v[160:161]\n"
      "v_pk_fma_f32 v[194:195], v[194:195], s[76:77], v[162:163]\n"
      "v_exp_f32 v212, v192\n"
      "v_exp_f32 v213, v193\n"
      "v_exp_f32 v214, v194\n"
      "v_exp_f32 v215, v195\n"
      "v_pk_mul_f32 v[68:69], v[68:69], v[192:193]\n"
      "v_pk_mul_f32 v[70:71], v[70:71], v[194:195]\n"
      "v_pk_add_f32 v[212:213], v[212:213], v[22:23]\n"
      "v_pk_add_f32 v[214:215], v[214:215], v[22:23]\n"
      "v_rcp_f32 v212, v212\n"
      "v_rcp_f32 v213, v213\n"
      "v_rcp_f32 v214, v214\n"
      "v_rcp_f32 v215, v215\n"
      "v_pk_fma_f32 v[68:69], v[68:69], v[212:213], v[48:49]\n"
      "v_pk_fma_f32 v[70:71], v[70:71], v[214:215], v[50:51]\n"
      "v_cvt_pk_bf16_f32 v226, v68, v69\n"
      "v_cvt_pk_bf16_f32 v227, v70, v71\n"
      "buffer_store_dwordx2 v[226:227], v4, s[48:51], s70 offen offset:128\n"
      "s_endpgm\n"
      ".LSPARE:\n"
      "s_and_b32 s70, s60, 7\n"
      "s_lshl2_add_u32 s70, s70, s61\n"
      "s_sub_u32 s70, s70, 28\n"
      "s_xor_b32 s71, s52, 4\n"
      "s_lshl_b32 s71, s71, 5\n"
      "s_add_u32 s71, s71, s70\n"
      "s_add_u32 s71, s71, 224\n"
      "v_lshrrev_b32 v20, 4, v1\n"
      "v_mul_u32_u24 v20, 1280, v20\n"
      "v_and_b32 v21, 15, v1\n"
      "v_lshl_add_u32 v20, v21, 4, v20\n"
      "v_add_u32 v21, 4080, v20\n"
      "v_add_u32 v22, 8160, v20\n"
      "v_add_u32 v23, 12240, v20\n"
      "s_mul_i32 s72, s71, 205\n"
      "s_lshr_b32 s72, s72, 10\n"
      "s_mul_i32 s73, s72, 5\n"
      "s_sub_u32 s73, s71, s73\n"
      "s_mul_i32 s74, s53, 8320\n"
      "s_add_u32 s74, s74, 53248\n"
      "s_cmp_lt_u32 s52, 4\n"
      "s_cbranch_scc1 .Ls_183\n"
      "s_waitcnt lgkmcnt(0)\n"
      "s_barrier\n"
      "buffer_load_dwordx2 v[42:43], off, s[20:23], 0 sc1\n"
      "s_cmp_lt_u32 s71, 270\n"
      "s_cbranch_scc0 .Ls_183\n"
      "s_mul_i32 s62, s72, 40960\n"
      "s_lshl_b32 s63, s73, 8\n"
      "s_add_u32 s62, s62, s63\n"
      "s_mov_b32 m0, s74\n"
      "s_add_u32 s63, s62, 20480\n"
      "buffer_load_dwordx4 v20, s[40:43], s62 offen offset:0 nt lds\n"
      "buffer_load_dwordx4 v21, s[40:43], s62 offen offset:1040 nt lds\n"
      "buffer_load_dwordx4 v22, s[40:43], s62 offen offset:2080 nt lds\n"
      "buffer_load_dwordx4 v23, s[40:43], s62 offen offset:3120 nt lds\n"
      "s_add_u32 m0, s74, 4160\n"
      "s_nop 0\n"
      "buffer_load_dwordx4 v20, s[40:43], s63 offen offset:0 nt lds\n"
      "buffer_load_dwordx4 v21, s[40:43], s63 offen offset:1040 nt lds\n"
      "buffer_load_dwordx4 v22, s[40:43], s63 offen offset:2080 nt lds\n"
      "buffer_load_dwordx4 v23, s[40:43], s63 offen offset:3120 nt lds\n"
      ".Ls_183:\n"
      "v_lshrrev_b32 v24, 3, v1\n"
      "v_mul_u32_u24 v25, 1280, v24\n"
      "v_and_b32 v26, 7, v1\n"
      "v_lshl_add_u32 v25, v26, 4, v25\n"
      "v_cmp_gt_u32 vcc, 40, v1\n"
      "v_mov_b32 v26, 1073741824\n"
      "v_lshlrev_b32 v24, 4, v24\n"
      "v_cndmask_b32 v26, v25, v26, vcc\n"
      "s_mul_i32 s62, s70, 3\n"
      "s_add_u32 s62, s62, 224\n"
      "s_mul_i32 s64, s52, 1040\n"
      "s_mul_i32 s65, s52, 10240\n"
      "s_mul_i32 s80, s62, 205\n"
      "s_lshr_b32 s80, s80, 11\n"
      "s_mul_i32 s81, s80, 10\n"
      "s_sub_u32 s81, s62, s81\n"
      "s_mul_i32 s86, s80, 81920\n"
      "s_lshl_b32 s63, s81, 7\n"
      "s_add_u32 s86, s86, s63\n"
      "s_add_u32 m0, s64, 1040\n"
      "s_add_u32 s63, s86, s65\n"
      "buffer_load_dwordx4 v25, s[12:15], s63 offen nt lds\n"
      "s_add_u32 m0, s64, 9360\n"
      "s_add_u32 s62, s62, 1\n"
      "buffer_load_dwordx4 v25, s[16:19], s63 offen nt lds\n"
      "s_mul_i32 s82, s62, 205\n"
      "s_lshr_b32 s82, s82, 11\n"
      "s_mul_i32 s83, s82, 10\n"
      "s_sub_u32 s83, s62, s83\n"
      "s_mul_i32 s87, s82, 81920\n"
      "s_lshl_b32 s63, s83, 7\n"
      "s_add_u32 s87, s87, s63\n"
      "s_add_u32 m0, s64, 18720\n"
      "s_add_u32 s63, s87, s65\n"
      "buffer_load_dwordx4 v25, s[12:15], s63 offen nt lds\n"
      "s_add_u32 m0, s64, 27040\n"
      "s_add_u32 s62, s62, 1\n"
      "buffer_load_dwordx4 v25, s[16:19], s63 offen nt lds\n"
      "s_mul_i32 s84, s62, 205\n"
      "s_lshr_b32 s84, s84, 11\n"
      "s_mul_i32 s85, s84, 10\n"
      "s_sub_u32 s85, s62, s85\n"
      "s_mul_i32 s88, s84, 81920\n"
      "s_lshl_b32 s63, s85, 7\n"
      "s_add_u32 s88, s88, s63\n"
      "s_add_u32 m0, s64, 36400\n"
      "s_add_u32 s63, s88, s65\n"
      "buffer_load_dwordx4 v25, s[12:15], s63 offen nt lds\n"
      "s_add_u32 m0, s64, 44720\n"
      "s_add_u32 s62, s62, 1\n"
      "buffer_load_dwordx4 v25, s[16:19], s63 offen nt lds\n"
      "s_cmp_eq_u32 s52, 0\n"
      "s_cselect_b32 s62, s86, s88\n"
      "s_cselect_b32 s63, 0, 35360\n"
      "s_cmp_eq_u32 s52, 1\n"
      "s_cselect_b32 s62, s87, s62\n"
      "s_cselect_b32 m0, 17680, s63\n"
      "s_sub_u32 s62, s62, 10240\n"
      "buffer_load_dwordx4 v26, s[12:15], s62 offen nt lds\n"
      "s_cmp_lt_u32 s52, 4\n"
      "s_cselect_b32 s80, s80, s82\n"
      "s_cselect_b32 s81, s81, s83\n"
      "s_cselect_b32 s89, 0, 17680\n"
      "s_lshl2_add_u32 s81, s81, s53\n"
      "s_lshl2_add_u32 s85, s85, s53\n"
      "s_lshl_b32 s62, s81, 7\n"
      "s_lshl_b32 s63, s85, 7\n"
      "buffer_load_dwordx4 v[176:179], v24, s[24:27], s62 offen\n"
      "buffer_load_dwordx4 v[180:183], v24, s[24:27], s63 offen\n"
      "v_mov_b64 v[36:37], 0\n"
      "v_mov_b64 v[38:39], 0\n"
      "s_lshl_b32 s63, s53, 10\n"
      "s_add_u32 s63, s63, 157696\n"
      "v_lshl_add_u32 v40, v1, 4, s63\n"
      "ds_write_b128 v40, v[36:39]\n"
      "v_lshlrev_b32 v40, 2, v1\n"
      "v_add_u32 v40, 161792, v40\n"
      "ds_write_b32 v40, v36\n"
      "v_lshrrev_b32 v20, 4, v1\n"
      "v_and_b32 v21, 1, v20\n"
      "v_lshrrev_b32 v20, 1, v20\n"
      "v_lshl_add_u32 v20, v21, 2, v20\n"
      "v_mul_u32_u24 v20, 1040, v20\n"
      "v_bfe_u32 v21, v1, 2, 2\n"
      "v_lshl_add_u32 v20, v21, 8, v20\n"
      "v_and_b32 v21, 3, v1\n"
      "v_lshl_add_u32 v20, v21, 3, v20\n"
      "v_lshlrev_b32 v22, 4, v1\n"
      "s_lshl3_add_u32 s62, s72, s73\n"
      "s_lshl_b32 s62, s62, 7\n"
      "s_add_u32 s62, s62, 18808832\n"
      "s_cmp_lt_u32 s71, 270\n"
      "s_cselect_b32 s62, s62, 1073741824\n"
      "v_mov_b32 v23, 1073741824\n"
      "s_mov_b32 exec_lo, 0xffff\n"
      "s_mov_b32 exec_hi, 0\n"
      "v_lshl_add_u32 v23, v1, 3, s62\n"
      "s_mov_b64 exec, -1\n"
      "v_add_u32 v21, s74, v20\n"
      "s_lshr_b32 s63, s72, 2\n"
      "s_and_b32 s62, s72, 3\n"
      "s_mul_i32 s62, s62, 7\n"
      "s_add_u32 s62, s62, s63\n"
      "s_cmp_ge_u32 s63, 7\n"
      "s_cselect_b32 s63, 21, 0\n"
      "s_add_u32 s62, s62, s63\n"
      "s_lshl_b32 s62, s62, 10\n"
      "s_mul_i32 s63, s73, 458752\n"
      "s_add_u32 s62, s62, s63\n"
      "s_add_u32 s62, s62, 5603328\n"
      "s_waitcnt vmcnt(9)\n"
      "v_cmp_eq_u32 vcc, 0, v1\n"
      "v_lshlrev_b32 v2, 2, v1\n"
      "v_add_u32 v2, 157696, v2\n"
      "v_cndmask_b32 v3, 0, 1, vcc\n"
      "v_readfirstlane_b32 s58, v42\n"
      "v_readfirstlane_b32 s59, v43\n"
      "s_add_u32 s58, s58, 1\n"
      "s_addc_u32 s59, s59, 0\n"
      "v_mov_b64 v[28:29], s[58:59]\n"
      "s_cmp_lt_u32 s71, 270\n"
      "s_cbranch_scc0 .Ls_184\n"
      "ds_read_b64_tr_b16 v[96:97], v21 offset:0\n"
      "ds_read_b64_tr_b16 v[98:99], v21 offset:2080\n"
      "ds_read_b64_tr_b16 v[100:101], v21 offset:32\n"
      "ds_read_b64_tr_b16 v[102:103], v21 offset:2112\n"
      "ds_read_b64_tr_b16 v[104:105], v21 offset:64\n"
      "ds_read_b64_tr_b16 v[106:107], v21 offset:2144\n"
      "ds_read_b64_tr_b16 v[108:109], v21 offset:96\n"
      "ds_read_b64_tr_b16 v[110:111], v21 offset:2176\n"
      "s_waitcnt lgkmcnt(6)\n"
      "ds_read_b64_tr_b16 v[112:113], v21 offset:128\n"
      "ds_read_b64_tr_b16 v[114:115], v21 offset:2208\n"
      "ds_read_b64_tr_b16 v[116:117], v21 offset:160\n"
      "ds_read_b64_tr_b16 v[118:119], v21 offset:2240\n"
      "ds_read_b64_tr_b16 v[120:121], v21 offset:192\n"
      "ds_read_b64_tr_b16 v[122:123], v21 offset:2272\n"
      "ds_read_b64_tr_b16 v[124:125], v21 offset:224\n"
      "ds_read_b64_tr_b16 v[126:127], v21 offset:2304\n"
      "s_waitcnt lgkmcnt(14)\n"
      "buffer_store_dwordx4 v[96:99], v22, s[20:23], s62 offen sc1\n"
      "s_add_u32 s62, s62, 57344\n"
      "s_waitcnt lgkmcnt(12)\n"
      "buffer_store_dwordx4 v[100:103], v22, s[20:23], s62 offen sc1\n"
      "s_add_u32 s62, s62, 57344\n"
      "s_waitcnt lgkmcnt(10)\n"
      "buffer_store_dwordx4 v[104:107], v22, s[20:23], s62 offen sc1\n"
      "s_add_u32 s62, s62, 57344\n"
      "s_waitcnt lgkmcnt(8)\n"
      "buffer_store_dwordx4 v[108:111], v22, s[20:23], s62 offen sc1\n"
      "s_add_u32 s62, s62, 57344\n"
      "s_waitcnt lgkmcnt(6)\n"
      "buffer_store_dwordx4 v[112:115], v22, s[20:23], s62 offen sc1\n"
      "s_add_u32 s62, s62, 57344\n"
      "s_waitcnt lgkmcnt(4)\n"
      "buffer_store_dwordx4 v[116:119], v22, s[20:23], s62 offen sc1\n"
      "s_add_u32 s62, s62, 57344\n"
      "s_waitcnt lgkmcnt(2)\n"
      "buffer_store_dwordx4 v[120:123], v22, s[20:23], s62 offen sc1\n"
      "s_add_u32 s62, s62, 57344\n"
      "s_waitcnt lgkmcnt(0)\n"
      "buffer_store_dwordx4 v[124:127], v22, s[20:23], s62 offen sc1\n"
      "s_waitcnt vmcnt(8)\n"
      "s_branch .Ls_185\n"
      ".Ls_184:\n"
      "s_waitcnt vmcnt(0)\n"
      ".Ls_185:\n"
      "s_cmp_lt_u32 s52, 4\n"
      "s_cbranch_scc1 .Ls_186\n"
      "s_lshl_b32 s94, s53, 12\n"
      "s_lshl_b32 s95, s53, 10\n"
      "v_lshlrev_b32 v5, 4, v1\n"
      "v_add_u32 v6, 4096, v5\n"
      "s_lshl_b32 s24, s98, 12\n"
      "s_add_u32 s24, s24, s95\n"
      "s_add_u32 s25, s24, 4096\n"
      "v_mov_b32 v32, s25\n"
      "v_writelane_b32 v32, s24, 0\n"
      "s_cmp_eq_u32 s53, 0\n"
      "s_cselect_b32 s63, 1073741824, -4\n"
      "v_add_u32 v32, s63, v32\n"
      "v_lshrrev_b32 v33, 11, v32\n"
      "v_xor_b32 v33, 524288, v33\n"
      "s_sub_u32 s91, s98, 4\n"
      "s_lshr_b32 s91, s91, 4\n"
      "s_lshr_b32 s63, s98, 4\n"
      "s_sub_u32 s63, s63, s91\n"
      "s_lshl_b32 s63, s63, 9\n"
      "v_and_b32 v72, 8, v1\n"
      "v_mul_u32_u24 v72, s63, v72\n"
      "v_and_b32 v73, 7, v1\n"
      "v_lshl_add_u32 v30, v73, 7, v72\n"
      "s_lshl_b32 s91, s91, 12\n"
      "s_add_u32 s91, s91, s95\n"
      "s_add_u32 s91, s91, 18644992\n"
      "v_add_u32 v30, s91, v30\n"
      "s_and_b32 s63, s53, 1\n"
      "s_mul_i32 s63, s63, 4608\n"
      "s_add_u32 s63, s63, 139520\n"
      "v_lshl_add_u32 v34, v1, 3, s63\n"
      "v_lshrrev_b32 v72, 1, v1\n"
      "v_min_u32 v72, 6, v72\n"
      "s_mul_i32 s62, s54, 28\n"
      "s_add_u32 s62, s62, s53\n"
      "v_lshl_add_u32 v72, v72, 2, s62\n"
      "v_min_u32 v72, 53, v72\n"
      "s_lshl_b32 s63, s56, 14\n"
      "s_add_u32 s63, s63, 18382848\n"
      "v_lshl_add_u32 v13, v72, 8, s63\n"
      "s_mul_i32 s63, s55, 80\n"
      "s_add_u32 s91, s63, 79\n"
      "s_lshr_b32 s63, s63, 7\n"
      "s_lshr_b32 s91, s91, 7\n"
      "s_sub_u32 s91, s91, s63\n"
      "v_and_b32 v73, s91, v1\n"
      "v_lshl_add_u32 v72, v72, 3, v73\n"
      "s_lshl_b32 s63, s63, 7\n"
      "s_add_u32 s63, s63, 18808832\n"
      "v_lshl_add_u32 v31, v72, 7, s63\n"
      "v_lshrrev_b32 v72, 3, v1\n"
      "v_and_b32 v72, 3, v72\n"
      "v_mul_u32_u24 v73, 4160, v72\n"
      "v_add_u32 v73, 122880, v73\n"
      "v_and_b32 v72, 7, v1\n"
      "v_lshl_add_u32 v35, v72, 3, v73\n"
      "s_add_u32 s62, s95, 122944\n"
      "v_add_u32 v40, s62, v5\n"
      "v_and_b32 v72, 31, v1\n"
      "v_lshlrev_b32 v42, 3, v72\n"
      "s_add_u32 s62, s95, 157696\n"
      "v_lshl_add_u32 v43, v72, 5, s62\n"
      "s_xor_b32 s31, s99, s53\n"
      "s_lshl2_add_u32 s31, s56, s31\n"
      "s_lshl_b32 s31, s31, 8\n"
      "s_add_u32 s31, s31, 8192\n"
      "s_lshl2_add_u32 s62, s54, s53\n"
      "s_cmp_ge_u32 s62, 6\n"
      "s_cselect_b32 s63, 1073741824, 6144\n"
      "v_add_u32 v7, s63, v5\n"
      "s_mul_i32 s62, s62, 7168\n"
      "s_mul_i32 s63, s55, 286720\n"
      "s_add_u32 s63, s63, s62\n"
      "s_add_u32 s27, s63, 5603328\n"
      "s_mul_i32 s63, s56, 458752\n"
      "s_add_u32 s63, s63, s62\n"
      "s_add_u32 s63, s63, 7897088\n"
      "s_mul_i32 s62, s54, 229376\n"
      "s_add_u32 s91, s63, s62\n"
      "s_sub_u32 s62, 229376, s62\n"
      "s_add_u32 s93, s63, s62\n"
      "s_sub_u32 s62, 4, s53\n"
      "s_and_b32 s62, s62, 3\n"
      "s_mul_i32 s62, s62, 57344\n"
      "s_add_u32 s44, s93, s62\n"
      "s_add_u32 s48, s91, s62\n"
      "s_sub_u32 s62, 5, s53\n"
      "s_and_b32 s62, s62, 3\n"
      "s_mul_i32 s62, s62, 57344\n"
      "s_add_u32 s45, s93, s62\n"
      "s_add_u32 s49, s91, s62\n"
      "s_sub_u32 s62, 6, s53\n"
      "s_and_b32 s62, s62, 3\n"
      "s_mul_i32 s62, s62, 57344\n"
      "s_add_u32 s46, s93, s62\n"
      "s_add_u32 s50, s91, s62\n"
      "s_sub_u32 s62, 7, s53\n"
      "s_and_b32 s62, s62, 3\n"
      "s_mul_i32 s62, s62, 57344\n"
      "s_add_u32 s47, s93, s62\n"
      "s_add_u32 s51, s91, s62\n"
      "s_add_u32 s91, s53, 3\n"
      "s_and_b32 s91, s91, 3\n"
      "s_lshl2_add_u32 s62, s91, 161888\n"
      "v_mov_b32 v15, s62\n"
      "s_mul_i32 s62, s53, 5120\n"
      "s_add_u32 s63, s62, 32768\n"
      "v_add_u32 v8, s63, v5\n"
      "s_mul_i32 s63, s91, 5120\n"
      "s_add_u32 s63, s63, 32768\n"
      "v_add_u32 v9, s63, v5\n"
      "s_xor_b32 s63, s60, 1\n"
      "s_lshl2_add_u32 s63, s63, s53\n"
      "s_mul_i32 s28, s63, 3072\n"
      "s_add_u32 s28, s28, 15237120\n"
      "v_cmp_eq_u32 vcc, s53, v1\n"
      "v_and_b32 v14, 48, v1\n"
      "v_add_u32 v14, 163072, v14\n"
      "v_cndmask_b32 v55, 0, 1, vcc\n"
      "buffer_load_dwordx2 v[76:77], v42, s[20:23], s31 offen sc1\n"
      "s_sub_u32 s62, s24, 16384\n"
      "buffer_load_dwordx4 v[140:143], v5, s[36:39], s62 offen nt\n"
      "s_sub_u32 s62, s25, 16384\n"
      "buffer_load_dwordx4 v[144:147], v5, s[36:39], s62 offen nt\n"
      "v_subrev_u32 v72, 16384, v32\n"
      "v_subrev_u32 v73, 8, v33\n"
      "buffer_load_dword v132, v72, s[36:39], 0 offen nt\n"
      "buffer_load_ushort v133, v73, s[32:35], 0 offen\n"
      "s_sub_u32 s62, s24, 8192\n"
      "buffer_load_dwordx4 v[148:151], v5, s[36:39], s62 offen nt\n"
      "s_sub_u32 s62, s25, 8192\n"
      "buffer_load_dwordx4 v[152:155], v5, s[36:39], s62 offen nt\n"
      "v_subrev_u32 v72, 8192, v32\n"
      "v_subrev_u32 v73, 4, v33\n"
      "buffer_load_dword v134, v72, s[36:39], 0 offen nt\n"
      "buffer_load_ushort v135, v73, s[32:35], 0 offen\n"
      "buffer_load_dwordx4 v[56:59], v5, s[36:39], s24 offen nt\n"
      "buffer_load_dwordx4 v[60:63], v5, s[36:39], s25 offen nt\n"
      "buffer_load_dword v52, v32, s[36:39], 0 offen nt\n"
      "buffer_load_ushort v53, v33, s[32:35], 0 offen\n"
      ".Ls_186:\n"
      "s_lshl_b32 s62, s53, 5\n"
      "s_add_u32 s62, s62, s89\n"
      "s_add_u32 s62, s62, 640\n"
      "v_and_b32 v68, 7, v1\n"
      "v_lshrrev_b32 v69, 3, v1\n"
      "v_mul_u32_u24 v192, 1040, v68\n"
      "v_lshl_add_u32 v192, v69, 2, v192\n"
      "v_add_u32 v192, s62, v192\n"
      "v_add_u32 v193, 8720, v192\n"
      "v_mul_u32_u24 v194, 8448, v69\n"
      "v_lshl_add_u32 v194, v68, 4, v194\n"
      "v_lshlrev_b32 v184, 16, v176\n"
      "v_lshlrev_b32 v185, 16, v178\n"
      "v_and_b32 v186, s92, v176\n"
      "v_and_b32 v187, s92, v178\n"
      "v_lshlrev_b32 v188, 16, v177\n"
      "v_lshlrev_b32 v189, 16, v179\n"
      "v_and_b32 v190, s92, v177\n"
      "v_and_b32 v191, s92, v179\n"
      "s_mul_i32 s62, s81, 67584\n"
      "s_lshl_b32 s63, s80, 7\n"
      "s_add_u32 s62, s62, s63\n"
      "s_add_u32 s62, s62, 32768\n"
      "s_add_u32 s63, s62, 4224\n"
      "s_mov_b32 s64, 0\n"
      "s_waitcnt lgkmcnt(0)\n"
      "s_barrier\n"
      ".LSKV:\n"
      "ds_read2_b32 v[196:197], v192 offset0:0 offset1:32\n"
      "ds_read2_b32 v[198:199], v192 offset0:64 offset1:100\n"
      "ds_read2_b32 v[200:201], v192 offset0:132 offset1:164\n"
      "ds_read2_b32 v[202:203], v192 offset0:196 offset1:228\n"
      "ds_read_b32 v204, v192 offset:1040\n"
      "ds_read_b32 v205, v192 offset:1168\n"
      "ds_read_b32 v206, v192 offset:1296\n"
      "ds_read2_b32 v[208:209], v193 offset0:0 offset1:32\n"
      "ds_read2_b32 v[210:211], v193 offset0:64 offset1:96\n"
      "ds_read2_b32 v[212:213], v193 offset0:128 offset1:160\n"
      "ds_read2_b32 v[214:215], v193 offset0:192 offset1:224\n"
      "s_waitcnt lgkmcnt(9)\n"
      "v_lshlrev_b32 v216, 16, v196\n"
      "v_and_b32 v217, s92, v196\n"
      "v_lshlrev_b32 v218, 16, v197\n"
      "v_and_b32 v219, s92, v197\n"
      "v_lshlrev_b32 v220, 16, v198\n"
      "v_and_b32 v221, s92, v198\n"
      "v_lshlrev_b32 v222, 16, v199\n"
      "v_and_b32 v223, s92, v199\n"
      "s_waitcnt lgkmcnt(7)\n"
      "v_lshlrev_b32 v224, 16, v200\n"
      "v_and_b32 v225, s92, v200\n"
      "v_lshlrev_b32 v226, 16, v201\n"
      "v_and_b32 v227, s92, v201\n"
      "v_lshlrev_b32 v228, 16, v202\n"
      "v_and_b32 v229, s92, v202\n"
      "v_lshlrev_b32 v230, 16, v203\n"
      "v_and_b32 v231, s92, v203\n"
      "s_waitcnt lgkmcnt(4)\n"
      "v_lshlrev_b32 v232, 16, v204\n"
      "v_and_b32 v233, s92, v204\n"
      "v_lshlrev_b32 v234, 16, v205\n"
      "v_and_b32 v235, s92, v205\n"
      "v_lshlrev_b32 v236, 16, v206\n"
      "v_and_b32 v237, s92, v206\n"
      "v_pk_mul_f32 v[240:241], v[216:217], v[190:191]\n"
      "v_pk_mul_f32 v[242:243], v[218:219], v[190:191]\n"
      "v_pk_mul_f32 v[244:245], v[220:221], v[190:191]\n"
      "v_pk_mul_f32 v[246:247], v[222:223], v[190:191]\n"
      "v_pk_mul_f32 v[248:249], v[224:225], v[190:191]\n"
      "v_pk_mul_f32 v[250:251], v[226:227], v[190:191]\n"
      "v_pk_mul_f32 v[252:253], v[228:229], v[190:191]\n"
      "v_pk_mul_f32 v[254:255], v[230:231], v[190:191]\n"
      "v_pk_fma_f32 v[240:241], v[218:219], v[188:189], v[240:241]\n"
      "v_pk_fma_f32 v[242:243], v[220:221], v[188:189], v[242:243]\n"
      "v_pk_fma_f32 v[244:245], v[222:223], v[188:189], v[244:245]\n"
      "v_pk_fma_f32 v[246:247], v[224:225], v[188:189], v[246:247]\n"
      "v_pk_fma_f32 v[248:249], v[226:227], v[188:189], v[248:249]\n"
      "v_pk_fma_f32 v[250:251], v[228:229], v[188:189], v[250:251]\n"
      "v_pk_fma_f32 v[252:253], v[230:231], v[188:189], v[252:253]\n"
      "v_pk_fma_f32 v[254:255], v[232:233], v[188:189], v[254:255]\n"
      "v_pk_fma_f32 v[240:241], v[220:221], v[186:187], v[240:241]\n"
      "v_pk_fma_f32 v[242:243], v[222:223], v[186:187], v[242:243]\n"
      "v_pk_fma_f32 v[244:245], v[224:225], v[186:187], v[244:245]\n"
      "v_pk_fma_f32 v[246:247], v[226:227], v[186:187], v[246:247]\n"
      "v_pk_fma_f32 v[248:249], v[228:229], v[186:187], v[248:249]\n"
      "v_pk_fma_f32 v[250:251], v[230:231], v[186:187], v[250:251]\n"
      "v_pk_fma_f32 v[252:253], v[232:233], v[186:187], v[252:253]\n"
      "v_pk_fma_f32 v[254:255], v[234:235], v[186:187], v[254:255]\n"
      "v_pk_fma_f32 v[240:241], v[222:223], v[184:185], v[240:241]\n"
      "v_pk_fma_f32 v[242:243], v[224:225], v[184:185], v[242:243]\n"
      "v_pk_fma_f32 v[244:245], v[226:227], v[184:185], v[244:245]\n"
      "v_pk_fma_f32 v[246:247], v[228:229], v[184:185], v[246:247]\n"
      "v_pk_fma_f32 v[248:249], v[230:231], v[184:185], v[248:249]\n"
      "v_pk_fma_f32 v[250:251], v[232:233], v[184:185], v[250:251]\n"
      "v_pk_fma_f32 v[252:253], v[234:235], v[184:185], v[252:253]\n"
      "v_pk_fma_f32 v[254:255], v[236:237], v[184:185], v[254:255]\n"
      "s_waitcnt lgkmcnt(0)\n"
      "v_lshlrev_b32 v216, 16, v208\n"
      "v_and_b32 v217, s92, v208\n"
      "v_lshlrev_b32 v218, 16, v209\n"
      "v_and_b32 v219, s92, v209\n"
      "v_lshlrev_b32 v220, 16, v210\n"
      "v_and_b32 v221, s92, v210\n"
      "v_lshlrev_b32 v222, 16, v211\n"
      "v_and_b32 v223, s92, v211\n"
      "v_lshlrev_b32 v224, 16, v212\n"
      "v_and_b32 v225, s92, v212\n"
      "v_lshlrev_b32 v226, 16, v213\n"
      "v_and_b32 v227, s92, v213\n"
      "v_lshlrev_b32 v228, 16, v214\n"
      "v_and_b32 v229, s92, v214\n"
      "v_lshlrev_b32 v230, 16, v215\n"
      "v_and_b32 v231, s92, v215\n"
      "v_pk_mul_f32 v[240:241], v[240:241], v[216:217]\n"
      "v_pk_mul_f32 v[242:243], v[242:243], v[218:219]\n"
      "v_pk_mul_f32 v[244:245], v[244:245], v[220:221]\n"
      "v_pk_mul_f32 v[246:247], v[246:247], v[222:223]\n"
      "v_pk_mul_f32 v[248:249], v[248:249], v[224:225]\n"
      "v_pk_mul_f32 v[250:251], v[250:251], v[226:227]\n"
      "v_pk_mul_f32 v[252:253], v[252:253], v[228:229]\n"
      "v_pk_mul_f32 v[254:255], v[254:255], v[230:231]\n"
      "v_cvt_pk_bf16_f32 v196, v242, v240\n"
      "v_cvt_pk_bf16_f32 v197, v246, v244\n"
      "v_cvt_pk_bf16_f32 v198, v250, v248\n"
      "v_cvt_pk_bf16_f32 v199, v254, v252\n"
      "buffer_store_dwordx4 v[196:199], v194, s[20:23], s62 offen sc1\n"
      "v_cvt_pk_bf16_f32 v200, v243, v241\n"
      "v_cvt_pk_bf16_f32 v201, v247, v245\n"
      "v_cvt_pk_bf16_f32 v202, v251, v249\n"
      "v_cvt_pk_bf16_f32 v203, v255, v253\n"
      "buffer_store_dwordx4 v[200:203], v194, s[20:23], s63 offen sc1\n"
      "s_waitcnt vmcnt(2)\n"
      "buffer_store_dwordx2 v[28:29], v23, s[20:23], 0 offen sc1\n"
      "s_lshl_b32 s62, s81, 5\n"
      "s_add_u32 s62, s62, s80\n"
      "s_lshl_b32 s62, s62, 7\n"
      "s_add_u32 s62, s62, 18644992\n"
      "v_mov_b32 v23, 1073741824\n"
      "s_mov_b32 exec_lo, 0xffff\n"
      "s_mov_b32 exec_hi, 0\n"
      "v_lshl_add_u32 v23, v1, 3, s62\n"
      "s_mov_b64 exec, -1\n"
      "s_mov_b32 s80, s84\n"
      "s_mov_b32 s81, s85\n"
      "v_lshlrev_b32 v184, 16, v180\n"
      "v_lshlrev_b32 v185, 16, v182\n"
      "v_and_b32 v186, s92, v180\n"
      "v_and_b32 v187, s92, v182\n"
      "v_lshlrev_b32 v188, 16, v181\n"
      "v_lshlrev_b32 v189, 16, v183\n"
      "v_and_b32 v190, s92, v181\n"
      "v_and_b32 v191, s92, v183\n"
      "v_add_u32 v192, 35360, v192\n"
      "v_add_u32 v193, 35360, v193\n"
      "s_mul_i32 s62, s81, 67584\n"
      "s_lshl_b32 s63, s80, 7\n"
      "s_add_u32 s62, s62, s63\n"
      "s_add_u32 s62, s62, 32768\n"
      "s_add_u32 s63, s62, 4224\n"
      "s_add_u32 s64, s64, 4\n"
      "s_add_u32 s65, s64, s52\n"
      "s_cmp_lt_u32 s65, 8\n"
      "s_cbranch_scc1 .LSKV\n"
      "s_lshl3_add_u32 s65, s60, 4096\n"
      "s_cmp_eq_u32 s52, 0\n"
      "s_cselect_b32 s65, s65, 1073741824\n"
      "v_writelane_b32 v23, s65, 32\n"
      "s_cmp_lt_u32 s52, 4\n"
      "s_cbranch_scc1 .Ls_187\n"
      "v_mov_b64 v[46:47], v[28:29]\n"
      "s_nop 0\n"
      "v_readfirstlane_b32 s58, v46\n"
      "v_readfirstlane_b32 s59, v47\n"
      "v_mov_b32 v1, v55\n"
      "v_cmp_ne_u32 vcc, s58, v77\n"
      "s_cbranch_vccnz .Lt_188\n"
      ".Lt_189:\n"
      "v_and_b32 v82, s92, v76\n"
      "v_rcp_f32 v77, v82\n"
      "v_xor_b32 v85, 0x8000, v76\n"
      "s_mov_b32 s62, 0x01000100\n"
      "v_mul_f32 v80, 0xc0135761, v77\n"
      "v_mul_f32 v81, v77, v77\n"
      "v_cmp_nle_f32 vcc, 0x3089705f, v77\n"
      "v_mul_f32 v81, 0x3d372713, v81\n"
      "v_perm_b32 v84, v85, v85, s62\n"
      "v_mul_f32 v81, v81, v80\n"
      "v_mov_b32 v83, 1.0\n"
      "ds_write_b64 v43, v[84:85] offset:16\n"
      "s_cbranch_vccnz .Lb_191\n"
      ".Lb_192:\n"
      "ds_write_b128 v43, v[80:83]\n"
      "v_subrev_u32 v136, 52736, v40\n"
      "v_subrev_u32 v137, 52736, v34\n"
      "v_subrev_u32 v138, 52736, v35\n"
      "ds_write_b64 v137, v[36:37] offset:0\n"
      "ds_write_b64 v138, v[36:37] offset:0\n"
      "s_cmp_eq_u32 s53, 0\n"
      "s_cbranch_scc0 .Ld_193\n"
      "v_readlane_b32 s62, v133, 0\n"
      "v_readlane_b32 s63, v133, 1\n"
      "s_mov_b64 exec, 1\n"
      "v_lshlrev_b32 v72, 16, v140\n"
      "s_lshl_b32 s62, s62, 16\n"
      "v_and_b32 v73, s92, v140\n"
      "v_add_f32 v72, s62, v72\n"
      "v_cvt_pk_bf16_f32 v140, v72, v73\n"
      "v_lshlrev_b32 v72, 16, v144\n"
      "s_lshl_b32 s63, s63, 16\n"
      "v_and_b32 v73, s92, v144\n"
      "v_add_f32 v72, s63, v72\n"
      "v_cvt_pk_bf16_f32 v144, v72, v73\n"
      "s_mov_b64 exec, -1\n"
      ".Ld_193:\n"
      "v_readlane_b32 s62, v132, 1\n"
      "ds_write_b128 v136, v[144:147] offset:8320\n"
      "v_alignbyte_b32 v103, v147, v146, 2\n"
      "v_writelane_b32 v72, s62, 0\n"
      "v_alignbyte_b32 v101, v145, v144, 2\n"
      "v_alignbyte_b32 v102, v146, v145, 2\n"
      "v_mov_b32_dpp v72, v147 wave_shr:1 row_mask:0xf bank_mask:0xf\n"
      "v_alignbyte_b32 v100, v144, v72, 2\n"
      "ds_write_b128 v136, v[100:103] offset:12480\n"
      "ds_write_b128 v136, v[140:143] offset:0\n"
      "v_alignbyte_b32 v99, v143, v142, 2\n"
      "v_alignbyte_b32 v97, v141, v140, 2\n"
      "v_alignbyte_b32 v98, v142, v141, 2\n"
      "v_mov_b32_dpp v132, v143 wave_shr:1 row_mask:0xf bank_mask:0xf\n"
      "v_alignbyte_b32 v96, v140, v132, 2\n"
      "ds_write_b128 v136, v[96:99] offset:4160\n"
      "s_waitcnt vmcnt(1)\n"
      "buffer_store_dwordx2 v[28:29], v23, s[20:23], 0 offen sc1\n"
      "ds_write_b64 v137, v[36:37] offset:26112\n"
      "ds_write_b64 v138, v[36:37] offset:26112\n"
      "s_cmp_eq_u32 s53, 0\n"
      "s_cbranch_scc0 .Ld_194\n"
      "v_readlane_b32 s62, v135, 0\n"
      "v_readlane_b32 s63, v135, 1\n"
      "s_mov_b64 exec, 1\n"
      "v_lshlrev_b32 v72, 16, v148\n"
      "s_lshl_b32 s62, s62, 16\n"
      "v_and_b32 v73, s92, v148\n"
      "v_add_f32 v72, s62, v72\n"
      "v_cvt_pk_bf16_f32 v148, v72, v73\n"
      "v_lshlrev_b32 v72, 16, v152\n"
      "s_lshl_b32 s63, s63, 16\n"
      "v_and_b32 v73, s92, v152\n"
      "v_add_f32 v72, s63, v72\n"
      "v_cvt_pk_bf16_f32 v152, v72, v73\n"
      "s_mov_b64 exec, -1\n"
      ".Ld_194:\n"
      "v_readlane_b32 s62, v134, 1\n"
      "ds_write_b128 v136, v[152:155] offset:34432\n"
      "v_alignbyte_b32 v103, v155, v154, 2\n"
      "v_writelane_b32 v72, s62, 0\n"
      "v_alignbyte_b32 v101, v153, v152, 2\n"
      "v_alignbyte_b32 v102, v154, v153, 2\n"
      "v_mov_b32_dpp v72, v155 wave_shr:1 row_mask:0xf bank_mask:0xf\n"
      "v_alignbyte_b32 v100, v152, v72, 2\n"
      "ds_write_b128 v136, v[100:103] offset:38592\n"
      "ds_write_b128 v136, v[148:151] offset:26112\n"
      "v_alignbyte_b32 v99, v151, v150, 2\n"
      "v_alignbyte_b32 v97, v149, v148, 2\n"
      "v_alignbyte_b32 v98, v150, v149, 2\n"
      "v_mov_b32_dpp v134, v151 wave_shr:1 row_mask:0xf bank_mask:0xf\n"
      "v_alignbyte_b32 v96, v148, v134, 2\n"
      "ds_write_b128 v136, v[96:99] offset:30272\n"
      "s_sleep 10\n"
      "buffer_load_dwordx2 v[50:51], v31, s[20:23], 0 offen sc1\n"
      "buffer_load_dwordx2 v[48:49], v30, s[20:23], 0 offen sc1\n"
      "s_waitcnt vmcnt(0)\n"
      "v_cmp_ne_u64 vcc, s[58:59], v[48:49]\n"
      "s_cbranch_vccnz .Lf_195\n"
      ".Lf_196:\n"
      "s_mul_i32 s62, s98, 4224\n"
      "s_add_u32 s62, s62, s95\n"
      "s_add_u32 m0, s95, 87296\n"
      "s_add_u32 s63, s62, 15872\n"
      "buffer_load_dwordx4 v5, s[20:23], s63 offen sc1 lds\n"
      "s_add_u32 m0, s95, 91904\n"
      "s_add_u32 s63, s62, 20096\n"
      "buffer_load_dwordx4 v5, s[20:23], s63 offen sc1 lds\n"
      "s_mul_i32 s62, s98, 4224\n"
      "s_add_u32 s62, s62, s95\n"
      "s_add_u32 m0, s95, 113408\n"
      "s_add_u32 s63, s62, 24320\n"
      "buffer_load_dwordx4 v5, s[20:23], s63 offen sc1 lds\n"
      "s_add_u32 m0, s95, 118016\n"
      "s_add_u32 s63, s62, 28544\n"
      "buffer_load_dwordx4 v5, s[20:23], s63 offen sc1 lds\n"
      "s_waitcnt vmcnt(2)\n"
      "ds_add_u32 v2, v3 offset:4240\n"
      "s_waitcnt vmcnt(0)\n"
      "ds_add_u32 v2, v3 offset:4272\n"
      "s_branch .LS3\n"
      ".Ls_187:\n"
      "s_waitcnt vmcnt(1) lgkmcnt(0)\n"
      "buffer_store_dwordx2 v[28:29], v23, s[20:23], 0 offen sc1\n"
      "v_mov_b64 v[12:13], v[28:29]\n"
      "ds_read_b32 v26, v2 offset:5120\n"
      "v_lshlrev_b32 v31, 3, v1\n"
      "v_add_u32 v31, 163072, v31\n"
      "s_waitcnt lgkmcnt(0)\n"
      "v_and_b32 v27, s92, v26\n"
      "v_lshlrev_b32 v26, 16, v26\n"
      "ds_write_b64 v31, v[26:27]\n"
      "ds_add_u32 v2, v3 offset:4168\n"
      "s_mov_b32 s81, 2\n"
      "s_lshl3_add_u32 s64, s60, s52\n"
      "s_cmp_eq_u32 s64, 2040\n"
      "s_cbranch_scc0 .Le_198\n"
      "v_lshlrev_b32 v25, 3, v1\n"
      "s_mov_b32 s64, 4096\n"
      "buffer_load_dwordx2 v[26:27], v25, s[20:23], s64 offen sc1\n"
      "s_waitcnt vmcnt(0)\n"
      "v_cmp_ne_u64 vcc, s[58:59], v[26:27]\n"
      "s_cbranch_vccnz .Lf_199\n"
      ".Lf_200:\n"
      "s_mov_b32 s64, 4608\n"
      "buffer_load_dwordx2 v[26:27], v25, s[20:23], s64 offen sc1\n"
      "s_waitcnt vmcnt(0)\n"
      "v_cmp_ne_u64 vcc, s[58:59], v[26:27]\n"
      "s_cbranch_vccnz .Lf_202\n"
      ".Lf_203:\n"
      "s_mov_b32 s64, 5120\n"
      "buffer_load_dwordx2 v[26:27], v25, s[20:23], s64 offen sc1\n"
      "s_waitcnt vmcnt(0)\n"
      "v_cmp_ne_u64 vcc, s[58:59], v[26:27]\n"
      "s_cbranch_vccnz .Lf_205\n"
      ".Lf_206:\n"
      "s_mov_b32 s64, 5632\n"
      "buffer_load_dwordx2 v[26:27], v25, s[20:23], s64 offen sc1\n"
      "s_waitcnt vmcnt(0)\n"
      "v_cmp_ne_u64 vcc, s[58:59], v[26:27]\n"
      "s_cbranch_vccnz .Lf_208\n"
      ".Lf_209:\n"
      "s_mov_b64 exec, 1\n"
      "buffer_store_dwordx2 v[28:29], off, s[20:23], 0 sc1\n"
      "s_mov_b64 exec, -1\n"
      ".Le_198:\n"
      "s_sub_u32 s66, 3, s53\n"
      "s_lshl1_add_u32 s62, s56, s54\n"
      "s_lshl2_add_u32 s63, s62, s66\n"
      "s_mul_i32 s62, s63, 20480\n"
      "s_mul_i32 s63, s55, 160\n"
      "s_add_u32 s62, s62, s63\n"
      "v_and_b32 v21, 15, v1\n"
      "v_mul_u32_u24 v21, 1280, v21\n"
      "v_lshrrev_b32 v22, 4, v1\n"
      "v_lshl_add_u32 v21, v22, 3, v21\n"
      "buffer_load_dwordx2 v[218:219], v21, s[4:7], s62 offen offset:0\n"
      "buffer_load_dwordx2 v[220:221], v21, s[4:7], s62 offen offset:32\n"
      "buffer_load_dwordx2 v[222:223], v21, s[4:7], s62 offen offset:64\n"
      "buffer_load_dwordx2 v[224:225], v21, s[4:7], s62 offen offset:96\n"
      "buffer_load_dwordx2 v[226:227], v21, s[4:7], s62 offen offset:128\n"
      "s_mov_b32 s79, 1073741824\n"
      "s_mov_b32 s84, 64\n"
      "s_mov_b32 s85, 64\n"
      "s_setprio 3\n"
      "v_and_b32 v10, 1, v1\n"
      "v_xor_b32 v10, 1, v10\n"
      "v_mul_u32_u24 v10, 4160, v10\n"
      "v_and_b32 v11, 14, v1\n"
      "v_lshl_add_u32 v10, v11, 1, v10\n"
      "v_and_b32 v11, 48, v1\n"
      "v_sub_u32 v10, v10, v11\n"
      "v_and_b32 v21, 15, v1\n"
      "v_lshl_add_u32 v11, v21, 5, v11\n"
      "s_lshr_b32 s64, s53, 1\n"
      "s_and_b32 s65, s53, 1\n"
      "s_mul_i32 s67, s64, 26112\n"
      "s_add_u32 s67, s67, 70144\n"
      "s_mul_i32 s62, s65, 8320\n"
      "s_add_u32 s62, s62, s67\n"
      "s_add_u32 s62, s62, 84\n"
      "v_add_u32 v212, s62, v10\n"
      "s_mul_i32 s62, s65, 4608\n"
      "s_add_u32 s62, s62, s67\n"
      "s_add_u32 s62, s62, 16672\n"
      "v_add_u32 v217, s62, v11\n"
      "v_lshrrev_b32 v22, 5, v1\n"
      "v_lshlrev_b32 v22, 4, v22\n"
      "v_lshl_add_u32 v22, v21, 5, v22\n"
      "v_bfe_u32 v21, v1, 4, 1\n"
      "v_lshl_add_u32 v5, v21, 9, v22\n"
      "s_add_u32 s63, s98, s53\n"
      "s_mul_i32 s63, s63, 4224\n"
      "s_add_u32 s63, s63, 2801152\n"
      "s_lshl_b32 s62, s64, 5\n"
      "v_add_u32 v14, s62, v2\n"
      "s_waitcnt vmcnt(0)\n"
      "s_mov_b32 s3, 0x80000\n"
      ".Lp_211:\n"
      "s_sleep 1\n"
      "ds_read_b32 v31, v14 offset:4240\n"
      "s_sub_u32 s3, s3, 1\n"
      "s_waitcnt lgkmcnt(0)\n"
      "v_readlane_b32 s2, v31, 0\n"
      "s_cselect_b32 s2, -1, s2\n"
      "s_cmp_lt_u32 s2, 4\n"
      "s_cbranch_scc1 .Lp_211\n"
      "ds_read2_b32 v[144:145], v212 offset0:3 offset1:2\n"
      "ds_read2_b32 v[146:147], v212 offset0:1 offset1:0\n"
      "ds_read_b128 v[204:207], v217 offset:4032\n"
      "ds_read_b128 v[200:203], v217 offset:3520\n"
      "ds_read_b128 v[196:199], v217 offset:3008\n"
      "ds_read_b128 v[192:195], v217 offset:2496\n"
      "ds_read_b128 v[188:191], v217 offset:1984\n"
      "ds_read_b128 v[184:187], v217 offset:1472\n"
      "ds_read_b128 v[180:183], v217 offset:960\n"
      "ds_read_b128 v[176:179], v217 offset:448\n"
      "ds_read2_b32 v[148:149], v212 offset0:131 offset1:130\n"
      "ds_read2_b32 v[150:151], v212 offset0:129 offset1:128\n"
      "v_add_u32 v213, 1024, v212\n"
      "v_add_u32 v214, 2048, v212\n"
      "v_add_u32 v215, 3072, v212\n"
      "s_waitcnt lgkmcnt(9)\n"
      "v_mfma_f32_16x16x32_bf16 v[124:127], v[144:147], v[204:207], 0\n"
      "ds_read2_b32 v[152:153], v213 offset0:3 offset1:2\n"
      "s_waitcnt lgkmcnt(9)\n"
      "v_mfma_f32_16x16x32_bf16 v[120:123], v[144:147], v[200:203], 0\n"
      "ds_read2_b32 v[154:155], v213 offset0:1 offset1:0\n"
      "s_waitcnt lgkmcnt(9)\n"
      "v_mfma_f32_16x16x32_bf16 v[116:119], v[144:147], v[196:199], 0\n"
      "ds_read2_b32 v[156:157], v213 offset0:131 offset1:130\n"
      "s_waitcnt lgkmcnt(9)\n"
      "v_mfma_f32_16x16x32_bf16 v[112:115], v[144:147], v[192:195], 0\n"
      "ds_read2_b32 v[158:159], v213 offset0:129 offset1:128\n"
      "s_waitcnt lgkmcnt(9)\n"
      "v_mfma_f32_16x16x32_bf16 v[108:111], v[144:147], v[188:191], 0\n"
      "ds_read2_b32 v[160:161], v214 offset0:3 offset1:2\n"
      "s_waitcnt lgkmcnt(9)\n"
      "v_mfma_f32_16x16x32_bf16 v[104:107], v[144:147], v[184:187], 0\n"
      "ds_read2_b32 v[162:163], v214 offset0:1 offset1:0\n"
      "s_waitcnt lgkmcnt(9)\n"
      "v_mfma_f32_16x16x32_bf16 v[100:103], v[144:147], v[180:183], 0\n"
      "ds_read2_b32 v[164:165], v214 offset0:131 offset1:130\n"
      "s_waitcnt lgkmcnt(7)\n"
      "v_mfma_f32_16x16x32_bf16 v[96:99], v[144:147], v[176:179], 0\n"
      "ds_read2_b32 v[166:167], v214 offset0:129 offset1:128\n"
      "v_mfma_f32_16x16x32_bf16 v[124:127], v[148:151], v[200:203], v[124:127]\n"
      "v_mfma_f32_16x16x32_bf16 v[120:123], v[148:151], v[196:199], v[120:123]\n"
      "ds_read2_b32 v[168:169], v215 offset0:3 offset1:2\n"
      "v_mfma_f32_16x16x32_bf16 v[116:119], v[148:151], v[192:195], v[116:119]\n"
      "ds_read2_b32 v[170:171], v215 offset0:1 offset1:0\n"
      "v_mfma_f32_16x16x32_bf16 v[112:115], v[148:151], v[188:191], v[112:115]\n"
      "ds_read2_b32 v[172:173], v215 offset0:131 offset1:130\n"
      "v_mfma_f32_16x16x32_bf16 v[108:111], v[148:151], v[184:187], v[108:111]\n"
      "ds_read2_b32 v[174:175], v215 offset0:129 offset1:128\n"
      "v_mfma_f32_16x16x32_bf16 v[104:107], v[148:151], v[180:183], v[104:107]\n"
      "ds_read2_b32 v[144:145], v212 offset0:19 offset1:18\n"
      "v_mfma_f32_16x16x32_bf16 v[100:103], v[148:151], v[176:179], v[100:103]\n"
      "ds_read2_b32 v[146:147], v212 offset0:17 offset1:16\n"
      "s_waitcnt lgkmcnt(6)\n"
      "v_mfma_f32_16x16x32_bf16 v[124:127], v[152:155], v[196:199], v[124:127]\n"
      "ds_read_b128 v[204:207], v217 offset:3968\n"
      "v_mfma_f32_16x16x32_bf16 v[120:123], v[152:155], v[192:195], v[120:123]\n"
      "ds_read_b128 v[200:203], v217 offset:3456\n"
      "v_mfma_f32_16x16x32_bf16 v[116:119], v[152:155], v[188:191], v[116:119]\n"
      "ds_read_b128 v[196:199], v217 offset:2944\n"
      "v_mfma_f32_16x16x32_bf16 v[112:115], v[152:155], v[184:187], v[112:115]\n"
      "v_mfma_f32_16x16x32_bf16 v[108:111], v[152:155], v[180:183], v[108:111]\n"
      "v_mfma_f32_16x16x32_bf16 v[104:107], v[152:155], v[176:179], v[104:107]\n"
      "ds_read2_b32 v[148:149], v212 offset0:147 offset1:146\n"
      "v_mfma_f32_16x16x32_bf16 v[124:127], v[156:159], v[192:195], v[124:127]\n"
      "ds_read_b128 v[192:195], v217 offset:2432\n"
      "v_mfma_f32_16x16x32_bf16 v[120:123], v[156:159], v[188:191], v[120:123]\n"
      "ds_read2_b32 v[150:151], v212 offset0:145 offset1:144\n"
      "v_mfma_f32_16x16x32_bf16 v[116:119], v[156:159], v[184:187], v[116:119]\n"
      "v_mfma_f32_16x16x32_bf16 v[112:115], v[156:159], v[180:183], v[112:115]\n"
      "v_mfma_f32_16x16x32_bf16 v[108:111], v[156:159], v[176:179], v[108:111]\n"
      "v_mfma_f32_16x16x32_bf16 v[124:127], v[160:163], v[188:191], v[124:127]\n"
      "ds_read_b128 v[188:191], v217 offset:1920\n"
      "v_mfma_f32_16x16x32_bf16 v[120:123], v[160:163], v[184:187], v[120:123]\n"
      "ds_read2_b32 v[152:153], v213 offset0:19 offset1:18\n"
      "v_mfma_f32_16x16x32_bf16 v[116:119], v[160:163], v[180:183], v[116:119]\n"
      "ds_read2_b32 v[154:155], v213 offset0:17 offset1:16\n"
      "v_mfma_f32_16x16x32_bf16 v[112:115], v[160:163], v[176:179], v[112:115]\n"
      "v_mfma_f32_16x16x32_bf16 v[124:127], v[164:167], v[184:187], v[124:127]\n"
      "ds_read_b128 v[184:187], v217 offset:1408\n"
      "v_mfma_f32_16x16x32_bf16 v[120:123], v[164:167], v[180:183], v[120:123]\n"
      "v_mfma_f32_16x16x32_bf16 v[116:119], v[164:167], v[176:179], v[116:119]\n"
      "s_waitcnt lgkmcnt(4)\n"
      "v_mfma_f32_16x16x32_bf16 v[124:127], v[168:171], v[180:183], v[124:127]\n"
      "ds_read_b128 v[180:183], v217 offset:896\n"
      "v_mfma_f32_16x16x32_bf16 v[120:123], v[168:171], v[176:179], v[120:123]\n"
      "ds_read2_b32 v[156:157], v213 offset0:147 offset1:146\n"
      "v_mfma_f32_16x16x32_bf16 v[124:127], v[172:175], v[176:179], v[124:127]\n"
      "ds_read_b128 v[176:179], v217 offset:384\n"
      "v_mfma_f32_16x16x32_bf16 v[124:127], v[144:147], v[204:207], v[124:127]\n"
      "ds_read2_b32 v[158:159], v213 offset0:145 offset1:144\n"
      "v_mfma_f32_16x16x32_bf16 v[120:123], v[144:147], v[200:203], v[120:123]\n"
      "v_mfma_f32_16x16x32_bf16 v[116:119], v[144:147], v[196:199], v[116:119]\n"
      "ds_read2_b32 v[160:161], v214 offset0:19 offset1:18\n"
      "v_mfma_f32_16x16x32_bf16 v[112:115], v[144:147], v[192:195], v[112:115]\n"
      "ds_read2_b32 v[162:163], v214 offset0:17 offset1:16\n"
      "s_waitcnt lgkmcnt(6)\n"
      "v_mfma_f32_16x16x32_bf16 v[108:111], v[144:147], v[188:191], v[108:111]\n"
      "v_mfma_f32_16x16x32_bf16 v[104:107], v[144:147], v[184:187], v[104:107]\n"
      "s_waitcnt lgkmcnt(4)\n"
      "v_mfma_f32_16x16x32_bf16 v[100:103], v[144:147], v[180:183], v[100:103]\n"
      "ds_read2_b32 v[164:165], v214 offset0:147 offset1:146\n"
      "s_waitcnt lgkmcnt(4)\n"
      "v_mfma_f32_16x16x32_bf16 v[96:99], v[144:147], v[176:179], v[96:99]\n"
      "ds_read2_b32 v[166:167], v214 offset0:145 offset1:144\n"
      "v_mfma_f32_16x16x32_bf16 v[124:127], v[148:151], v[200:203], v[124:127]\n"
      "v_mfma_f32_16x16x32_bf16 v[120:123], v[148:151], v[196:199], v[120:123]\n"
      "ds_read2_b32 v[168:169], v215 offset0:19 offset1:18\n"
      "v_mfma_f32_16x16x32_bf16 v[116:119], v[148:151], v[192:195], v[116:119]\n"
      "ds_read2_b32 v[170:171], v215 offset0:17 offset1:16\n"
      "v_mfma_f32_16x16x32_bf16 v[112:115], v[148:151], v[188:191], v[112:115]\n"
      "ds_read2_b32 v[172:173], v215 offset0:147 offset1:146\n"
      "v_mfma_f32_16x16x32_bf16 v[108:111], v[148:151], v[184:187], v[108:111]\n"
      "ds_read2_b32 v[174:175], v215 offset0:145 offset1:144\n"
      "v_mfma_f32_16x16x32_bf16 v[104:107], v[148:151], v[180:183], v[104:107]\n"
      "ds_read2_b32 v[144:145], v212 offset0:35 offset1:34\n"
      "v_mfma_f32_16x16x32_bf16 v[100:103], v[148:151], v[176:179], v[100:103]\n"
      "ds_read2_b32 v[146:147], v212 offset0:33 offset1:32\n"
      "v_mfma_f32_16x16x32_bf16 v[124:127], v[152:155], v[196:199], v[124:127]\n"
      "ds_read_b128 v[204:207], v217 offset:3904\n"
      "v_mfma_f32_16x16x32_bf16 v[120:123], v[152:155], v[192:195], v[120:123]\n"
      "ds_read_b128 v[200:203], v217 offset:3392\n"
      "v_mfma_f32_16x16x32_bf16 v[116:119], v[152:155], v[188:191], v[116:119]\n"
      "ds_read_b128 v[196:199], v217 offset:2880\n"
      "v_mfma_f32_16x16x32_bf16 v[112:115], v[152:155], v[184:187], v[112:115]\n"
      "v_mfma_f32_16x16x32_bf16 v[108:111], v[152:155], v[180:183], v[108:111]\n"
      "v_mfma_f32_16x16x32_bf16 v[104:107], v[152:155], v[176:179], v[104:107]\n"
      "ds_read2_b32 v[148:149], v212 offset0:163 offset1:162\n"
      "s_waitcnt lgkmcnt(5)\n"
      "v_mfma_f32_16x16x32_bf16 v[124:127], v[156:159], v[192:195], v[124:127]\n"
      "ds_read_b128 v[192:195], v217 offset:2368\n"
      "v_mfma_f32_16x16x32_bf16 v[120:123], v[156:159], v[188:191], v[120:123]\n"
      "ds_read2_b32 v[150:151], v212 offset0:161 offset1:160\n"
      "v_mfma_f32_16x16x32_bf16 v[116:119], v[156:159], v[184:187], v[116:119]\n"
      "v_mfma_f32_16x16x32_bf16 v[112:115], v[156:159], v[180:183], v[112:115]\n"
      "v_mfma_f32_16x16x32_bf16 v[108:111], v[156:159], v[176:179], v[108:111]\n"
      "v_mfma_f32_16x16x32_bf16 v[124:127], v[160:163], v[188:191], v[124:127]\n"
      "ds_read_b128 v[188:191], v217 offset:1856\n"
      "v_mfma_f32_16x16x32_bf16 v[120:123], v[160:163], v[184:187], v[120:123]\n"
      "ds_read2_b32 v[152:153], v213 offset0:35 offset1:34\n"
      "v_mfma_f32_16x16x32_bf16 v[116:119], v[160:163], v[180:183], v[116:119]\n"
      "ds_read2_b32 v[154:155], v213 offset0:33 offset1:32\n"
      "v_mfma_f32_16x16x32_bf16 v[112:115], v[160:163], v[176:179], v[112:115]\n"
      "v_mfma_f32_16x16x32_bf16 v[124:127], v[164:167], v[184:187], v[124:127]\n"
      "ds_read_b128 v[184:187], v217 offset:1344\n"
      "v_mfma_f32_16x16x32_bf16 v[120:123], v[164:167], v[180:183], v[120:123]\n"
      "v_mfma_f32_16x16x32_bf16 v[116:119], v[164:167], v[176:179], v[116:119]\n"
      "v_mfma_f32_16x16x32_bf16 v[124:127], v[168:171], v[180:183], v[124:127]\n"
      "ds_read_b128 v[180:183], v217 offset:832\n"
      "v_mfma_f32_16x16x32_bf16 v[120:123], v[168:171], v[176:179], v[120:123]\n"
      "ds_read2_b32 v[156:157], v213 offset0:163 offset1:162\n"
      "v_mfma_f32_16x16x32_bf16 v[124:127], v[172:175], v[176:179], v[124:127]\n"
      "ds_read_b128 v[176:179], v217 offset:320\n"
      "s_waitcnt lgkmcnt(4)\n"
      "v_mfma_f32_16x16x32_bf16 v[124:127], v[144:147], v[204:207], v[124:127]\n"
      "ds_read2_b32 v[158:159], v213 offset0:161 offset1:160\n"
      "v_mfma_f32_16x16x32_bf16 v[120:123], v[144:147], v[200:203], v[120:123]\n"
      "v_mfma_f32_16x16x32_bf16 v[116:119], v[144:147], v[196:199], v[116:119]\n"
      "ds_read2_b32 v[160:161], v214 offset0:35 offset1:34\n"
      "v_mfma_f32_16x16x32_bf16 v[112:115], v[144:147], v[192:195], v[112:115]\n"
      "ds_read2_b32 v[162:163], v214 offset0:33 offset1:32\n"
      "v_mfma_f32_16x16x32_bf16 v[108:111], v[144:147], v[188:191], v[108:111]\n"
      "s_waitcnt lgkmcnt(5)\n"
      "v_mfma_f32_16x16x32_bf16 v[104:107], v[144:147], v[184:187], v[104:107]\n"
      "v_mfma_f32_16x16x32_bf16 v[100:103], v[144:147], v[180:183], v[100:103]\n"
      "ds_read2_b32 v[164:165], v214 offset0:163 offset1:162\n"
      "s_waitcnt lgkmcnt(4)\n"
      "v_mfma_f32_16x16x32_bf16 v[96:99], v[144:147], v[176:179], v[96:99]\n"
      "ds_read2_b32 v[166:167], v214 offset0:161 offset1:160\n"
      "v_mfma_f32_16x16x32_bf16 v[124:127], v[148:151], v[200:203], v[124:127]\n"
      "v_mfma_f32_16x16x32_bf16 v[120:123], v[148:151], v[196:199], v[120:123]\n"
      "ds_read2_b32 v[168:169], v215 offset0:35 offset1:34\n"
      "v_mfma_f32_16x16x32_bf16 v[116:119], v[148:151], v[192:195], v[116:119]\n"
      "ds_read2_b32 v[170:171], v215 offset0:33 offset1:32\n"
      "v_mfma_f32_16x16x32_bf16 v[112:115], v[148:151], v[188:191], v[112:115]\n"
      "ds_read2_b32 v[172:173], v215 offset0:163 offset1:162\n"
      "v_mfma_f32_16x16x32_bf16 v[108:111], v[148:151], v[184:187], v[108:111]\n"
      "ds_read2_b32 v[174:175], v215 offset0:161 offset1:160\n"
      "v_mfma_f32_16x16x32_bf16 v[104:107], v[148:151], v[180:183], v[104:107]\n"
      "ds_read2_b32 v[144:145], v212 offset0:51 offset1:50\n"
      "v_mfma_f32_16x16x32_bf16 v[100:103], v[148:151], v[176:179], v[100:103]\n"
      "ds_read2_b32 v[146:147], v212 offset0:49 offset1:48\n"
      "v_mfma_f32_16x16x32_bf16 v[124:127], v[152:155], v[196:199], v[124:127]\n"
      "ds_read_b128 v[204:207], v217 offset:3840\n"
      "v_mfma_f32_16x16x32_bf16 v[120:123], v[152:155], v[192:195], v[120:123]\n"
      "ds_read_b128 v[200:203], v217 offset:3328\n"
      "v_mfma_f32_16x16x32_bf16 v[116:119], v[152:155], v[188:191], v[116:119]\n"
      "ds_read_b128 v[196:199], v217 offset:2816\n"
      "v_mfma_f32_16x16x32_bf16 v[112:115], v[152:155], v[184:187], v[112:115]\n"
      "v_mfma_f32_16x16x32_bf16 v[108:111], v[152:155], v[180:183], v[108:111]\n"
      "v_mfma_f32_16x16x32_bf16 v[104:107], v[152:155], v[176:179], v[104:107]\n"
      "ds_read2_b32 v[148:149], v212 offset0:179 offset1:178\n"
      "s_waitcnt lgkmcnt(5)\n"
      "v_mfma_f32_16x16x32_bf16 v[124:127], v[156:159], v[192:195], v[124:127]\n"
      "ds_read_b128 v[192:195], v217 offset:2304\n"
      "v_mfma_f32_16x16x32_bf16 v[120:123], v[156:159], v[188:191], v[120:123]\n"
      "ds_read2_b32 v[150:151], v212 offset0:177 offset1:176\n"
      "v_mfma_f32_16x16x32_bf16 v[116:119], v[156:159], v[184:187], v[116:119]\n"
      "v_mfma_f32_16x16x32_bf16 v[112:115], v[156:159], v[180:183], v[112:115]\n"
      "v_mfma_f32_16x16x32_bf16 v[108:111], v[156:159], v[176:179], v[108:111]\n"
      "v_mfma_f32_16x16x32_bf16 v[124:127], v[160:163], v[188:191], v[124:127]\n"
      "ds_read_b128 v[188:191], v217 offset:1792\n"
      "v_mfma_f32_16x16x32_bf16 v[120:123], v[160:163], v[184:187], v[120:123]\n"
      "ds_read2_b32 v[152:153], v213 offset0:51 offset1:50\n"
      "v_mfma_f32_16x16x32_bf16 v[116:119], v[160:163], v[180:183], v[116:119]\n"
      "ds_read2_b32 v[154:155], v213 offset0:49 offset1:48\n"
      "v_mfma_f32_16x16x32_bf16 v[112:115], v[160:163], v[176:179], v[112:115]\n"
      "v_mfma_f32_16x16x32_bf16 v[124:127], v[164:167], v[184:187], v[124:127]\n"
      "ds_read_b128 v[184:187], v217 offset:1280\n"
      "v_mfma_f32_16x16x32_bf16 v[120:123], v[164:167], v[180:183], v[120:123]\n"
      "v_mfma_f32_16x16x32_bf16 v[116:119], v[164:167], v[176:179], v[116:119]\n"
      "v_mfma_f32_16x16x32_bf16 v[124:127], v[168:171], v[180:183], v[124:127]\n"
      "ds_read_b128 v[180:183], v217 offset:768\n"
      "v_mfma_f32_16x16x32_bf16 v[120:123], v[168:171], v[176:179], v[120:123]\n"
      "ds_read2_b32 v[156:157], v213 offset0:179 offset1:178\n"
      "v_mfma_f32_16x16x32_bf16 v[124:127], v[172:175], v[176:179], v[124:127]\n"
      "ds_read_b128 v[176:179], v217 offset:256\n"
      "s_waitcnt lgkmcnt(4)\n"
      "v_mfma_f32_16x16x32_bf16 v[124:127], v[144:147], v[204:207], v[124:127]\n"
      "ds_read2_b32 v[158:159], v213 offset0:177 offset1:176\n"
      "v_mfma_f32_16x16x32_bf16 v[120:123], v[144:147], v[200:203], v[120:123]\n"
      "v_mfma_f32_16x16x32_bf16 v[116:119], v[144:147], v[196:199], v[116:119]\n"
      "ds_read2_b32 v[160:161], v214 offset0:51 offset1:50\n"
      "v_mfma_f32_16x16x32_bf16 v[112:115], v[144:147], v[192:195], v[112:115]\n"
      "ds_read2_b32 v[162:163], v214 offset0:49 offset1:48\n"
      "v_mfma_f32_16x16x32_bf16 v[108:111], v[144:147], v[188:191], v[108:111]\n"
      "s_waitcnt lgkmcnt(5)\n"
      "v_mfma_f32_16x16x32_bf16 v[104:107], v[144:147], v[184:187], v[104:107]\n"
      "v_mfma_f32_16x16x32_bf16 v[100:103], v[144:147], v[180:183], v[100:103]\n"
      "ds_read2_b32 v[164:165], v214 offset0:179 offset1:178\n"
      "s_waitcnt lgkmcnt(4)\n"
      "v_mfma_f32_16x16x32_bf16 v[96:99], v[144:147], v[176:179], v[96:99]\n"
      "ds_read2_b32 v[166:167], v214 offset0:177 offset1:176\n"
      "v_mfma_f32_16x16x32_bf16 v[124:127], v[148:151], v[200:203], v[124:127]\n"
      "v_mfma_f32_16x16x32_bf16 v[120:123], v[148:151], v[196:199], v[120:123]\n"
      "ds_read2_b32 v[168:169], v215 offset0:51 offset1:50\n"
      "v_mfma_f32_16x16x32_bf16 v[116:119], v[148:151], v[192:195], v[116:119]\n"
      "ds_read2_b32 v[170:171], v215 offset0:49 offset1:48\n"
      "v_mfma_f32_16x16x32_bf16 v[112:115], v[148:151], v[188:191], v[112:115]\n"
      "ds_read2_b32 v[172:173], v215 offset0:179 offset1:178\n"
      "v_mfma_f32_16x16x32_bf16 v[108:111], v[148:151], v[184:187], v[108:111]\n"
      "ds_read2_b32 v[174:175], v215 offset0:177 offset1:176\n"
      "v_mfma_f32_16x16x32_bf16 v[104:107], v[148:151], v[180:183], v[104:107]\n"
      "ds_read2_b32 v[144:145], v212 offset0:67 offset1:66\n"
      "v_mfma_f32_16x16x32_bf16 v[100:103], v[148:151], v[176:179], v[100:103]\n"
      "ds_read2_b32 v[146:147], v212 offset0:65 offset1:64\n"
      "v_mfma_f32_16x16x32_bf16 v[124:127], v[152:155], v[196:199], v[124:127]\n"
      "ds_read_b128 v[204:207], v217 offset:3776\n"
      "v_mfma_f32_16x16x32_bf16 v[120:123], v[152:155], v[192:195], v[120:123]\n"
      "ds_read_b128 v[200:203], v217 offset:3264\n"
      "v_mfma_f32_16x16x32_bf16 v[116:119], v[152:155], v[188:191], v[116:119]\n"
      "ds_read_b128 v[196:199], v217 offset:2752\n"
      "v_mfma_f32_16x16x32_bf16 v[112:115], v[152:155], v[184:187], v[112:115]\n"
      "v_mfma_f32_16x16x32_bf16 v[108:111], v[152:155], v[180:183], v[108:111]\n"
      "v_mfma_f32_16x16x32_bf16 v[104:107], v[152:155], v[176:179], v[104:107]\n"
      "ds_read2_b32 v[148:149], v212 offset0:195 offset1:194\n"
      "s_waitcnt lgkmcnt(5)\n"
      "v_mfma_f32_16x16x32_bf16 v[124:127], v[156:159], v[192:195], v[124:127]\n"
      "ds_read_b128 v[192:195], v217 offset:2240\n"
      "v_mfma_f32_16x16x32_bf16 v[120:123], v[156:159], v[188:191], v[120:123]\n"
      "ds_read2_b32 v[150:151], v212 offset0:193 offset1:192\n"
      "v_mfma_f32_16x16x32_bf16 v[116:119], v[156:159], v[184:187], v[116:119]\n"
      "v_mfma_f32_16x16x32_bf16 v[112:115], v[156:159], v[180:183], v[112:115]\n"
      "v_mfma_f32_16x16x32_bf16 v[108:111], v[156:159], v[176:179], v[108:111]\n"
      "v_mfma_f32_16x16x32_bf16 v[124:127], v[160:163], v[188:191], v[124:127]\n"
      "ds_read_b128 v[188:191], v217 offset:1728\n"
      "v_mfma_f32_16x16x32_bf16 v[120:123], v[160:163], v[184:187], v[120:123]\n"
      "ds_read2_b32 v[152:153], v213 offset0:67 offset1:66\n"
      "v_mfma_f32_16x16x32_bf16 v[116:119], v[160:163], v[180:183], v[116:119]\n"
      "ds_read2_b32 v[154:155], v213 offset0:65 offset1:64\n"
      "v_mfma_f32_16x16x32_bf16 v[112:115], v[160:163], v[176:179], v[112:115]\n"
      "v_mfma_f32_16x16x32_bf16 v[124:127], v[164:167], v[184:187], v[124:127]\n"
      "ds_read_b128 v[184:187], v217 offset:1216\n"
      "v_mfma_f32_16x16x32_bf16 v[120:123], v[164:167], v[180:183], v[120:123]\n"
      "v_mfma_f32_16x16x32_bf16 v[116:119], v[164:167], v[176:179], v[116:119]\n"
      "v_mfma_f32_16x16x32_bf16 v[124:127], v[168:171], v[180:183], v[124:127]\n"
      "ds_read_b128 v[180:183], v217 offset:704\n"
      "v_mfma_f32_16x16x32_bf16 v[120:123], v[168:171], v[176:179], v[120:123]\n"
      "ds_read2_b32 v[156:157], v213 offset0:195 offset1:194\n"
      "v_mfma_f32_16x16x32_bf16 v[124:127], v[172:175], v[176:179], v[124:127]\n"
      "ds_read_b128 v[176:179], v217 offset:192\n"
      "s_waitcnt lgkmcnt(4)\n"
      "v_mfma_f32_16x16x32_bf16 v[124:127], v[144:147], v[204:207], v[124:127]\n"
      "ds_read2_b32 v[158:159], v213 offset0:193 offset1:192\n"
      "v_mfma_f32_16x16x32_bf16 v[120:123], v[144:147], v[200:203], v[120:123]\n"
      "v_mfma_f32_16x16x32_bf16 v[116:119], v[144:147], v[196:199], v[116:119]\n"
      "ds_read2_b32 v[160:161], v214 offset0:67 offset1:66\n"
      "v_mfma_f32_16x16x32_bf16 v[112:115], v[144:147], v[192:195], v[112:115]\n"
      "ds_read2_b32 v[162:163], v214 offset0:65 offset1:64\n"
      "v_mfma_f32_16x16x32_bf16 v[108:111], v[144:147], v[188:191], v[108:111]\n"
      "s_waitcnt lgkmcnt(5)\n"
      "v_mfma_f32_16x16x32_bf16 v[104:107], v[144:147], v[184:187], v[104:107]\n"
      "v_mfma_f32_16x16x32_bf16 v[100:103], v[144:147], v[180:183], v[100:103]\n"
      "ds_read2_b32 v[164:165], v214 offset0:195 offset1:194\n"
      "s_waitcnt lgkmcnt(4)\n"
      "v_mfma_f32_16x16x32_bf16 v[96:99], v[144:147], v[176:179], v[96:99]\n"
      "ds_read2_b32 v[166:167], v214 offset0:193 offset1:192\n"
      "v_mfma_f32_16x16x32_bf16 v[124:127], v[148:151], v[200:203], v[124:127]\n"
      "v_mfma_f32_16x16x32_bf16 v[120:123], v[148:151], v[196:199], v[120:123]\n"
      "ds_read2_b32 v[168:169], v215 offset0:67 offset1:66\n"
      "v_mfma_f32_16x16x32_bf16 v[116:119], v[148:151], v[192:195], v[116:119]\n"
      "ds_read2_b32 v[170:171], v215 offset0:65 offset1:64\n"
      "v_mfma_f32_16x16x32_bf16 v[112:115], v[148:151], v[188:191], v[112:115]\n"
      "ds_read2_b32 v[172:173], v215 offset0:195 offset1:194\n"
      "v_mfma_f32_16x16x32_bf16 v[108:111], v[148:151], v[184:187], v[108:111]\n"
      "ds_read2_b32 v[174:175], v215 offset0:193 offset1:192\n"
      "v_mfma_f32_16x16x32_bf16 v[104:107], v[148:151], v[180:183], v[104:107]\n"
      "ds_read2_b32 v[144:145], v212 offset0:83 offset1:82\n"
      "v_mfma_f32_16x16x32_bf16 v[100:103], v[148:151], v[176:179], v[100:103]\n"
      "ds_read2_b32 v[146:147], v212 offset0:81 offset1:80\n"
      "v_mfma_f32_16x16x32_bf16 v[124:127], v[152:155], v[196:199], v[124:127]\n"
      "ds_read_b128 v[204:207], v217 offset:3712\n"
      "v_mfma_f32_16x16x32_bf16 v[120:123], v[152:155], v[192:195], v[120:123]\n"
      "ds_read_b128 v[200:203], v217 offset:3200\n"
      "v_mfma_f32_16x16x32_bf16 v[116:119], v[152:155], v[188:191], v[116:119]\n"
      "ds_read_b128 v[196:199], v217 offset:2688\n"
      "v_mfma_f32_16x16x32_bf16 v[112:115], v[152:155], v[184:187], v[112:115]\n"
      "v_mfma_f32_16x16x32_bf16 v[108:111], v[152:155], v[180:183], v[108:111]\n"
      "v_mfma_f32_16x16x32_bf16 v[104:107], v[152:155], v[176:179], v[104:107]\n"
      "ds_read2_b32 v[148:149], v212 offset0:211 offset1:210\n"
      "s_waitcnt lgkmcnt(5)\n"
      "v_mfma_f32_16x16x32_bf16 v[124:127], v[156:159], v[192:195], v[124:127]\n"
      "ds_read_b128 v[192:195], v217 offset:2176\n"
      "v_mfma_f32_16x16x32_bf16 v[120:123], v[156:159], v[188:191], v[120:123]\n"
      "ds_read2_b32 v[150:151], v212 offset0:209 offset1:208\n"
      "v_mfma_f32_16x16x32_bf16 v[116:119], v[156:159], v[184:187], v[116:119]\n"
      "v_mfma_f32_16x16x32_bf16 v[112:115], v[156:159], v[180:183], v[112:115]\n"
      "v_mfma_f32_16x16x32_bf16 v[108:111], v[156:159], v[176:179], v[108:111]\n"
      "v_mfma_f32_16x16x32_bf16 v[124:127], v[160:163], v[188:191], v[124:127]\n"
      "ds_read_b128 v[188:191], v217 offset:1664\n"
      "v_mfma_f32_16x16x32_bf16 v[120:123], v[160:163], v[184:187], v[120:123]\n"
      "ds_read2_b32 v[152:153], v213 offset0:83 offset1:82\n"
      "v_mfma_f32_16x16x32_bf16 v[116:119], v[160:163], v[180:183], v[116:119]\n"
      "ds_read2_b32 v[154:155], v213 offset0:81 offset1:80\n"
      "v_mfma_f32_16x16x32_bf16 v[112:115], v[160:163], v[176:179], v[112:115]\n"
      "v_mfma_f32_16x16x32_bf16 v[124:127], v[164:167], v[184:187], v[124:127]\n"
      "ds_read_b128 v[184:187], v217 offset:1152\n"
      "v_mfma_f32_16x16x32_bf16 v[120:123], v[164:167], v[180:183], v[120:123]\n"
      "v_mfma_f32_16x16x32_bf16 v[116:119], v[164:167], v[176:179], v[116:119]\n"
      "v_mfma_f32_16x16x32_bf16 v[124:127], v[168:171], v[180:183], v[124:127]\n"
      "ds_read_b128 v[180:183], v217 offset:640\n"
      "v_mfma_f32_16x16x32_bf16 v[120:123], v[168:171], v[176:179], v[120:123]\n"
      "ds_read2_b32 v[156:157], v213 offset0:211 offset1:210\n"
      "v_mfma_f32_16x16x32_bf16 v[124:127], v[172:175], v[176:179], v[124:127]\n"
      "ds_read_b128 v[176:179], v217 offset:128\n"
      "s_waitcnt lgkmcnt(4)\n"
      "v_mfma_f32_16x16x32_bf16 v[124:127], v[144:147], v[204:207], v[124:127]\n"
      "ds_read2_b32 v[158:159], v213 offset0:209 offset1:208\n"
      "v_mfma_f32_16x16x32_bf16 v[120:123], v[144:147], v[200:203], v[120:123]\n"
      "v_mfma_f32_16x16x32_bf16 v[116:119], v[144:147], v[196:199], v[116:119]\n"
      "ds_read2_b32 v[160:161], v214 offset0:83 offset1:82\n"
      "v_mfma_f32_16x16x32_bf16 v[112:115], v[144:147], v[192:195], v[112:115]\n"
      "ds_read2_b32 v[162:163], v214 offset0:81 offset1:80\n"
      "v_mfma_f32_16x16x32_bf16 v[108:111], v[144:147], v[188:191], v[108:111]\n"
      "s_waitcnt lgkmcnt(5)\n"
      "v_mfma_f32_16x16x32_bf16 v[104:107], v[144:147], v[184:187], v[104:107]\n"
      "v_mfma_f32_16x16x32_bf16 v[100:103], v[144:147], v[180:183], v[100:103]\n"
      "ds_read2_b32 v[164:165], v214 offset0:211 offset1:210\n"
      "s_waitcnt lgkmcnt(4)\n"
      "v_mfma_f32_16x16x32_bf16 v[96:99], v[144:147], v[176:179], v[96:99]\n"
      "ds_read2_b32 v[166:167], v214 offset0:209 offset1:208\n"
      "v_mfma_f32_16x16x32_bf16 v[124:127], v[148:151], v[200:203], v[124:127]\n"
      "v_mfma_f32_16x16x32_bf16 v[120:123], v[148:151], v[196:199], v[120:123]\n"
      "ds_read2_b32 v[168:169], v215 offset0:83 offset1:82\n"
      "v_mfma_f32_16x16x32_bf16 v[116:119], v[148:151], v[192:195], v[116:119]\n"
      "ds_read2_b32 v[170:171], v215 offset0:81 offset1:80\n"
      "v_mfma_f32_16x16x32_bf16 v[112:115], v[148:151], v[188:191], v[112:115]\n"
      "ds_read2_b32 v[172:173], v215 offset0:211 offset1:210\n"
      "v_mfma_f32_16x16x32_bf16 v[108:111], v[148:151], v[184:187], v[108:111]\n"
      "ds_read2_b32 v[174:175], v215 offset0:209 offset1:208\n"
      "v_mfma_f32_16x16x32_bf16 v[104:107], v[148:151], v[180:183], v[104:107]\n"
      "ds_read2_b32 v[144:145], v212 offset0:99 offset1:98\n"
      "v_mfma_f32_16x16x32_bf16 v[100:103], v[148:151], v[176:179], v[100:103]\n"
      "ds_read2_b32 v[146:147], v212 offset0:97 offset1:96\n"
      "v_mfma_f32_16x16x32_bf16 v[124:127], v[152:155], v[196:199], v[124:127]\n"
      "ds_read_b128 v[204:207], v217 offset:3648\n"
      "v_mfma_f32_16x16x32_bf16 v[120:123], v[152:155], v[192:195], v[120:123]\n"
      "ds_read_b128 v[200:203], v217 offset:3136\n"
      "v_mfma_f32_16x16x32_bf16 v[116:119], v[152:155], v[188:191], v[116:119]\n"
      "ds_read_b128 v[196:199], v217 offset:2624\n"
      "v_mfma_f32_16x16x32_bf16 v[112:115], v[152:155], v[184:187], v[112:115]\n"
      "v_mfma_f32_16x16x32_bf16 v[108:111], v[152:155], v[180:183], v[108:111]\n"
      "v_mfma_f32_16x16x32_bf16 v[104:107], v[152:155], v[176:179], v[104:107]\n"
      "ds_read2_b32 v[148:149], v212 offset0:227 offset1:226\n"
      "s_waitcnt lgkmcnt(5)\n"
      "v_mfma_f32_16x16x32_bf16 v[124:127], v[156:159], v[192:195], v[124:127]\n"
      "ds_read_b128 v[192:195], v217 offset:2112\n"
      "v_mfma_f32_16x16x32_bf16 v[120:123], v[156:159], v[188:191], v[120:123]\n"
      "ds_read2_b32 v[150:151], v212 offset0:225 offset1:224\n"
      "v_mfma_f32_16x16x32_bf16 v[116:119], v[156:159], v[184:187], v[116:119]\n"
      "v_mfma_f32_16x16x32_bf16 v[112:115], v[156:159], v[180:183], v[112:115]\n"
      "v_mfma_f32_16x16x32_bf16 v[108:111], v[156:159], v[176:179], v[108:111]\n"
      "v_mfma_f32_16x16x32_bf16 v[124:127], v[160:163], v[188:191], v[124:127]\n"
      "ds_read_b128 v[188:191], v217 offset:1600\n"
      "v_mfma_f32_16x16x32_bf16 v[120:123], v[160:163], v[184:187], v[120:123]\n"
      "ds_read2_b32 v[152:153], v213 offset0:99 offset1:98\n"
      "v_mfma_f32_16x16x32_bf16 v[116:119], v[160:163], v[180:183], v[116:119]\n"
      "ds_read2_b32 v[154:155], v213 offset0:97 offset1:96\n"
      "v_mfma_f32_16x16x32_bf16 v[112:115], v[160:163], v[176:179], v[112:115]\n"
      "v_mfma_f32_16x16x32_bf16 v[124:127], v[164:167], v[184:187], v[124:127]\n"
      "ds_read_b128 v[184:187], v217 offset:1088\n"
      "v_mfma_f32_16x16x32_bf16 v[120:123], v[164:167], v[180:183], v[120:123]\n"
      "v_mfma_f32_16x16x32_bf16 v[116:119], v[164:167], v[176:179], v[116:119]\n"
      "v_mfma_f32_16x16x32_bf16 v[124:127], v[168:171], v[180:183], v[124:127]\n"
      "ds_read_b128 v[180:183], v217 offset:576\n"
      "v_mfma_f32_16x16x32_bf16 v[120:123], v[168:171], v[176:179], v[120:123]\n"
      "ds_read2_b32 v[156:157], v213 offset0:227 offset1:226\n"
      "v_mfma_f32_16x16x32_bf16 v[124:127], v[172:175], v[176:179], v[124:127]\n"
      "ds_read_b128 v[176:179], v217 offset:64\n"
      "s_waitcnt lgkmcnt(4)\n"
      "v_mfma_f32_16x16x32_bf16 v[124:127], v[144:147], v[204:207], v[124:127]\n"
      "ds_read2_b32 v[158:159], v213 offset0:225 offset1:224\n"
      "v_mfma_f32_16x16x32_bf16 v[120:123], v[144:147], v[200:203], v[120:123]\n"
      "v_mfma_f32_16x16x32_bf16 v[116:119], v[144:147], v[196:199], v[116:119]\n"
      "ds_read2_b32 v[160:161], v214 offset0:99 offset1:98\n"
      "v_mfma_f32_16x16x32_bf16 v[112:115], v[144:147], v[192:195], v[112:115]\n"
      "ds_read2_b32 v[162:163], v214 offset0:97 offset1:96\n"
      "v_mfma_f32_16x16x32_bf16 v[108:111], v[144:147], v[188:191], v[108:111]\n"
      "s_waitcnt lgkmcnt(5)\n"
      "v_mfma_f32_16x16x32_bf16 v[104:107], v[144:147], v[184:187], v[104:107]\n"
      "v_mfma_f32_16x16x32_bf16 v[100:103], v[144:147], v[180:183], v[100:103]\n"
      "ds_read2_b32 v[164:165], v214 offset0:227 offset1:226\n"
      "s_waitcnt lgkmcnt(4)\n"
      "v_mfma_f32_16x16x32_bf16 v[96:99], v[144:147], v[176:179], v[96:99]\n"
      "ds_read2_b32 v[166:167], v214 offset0:225 offset1:224\n"
      "v_mfma_f32_16x16x32_bf16 v[124:127], v[148:151], v[200:203], v[124:127]\n"
      "v_mfma_f32_16x16x32_bf16 v[120:123], v[148:151], v[196:199], v[120:123]\n"
      "ds_read2_b32 v[168:169], v215 offset0:99 offset1:98\n"
      "v_mfma_f32_16x16x32_bf16 v[116:119], v[148:151], v[192:195], v[116:119]\n"
      "ds_read2_b32 v[170:171], v215 offset0:97 offset1:96\n"
      "v_mfma_f32_16x16x32_bf16 v[112:115], v[148:151], v[188:191], v[112:115]\n"
      "ds_read2_b32 v[172:173], v215 offset0:227 offset1:226\n"
      "v_mfma_f32_16x16x32_bf16 v[108:111], v[148:151], v[184:187], v[108:111]\n"
      "ds_read2_b32 v[174:175], v215 offset0:225 offset1:224\n"
      "v_mfma_f32_16x16x32_bf16 v[104:107], v[148:151], v[180:183], v[104:107]\n"
      "ds_read2_b32 v[144:145], v212 offset0:115 offset1:114\n"
      "v_mfma_f32_16x16x32_bf16 v[100:103], v[148:151], v[176:179], v[100:103]\n"
      "ds_read2_b32 v[146:147], v212 offset0:113 offset1:112\n"
      "v_mfma_f32_16x16x32_bf16 v[124:127], v[152:155], v[196:199], v[124:127]\n"
      "ds_read_b128 v[204:207], v217 offset:3584\n"
      "v_mfma_f32_16x16x32_bf16 v[120:123], v[152:155], v[192:195], v[120:123]\n"
      "ds_read_b128 v[200:203], v217 offset:3072\n"
      "v_mfma_f32_16x16x32_bf16 v[116:119], v[152:155], v[188:191], v[116:119]\n"
      "ds_read_b128 v[196:199], v217 offset:2560\n"
      "v_mfma_f32_16x16x32_bf16 v[112:115], v[152:155], v[184:187], v[112:115]\n"
      "v_mfma_f32_16x16x32_bf16 v[108:111], v[152:155], v[180:183], v[108:111]\n"
      "v_mfma_f32_16x16x32_bf16 v[104:107], v[152:155], v[176:179], v[104:107]\n"
      "ds_read2_b32 v[148:149], v212 offset0:243 offset1:242\n"
      "s_waitcnt lgkmcnt(5)\n"
      "v_mfma_f32_16x16x32_bf16 v[124:127], v[156:159], v[192:195], v[124:127]\n"
      "ds_read_b128 v[192:195], v217 offset:2048\n"
      "v_mfma_f32_16x16x32_bf16 v[120:123], v[156:159], v[188:191], v[120:123]\n"
      "ds_read2_b32 v[150:151], v212 offset0:241 offset1:240\n"
      "v_mfma_f32_16x16x32_bf16 v[116:119], v[156:159], v[184:187], v[116:119]\n"
      "v_mfma_f32_16x16x32_bf16 v[112:115], v[156:159], v[180:183], v[112:115]\n"
      "v_mfma_f32_16x16x32_bf16 v[108:111], v[156:159], v[176:179], v[108:111]\n"
      "v_mfma_f32_16x16x32_bf16 v[124:127], v[160:163], v[188:191], v[124:127]\n"
      "ds_read_b128 v[188:191], v217 offset:1536\n"
      "v_mfma_f32_16x16x32_bf16 v[120:123], v[160:163], v[184:187], v[120:123]\n"
      "ds_read2_b32 v[152:153], v213 offset0:115 offset1:114\n"
      "v_mfma_f32_16x16x32_bf16 v[116:119], v[160:163], v[180:183], v[116:119]\n"
      "ds_read2_b32 v[154:155], v213 offset0:113 offset1:112\n"
      "v_mfma_f32_16x16x32_bf16 v[112:115], v[160:163], v[176:179], v[112:115]\n"
      "v_mfma_f32_16x16x32_bf16 v[124:127], v[164:167], v[184:187], v[124:127]\n"
      "ds_read_b128 v[184:187], v217 offset:1024\n"
      "v_mfma_f32_16x16x32_bf16 v[120:123], v[164:167], v[180:183], v[120:123]\n"
      "v_mfma_f32_16x16x32_bf16 v[116:119], v[164:167], v[176:179], v[116:119]\n"
      "v_mfma_f32_16x16x32_bf16 v[124:127], v[168:171], v[180:183], v[124:127]\n"
      "ds_read_b128 v[180:183], v217 offset:512\n"
      "v_mfma_f32_16x16x32_bf16 v[120:123], v[168:171], v[176:179], v[120:123]\n"
      "ds_read2_b32 v[156:157], v213 offset0:243 offset1:242\n"
      "v_mfma_f32_16x16x32_bf16 v[124:127], v[172:175], v[176:179], v[124:127]\n"
      "ds_read_b128 v[176:179], v217 offset:0\n"
      "s_waitcnt lgkmcnt(4)\n"
      "v_mfma_f32_16x16x32_bf16 v[124:127], v[144:147], v[204:207], v[124:127]\n"
      "ds_read2_b32 v[158:159], v213 offset0:241 offset1:240\n"
      "v_mfma_f32_16x16x32_bf16 v[120:123], v[144:147], v[200:203], v[120:123]\n"
      "v_mfma_f32_16x16x32_bf16 v[116:119], v[144:147], v[196:199], v[116:119]\n"
      "ds_read2_b32 v[160:161], v214 offset0:115 offset1:114\n"
      "v_mfma_f32_16x16x32_bf16 v[112:115], v[144:147], v[192:195], v[112:115]\n"
      "ds_read2_b32 v[162:163], v214 offset0:113 offset1:112\n"
      "v_mfma_f32_16x16x32_bf16 v[108:111], v[144:147], v[188:191], v[108:111]\n"
      "s_waitcnt lgkmcnt(5)\n"
      "v_mfma_f32_16x16x32_bf16 v[104:107], v[144:147], v[184:187], v[104:107]\n"
      "v_mfma_f32_16x16x32_bf16 v[100:103], v[144:147], v[180:183], v[100:103]\n"
      "ds_read2_b32 v[164:165], v214 offset0:243 offset1:242\n"
      "s_waitcnt lgkmcnt(4)\n"
      "v_mfma_f32_16x16x32_bf16 v[96:99], v[144:147], v[176:179], v[96:99]\n"
      "ds_read2_b32 v[166:167], v214 offset0:241 offset1:240\n"
      "v_mfma_f32_16x16x32_bf16 v[124:127], v[148:151], v[200:203], v[124:127]\n"
      "v_mfma_f32_16x16x32_bf16 v[120:123], v[148:151], v[196:199], v[120:123]\n"
      "ds_read2_b32 v[168:169], v215 offset0:115 offset1:114\n"
      "v_mfma_f32_16x16x32_bf16 v[116:119], v[148:151], v[192:195], v[116:119]\n"
      "ds_read2_b32 v[170:171], v215 offset0:113 offset1:112\n"
      "v_mfma_f32_16x16x32_bf16 v[112:115], v[148:151], v[188:191], v[112:115]\n"
      "ds_read2_b32 v[172:173], v215 offset0:243 offset1:242\n"
      "v_mfma_f32_16x16x32_bf16 v[108:111], v[148:151], v[184:187], v[108:111]\n"
      "ds_read2_b32 v[174:175], v215 offset0:241 offset1:240\n"
      "v_mfma_f32_16x16x32_bf16 v[104:107], v[148:151], v[180:183], v[104:107]\n"
      "v_mfma_f32_16x16x32_bf16 v[100:103], v[148:151], v[176:179], v[100:103]\n"
      "v_mfma_f32_16x16x32_bf16 v[124:127], v[152:155], v[196:199], v[124:127]\n"
      "v_mfma_f32_16x16x32_bf16 v[120:123], v[152:155], v[192:195], v[120:123]\n"
      "v_mfma_f32_16x16x32_bf16 v[116:119], v[152:155], v[188:191], v[116:119]\n"
      "v_mfma_f32_16x16x32_bf16 v[112:115], v[152:155], v[184:187], v[112:115]\n"
      "v_mfma_f32_16x16x32_bf16 v[108:111], v[152:155], v[180:183], v[108:111]\n"
      "v_mfma_f32_16x16x32_bf16 v[104:107], v[152:155], v[176:179], v[104:107]\n"
      "s_waitcnt lgkmcnt(0)\n"
      "v_mfma_f32_16x16x32_bf16 v[124:127], v[156:159], v[192:195], v[124:127]\n"
      "v_mfma_f32_16x16x32_bf16 v[120:123], v[156:159], v[188:191], v[120:123]\n"
      "v_mfma_f32_16x16x32_bf16 v[116:119], v[156:159], v[184:187], v[116:119]\n"
      "v_mfma_f32_16x16x32_bf16 v[112:115], v[156:159], v[180:183], v[112:115]\n"
      "v_mfma_f32_16x16x32_bf16 v[108:111], v[156:159], v[176:179], v[108:111]\n"
      "v_mfma_f32_16x16x32_bf16 v[124:127], v[160:163], v[188:191], v[124:127]\n"
      "v_mfma_f32_16x16x32_bf16 v[120:123], v[160:163], v[184:187], v[120:123]\n"
      "v_mfma_f32_16x16x32_bf16 v[116:119], v[160:163], v[180:183], v[116:119]\n"
      "v_mfma_f32_16x16x32_bf16 v[112:115], v[160:163], v[176:179], v[112:115]\n"
      "v_mfma_f32_16x16x32_bf16 v[124:127], v[164:167], v[184:187], v[124:127]\n"
      "v_mfma_f32_16x16x32_bf16 v[120:123], v[164:167], v[180:183], v[120:123]\n"
      "v_mfma_f32_16x16x32_bf16 v[116:119], v[164:167], v[176:179], v[116:119]\n"
      "v_mfma_f32_16x16x32_bf16 v[124:127], v[168:171], v[180:183], v[124:127]\n"
      "v_mfma_f32_16x16x32_bf16 v[120:123], v[168:171], v[176:179], v[120:123]\n"
      "v_mfma_f32_16x16x32_bf16 v[124:127], v[172:175], v[176:179], v[124:127]\n"
      "ds_add_u32 v2, v3 offset:4276\n"
      "ds_read_b32 v31, v2 offset:4304\n"
      "v_cvt_pk_bf16_f32 v72, v96, v97\n"
      "v_cvt_pk_bf16_f32 v74, v100, v101\n"
      "v_cvt_pk_bf16_f32 v73, v98, v99\n"
      "v_cvt_pk_bf16_f32 v75, v102, v103\n"
      "v_permlane16_swap_b32 v72, v74\n"
      "s_nop 0\n"
      "v_permlane16_swap_b32 v73, v75\n"
      "buffer_store_dwordx4 v[72:75], v5, s[20:23], s63 offen offset:0 sc1\n"
      "v_cvt_pk_bf16_f32 v76, v104, v105\n"
      "v_cvt_pk_bf16_f32 v78, v108, v109\n"
      "v_cvt_pk_bf16_f32 v77, v106, v107\n"
      "v_cvt_pk_bf16_f32 v79, v110, v111\n"
      "v_permlane16_swap_b32 v76, v78\n"
      "s_nop 0\n"
      "v_permlane16_swap_b32 v77, v79\n"
      "buffer_store_dwordx4 v[76:79], v5, s[20:23], s63 offen offset:1024 sc1\n"
      "v_cvt_pk_bf16_f32 v80, v112, v113\n"
      "v_cvt_pk_bf16_f32 v82, v116, v117\n"
      "v_cvt_pk_bf16_f32 v81, v114, v115\n"
      "v_cvt_pk_bf16_f32 v83, v118, v119\n"
      "v_permlane16_swap_b32 v80, v82\n"
      "s_nop 0\n"
      "v_permlane16_swap_b32 v81, v83\n"
      "buffer_store_dwordx4 v[80:83], v5, s[20:23], s63 offen offset:2048 sc1\n"
      "v_cvt_pk_bf16_f32 v84, v120, v121\n"
      "v_cvt_pk_bf16_f32 v86, v124, v125\n"
      "v_cvt_pk_bf16_f32 v85, v122, v123\n"
      "v_cvt_pk_bf16_f32 v87, v126, v127\n"
      "v_permlane16_swap_b32 v84, v86\n"
      "s_nop 0\n"
      "v_permlane16_swap_b32 v85, v87\n"
      "buffer_store_dwordx4 v[84:87], v5, s[20:23], s63 offen offset:3072 sc1\n"
      "s_branch .LCONV\n"
      ".Lt_1:\n"
      "s_mov_b32 s3, 0x10000\n"
      ".Lt_3:\n"
      "s_sleep 4\n"
      "buffer_load_dwordx2 v[76:77], v42, s[20:23], s31 offen sc1\n"
      "s_waitcnt vmcnt(0)\n"
      "v_cmp_ne_u32 vcc, s58, v77\n"
      "s_sub_u32 s3, s3, 1\n"
      "s_cselect_b64 vcc, 0, vcc\n"
      "s_cbranch_vccnz .Lt_3\n"
      "s_branch .Lt_2\n"
      ".Lb_4:\n"
      "s_mov_b64 exec, vcc\n"
      "v_mov_b32 v80, 0xc0135761\n"
      "v_mov_b32 v81, 0xbdd2d3e8\n"
      "v_mov_b32 v82, 1.0\n"
      "v_mov_b32 v83, v77\n"
      "s_mov_b64 exec, -1\n"
      "ds_add_u32 v2, v3 offset:4144\n"
      "s_branch .Lb_5\n"
      ".Lw_6:\n"
      "s_mov_b32 s3, 0x80000\n"
      ".Lw_8:\n"
      "ds_read_b32 v4, v2 offset:4152\n"
      "s_waitcnt lgkmcnt(0)\n"
      "v_readfirstlane_b32 s2, v4\n"
      "s_sub_u32 s3, s3, 1\n"
      "s_cselect_b32 s2, -1, s2\n"
      "s_cmp_lt_u32 s2, 4\n"
      "s_cbranch_scc1 .Lw_8\n"
      "s_branch .Lw_7\n"
      ".Lf_9:\n"
      "s_mov_b32 s3, 0x10000\n"
      ".Lf_11:\n"
      "s_sleep 4\n"
      "buffer_load_dwordx2 v[50:51], v31, s[20:23], 0 offen sc1\n"
      "s_waitcnt vmcnt(0)\n"
      "v_cmp_ne_u64 vcc, s[58:59], v[50:51]\n"
      "s_sub_u32 s3, s3, 1\n"
      "s_cselect_b64 vcc, 0, vcc\n"
      "s_cbranch_vccnz .Lf_11\n"
      "s_branch .Lf_10\n"
      ".Lf_12:\n"
      "s_mov_b32 s3, 0x10000\n"
      ".Lf_14:\n"
      "s_sleep 4\n"
      "buffer_load_dwordx2 v[48:49], v30, s[20:23], 0 offen sc1\n"
      "s_waitcnt vmcnt(0)\n"
      "v_cmp_ne_u64 vcc, s[58:59], v[48:49]\n"
      "s_sub_u32 s3, s3, 1\n"
      "s_cselect_b64 vcc, 0, vcc\n"
      "s_cbranch_vccnz .Lf_14\n"
      "s_branch .Lf_13\n"
      ".Lw_16:\n"
      "s_mov_b32 s3, 0x80000\n"
      ".Lw_18:\n"
      "ds_read_b32 v4, v2 offset:4168\n"
      "s_waitcnt lgkmcnt(0)\n"
      "v_readfirstlane_b32 s2, v4\n"
      "s_sub_u32 s3, s3, 1\n"
      "s_cselect_b32 s2, -1, s2\n"
      "s_cmp_lt_u32 s2, 4\n"
      "s_cbranch_scc1 .Lw_18\n"
      "s_branch .Lw_17\n"
      ".Lf_19:\n"
      "s_mov_b32 s3, 0x10000\n"
      ".Lf_21:\n"
      "s_sleep 4\n"
      "buffer_load_dwordx2 v[10:11], v13, s[20:23], 0 offen sc1\n"
      "s_waitcnt vmcnt(0)\n"
      "v_cmp_ne_u64 vcc, s[58:59], v[10:11]\n"
      "s_sub_u32 s3, s3, 1\n"
      "s_cselect_b64 vcc, 0, vcc\n"
      "s_cbranch_vccnz .Lf_21\n"
      "s_branch .Lf_20\n"
      ".Lw_22:\n"
      "s_mov_b32 s3, 0x80000\n"
      ".Lw_24:\n"
      "ds_read_b32 v4, v2 offset:4276\n"
      "s_waitcnt lgkmcnt(0)\n"
      "v_readfirstlane_b32 s2, v4\n"
      "s_sub_u32 s3, s3, 1\n"
      "s_cselect_b32 s2, -1, s2\n"
      "s_cmp_lt_u32 s2, 4\n"
      "s_cbranch_scc1 .Lw_24\n"
      "s_branch .Lw_23\n"
      ".Lf_39:\n"
      "s_mov_b32 s3, 0x10000\n"
      ".Lf_41:\n"
      "s_sleep 4\n"
      "buffer_load_dwordx2 v[10:11], v13, s[20:23], s74 offen sc1\n"
      "s_waitcnt vmcnt(0)\n"
      "v_cmp_ne_u64 vcc, s[58:59], v[10:11]\n"
      "s_sub_u32 s3, s3, 1\n"
      "s_cselect_b64 vcc, 0, vcc\n"
      "s_cbranch_vccnz .Lf_41\n"
      "s_branch .Lf_40\n"
      ".Lw_58:\n"
      "s_mov_b32 s3, 0x80000\n"
      ".Lw_60:\n"
      "ds_read_b32 v4, v2 offset:4308\n"
      "s_waitcnt lgkmcnt(0)\n"
      "v_readfirstlane_b32 s2, v4\n"
      "s_sub_u32 s3, s3, 1\n"
      "s_cselect_b32 s2, -1, s2\n"
      "s_cmp_lt_u32 s2, 4\n"
      "s_cbranch_scc1 .Lw_60\n"
      "s_branch .Lw_59\n"
      ".Lg_121:\n"
      "s_waitcnt lgkmcnt(0)\n"
      "s_nop 11\n"
      "v_mul_f32 v80, v80, v27\n"
      "v_mul_f32 v81, v81, v27\n"
      "v_mul_f32 v82, v82, v27\n"
      "v_mul_f32 v83, v83, v27\n"
      "v_mul_f32 v84, v84, v27\n"
      "v_mul_f32 v85, v85, v27\n"
      "v_mul_f32 v86, v86, v27\n"
      "v_mul_f32 v87, v87, v27\n"
      "v_mul_f32 v88, v88, v27\n"
      "v_mul_f32 v89, v89, v27\n"
      "v_mul_f32 v90, v90, v27\n"
      "v_mul_f32 v91, v91, v27\n"
      "v_mul_f32 v92, v92, v27\n"
      "v_mul_f32 v93, v93, v27\n"
      "v_mul_f32 v94, v94, v27\n"
      "v_mul_f32 v95, v95, v27\n"
      "s_branch .Lg_122\n"
      ".Lg_123:\n"
      "s_waitcnt lgkmcnt(0)\n"
      "s_nop 11\n"
      "v_mul_f32 v64, v64, v27\n"
      "v_mul_f32 v65, v65, v27\n"
      "v_mul_f32 v66, v66, v27\n"
      "v_mul_f32 v67, v67, v27\n"
      "v_mul_f32 v68, v68, v27\n"
      "v_mul_f32 v69, v69, v27\n"
      "v_mul_f32 v70, v70, v27\n"
      "v_mul_f32 v71, v71, v27\n"
      "v_mul_f32 v72, v72, v27\n"
      "v_mul_f32 v73, v73, v27\n"
      "v_mul_f32 v74, v74, v27\n"
      "v_mul_f32 v75, v75, v27\n"
      "v_mul_f32 v76, v76, v27\n"
      "v_mul_f32 v77, v77, v27\n"
      "v_mul_f32 v78, v78, v27\n"
      "v_mul_f32 v79, v79, v27\n"
      "s_branch .Lg_124\n"
      ".Lw_125:\n"
      "s_mov_b32 s3, 0x80000\n"
      ".Lw_127:\n"
      "ds_read_b32 v31, v2 offset:2072\n"
      "s_waitcnt lgkmcnt(0)\n"
      "v_readfirstlane_b32 s2, v31\n"
      "s_sub_u32 s3, s3, 1\n"
      "s_cselect_b32 s2, -1, s2\n"
      "s_cmp_lt_u32 s2, 4\n"
      "s_cbranch_scc1 .Lw_127\n"
      "s_branch .Lw_126\n"
      ".Lw_128:\n"
      "s_mov_b32 s3, 0x80000\n"
      ".Lw_130:\n"
      "ds_read_b32 v31, v2 offset:3096\n"
      "s_waitcnt lgkmcnt(0)\n"
      "v_readfirstlane_b32 s2, v31\n"
      "s_sub_u32 s3, s3, 1\n"
      "s_cselect_b32 s2, -1, s2\n"
      "s_cmp_lt_u32 s2, 4\n"
      "s_cbranch_scc1 .Lw_130\n"
      "s_branch .Lw_129\n"
      ".Lg_131:\n"
      "s_waitcnt lgkmcnt(0)\n"
      "s_nop 11\n"
      "v_mul_f32 v64, v64, v27\n"
      "v_mul_f32 v65, v65, v27\n"
      "v_mul_f32 v66, v66, v27\n"
      "v_mul_f32 v67, v67, v27\n"
      "v_mul_f32 v68, v68, v27\n"
      "v_mul_f32 v69, v69, v27\n"
      "v_mul_f32 v70, v70, v27\n"
      "v_mul_f32 v71, v71, v27\n"
      "v_mul_f32 v72, v72, v27\n"
      "v_mul_f32 v73, v73, v27\n"
      "v_mul_f32 v74, v74, v27\n"
      "v_mul_f32 v75, v75, v27\n"
      "v_mul_f32 v76, v76, v27\n"
      "v_mul_f32 v77, v77, v27\n"
      "v_mul_f32 v78, v78, v27\n"
      "v_mul_f32 v79, v79, v27\n"
      "s_branch .Lg_132\n"
      ".Lh_133:\n"
      "s_waitcnt vmcnt(0)\n"
      "s_mov_b64 exec, 255\n"
      "buffer_store_dwordx4 v[12:15], v29, s[20:23], s79 offen offset:0 sc1\n"
      "s_mov_b64 exec, -1\n"
      "s_mov_b32 s84, 64\n"
      "ds_add_u32 v2, v3 offset:4168\n"
      "s_branch .Lh_134\n"
      ".Lh_135:\n"
      "s_waitcnt vmcnt(1)\n"
      "s_mov_b64 exec, 255\n"
      "buffer_store_dwordx4 v[12:15], v29, s[20:23], s79 offen offset:0 sc1\n"
      "s_mov_b64 exec, -1\n"
      "s_mov_b32 s84, 64\n"
      "ds_add_u32 v2, v3 offset:4168\n"
      "s_branch .Lh_136\n"
      ".Lh_137:\n"
      "s_waitcnt vmcnt(1)\n"
      "s_mov_b64 exec, 255\n"
      "buffer_store_dwordx4 v[12:15], v29, s[20:23], s79 offen offset:0 sc1\n"
      "s_mov_b64 exec, -1\n"
      "s_mov_b32 s84, 64\n"
      "ds_add_u32 v2, v3 offset:4168\n"
      "s_branch .Lh_138\n"
      ".Lh_139:\n"
      "s_waitcnt vmcnt(2)\n"
      "s_mov_b64 exec, 255\n"
      "buffer_store_dwordx4 v[12:15], v29, s[20:23], s79 offen offset:0 sc1\n"
      "s_mov_b64 exec, -1\n"
      "s_mov_b32 s84, 64\n"
      "ds_add_u32 v2, v3 offset:4168\n"
      "s_branch .Lh_140\n"
      ".Lg_141:\n"
      "s_waitcnt lgkmcnt(0)\n"
      "s_nop 11\n"
      "v_mul_f32 v64, v64, v27\n"
      "v_mul_f32 v65, v65, v27\n"
      "v_mul_f32 v66, v66, v27\n"
      "v_mul_f32 v67, v67, v27\n"
      "v_mul_f32 v68, v68, v27\n"
      "v_mul_f32 v69, v69, v27\n"
      "v_mul_f32 v70, v70, v27\n"
      "v_mul_f32 v71, v71, v27\n"
      "v_mul_f32 v72, v72, v27\n"
      "v_mul_f32 v73, v73, v27\n"
      "v_mul_f32 v74, v74, v27\n"
      "v_mul_f32 v75, v75, v27\n"
      "v_mul_f32 v76, v76, v27\n"
      "v_mul_f32 v77, v77, v27\n"
      "v_mul_f32 v78, v78, v27\n"
      "v_mul_f32 v79, v79, v27\n"
      "s_branch .Lg_142\n"
      ".Lh_143:\n"
      "s_waitcnt vmcnt(4)\n"
      "s_mov_b64 exec, 255\n"
      "buffer_store_dwordx4 v[12:15], v29, s[20:23], s79 offen offset:0 sc1\n"
      "s_mov_b64 exec, -1\n"
      "s_mov_b32 s84, 64\n"
      "ds_add_u32 v2, v3 offset:4168\n"
      "s_branch .Lh_144\n"
      ".Lw_145:\n"
      "s_mov_b32 s3, 0x80000\n"
      ".Lw_147:\n"
      "ds_read_b32 v31, v18 offset:4240\n"
      "s_waitcnt lgkmcnt(0)\n"
      "v_readfirstlane_b32 s2, v31\n"
      "s_sub_u32 s3, s3, 1\n"
      "s_cselect_b32 s2, -1, s2\n"
      "s_cmp_lt_u32 s2, 4\n"
      "s_cbranch_scc1 .Lw_147\n"
      "s_branch .Lw_146\n"
      ".Lh_148:\n"
      "s_waitcnt vmcnt(4)\n"
      "s_mov_b64 exec, 255\n"
      "buffer_store_dwordx4 v[12:15], v29, s[20:23], s79 offen offset:0 sc1\n"
      "s_mov_b64 exec, -1\n"
      "s_mov_b32 s84, 64\n"
      "ds_add_u32 v2, v3 offset:4168\n"
      "s_branch .Lh_149\n"
      ".Lh_150:\n"
      "s_waitcnt vmcnt(0)\n"
      "s_mov_b64 exec, 255\n"
      "buffer_store_dwordx4 v[12:15], v29, s[20:23], s79 offen offset:128 sc1\n"
      "s_mov_b64 exec, -1\n"
      "s_mov_b32 s85, 64\n"
      "s_branch .Lh_151\n"
      ".Lh_152:\n"
      "s_waitcnt vmcnt(4)\n"
      "s_mov_b64 exec, 255\n"
      "buffer_store_dwordx4 v[12:15], v29, s[20:23], s79 offen offset:0 sc1\n"
      "s_mov_b64 exec, -1\n"
      "s_mov_b32 s84, 64\n"
      "ds_add_u32 v2, v3 offset:4168\n"
      "s_branch .Lh_153\n"
      ".Lh_154:\n"
      "s_waitcnt vmcnt(0)\n"
      "s_mov_b64 exec, 255\n"
      "buffer_store_dwordx4 v[12:15], v29, s[20:23], s79 offen offset:128 sc1\n"
      "s_mov_b64 exec, -1\n"
      "s_mov_b32 s85, 64\n"
      "s_branch .Lh_155\n"
      ".Lh_156:\n"
      "s_waitcnt vmcnt(0)\n"
      "s_mov_b64 exec, 255\n"
      "buffer_store_dwordx4 v[12:15], v29, s[20:23], s79 offen offset:128 sc1\n"
      "s_mov_b64 exec, -1\n"
      "s_mov_b32 s85, 64\n"
      "s_branch .Lh_157\n"
      ".Lh_158:\n"
      "s_waitcnt vmcnt(0)\n"
      "s_mov_b64 exec, 255\n"
      "buffer_store_dwordx4 v[12:15], v29, s[20:23], s79 offen offset:128 sc1\n"
      "s_mov_b64 exec, -1\n"
      "s_mov_b32 s85, 64\n"
      "s_branch .Lh_159\n"
      ".Lh_160:\n"
      "s_waitcnt vmcnt(0)\n"
      "s_mov_b64 exec, 255\n"
      "buffer_store_dwordx4 v[12:15], v29, s[20:23], s79 offen offset:128 sc1\n"
      "s_mov_b64 exec, -1\n"
      "s_mov_b32 s85, 64\n"
      "s_branch .Lh_161\n"
      ".Lw_162:\n"
      "s_mov_b32 s3, 0x80000\n"
      ".Lw_164:\n"
      "ds_read_b32 v31, v2 offset:4276\n"
      "s_waitcnt lgkmcnt(0)\n"
      "v_readfirstlane_b32 s2, v31\n"
      "s_sub_u32 s3, s3, 1\n"
      "s_cselect_b32 s2, -1, s2\n"
      "s_cmp_lt_u32 s2, 4\n"
      "s_cbranch_scc1 .Lw_164\n"
      "s_branch .Lw_163\n"
      ".Lw_168:\n"
      "s_mov_b32 s3, 0x80000\n"
      ".Lw_170:\n"
      "ds_read_b32 v31, v2 offset:4308\n"
      "s_waitcnt lgkmcnt(0)\n"
      "v_readfirstlane_b32 s2, v31\n"
      "s_sub_u32 s3, s3, 1\n"
      "s_cselect_b32 s2, -1, s2\n"
      "s_cmp_lt_u32 s2, 4\n"
      "s_cbranch_scc1 .Lw_170\n"
      "s_branch .Lw_169\n"
      ".Lf_172:\n"
      "s_mov_b32 s3, 0x10000\n"
      ".Lf_174:\n"
      "s_sleep 4\n"
      "buffer_load_dwordx2 v[26:27], v25, s[20:23], 0 offen sc1\n"
      "s_waitcnt vmcnt(0)\n"
      "v_cmp_ne_u64 vcc, s[58:59], v[26:27]\n"
      "s_sub_u32 s3, s3, 1\n"
      "s_cselect_b64 vcc, 0, vcc\n"
      "s_cbranch_vccnz .Lf_174\n"
      "s_branch .Lf_173\n"
      ".Ly_177:\n"
      "s_mov_b32 s3, 0x10000\n"
      ".Ly_179:\n"
      "s_sleep 4\n"
      "buffer_load_dwordx4 v[164:167], v9, s[20:23], s72 offen offset:0 sc1\n"
      "buffer_load_dwordx4 v[168:171], v9, s[20:23], s72 offen offset:1024 sc1\n"
      "buffer_load_dwordx4 v[172:175], v9, s[20:23], s72 offen offset:2048 sc1\n"
      "s_waitcnt vmcnt(0)\n"
      "v_xor_b32 v21, s74, v167\n"
      "v_bitop3_b32 v21, v21, v171, s74 bitop3:0xf6\n"
      "v_bitop3_b32 v21, v21, v175, s74 bitop3:0xf6\n"
      "v_cmp_lt_u32 vcc, 0xffff, v21\n"
      "s_sub_u32 s3, s3, 1\n"
      "s_cselect_b64 vcc, 0, vcc\n"
      "s_cbranch_vccnz .Ly_179\n"
      "s_branch .Ly_178\n"
      ".Lt_188:\n"
      "s_mov_b32 s3, 0x10000\n"
      ".Lt_190:\n"
      "s_sleep 4\n"
      "buffer_load_dwordx2 v[76:77], v42, s[20:23], s31 offen sc1\n"
      "s_waitcnt vmcnt(0)\n"
      "v_cmp_ne_u32 vcc, s58, v77\n"
      "s_sub_u32 s3, s3, 1\n"
      "s_cselect_b64 vcc, 0, vcc\n"
      "s_cbranch_vccnz .Lt_190\n"
      "s_branch .Lt_189\n"
      ".Lb_191:\n"
      "s_mov_b64 exec, vcc\n"
      "v_mov_b32 v80, 0xc0135761\n"
      "v_mov_b32 v81, 0xbdd2d3e8\n"
      "v_mov_b32 v82, 1.0\n"
      "v_mov_b32 v83, v77\n"
      "s_mov_b64 exec, -1\n"
      "ds_add_u32 v2, v3 offset:4144\n"
      "s_branch .Lb_192\n"
      ".Lf_195:\n"
      "s_mov_b32 s3, 0x10000\n"
      ".Lf_197:\n"
      "s_sleep 4\n"
      "buffer_load_dwordx2 v[48:49], v30, s[20:23], 0 offen sc1\n"
      "s_waitcnt vmcnt(0)\n"
      "v_cmp_ne_u64 vcc, s[58:59], v[48:49]\n"
      "s_sub_u32 s3, s3, 1\n"
      "s_cselect_b64 vcc, 0, vcc\n"
      "s_cbranch_vccnz .Lf_197\n"
      "s_branch .Lf_196\n"
      ".Lf_199:\n"
      "s_mov_b32 s3, 0x10000\n"
      ".Lf_201:\n"
      "s_sleep 4\n"
      "buffer_load_dwordx2 v[26:27], v25, s[20:23], s64 offen sc1\n"
      "s_waitcnt vmcnt(0)\n"
      "v_cmp_ne_u64 vcc, s[58:59], v[26:27]\n"
      "s_sub_u32 s3, s3, 1\n"
      "s_cselect_b64 vcc, 0, vcc\n"
      "s_cbranch_vccnz .Lf_201\n"
      "s_branch .Lf_200\n"
      ".Lf_202:\n"
      "s_mov_b32 s3, 0x10000\n"
      ".Lf_204:\n"
      "s_sleep 4\n"
      "buffer_load_dwordx2 v[26:27], v25, s[20:23], s64 offen sc1\n"
      "s_waitcnt vmcnt(0)\n"
      "v_cmp_ne_u64 vcc, s[58:59], v[26:27]\n"
      "s_sub_u32 s3, s3, 1\n"
      "s_cselect_b64 vcc, 0, vcc\n"
      "s_cbranch_vccnz .Lf_204\n"
      "s_branch .Lf_203\n"
      ".Lf_205:\n"
      "s_mov_b32 s3, 0x10000\n"
      ".Lf_207:\n"
      "s_sleep 4\n"
      "buffer_load_dwordx2 v[26:27], v25, s[20:23], s64 offen sc1\n"
      "s_waitcnt vmcnt(0)\n"
      "v_cmp_ne_u64 vcc, s[58:59], v[26:27]\n"
      "s_sub_u32 s3, s3, 1\n"
      "s_cselect_b64 vcc, 0, vcc\n"
      "s_cbranch_vccnz .Lf_207\n"
      "s_branch .Lf_206\n"
      ".Lf_208:\n"
      "s_mov_b32 s3, 0x10000\n"
      ".Lf_210:\n"
      "s_sleep 4\n"
      "buffer_load_dwordx2 v[26:27], v25, s[20:23], s64 offen sc1\n"
      "s_waitcnt vmcnt(0)\n"
      "v_cmp_ne_u64 vcc, s[58:59], v[26:27]\n"
      "s_sub_u32 s3, s3, 1\n"
      "s_cselect_b64 vcc, 0, vcc\n"
      "s_cbranch_vccnz .Lf_210\n"
      "s_branch .Lf_209\n"
      ".Lfz_end:\n"
      ".fill 320, 4, 0xBF800000\n"
      : "+{v0}"(tid) : "{s[0:1]}"(__builtin_amdgcn_kernarg_segment_ptr()), "{s2}"(blockIdx.x) : "v1", "v2", "v3", "v4", "v5", "v6", "v7", "v8", "v9", "v10", "v11", "v12", "v13", "v14", "v15", "v16", "v17", "v18", "v19", "v20", "v21", "v22", "v23", "v24", "v25", "v26", "v27", "v28", "v29", "v30", "v31", "v32", "v33", "v34", "v35", "v36", "v37", "v38", "v39", "v40", "v41", "v42", "v43", "v44", "v45", "v46", "v47", "v48", "v49", "v50", "v51", "v52", "v53", "v54", "v55", "v56", "v57", "v58", "v59", "v60", "v61", "v62", "v63", "v64", "v65", "v66", "v67", "v68", "v69", "v70", "v71", "v72", "v73", "v74", "v75", "v76", "v77", "v78", "v79", "v80", "v81", "v82", "v83", "v84", "v85", "v86", "v87", "v88", "v89", "v90", "v91", "v92", "v93", "v94", "v95", "v96", "v97", "v98", "v99", "v100", "v101", "v102", "v103", "v104", "v105", "v106", "v107", "v108", "v109", "v110", "v111", "v112", "v113", "v114", "v115", "v116", "v117", "v118", "v119", "v120", "v121", "v122", "v123", "v124", "v125", "v126", "v127", "v128", "v129", "v130", "v131", "v132", "v133", "v134", "v135", "v136", "v137", "v138", "v139", "v140", "v141", "v142", "v143", "v144", "v145", "v146", "v147", "v148", "v149", "v150", "v151", "v152", "v153", "v154", "v155", "v156", "v157", "v158", "v159", "v160", "v161", "v162", "v163", "v164", "v165", "v166", "v167", "v168", "v169", "v170", "v171", "v172", "v173", "v174", "v175", "v176", "v177", "v178", "v179", "v180", "v181", "v182", "v183", "v184", "v185", "v186", "v187", "v188", "v189", "v190", "v191", "v192", "v193", "v194", "v195", "v196", "v197", "v198", "v199", "v200", "v201", "v202", "v203", "v204", "v205", "v206", "v207", "v208", "v209", "v210", "v211", "v212", "v213", "v214", "v215", "v216", "v217", "v218", "v219", "v220", "v221", "v222", "v223", "v224", "v225", "v226", "v227", "v228", "v229", "v230", "v231", "v232", "v233", "v234", "v235", "v236", "v237", "v238", "v239", "v240", "v241", "v242", "v243", "v244", "v245", "v246", "v247", "v248", "v249", "v250", "v251", "v252", "v253", "v254", "v255", "s3", "s4", "s5", "s6", "s7", "s8", "s9", "s10", "s11", "s12", "s13", "s14", "s15", "s16", "s17", "s18", "s19", "s20", "s21", "s22", "s23", "s24", "s25", "s26", "s27", "s28", "s29", "s30", "s31", "s33", "s34", "s35", "s36", "s37", "s38", "s39", "s40", "s41", "s42", "s43", "s44", "s45", "s46", "s47", "s48", "s49", "s50", "s51", "s52", "s53", "s54", "s55", "s56", "s57", "s58", "s59", "s60", "s61", "s62", "s63", "s64", "s65", "s66", "s67", "s68", "s69", "s70", "s71", "s72", "s73", "s74", "s75", "s76", "s77", "s78", "s79", "s80", "s81", "s82", "s83", "s84", "s85", "s86", "s87", "s88", "s89", "s90", "s91", "s92", "s93", "s94", "s95", "s96", "s97", "s98", "s99", "vcc", "scc", "memory");
#endif
}
}

extern "C" void kernel_launch(void* const* d_in, const int* in_sizes, int n_in, void* d_out, int out_size, void* d_ws,
                              size_t ws_size, hipStream_t stream) {
  (void)hipFuncSetAttribute((const void*)fused_kernel, hipFuncAttributeMaxDynamicSharedMemorySize, 163840);
  fused_kernel<<<256, 512, 163840, stream>>>(d_in[0], 2621440u, 0x20000u, d_in[7], 2211840u, 0x20000u, d_in[1], 2621440u, 0x20000u, d_in[2], 2621440u, 0x20000u, d_ws, 18896896u, 0x20000u, d_in[3], 5120u, 0x20000u, d_in[8], 3456u, 0x20000u, d_in[5], 1280u, 0x20000u, d_in[4], 2621440u, 0x20000u, d_in[9], 2211840u, 0x20000u, d_in[10], 1280u, 0x20000u, d_out, 2621440u, 0x20000u);
}
```

```cpp
#include <hip/hip_runtime.h>
#include <stdint.h>
namespace {
constexpr unsigned WS_STATS = 8192, WS_KV = 32768, WS_Y2 = 2818048, WS_W2T = 5603328, WS_HT = 7897088, WS_EX = 15237120, WS_END = 18896896;
constexpr unsigned KVPI = 4224, Y2PI = 4224;
#define BUF const void*, unsigned, unsigned
__global__ __launch_bounds__(512, 2) void fused_kernel(BUF, BUF, BUF, BUF, BUF, BUF, BUF, BUF, BUF, BUF, BUF, BUF) {
#if defined(__HIP_DEVICE_COMPILE__)
  unsigned tid = threadIdx.x;
  asm volatile(
      "s_load_dwordx16 s[4:19], s[0:1], 0x0\n"
      "s_load_dwordx16 s[20:35], s[0:1], 0x40\n"
      "s_load_dwordx16 s[36:51], s[0:1], 0x80\n"
      "s_getpc_b64 s[64:65]\n"
      ".Lfz_pc:\n"
      "v_lshrrev_b32 v2, 6, v0\n"
      "v_and_b32 v1, 63, v0\n"
      "s_mov_b32 s60, s2\n"
      "v_readfirstlane_b32 s52, v2\n"
      "s_lshr_b32 s61, s2, 3\n"
      "s_mov_b32 s66, .Lfz_end-.Lfz_pc\n"
      "s_mov_b32 s67, 0x20000\n"
      "s_lshl3_add_u32 s62, s61, s52\n"
      "s_lshl_b32 s62, s62, 7\n"
      "v_and_b32 v2, 1, v0\n"
      "v_lshlrev_b32 v2, 15, v2\n"
      "buffer_load_dword v3, v2, s[64:67], s62 offen\n"
      "s_and_b32 s53, s52, 3\n"
      "s_and_b32 s54, s2, 1\n"
      "s_mul_i32 s62, s61, 37\n"
      "s_lshr_b32 s62, s62, 8\n"
      "s_mul_i32 s63, s62, 7\n"
      "s_sub_u32 s63, s61, s63\n"
      "s_cmp_lt_u32 s61, 28\n"
      "s_cselect_b32 s62, s62, s63\n"
      "s_cselect_b32 s55, s63, 7\n"
      "s_bfe_u32 s63, s2, 0x20001\n"
      "s_lshl2_add_u32 s56, s63, s62\n"
      "s_mul_i32 s63, s54, 7\n"
      "s_add_u32 s57, s63, s55\n"
      "s_mov_b32 s92, 0xffff0000\n"
      "s_mul_i32 s62, s2, 6\n"
      "s_sub_u32 s62, s62, 892\n"
      "s_lshl_b32 s63, s2, 1\n"
      "s_max_i32 s98, s62, s63\n"
      "s_and_b32 s62, s57, 1\n"
      "s_xor_b32 s63, s54, 1\n"
      "s_lshl1_add_u32 s99, s63, s62\n"
      "s_cmp_lt_u32 s52, 4\n"
      "s_cbranch_scc1 .LM\n"
      "s_cmp_lt_u32 s61, 28\n"
      "s_cbranch_scc0 .LSPARE\n"
      "v_lshrrev_b32 v72, 3, v1\n"
      "v_and_b32 v73, 7, v1\n"
      "v_mul_u32_u24 v74, 1280, v72\n"
      "v_lshl_add_u32 v24, v73, 4, v74\n"
      "v_lshrrev_b32 v75, 4, v1\n"
      "s_and_b32 s62, s53, 1\n"
      "s_lshl_b32 s62, s62, 2\n"
      "v_xor_b32 v73, v73, v75\n"
      "v_xor_b32 v73, s62, v73\n"
      "v_lshl_add_u32 v74, v73, 4, v74\n"
      "s_mul_i32 s62, s99, 40960\n"
      "v_add_u32 v20, s62, v74\n"
      "s_xor_b32 s62, s99, 1\n"
      "s_mul_i32 s62, s62, 40960\n"
      "v_add_u32 v21, s62, v74\n"
      "v_and_b32 v73, 15, v1\n"
      "v_lshlrev_b32 v76, 2, v75\n"
      "v_xor_b32 v73, v73, v76\n"
      "s_cmp_eq_u32 s57, 13\n"
      "s_cselect_b32 s63, 7, 15\n"
      "v_and_b32 v73, s63, v73\n"
      "v_mul_u32_u24 v75, 3456, v75\n"
      "v_lshl_add_u32 v16, v73, 4, v75\n"
      "v_add_u32 v17, 12800, v16\n"
      "v_add_u32 v18, 25600, v16\n"
      "v_add_u32 v19, 38400, v16\n"
      "s_mul_i32 s63, s56, 3\n"
      "s_add_u32 s63, s63, s57\n"
      "s_mul_i32 s64, s63, 205\n"
      "s_lshr_b32 s64, s64, 11\n"
      "s_mul_i32 s64, s64, 10\n"
      "s_sub_u32 s63, s63, s64\n"
      "s_lshl_b32 s64, s56, 4\n"
      "s_add_u32 s64, s64, s53\n"
      "s_mul_i32 s68, s64, 10240\n"
      "s_mul_i32 s69, s53, 55296\n"
      "s_lshl_b32 s64, s57, 8\n"
      "s_add_u32 s69, s69, s64\n"
      "s_lshl_b32 s64, s63, 7\n"
      "s_add_u32 s70, s68, s64\n"
      "s_mul_i32 s64, s63, 221184\n"
      "s_add_u32 s80, s69, s64\n"
      "s_add_u32 s64, s68, 1280\n"
      "s_and_b32 s91, s2, 7\n"
      "s_mul_i32 s91, s91, 28\n"
      "s_add_u32 s91, s91, s61\n"
      "s_mul_i32 s93, s91, 205\n"
      "s_lshr_b32 s90, s93, 10\n"
      "s_lshr_b32 s93, s93, 11\n"
      "s_mul_i32 s65, s93, 80640\n"
      "s_lshl_b32 s67, s91, 7\n"
      "s_add_u32 s65, s65, s67\n"
      "s_sub_u32 s66, s65, 10240\n"
      "s_cselect_b32 s66, 1073741824, s66\n"
      "s_mul_i32 s67, s53, 20480\n"
      "s_add_u32 s65, s65, s67\n"
      "s_lshr_b32 s67, s67, 1\n"
      "s_mul_i32 s90, s90, 39680\n"
      "s_add_u32 s90, s90, s67\n"
      "s_lshl_b32 s67, s91, 8\n"
      "s_add_u32 s90, s90, s67\n"
      "v_add_u32 v25, 9200, v24\n"
      "v_cmp_gt_u32 vcc, 40, v1\n"
      "v_mov_b32 v72, 1073741824\n"
      "s_mul_i32 s67, s53, 2080\n"
      "v_cndmask_b32 v26, v24, v72, vcc\n"
      "v_lshrrev_b32 v72, 4, v1\n"
      "v_mul_u32_u24 v72, 1280, v72\n"
      "v_and_b32 v73, 15, v1\n"
      "v_lshl_add_u32 v27, v73, 4, v72\n"
      "v_add_u32 v28, 4080, v27\n"
      "s_add_u32 s71, s70, 128\n"
      "s_add_u32 s81, s80, 221184\n"
      "s_cmp_eq_u32 s71, s64\n"
      "s_cselect_b32 s71, s68, s71\n"
      "s_cselect_b32 s81, s69, s81\n"
      "s_lshl_b32 s94, s53, 12\n"
      "s_lshl_b32 s95, s53, 10\n"
      "s_setprio 2\n"
      "s_waitcnt lgkmcnt(0)\n"
      "s_barrier\n"
      "s_add_u32 m0, s94, 0\n"
      "s_nop 0\n"
      "buffer_load_dwordx4 v16, s[8:11], s80 offen offset:0 lds\n"
      "buffer_load_dwordx4 v17, s[8:11], s80 offen offset:1024 lds\n"
      "buffer_load_dwordx4 v18, s[8:11], s80 offen offset:2048 lds\n"
      "buffer_load_dwordx4 v19, s[8:11], s80 offen offset:3072 lds\n"
      "s_add_u32 m0, s95, 16384\n"
      "s_nop 0\n"
      "buffer_load_dwordx4 v20, s[4:7], s70 offen lds\n"
      "s_add_u32 m0, s95, 20480\n"
      "s_nop 0\n"
      "buffer_load_dwordx4 v21, s[4:7], s70 offen lds\n"
      "s_mov_b32 m0, 139520\n"
      "s_nop 0\n"
      "buffer_load_dwordx4 v26, s[12:15], s66 offen nt lds\n"
      "s_add_u32 m0, s67, 140560\n"
      "s_nop 0\n"
      "buffer_load_dwordx4 v24, s[12:15], s65 offen nt lds\n"
      "buffer_load_dwordx4 v25, s[12:15], s65 offen offset:1040 nt lds\n"
      "s_add_u32 m0, s67, 148880\n"
      "s_nop 0\n"
      "buffer_load_dwordx4 v24, s[16:19], s65 offen nt lds\n"
      "buffer_load_dwordx4 v25, s[16:19], s65 offen offset:1040 nt lds\n"
      "s_add_u32 m0, s67, 122880\n"
      "s_nop 0\n"
      "buffer_load_dwordx4 v27, s[40:43], s90 offen nt lds\n"
      "buffer_load_dwordx4 v28, s[40:43], s90 offen offset:1040 nt lds\n"
      "s_add_u32 s72, s71, 128\n"
      "s_add_u32 s82, s81, 221184\n"
      "s_cmp_eq_u32 s72, s64\n"
      "s_cselect_b32 s72, s68, s72\n"
      "s_cselect_b32 s82, s69, s82\n"
      "s_add_u32 s73, s72, 128\n"
      "s_add_u32 s83, s82, 221184\n"
      "s_cmp_eq_u32 s73, s64\n"
      "s_cselect_b32 s73, s68, s73\n"
      "s_cselect_b32 s83, s69, s83\n"
      "s_add_u32 s74, s73, 128\n"
      "s_add_u32 s84, s83, 221184\n"
      "s_cmp_eq_u32 s74, s64\n"
      "s_cselect_b32 s74, s68, s74\n"
      "s_cselect_b32 s84, s69, s84\n"
      "s_add_u32 s75, s74, 128\n"
      "s_add_u32 s85, s84, 221184\n"
      "s_cmp_eq_u32 s75, s64\n"
      "s_cselect_b32 s75, s68, s75\n"
      "s_cselect_b32 s85, s69, s85\n"
      "s_add_u32 s76, s75, 128\n"
      "s_add_u32 s86, s85, 221184\n"
      "s_cmp_eq_u32 s76, s64\n"
      "s_cselect_b32 s76, s68, s76\n"
      "s_cselect_b32 s86, s69, s86\n"
      "s_add_u32 s77, s76, 128\n"
      "s_add_u32 s87, s86, 221184\n"
      "s_cmp_eq_u32 s77, s64\n"
      "s_cselect_b32 s77, s68, s77\n"
      "s_cselect_b32 s87, s69, s87\n"
      "s_add_u32 s78, s77, 128\n"
      "s_add_u32 s88, s87, 221184\n"
      "s_cmp_eq_u32 s78, s64\n"
      "s_cselect_b32 s78, s68, s78\n"
      "s_cselect_b32 s88, s69, s88\n"
      "s_add_u32 s79, s78, 128\n"
      "s_add_u32 s89, s88, 221184\n"
      "s_cmp_eq_u32 s79, s64\n"
      "s_cselect_b32 s79, s68, s79\n"
      "s_cselect_b32 s89, s69, s89\n"
      "s_xor_b32 s62, s99, 2\n"
      "s_mul_i32 s62, s62, 40960\n"
      "v_add_u32 v22, s62, v74\n"
      "s_xor_b32 s62, s99, 3\n"
      "s_mul_i32 s62, s62, 40960\n"
      "v_add_u32 v23, s62, v74\n"
      "s_mul_i32 s62, s93, 10\n"
      "s_sub_u32 s62, s91, s62\n"
      "s_lshl2_add_u32 s62, s62, s53\n"
      "s_lshl_b32 s62, s62, 5\n"
      "s_add_u32 s62, s62, s93\n"
      "s_lshl_b32 s62, s62, 7\n"
      "s_add_u32 s62, s62, 18644992\n"
      "v_mov_b32 v29, 1073741824\n"
      "s_mov_b32 exec_lo, 0xffff\n"
      "s_mov_b32 exec_hi, 0x0\n"
      "v_lshl_add_u32 v29, v1, 3, s62\n"
      "s_mov_b64 exec, -1\n"
      "s_mul_i32 s93, s91, 205\n"
      "s_lshr_b32 s93, s93, 10\n"
      "s_mul_i32 s62, s93, 5\n"
      "s_sub_u32 s91, s91, s62\n"
      "s_lshr_b32 s30, s93, 2\n"
      "s_and_b32 s29, s93, 3\n"
      "s_mul_i32 s29, s29, 7\n"
      "s_add_u32 s29, s29, s30\n"
      "s_cmp_ge_u32 s30, 7\n"
      "s_cselect_b32 s30, 21, 0\n"
      "s_add_u32 s29, s29, s30\n"
      "s_lshl_b32 s29, s29, 10\n"
      "s_mul_i32 s30, s91, 458752\n"
      "s_add_u32 s29, s29, s30\n"
      "s_add_u32 s29, s29, 5603328\n"
      "s_mul_i32 s30, s53, 114688\n"
      "s_add_u32 s29, s29, s30\n"
      "s_add_u32 s30, s29, 57344\n"
      "s_lshl3_add_u32 s62, s93, s91\n"
      "s_lshl_b32 s62, s62, 7\n"
      "s_add_u32 s62, s62, 18808832\n"
      "s_lshl3_add_u32 s63, s60, 4096\n"
      "s_cmp_eq_u32 s53, 0\n"
      "s_cselect_b32 s62, s62, 1073741824\n"
      "s_cselect_b32 s63, s63, 1073741824\n"
      "s_mov_b32 exec_lo, 0xffff0000\n"
      "s_mov_b32 exec_hi, 0x0\n"
      "s_sub_u32 s62, s62, 128\n"
      "v_lshl_add_u32 v29, v1, 3, s62\n"
      "s_mov_b64 exec, -1\n"
      "v_writelane_b32 v29, s63, 32\n"
      "v_lshrrev_b32 v44, 4, v1\n"
      "v_and_b32 v72, 1, v44\n"
      "v_lshrrev_b32 v44, 1, v44\n"
      "v_lshl_add_u32 v44, v72, 2, v44\n"
      "v_mul_u32_u24 v44, 1040, v44\n"
      "v_bfe_u32 v72, v1, 2, 2\n"
      "v_lshl_add_u32 v44, v72, 8, v44\n"
      "v_and_b32 v72, 3, v1\n"
      "v_lshl_add_u32 v44, v72, 3, v44\n"
      "s_lshl_b32 s64, s53, 6\n"
      "s_add_u32 s64, s64, 122880\n"
      "v_add_u32 v44, s64, v44\n"
      "v_mov_b64 v[36:37], 0\n"
      "v_mov_b64 v[38:39], 0\n"
      "s_lshl_b32 s63, s53, 10\n"
      "s_add_u32 s63, s63, 157696\n"
      "v_lshl_add_u32 v40, v1, 4, s63\n"
      "ds_write_b128 v40, v[36:39]\n"
      "v_lshlrev_b32 v40, 2, v1\n"
      "v_add_u32 v40, 161792, v40\n"
      "ds_write_b32 v40, v36\n"
      "v_lshlrev_b32 v5, 4, v1\n"
      "v_add_u32 v6, 4096, v5\n"
      "s_lshl_b32 s24, s98, 12\n"
      "s_add_u32 s24, s24, s95\n"
      "s_add_u32 s25, s24, 4096\n"
      "v_mov_b32 v32, s25\n"
      "v_writelane_b32 v32, s24, 0\n"
      "s_cmp_eq_u32 s53, 0\n"
      "s_cselect_b32 s63, 1073741824, -4\n"
      "v_add_u32 v32, s63, v32\n"
      "v_lshrrev_b32 v33, 11, v32\n"
      "v_xor_b32 v33, 524288, v33\n"
      "s_sub_u32 s91, s98, 0\n"
      "s_lshr_b32 s91, s91, 4\n"
      "s_lshr_b32 s63, s98, 4\n"
      "s_sub_u32 s63, s63, s91\n"
      "s_lshl_b32 s63, s63, 9\n"
      "v_and_b32 v72, 8, v1\n"
      "v_mul_u32_u24 v72, s63, v72\n"
      "v_and_b32 v73, 7, v1\n"
      "v_lshl_add_u32 v30, v73, 7, v72\n"
      "s_lshl_b32 s91, s91, 12\n"
      "s_add_u32 s91, s91, s95\n"
      "s_add_u32 s91, s91, 18644992\n"
      "v_add_u32 v30, s91, v30\n"
      "s_and_b32 s63, s53, 1\n"
      "s_mul_i32 s63, s63, 4608\n"
      "s_add_u32 s63, s63, 139520\n"
      "v_lshl_add_u32 v34, v1, 3, s63\n"
      "v_lshrrev_b32 v72, 1, v1\n"
      "v_min_u32 v72, 6, v72\n"
      "s_mul_i32 s62, s54, 28\n"
      "s_add_u32 s62, s62, s53\n"
      "v_lshl_add_u32 v72, v72, 2, s62\n"
      "v_min_u32 v72, 53, v72\n"
      "s_lshl_b32 s63, s56, 14\n"
      "s_add_u32 s63, s63, 18382848\n"
      "v_lshl_add_u32 v13, v72, 8, s63\n"
      "s_mul_i32 s63, s55, 80\n"
      "s_add_u32 s91, s63, 79\n"
      "s_lshr_b32 s63, s63, 7\n"
      "s_lshr_b32 s91, s91, 7\n"
      "s_sub_u32 s91, s91, s63\n"
      "v_and_b32 v73, s91, v1\n"
      "v_lshl_add_u32 v72, v72, 3, v73\n"
      "s_lshl_b32 s63, s63, 7\n"
      "s_add_u32 s63, s63, 18808832\n"
      "v_lshl_add_u32 v31, v72, 7, s63\n"
      "v_lshrrev_b32 v72, 3, v1\n"
      "v_and_b32 v72, 3, v72\n"
      "v_mul_u32_u24 v73, 4160, v72\n"
      "v_add_u32 v73, 122880, v73\n"
      "v_and_b32 v72, 7, v1\n"
      "v_lshl_add_u32 v35, v72, 3, v73\n"
      "s_add_u32 s62, s95, 122944\n"
      "v_add_u32 v40, s62, v5\n"
      "v_and_b32 v72, 31, v1\n"
      "v_lshlrev_b32 v42, 3, v72\n"
      "s_add_u32 s62, s95, 157696\n"
      "v_lshl_add_u32 v43, v72, 5, s62\n"
      "s_xor_b32 s31, s99, s53\n"
      "s_lshl2_add_u32 s31, s56, s31\n"
      "s_lshl_b32 s31, s31, 8\n"
      "s_add_u32 s31, s31, 8192\n"
      "s_lshl2_add_u32 s62, s54, s53\n"
      "s_cmp_ge_u32 s62, 6\n"
      "s_cselect_b32 s63, 1073741824, 6144\n"
      "v_add_u32 v7, s63, v5\n"
      "s_mul_i32 s62, s62, 7168\n"
      "s_mul_i32 s63, s55, 286720\n"
      "s_add_u32 s63, s63, s62\n"
      "s_add_u32 s27, s63, 5603328\n"
      "s_mul_i32 s63, s56, 458752\n"
      "s_add_u32 s63, s63, s62\n"
      "s_add_u32 s63, s63, 7897088\n"
      "s_mul_i32 s62, s54, 229376\n"
      "s_add_u32 s91, s63, s62\n"
      "s_sub_u32 s62, 229376, s62\n"
      "s_add_u32 s93, s63, s62\n"
      "s_sub_u32 s62, 4, s53\n"
      "s_and_b32 s62, s62, 3\n"
      "s_mul_i32 s62, s62, 57344\n"
      "s_add_u32 s44, s93, s62\n"
      "s_add_u32 s48, s91, s62\n"
      "s_sub_u32 s62, 5, s53\n"
      "s_and_b32 s62, s62, 3\n"
      "s_mul_i32 s62, s62, 57344\n"
      "s_add_u32 s45, s93, s62\n"
      "s_add_u32 s49, s91, s62\n"
      "s_sub_u32 s62, 6, s53\n"
      "s_and_b32 s62, s62, 3\n"
      "s_mul_i32 s62, s62, 57344\n"
      "s_add_u32 s46, s93, s62\n"
      "s_add_u32 s50, s91, s62\n"
      "s_sub_u32 s62, 7, s53\n"
      "s_and_b32 s62, s62, 3\n"
      "s_mul_i32 s62, s62, 57344\n"
      "s_add_u32 s47, s93, s62\n"
      "s_add_u32 s51, s91, s62\n"
      "s_add_u32 s91, s53, 3\n"
      "s_and_b32 s91, s91, 3\n"
      "s_lshl2_add_u32 s62, s91, 161888\n"
      "v_mov_b32 v15, s62\n"
      "s_mul_i32 s62, s53, 5120\n"
      "s_add_u32 s63, s62, 32768\n"
      "v_add_u32 v8, s63, v5\n"
      "s_mul_i32 s63, s91, 5120\n"
      "s_add_u32 s63, s63, 32768\n"
      "v_add_u32 v9, s63, v5\n"
      "s_xor_b32 s63, s60, 1\n"
      "s_lshl2_add_u32 s63, s63, s53\n"
      "s_mul_i32 s28, s63, 3072\n"
      "s_add_u32 s28, s28, 15237120\n"
      "v_cmp_eq_u32 vcc, s53, v1\n"
      "v_and_b32 v14, 48, v1\n"
      "v_add_u32 v14, 163072, v14\n"
      "v_cndmask_b32 v55, 0, 1, vcc\n"
      ".LWARME:\n"
      "v_mov_b32 v45, 161920\n"
      "s_add_u32 m0, s94, 24576\n"
      "s_nop 0\n"
      "buffer_load_dwordx4 v16, s[8:11], s81 offen offset:0 lds\n"
      "buffer_load_dwordx4 v17, s[8:11], s81 offen offset:1024 lds\n"
      "buffer_load_dwordx4 v18, s[8:11], s81 offen offset:2048 lds\n"
      "buffer_load_dwordx4 v19, s[8:11], s81 offen offset:3072 lds\n"
      "s_add_u32 m0, s95, 40960\n"
      "s_nop 0\n"
      "buffer_load_dwordx4 v20, s[4:7], s71 offen lds\n"
      "s_add_u32 m0, s95, 45056\n"
      "s_nop 0\n"
      "buffer_load_dwordx4 v21, s[4:7], s71 offen lds\n"
      "s_waitcnt vmcnt(13) lgkmcnt(0)\n"
      "v_cmp_eq_u32 vcc, 0, v1\n"
      "v_lshlrev_b32 v2, 2, v1\n"
      "v_add_u32 v2, 157696, v2\n"
      "v_cndmask_b32 v3, 0, 1, vcc\n"
      "s_barrier\n"
      "s_add_u32 m0, s94, 49152\n"
      "s_nop 0\n"
      "buffer_load_dwordx4 v16, s[8:11], s82 offen offset:0 lds\n"
      "buffer_load_dwordx4 v17, s[8:11], s82 offen offset:1024 lds\n"
      "buffer_load_dwordx4 v18, s[8:11], s82 offen offset:2048 lds\n"
      "buffer_load_dwordx4 v19, s[8:11], s82 offen offset:3072 lds\n"
      "s_add_u32 m0, s95, 65536\n"
      "s_nop 0\n"
      "buffer_load_dwordx4 v20, s[4:7], s72 offen lds\n"
      "s_add_u32 m0, s95, 69632\n"
      "s_nop 0\n"
      "buffer_load_dwordx4 v21, s[4:7], s72 offen lds\n"
      "s_waitcnt vmcnt(12)\n"
      "s_barrier\n"
      "ds_read_b64_tr_b16 v[88:89], v44 offset:0\n"
      "ds_read_b64_tr_b16 v[90:91], v44 offset:2080\n"
      "ds_read_b64_tr_b16 v[92:93], v44 offset:32\n"
      "ds_read_b64_tr_b16 v[94:95], v44 offset:2112\n"
      "ds_read_b64 v[46:47], v45\n"
      "s_waitcnt lgkmcnt(0)\n"
      "buffer_store_dwordx4 v[88:91], v5, s[20:23], s29 offen sc1\n"
      "buffer_store_dwordx4 v[92:95], v5, s[20:23], s30 offen sc1\n"
      "s_waitcnt lgkmcnt(0)\n"
      "v_readfirstlane_b32 s58, v46\n"
      "v_readfirstlane_b32 s59, v47\n"
      "v_mov_b32 v1, v55\n"
      "s_add_u32 m0, s94, 73728\n"
      "s_nop 0\n"
      "buffer_load_dwordx4 v16, s[8:11], s83 offen offset:0 lds\n"
      "buffer_load_dwordx4 v17, s[8:11], s83 offen offset:1024 lds\n"
      "buffer_load_dwordx4 v18, s[8:11], s83 offen offset:2048 lds\n"
      "buffer_load_dwordx4 v19, s[8:11], s83 offen offset:3072 lds\n"
      "s_add_u32 m0, s95, 90112\n"
      "s_nop 0\n"
      "buffer_load_dwordx4 v20, s[4:7], s73 offen lds\n"
      "s_add_u32 m0, s95, 94208\n"
      "s_nop 0\n"
      "buffer_load_dwordx4 v21, s[4:7], s73 offen lds\n"
      "s_waitcnt vmcnt(14)\n"
      "s_barrier\n"
      "s_add_u32 m0, s94, 98304\n"
      "s_nop 0\n"
      "buffer_load_dwordx4 v16, s[8:11], s84 offen offset:0 lds\n"
      "buffer_load_dwordx4 v17, s[8:11], s84 offen offset:1024 lds\n"
      "buffer_load_dwordx4 v18, s[8:11], s84 offen offset:2048 lds\n"
      "buffer_load_dwordx4 v19, s[8:11], s84 offen offset:3072 lds\n"
      "s_add_u32 m0, s95, 114688\n"
      "s_nop 0\n"
      "buffer_load_dwordx4 v20, s[4:7], s74 offen lds\n"
      "s_add_u32 m0, s95, 118784\n"
      "s_nop 0\n"
      "buffer_load_dwordx4 v21, s[4:7], s74 offen lds\n"
      "s_waitcnt vmcnt(14)\n"
      "s_barrier\n"
      "s_add_u32 m0, s94, 0\n"
      "s_nop 0\n"
      "buffer_load_dwordx4 v16, s[8:11], s85 offen offset:0 lds\n"
      "buffer_load_dwordx4 v17, s[8:11], s85 offen offset:1024 lds\n"
      "buffer_load_dwordx4 v18, s[8:11], s85 offen offset:2048 lds\n"
      "buffer_load_dwordx4 v19, s[8:11], s85 offen offset:3072 lds\n"
      "s_add_u32 m0, s95, 16384\n"
      "s_nop 0\n"
      "buffer_load_dwordx4 v20, s[4:7], s75 offen lds\n"
      "s_add_u32 m0, s95, 20480\n"
      "s_nop 0\n"
      "buffer_load_dwordx4 v21, s[4:7], s75 offen lds\n"
      "buffer_load_dwordx2 v[76:77], v42, s[20:23], s31 offen sc1\n"
      "s_waitcnt vmcnt(13)\n"
      "s_barrier\n"
      "s_add_u32 m0, s94, 24576\n"
      "s_nop 0\n"
      "buffer_load_dwordx4 v16, s[8:11], s86 offen offset:0 lds\n"
      "buffer_load_dwordx4 v17, s[8:11], s86 offen offset:1024 lds\n"
      "buffer_load_dwordx4 v18, s[8:11], s86 offen offset:2048 lds\n"
      "buffer_load_dwordx4 v19, s[8:11], s86 offen offset:3072 lds\n"
      "s_add_u32 m0, s95, 40960\n"
      "s_nop 0\n"
      "buffer_load_dwordx4 v20, s[4:7], s76 offen lds\n"
      "s_add_u32 m0, s95, 45056\n"
      "s_nop 0\n"
      "buffer_load_dwordx4 v21, s[4:7], s76 offen lds\n"
      "s_waitcnt vmcnt(13)\n"
      "s_barrier\n"
      "s_add_u32 m0, s94, 49152\n"
      "s_nop 0\n"
      "buffer_load_dwordx4 v16, s[8:11], s87 offen offset:0 lds\n"
      "buffer_load_dwordx4 v17, s[8:11], s87 offen offset:1024 lds\n"
      "buffer_load_dwordx4 v18, s[8:11], s87 offen offset:2048 lds\n"
      "buffer_load_dwordx4 v19, s[8:11], s87 offen offset:3072 lds\n"
      "s_add_u32 m0, s95, 65536\n"
      "s_nop 0\n"
      "buffer_load_dwordx4 v20, s[4:7], s77 offen lds\n"
      "s_add_u32 m0, s95, 69632\n"
      "s_nop 0\n"
      "buffer_load_dwordx4 v21, s[4:7], s77 offen lds\n"
      "s_waitcnt vmcnt(12)\n"
      "s_barrier\n"
      "buffer_store_dwordx2 v[46:47], v29, s[20:23], 0 offen sc1\n"
      "v_cmp_ne_u32 vcc, s58, v77\n"
      "s_cbranch_vccnz .Lt_1\n"
      ".Lt_2:\n"
      "v_and_b32 v82, s92, v76\n"
      "v_rcp_f32 v77, v82\n"
      "v_xor_b32 v85, 0x8000, v76\n"
      "s_mov_b32 s62, 0x01000100\n"
      "v_mul_f32 v80, 0xc0135761, v77\n"
      "v_mul_f32 v81, v77, v77\n"
      "v_cmp_nle_f32 vcc, 0x3089705f, v77\n"
      "v_mul_f32 v81, 0x3d372713, v81\n"
      "v_perm_b32 v84, v85, v85, s62\n"
      "v_mul_f32 v81, v81, v80\n"
      "v_mov_b32 v83, 1.0\n"
      "ds_write_b64 v43, v[84:85] offset:16\n"
      "s_cbranch_vccnz .Lb_4\n"
      ".Lb_5:\n"
      "ds_write_b128 v43, v[80:83]\n"
      "s_add_u32 m0, s94, 73728\n"
      "s_nop 0\n"
      "buffer_load_dwordx4 v16, s[8:11], s88 offen offset:0 lds\n"
      "buffer_load_dwordx4 v17, s[8:11], s88 offen offset:1024 lds\n"
      "buffer_load_dwordx4 v18, s[8:11], s88 offen offset:2048 lds\n"
      "buffer_load_dwordx4 v19, s[8:11], s88 offen offset:3072 lds\n"
      "s_add_u32 m0, s95, 90112\n"
      "s_nop 0\n"
      "buffer_load_dwordx4 v20, s[4:7], s78 offen lds\n"
      "s_add_u32 m0, s95, 94208\n"
      "s_nop 0\n"
      "buffer_load_dwordx4 v21, s[4:7], s78 offen lds\n"
      "s_waitcnt vmcnt(13)\n"
      "s_barrier\n"
      "s_add_u32 m0, s94, 98304\n"
      "s_nop 0\n"
      "buffer_load_dwordx4 v16, s[8:11], s89 offen offset:0 lds\n"
      "buffer_load_dwordx4 v17, s[8:11], s89 offen offset:1024 lds\n"
      "buffer_load_dwordx4 v18, s[8:11], s89 offen offset:2048 lds\n"
      "buffer_load_dwordx4 v19, s[8:11], s89 offen offset:3072 lds\n"
      "s_add_u32 m0, s95, 114688\n"
      "s_nop 0\n"
      "buffer_load_dwordx4 v20, s[4:7], s79 offen lds\n"
      "s_add_u32 m0, s95, 118784\n"
      "s_nop 0\n"
      "buffer_load_dwordx4 v21, s[4:7], s79 offen lds\n"
      "s_waitcnt vmcnt(13)\n"
      "s_barrier\n"
      "s_add_u32 m0, s95, 0\n"
      "s_nop 0\n"
      "buffer_load_dwordx4 v22, s[4:7], s70 offen lds\n"
      "s_add_u32 m0, s95, 4096\n"
      "s_nop 0\n"
      "buffer_load_dwordx4 v22, s[4:7], s71 offen lds\n"
      "s_add_u32 m0, s95, 8192\n"
      "s_nop 0\n"
      "buffer_load_dwordx4 v22, s[4:7], s72 offen lds\n"
      "s_add_u32 m0, s95, 12288\n"
      "s_nop 0\n"
      "buffer_load_dwordx4 v22, s[4:7], s73 offen lds\n"
      "s_add_u32 m0, s95, 16384\n"
      "s_nop 0\n"
      "buffer_load_dwordx4 v22, s[4:7], s74 offen lds\n"
      "s_waitcnt vmcnt(11)\n"
      "s_barrier\n"
      "s_add_u32 m0, s95, 20480\n"
      "s_nop 0\n"
      "buffer_load_dwordx4 v22, s[4:7], s75 offen lds\n"
      "s_add_u32 m0, s95, 24576\n"
      "s_nop 0\n"
      "buffer_load_dwordx4 v22, s[4:7], s76 offen lds\n"
      "s_add_u32 m0, s95, 28672\n"
      "s_nop 0\n"
      "buffer_load_dwordx4 v22, s[4:7], s77 offen lds\n"
      "s_add_u32 m0, s95, 32768\n"
      "s_nop 0\n"
      "buffer_load_dwordx4 v22, s[4:7], s78 offen lds\n"
      "s_add_u32 m0, s95, 36864\n"
      "s_nop 0\n"
      "buffer_load_dwordx4 v22, s[4:7], s79 offen lds\n"
      "s_waitcnt vmcnt(10) lgkmcnt(0)\n"
      "s_barrier\n"
      "s_add_u32 m0, s95, 49152\n"
      "s_nop 0\n"
      "buffer_load_dwordx4 v23, s[4:7], s70 offen lds\n"
      "s_add_u32 m0, s95, 53248\n"
      "s_nop 0\n"
      "buffer_load_dwordx4 v23, s[4:7], s71 offen lds\n"
      "s_add_u32 m0, s95, 57344\n"
      "s_nop 0\n"
      "buffer_load_dwordx4 v23, s[4:7], s72 offen lds\n"
      "s_add_u32 m0, s95, 61440\n"
      "s_nop 0\n"
      "buffer_load_dwordx4 v23, s[4:7], s73 offen lds\n"
      "s_add_u32 m0, s95, 65536\n"
      "s_nop 0\n"
      "buffer_load_dwordx4 v23, s[4:7], s74 offen lds\n"
      "buffer_load_dwordx2 v[50:51], v31, s[20:23], 0 offen sc1\n"
      "s_cmp_lt_u32 s53, 2\n"
      "s_cselect_b32 s62, 0, 1073741824\n"
      "buffer_load_dwordx2 v[48:49], v30, s[20:23], s62 offen sc1\n"
      "s_waitcnt vmcnt(12)\n"
      "s_barrier\n"
      "s_add_u32 m0, s95, 69632\n"
      "s_nop 0\n"
      "buffer_load_dwordx4 v23, s[4:7], s75 offen lds\n"
      "s_add_u32 m0, s95, 73728\n"
      "s_nop 0\n"
      "buffer_load_dwordx4 v23, s[4:7], s76 offen lds\n"
      "s_add_u32 m0, s95, 77824\n"
      "s_nop 0\n"
      "buffer_load_dwordx4 v23, s[4:7], s77 offen lds\n"
      "s_add_u32 m0, s95, 81920\n"
      "s_nop 0\n"
      "buffer_load_dwordx4 v23, s[4:7], s78 offen lds\n"
      "s_add_u32 m0, s95, 86016\n"
      "s_nop 0\n"
      "buffer_load_dwordx4 v23, s[4:7], s79 offen lds\n"
      "ds_add_u32 v2, v3 offset:4152\n"
      "s_waitcnt vmcnt(12)\n"
      "ds_add_u32 v2, v3 offset:2072\n"
      "ds_read_b32 v4, v2 offset:4152\n"
      "s_waitcnt lgkmcnt(0)\n"
      "v_readfirstlane_b32 s2, v4\n"
      "s_cmp_lt_u32 s2, 4\n"
      "s_cbranch_scc1 .Lw_6\n"
      ".Lw_7:\n"
      ".LS2:\n"
      "buffer_load_dwordx4 v[56:59], v5, s[36:39], s24 offen nt\n"
      "buffer_load_dwordx4 v[60:63], v5, s[36:39], s25 offen nt\n"
      "buffer_load_dword v52, v32, s[36:39], 0 offen nt\n"
      "buffer_load_ushort v53, v33, s[32:35], 0 offen\n"
      ".LS3:\n"
      "ds_write_b64 v34, v[36:37]\n"
      "ds_write_b64 v35, v[36:37]\n"
      "s_waitcnt vmcnt(4)\n"
      "ds_add_u32 v2, v3 offset:3096\n"
      "s_waitcnt vmcnt(10)\n"
      "v_cmp_ne_u64 vcc, s[58:59], v[50:51]\n"
      "s_cbranch_vccnz .Lf_9\n"
      ".Lf_10:\n"
      "s_add_u32 s66, s27, 57344\n"
      "s_add_u32 s67, s27, 114688\n"
      "s_add_u32 s68, s27, 172032\n"
      "s_add_u32 s69, s27, 229376\n"
      "s_cmp_lt_u32 s53, 2\n"
      "s_cbranch_scc0 .Lk_12\n"
      "s_waitcnt vmcnt(9)\n"
      "v_cmp_ne_u64 vcc, s[58:59], v[48:49]\n"
      "s_cbranch_vccnz .Lf_13\n"
      ".Lf_14:\n"
      "s_mul_i32 s62, s98, 4224\n"
      "s_add_u32 s62, s62, s95\n"
      "s_add_u32 m0, s95, 140032\n"
      "s_add_u32 s63, s62, 32768\n"
      "buffer_load_dwordx4 v5, s[20:23], s63 offen sc1 lds\n"
      "s_add_u32 m0, s95, 144640\n"
      "s_add_u32 s63, s62, 36992\n"
      "buffer_load_dwordx4 v5, s[20:23], s63 offen sc1 lds\n"
      ".Lk_12:\n"
      "s_waitcnt vmcnt(6)\n"
      "buffer_load_dwordx4 v[116:119], v5, s[20:23], s27 offen offset:0 sc1\n"
      "buffer_load_dwordx4 v[136:139], v5, s[20:23], s27 offen offset:1024 sc1\n"
      "s_waitcnt vmcnt(6)\n"
      "buffer_load_dwordx4 v[156:159], v5, s[20:23], s27 offen offset:2048 sc1\n"
      "buffer_load_dwordx4 v[176:179], v5, s[20:23], s27 offen offset:3072 sc1\n"
      "s_waitcnt vmcnt(4)\n"
      "s_cmp_eq_u32 s53, 0\n"
      "s_cbranch_scc0 .Ld_16\n"
      "v_readlane_b32 s62, v53, 0\n"
      "v_readlane_b32 s63, v53, 1\n"
      "s_mov_b64 exec, 1\n"
      "v_lshlrev_b32 v72, 16, v56\n"
      "s_lshl_b32 s62, s62, 16\n"
      "v_and_b32 v73, s92, v56\n"
      "v_add_f32 v72, s62, v72\n"
      "v_cvt_pk_bf16_f32 v56, v72, v73\n"
      "v_lshlrev_b32 v72, 16, v60\n"
      "s_lshl_b32 s63, s63, 16\n"
      "v_and_b32 v73, s92, v60\n"
      "v_add_f32 v72, s63, v72\n"
      "v_cvt_pk_bf16_f32 v60, v72, v73\n"
      "s_mov_b64 exec, -1\n"
      ".Ld_16:\n"
      "v_readlane_b32 s62, v52, 1\n"
      "ds_write_b128 v40, v[60:63] offset:8320\n"
      "v_alignbyte_b32 v103, v63, v62, 2\n"
      "v_writelane_b32 v72, s62, 0\n"
      "v_alignbyte_b32 v101, v61, v60, 2\n"
      "v_alignbyte_b32 v102, v62, v61, 2\n"
      "v_mov_b32_dpp v72, v63 wave_shr:1 row_mask:0xf bank_mask:0xf\n"
      "v_alignbyte_b32 v100, v60, v72, 2\n"
      "ds_write_b128 v40, v[100:103] offset:12480\n"
      "s_waitcnt vmcnt(6)\n"
      "buffer_load_dwordx4 v[196:199], v6, s[20:23], s27 offen offset:0 sc1\n"
      "buffer_load_dwordx4 v[216:219], v6, s[20:23], s27 offen offset:1024 sc1\n"
      "ds_write_b128 v40, v[56:59] offset:0\n"
      "v_alignbyte_b32 v99, v59, v58, 2\n"
      "v_alignbyte_b32 v97, v57, v56, 2\n"
      "v_alignbyte_b32 v98, v58, v57, 2\n"
      "v_mov_b32_dpp v52, v59 wave_shr:1 row_mask:0xf bank_mask:0xf\n"
      "v_alignbyte_b32 v96, v56, v52, 2\n"
      "ds_write_b128 v40, v[96:99] offset:4160\n"
      "s_waitcnt vmcnt(6)\n"
      "buffer_load_dwordx4 v[236:239], v7, s[20:23], s27 offen offset:0 sc1\n"
      "buffer_load_dwordx4 v[120:123], v5, s[20:23], s66 offen offset:0 sc1\n"
      "s_cmp_lt_u32 s53, 2\n"
      "s_cselect_b32 s62, 1073741824, 0\n"
      "buffer_load_dwordx2 v[48:49], v30, s[20:23], s62 offen sc1\n"
      "s_waitcnt vmcnt(9)\n"
      "ds_add_u32 v2, v3 offset:4304\n"
      "s_waitcnt vmcnt(6)\n"
      "buffer_load_dwordx4 v[140:143], v5, s[20:23], s66 offen offset:1024 sc1\n"
      "buffer_load_dwordx4 v[160:163], v5, s[20:23], s66 offen offset:2048 sc1\n"
      "s_waitcnt vmcnt(6)\n"
      "buffer_load_dwordx4 v[180:183], v5, s[20:23], s66 offen offset:3072 sc1\n"
      "buffer_load_dwordx4 v[200:203], v6, s[20:23], s66 offen offset:0 sc1\n"
      "s_waitcnt vmcnt(6)\n"
      "buffer_load_dwordx4 v[220:223], v6, s[20:23], s66 offen offset:1024 sc1\n"
      "buffer_load_dwordx4 v[240:243], v7, s[20:23], s66 offen offset:0 sc1\n"
      "s_cmp_lt_u32 s53, 2\n"
      "s_cbranch_scc1 .Lk_17\n"
      "s_waitcnt vmcnt(6)\n"
      "v_cmp_ne_u64 vcc, s[58:59], v[48:49]\n"
      "s_cbranch_vccnz .Lf_18\n"
      ".Lf_19:\n"
      "s_mul_i32 s62, s98, 4224\n"
      "s_add_u32 s62, s62, s95\n"
      "s_add_u32 m0, s95, 140032\n"
      "s_add_u32 s63, s62, 32768\n"
      "buffer_load_dwordx4 v5, s[20:23], s63 offen sc1 lds\n"
      "s_add_u32 m0, s95, 144640\n"
      "s_add_u32 s63, s62, 36992\n"
      "buffer_load_dwordx4 v5, s[20:23], s63 offen sc1 lds\n"
      ".Lk_17:\n"
      "s_waitcnt vmcnt(6)\n"
      "buffer_load_dwordx4 v[124:127], v5, s[20:23], s67 offen offset:0 sc1\n"
      "buffer_load_dwordx4 v[144:147], v5, s[20:23], s67 offen offset:1024 sc1\n"
      "s_waitcnt vmcnt(6)\n"
      "buffer_load_dwordx4 v[164:167], v5, s[20:23], s67 offen offset:2048 sc1\n"
      "buffer_load_dwordx4 v[184:187], v5, s[20:23], s67 offen offset:3072 sc1\n"
      "s_waitcnt vmcnt(6)\n"
      "buffer_load_dwordx4 v[204:207], v6, s[20:23], s67 offen offset:0 sc1\n"
      "buffer_load_dwordx4 v[224:227], v6, s[20:23], s67 offen offset:1024 sc1\n"
      "s_waitcnt vmcnt(6)\n"
      "ds_add_u32 v2, v3 offset:4280\n"
      "s_waitcnt vmcnt(6)\n"
      "buffer_load_dwordx4 v[244:247], v7, s[20:23], s67 offen offset:0 sc1\n"
      "buffer_load_dwordx4 v[128:131], v5, s[20:23], s68 offen offset:0 sc1\n"
      "s_waitcnt vmcnt(6)\n"
      "buffer_load_dwordx4 v[148:151], v5, s[20:23], s68 offen offset:1024 sc1\n"
      "buffer_load_dwordx4 v[168:171], v5, s[20:23], s68 offen offset:2048 sc1\n"
      "s_waitcnt vmcnt(6)\n"
      "buffer_load_dwordx4 v[188:191], v5, s[20:23], s68 offen offset:3072 sc1\n"
      "buffer_load_dwordx4 v[208:211], v6, s[20:23], s68 offen offset:0 sc1\n"
      "s_waitcnt vmcnt(6)\n"
      "buffer_load_dwordx4 v[228:231], v6, s[20:23], s68 offen offset:1024 sc1\n"
      "buffer_load_dwordx4 v[248:251], v7, s[20:23], s68 offen offset:0 sc1\n"
      "s_waitcnt vmcnt(6)\n"
      "buffer_load_dwordx4 v[132:135], v5, s[20:23], s69 offen offset:0 sc1\n"
      "buffer_load_dwordx4 v[152:155], v5, s[20:23], s69 offen offset:1024 sc1\n"
      "s_waitcnt vmcnt(6)\n"
      "buffer_load_dwordx4 v[172:175], v5, s[20:23], s69 offen offset:2048 sc1\n"
      "buffer_load_dwordx4 v[192:195], v5, s[20:23], s69 offen offset:3072 sc1\n"
      "s_waitcnt vmcnt(6)\n"
      "buffer_load_dwordx4 v[212:215], v6, s[20:23], s69 offen offset:0 sc1\n"
      "buffer_load_dwordx4 v[232:235], v6, s[20:23], s69 offen offset:1024 sc1\n"
      "s_waitcnt vmcnt(6)\n"
      "buffer_load_dwordx4 v[252:255], v7, s[20:23], s69 offen offset:0 sc1\n"
      "s_and_b32 s75, s58, 0x1fff\n"
      "s_or_b32 s75, s75, 0x4000\n"
      "s_lshl_b32 s75, s75, 16\n"
      "s_mov_b32 s74, 128\n"
      "s_mov_b32 s73, 0x100000\n"
      "ds_read_b32 v4, v2 offset:4168\n"
      "s_waitcnt lgkmcnt(0)\n"
      "v_readfirstlane_b32 s2, v4\n"
      "s_cmp_lt_u32 s2, 4\n"
      "s_cbranch_scc1 .Lw_21\n"
      ".Lw_22:\n"
      "buffer_load_dwordx2 v[10:11], v13, s[20:23], 0 offen sc1\n"
      "s_waitcnt vmcnt(0)\n"
      "v_cmp_ne_u64 vcc, s[58:59], v[10:11]\n"
      "s_cbranch_vccnz .Lf_24\n"
      ".Lf_25:\n"
      "buffer_load_dwordx4 v[32:35], v5, s[20:23], s44 offen offset:0 sc1\n"
      "buffer_load_dwordx4 v[36:39], v5, s[20:23], s44 offen offset:1024 sc1\n"
      "buffer_load_dwordx4 v[40:43], v5, s[20:23], s44 offen offset:2048 sc1\n"
      "buffer_load_dwordx4 v[44:47], v5, s[20:23], s44 offen offset:3072 sc1\n"
      "buffer_load_dwordx4 v[48:51], v6, s[20:23], s44 offen offset:0 sc1\n"
      "buffer_load_dwordx4 v[52:55], v6, s[20:23], s44 offen offset:1024 sc1\n"
      "buffer_load_dwordx4 v[56:59], v7, s[20:23], s44 offen offset:0 sc1\n"
      "buffer_load_dwordx4 v[60:63], v5, s[20:23], s45 offen offset:0 sc1\n"
      "s_waitcnt vmcnt(6)\n"
      "buffer_load_dwordx4 v[64:67], v5, s[20:23], s45 offen offset:1024 sc1\n"
      "s_setprio 1\n"
      "s_waitcnt vmcnt(6)\n"
      "v_mfma_f32_16x16x32_bf16 v[16:19], v[116:119], v[32:35], 0\n"
      "v_mfma_f32_16x16x32_bf16 v[16:19], v[136:139], v[36:39], v[16:19]\n"
      "buffer_load_dwordx4 v[68:71], v5, s[20:23], s45 offen offset:2048 sc1\n"
      "v_mfma_f32_16x16x32_bf16 v[16:19], v[156:159], v[40:43], v[16:19]\n"
      "s_waitcnt vmcnt(3)\n"
      "v_mfma_f32_16x16x32_bf16 v[16:19], v[176:179], v[44:47], v[16:19]\n"
      "buffer_load_dwordx4 v[72:75], v5, s[20:23], s45 offen offset:3072 sc1\n"
      "ds_read_b32 v4, v2 offset:4276\n"
      "v_mfma_f32_16x16x32_bf16 v[16:19], v[196:199], v[48:51], v[16:19]\n"
      "v_mfma_f32_16x16x32_bf16 v[16:19], v[216:219], v[52:55], v[16:19]\n"
      "buffer_load_dwordx4 v[76:79], v6, s[20:23], s45 offen offset:0 sc1\n"
      "v_mfma_f32_16x16x32_bf16 v[16:19], v[236:239], v[56:59], v[16:19]\n"
      "v_mfma_f32_16x16x32_bf16 v[20:23], v[120:123], v[32:35], 0\n"
      "buffer_load_dwordx4 v[80:83], v6, s[20:23], s45 offen offset:1024 sc1\n"
      "v_mfma_f32_16x16x32_bf16 v[20:23], v[140:143], v[36:39], v[20:23]\n"
      "s_waitcnt lgkmcnt(0)\n"
      "v_readfirstlane_b32 s2, v4\n"
      "s_cmp_lt_u32 s2, 4\n"
      "s_cbranch_scc1 .Lw_27\n"
      ".Lw_28:\n"
      "v_mfma_f32_16x16x32_bf16 v[20:23], v[160:163], v[40:43], v[20:23]\n"
      "buffer_load_dwordx4 v[84:87], v7, s[20:23], s45 offen offset:0 sc1\n"
      "v_mfma_f32_16x16x32_bf16 v[20:23], v[180:183], v[44:47], v[20:23]\n"
      "v_mfma_f32_16x16x32_bf16 v[20:23], v[200:203], v[48:51], v[20:23]\n"
      "buffer_load_dwordx4 v[88:91], v5, s[20:23], s46 offen offset:0 sc1\n"
      "ds_write_b128 v8, v[16:19] offset:0\n"
      "v_mfma_f32_16x16x32_bf16 v[20:23], v[220:223], v[52:55], v[20:23]\n"
      "v_mfma_f32_16x16x32_bf16 v[20:23], v[240:243], v[56:59], v[20:23]\n"
      "buffer_load_dwordx4 v[92:95], v5, s[20:23], s46 offen offset:1024 sc1\n"
      "v_mfma_f32_16x16x32_bf16 v[24:27], v[124:127], v[32:35], 0\n"
      "v_mfma_f32_16x16x32_bf16 v[24:27], v[144:147], v[36:39], v[24:27]\n"
      "buffer_load_dwordx4 v[96:99], v5, s[20:23], s46 offen offset:2048 sc1\n"
      "ds_add_u32 v2, v1 offset:4192\n"
      "v_mfma_f32_16x16x32_bf16 v[24:27], v[164:167], v[40:43], v[24:27]\n"
      "v_mfma_f32_16x16x32_bf16 v[24:27], v[184:187], v[44:47], v[24:27]\n"
      "buffer_load_dwordx4 v[100:103], v5, s[20:23], s46 offen offset:3072 sc1\n"
      "v_mfma_f32_16x16x32_bf16 v[24:27], v[204:207], v[48:51], v[24:27]\n"
      "ds_write_b128 v8, v[20:23] offset:1024\n"
      "v_mfma_f32_16x16x32_bf16 v[24:27], v[224:227], v[52:55], v[24:27]\n"
      "buffer_load_dwordx4 v[104:107], v6, s[20:23], s46 offen offset:0 sc1\n"
      "v_mfma_f32_16x16x32_bf16 v[24:27], v[244:247], v[56:59], v[24:27]\n"
      "v_mfma_f32_16x16x32_bf16 v[28:31], v[128:131], v[32:35], 0\n"
      "buffer_load_dwordx4 v[108:111], v6, s[20:23], s46 offen offset:1024 sc1\n"
      "v_mfma_f32_16x16x32_bf16 v[28:31], v[148:151], v[36:39], v[28:31]\n"
      "ds_add_u32 v2, v1 offset:4192\n"
      "v_mfma_f32_16x16x32_bf16 v[28:31], v[168:171], v[40:43], v[28:31]\n"
      "buffer_load_dwordx4 v[112:115], v7, s[20:23], s46 offen offset:0 sc1\n"
      "v_mfma_f32_16x16x32_bf16 v[28:31], v[188:191], v[44:47], v[28:31]\n"
      "v_mfma_f32_16x16x32_bf16 v[28:31], v[208:211], v[48:51], v[28:31]\n"
      "ds_write_b128 v8, v[24:27] offset:2048\n"
      "v_mfma_f32_16x16x32_bf16 v[28:31], v[228:231], v[52:55], v[28:31]\n"
      "v_mfma_f32_16x16x32_bf16 v[28:31], v[248:251], v[56:59], v[28:31]\n"
      "v_mfma_f32_16x16x32_bf16 v[16:19], v[132:135], v[32:35], 0\n"
      "buffer_load_dwordx4 v[32:35], v5, s[20:23], s47 offen offset:0 sc1\n"
      "ds_read_b32 v4, v15\n"
      "ds_read_b128 v[20:23], v9 offset:0\n"
      "v_mfma_f32_16x16x32_bf16 v[16:19], v[152:155], v[36:39], v[16:19]\n"
      "ds_add_u32 v2, v1 offset:4192\n"
      "v_mfma_f32_16x16x32_bf16 v[16:19], v[172:175], v[40:43], v[16:19]\n"
      "v_mfma_f32_16x16x32_bf16 v[16:19], v[192:195], v[44:47], v[16:19]\n"
      "s_waitcnt lgkmcnt(2)\n"
      "v_cmp_gt_u32 vcc, 1, v4\n"
      "v_mfma_f32_16x16x32_bf16 v[16:19], v[212:215], v[48:51], v[16:19]\n"
      "buffer_load_dwordx4 v[36:39], v5, s[20:23], s47 offen offset:1024 sc1\n"
      "ds_write_b128 v8, v[28:31] offset:3072\n"
      "v_mfma_f32_16x16x32_bf16 v[16:19], v[232:235], v[52:55], v[16:19]\n"
      "s_cbranch_vccnz .Lp_30\n"
      ".Lp_31:\n"
      "v_mfma_f32_16x16x32_bf16 v[16:19], v[252:255], v[56:59], v[16:19]\n"
      "buffer_load_dwordx2 v[10:11], v13, s[20:23], s74 offen sc1\n"
      "s_waitcnt vmcnt(14)\n"
      "s_waitcnt lgkmcnt(2)\n"
      "v_mfma_f32_16x16x32_bf16 v[20:23], v[116:119], v[60:63], v[20:23]\n"
      "ds_read_b32 v4, v15\n"
      "ds_read_b128 v[24:27], v9 offset:1024\n"
      "v_mfma_f32_16x16x32_bf16 v[20:23], v[136:139], v[64:67], v[20:23]\n"
      "buffer_load_dwordx4 v[40:43], v5, s[20:23], s47 offen offset:2048 sc1\n"
      "ds_add_u32 v2, v1 offset:4192\n"
      "v_mfma_f32_16x16x32_bf16 v[20:23], v[156:159], v[68:71], v[20:23]\n"
      "s_waitcnt vmcnt(11)\n"
      "v_mfma_f32_16x16x32_bf16 v[20:23], v[176:179], v[72:75], v[20:23]\n"
      "s_waitcnt lgkmcnt(2)\n"
      "v_cmp_gt_u32 vcc, 2, v4\n"
      "v_mfma_f32_16x16x32_bf16 v[20:23], v[196:199], v[76:79], v[20:23]\n"
      "ds_write_b128 v8, v[16:19] offset:4096\n"
      "v_mfma_f32_16x16x32_bf16 v[20:23], v[216:219], v[80:83], v[20:23]\n"
      "buffer_load_dwordx4 v[44:47], v5, s[20:23], s47 offen offset:3072 sc1\n"
      "s_cbranch_vccnz .Lp_32\n"
      ".Lp_33:\n"
      "v_mfma_f32_16x16x32_bf16 v[20:23], v[236:239], v[84:87], v[20:23]\n"
      "s_waitcnt lgkmcnt(2)\n"
      "v_mfma_f32_16x16x32_bf16 v[24:27], v[120:123], v[60:63], v[24:27]\n"
      "ds_read_b32 v4, v15\n"
      "ds_read_b128 v[28:31], v9 offset:2048\n"
      "v_mfma_f32_16x16x32_bf16 v[24:27], v[140:143], v[64:67], v[24:27]\n"
      "ds_add_u32 v2, v1 offset:4192\n"
      "v_mfma_f32_16x16x32_bf16 v[24:27], v[160:163], v[68:71], v[24:27]\n"
      "buffer_load_dwordx4 v[48:51], v6, s[20:23], s47 offen offset:0 sc1\n"
      "v_mfma_f32_16x16x32_bf16 v[24:27], v[180:183], v[72:75], v[24:27]\n"
      "s_waitcnt lgkmcnt(2)\n"
      "v_cmp_gt_u32 vcc, 3, v4\n"
      "v_mfma_f32_16x16x32_bf16 v[24:27], v[200:203], v[76:79], v[24:27]\n"
      "ds_write_b128 v8, v[20:23] offset:20480\n"
      "v_mfma_f32_16x16x32_bf16 v[24:27], v[220:223], v[80:83], v[24:27]\n"
      "s_cbranch_vccnz .Lp_34\n"
      ".Lp_35:\n"
      "v_mfma_f32_16x16x32_bf16 v[24:27], v[240:243], v[84:87], v[24:27]\n"
      "buffer_load_dwordx4 v[52:55], v6, s[20:23], s47 offen offset:1024 sc1\n"
      "s_waitcnt lgkmcnt(2)\n"
      "v_mfma_f32_16x16x32_bf16 v[28:31], v[124:127], v[60:63], v[28:31]\n"
      "ds_read_b32 v4, v15\n"
      "ds_read_b128 v[16:19], v9 offset:3072\n"
      "v_mfma_f32_16x16x32_bf16 v[28:31], v[144:147], v[64:67], v[28:31]\n"
      "ds_add_u32 v2, v1 offset:4192\n"
      "v_mfma_f32_16x16x32_bf16 v[28:31], v[164:167], v[68:71], v[28:31]\n"
      "v_mfma_f32_16x16x32_bf16 v[28:31], v[184:187], v[72:75], v[28:31]\n"
      "buffer_load_dwordx4 v[56:59], v7, s[20:23], s47 offen offset:0 sc1\n"
      "s_waitcnt lgkmcnt(2)\n"
      "v_cmp_gt_u32 vcc, 4, v4\n"
      "v_mfma_f32_16x16x32_bf16 v[28:31], v[204:207], v[76:79], v[28:31]\n"
      "ds_write_b128 v8, v[24:27] offset:21504\n"
      "v_mfma_f32_16x16x32_bf16 v[28:31], v[224:227], v[80:83], v[28:31]\n"
      "s_cbranch_vccnz .Lp_36\n"
      ".Lp_37:\n"
      "v_mfma_f32_16x16x32_bf16 v[28:31], v[244:247], v[84:87], v[28:31]\n"
      "s_waitcnt lgkmcnt(2)\n"
      "v_mfma_f32_16x16x32_bf16 v[16:19], v[128:131], v[60:63], v[16:19]\n"
      "ds_read_b32 v4, v15\n"
      "ds_read_b128 v[20:23], v9 offset:4096\n"
      "v_mfma_f32_16x16x32_bf16 v[16:19], v[148:151], v[64:67], v[16:19]\n"
      "ds_add_u32 v2, v1 offset:4192\n"
      "v_mfma_f32_16x16x32_bf16 v[16:19], v[168:171], v[68:71], v[16:19]\n"
      "v_mfma_f32_16x16x32_bf16 v[16:19], v[188:191], v[72:75], v[16:19]\n"
      "s_waitcnt lgkmcnt(2)\n"
      "v_cmp_gt_u32 vcc, 5, v4\n"
      "v_mfma_f32_16x16x32_bf16 v[16:19], v[208:211], v[76:79], v[16:19]\n"
      "ds_write_b128 v8, v[28:31] offset:22528\n"
      "v_mfma_f32_16x16x32_bf16 v[16:19], v[228:231], v[80:83], v[16:19]\n"
      "s_cbranch_vccnz .Lp_38\n"
      ".Lp_39:\n"
      "v_mfma_f32_16x16x32_bf16 v[16:19], v[248:251], v[84:87], v[16:19]\n"
      "s_waitcnt lgkmcnt(2)\n"
      "v_mfma_f32_16x16x32_bf16 v[20:23], v[132:135], v[60:63], v[20:23]\n"
      "ds_read_b32 v4, v15\n"
      "ds_read_b128 v[24:27], v9 offset:20480\n"
      "v_mfma_f32_16x16x32_bf16 v[20:23], v[152:155], v[64:67], v[20:23]\n"
      "ds_add_u32 v2, v1 offset:4192\n"
      "v_mfma_f32_16x16x32_bf16 v[20:23], v[172:175], v[68:71], v[20:23]\n"
      "v_mfma_f32_16x16x32_bf16 v[20:23], v[192:195], v[72:75], v[20:23]\n"
      "s_waitcnt lgkmcnt(2)\n"
      "v_cmp_gt_u32 vcc, 6, v4\n"
      "v_mfma_f32_16x16x32_bf16 v[20:23], v[212:215], v[76:79], v[20:23]\n"
      "ds_write_b128 v8, v[16:19] offset:23552\n"
      "v_mfma_f32_16x16x32_bf16 v[20:23], v[232:235], v[80:83], v[20:23]\n"
      "s_cbranch_vccnz .Lp_40\n"
      ".Lp_41:\n"
      "v_mfma_f32_16x16x32_bf16 v[20:23], v[252:255], v[84:87], v[20:23]\n"
      "s_waitcnt vmcnt(12)\n"
      "s_waitcnt lgkmcnt(2)\n"
      "v_mfma_f32_16x16x32_bf16 v[24:27], v[116:119], v[88:91], v[24:27]\n"
      "ds_read_b32 v4, v15\n"
      "ds_read_b128 v[28:31], v9 offset:21504\n"
      "v_mfma_f32_16x16x32_bf16 v[24:27], v[136:139], v[92:95], v[24:27]\n"
      "ds_add_u32 v2, v1 offset:4192\n"
      "v_mfma_f32_16x16x32_bf16 v[24:27], v[156:159], v[96:99], v[24:27]\n"
      "s_waitcnt vmcnt(8)\n"
      "v_mfma_f32_16x16x32_bf16 v[24:27], v[176:179], v[100:103], v[24:27]\n"
      "s_waitcnt lgkmcnt(2)\n"
      "v_cmp_gt_u32 vcc, 7, v4\n"
      "v_mfma_f32_16x16x32_bf16 v[24:27], v[196:199], v[104:107], v[24:27]\n"
      "ds_write_b128 v8, v[20:23] offset:24576\n"
      "v_mfma_f32_16x16x32_bf16 v[24:27], v[216:219], v[108:111], v[24:27]\n"
      "s_cbranch_vccnz .Lp_42\n"
      ".Lp_43:\n"
      "v_mfma_f32_16x16x32_bf16 v[24:27], v[236:239], v[112:115], v[24:27]\n"
      "s_waitcnt lgkmcnt(2)\n"
      "v_mfma_f32_16x16x32_bf16 v[28:31], v[120:123], v[88:91], v[28:31]\n"
      "ds_read_b32 v4, v15\n"
      "ds_read_b128 v[16:19], v9 offset:22528\n"
      "v_mfma_f32_16x16x32_bf16 v[28:31], v[140:143], v[92:95], v[28:31]\n"
      "ds_add_u32 v2, v1 offset:4192\n"
      "v_mfma_f32_16x16x32_bf16 v[28:31], v[160:163], v[96:99], v[28:31]\n"
      "v_mfma_f32_16x16x32_bf16 v[28:31], v[180:183], v[100:103], v[28:31]\n"
      "s_waitcnt lgkmcnt(2)\n"
      "v_cmp_gt_u32 vcc, 8, v4\n"
      "v_mfma_f32_16x16x32_bf16 v[28:31], v[200:203], v[104:107], v[28:31]\n"
      "ds_write_b128 v8, v[24:27] offset:40960\n"
      "v_mfma_f32_16x16x32_bf16 v[28:31], v[220:223], v[108:111], v[28:31]\n"
      "s_cbranch_vccnz .Lp_44\n"
      ".Lp_45:\n"
      "v_mfma_f32_16x16x32_bf16 v[28:31], v[240:243], v[112:115], v[28:31]\n"
      "s_waitcnt lgkmcnt(2)\n"
      "v_mfma_f32_16x16x32_bf16 v[16:19], v[124:127], v[88:91], v[16:19]\n"
      "s_waitcnt vmcnt(5)\n"
      "v_cmp_ne_u64 vcc, s[58:59], v[10:11]\n"
      "s_cbranch_vccnz .Lf_46\n"
      ".Lf_47:\n"
      "buffer_load_dwordx4 v[60:63], v5, s[20:23], s48 offen offset:0 sc1\n"
      "ds_read_b32 v4, v15\n"
      "ds_read_b128 v[20:23], v9 offset:23552\n"
      "v_mfma_f32_16x16x32_bf16 v[16:19], v[144:147], v[92:95], v[16:19]\n"
      "ds_add_u32 v2, v1 offset:4192\n"
      "v_mfma_f32_16x16x32_bf16 v[16:19], v[164:167], v[96:99], v[16:19]\n"
      "buffer_load_dwordx4 v[64:67], v5, s[20:23], s48 offen offset:1024 sc1\n"
      "v_mfma_f32_16x16x32_bf16 v[16:19], v[184:187], v[100:103], v[16:19]\n"
      "s_waitcnt lgkmcnt(2)\n"
      "v_cmp_gt_u32 vcc, 9, v4\n"
      "v_mfma_f32_16x16x32_bf16 v[16:19], v[204:207], v[104:107], v[16:19]\n"
      "buffer_load_dwordx4 v[68:71], v5, s[20:23], s48 offen offset:2048 sc1\n"
      "ds_write_b128 v8, v[28:31] offset:41984\n"
      "v_mfma_f32_16x16x32_bf16 v[16:19], v[224:227], v[108:111], v[16:19]\n"
      "s_cbranch_vccnz .Lp_49\n"
      ".Lp_50:\n"
      "v_mfma_f32_16x16x32_bf16 v[16:19], v[244:247], v[112:115], v[16:19]\n"
      "buffer_load_dwordx4 v[72:75], v5, s[20:23], s48 offen offset:3072 sc1\n"
      "s_waitcnt lgkmcnt(2)\n"
      "v_mfma_f32_16x16x32_bf16 v[20:23], v[128:131], v[88:91], v[20:23]\n"
      "ds_read_b32 v4, v15\n"
      "ds_read_b128 v[24:27], v9 offset:24576\n"
      "v_mfma_f32_16x16x32_bf16 v[20:23], v[148:151], v[92:95], v[20:23]\n"
      "buffer_load_dwordx4 v[76:79], v6, s[20:23], s48 offen offset:0 sc1\n"
      "ds_add_u32 v2, v1 offset:4192\n"
      "v_mfma_f32_16x16x32_bf16 v[20:23], v[168:171], v[96:99], v[20:23]\n"
      "v_mfma_f32_16x16x32_bf16 v[20:23], v[188:191], v[100:103], v[20:23]\n"
      "buffer_load_dwordx4 v[80:83], v6, s[20:23], s48 offen offset:1024 sc1\n"
      "s_waitcnt lgkmcnt(2)\n"
      "v_cmp_gt_u32 vcc, 10, v4\n"
      "v_mfma_f32_16x16x32_bf16 v[20:23], v[208:211], v[104:107], v[20:23]\n"
      "ds_write_b128 v8, v[16:19] offset:43008\n"
      "v_mfma_f32_16x16x32_bf16 v[20:23], v[228:231], v[108:111], v[20:23]\n"
      "buffer_load_dwordx4 v[84:87], v7, s[20:23], s48 offen offset:0 sc1\n"
      "s_cbranch_vccnz .Lp_51\n"
      ".Lp_52:\n"
      "v_mfma_f32_16x16x32_bf16 v[20:23], v[248:251], v[112:115], v[20:23]\n"
      "s_waitcnt lgkmcnt(2)\n"
      "v_mfma_f32_16x16x32_bf16 v[24:27], v[132:135], v[88:91], v[24:27]\n"
      "buffer_load_dwordx4 v[88:91], v5, s[20:23], s49 offen offset:0 sc1\n"
      "ds_read_b32 v4, v15\n"
      "ds_read_b128 v[28:31], v9 offset:40960\n"
      "v_mfma_f32_16x16x32_bf16 v[24:27], v[152:155], v[92:95], v[24:27]\n"
      "ds_add_u32 v2, v1 offset:4192\n"
      "v_mfma_f32_16x16x32_bf16 v[24:27], v[172:175], v[96:99], v[24:27]\n"
      "v_mfma_f32_16x16x32_bf16 v[24:27], v[192:195], v[100:103], v[24:27]\n"
      "s_waitcnt lgkmcnt(2)\n"
      "v_cmp_gt_u32 vcc, 11, v4\n"
      "v_mfma_f32_16x16x32_bf16 v[24:27], v[212:215], v[104:107], v[24:27]\n"
      "buffer_load_dwordx4 v[92:95], v5, s[20:23], s49 offen offset:1024 sc1\n"
      "ds_write_b128 v8, v[20:23] offset:44032\n"
      "v_mfma_f32_16x16x32_bf16 v[24:27], v[232:235], v[108:111], v[24:27]\n"
      "s_cbranch_vccnz .Lp_53\n"
      ".Lp_54:\n"
      "v_mfma_f32_16x16x32_bf16 v[24:27], v[252:255], v[112:115], v[24:27]\n"
      "s_waitcnt vmcnt(13)\n"
      "s_waitcnt lgkmcnt(2)\n"
      "v_mfma_f32_16x16x32_bf16 v[28:31], v[116:119], v[32:35], v[28:31]\n"
      "ds_read_b32 v4, v15\n"
      "ds_read_b128 v[16:19], v9 offset:41984\n"
      "v_mfma_f32_16x16x32_bf16 v[28:31], v[136:139], v[36:39], v[28:31]\n"
      "buffer_load_dwordx4 v[96:99], v5, s[20:23], s49 offen offset:2048 sc1\n"
      "ds_add_u32 v2, v1 offset:4192\n"
      "v_mfma_f32_16x16x32_bf16 v[28:31], v[156:159], v[40:43], v[28:31]\n"
      "s_waitcnt vmcnt(10)\n"
      "v_mfma_f32_16x16x32_bf16 v[28:31], v[176:179], v[44:47], v[28:31]\n"
      "s_waitcnt lgkmcnt(2)\n"
      "v_cmp_gt_u32 vcc, 12, v4\n"
      "v_mfma_f32_16x16x32_bf16 v[28:31], v[196:199], v[48:51], v[28:31]\n"
      "ds_write_b128 v8, v[24:27] offset:45056\n"
      "v_mfma_f32_16x16x32_bf16 v[28:31], v[216:219], v[52:55], v[28:31]\n"
      "buffer_load_dwordx4 v[100:103], v5, s[20:23], s49 offen offset:3072 sc1\n"
      "s_cbranch_vccnz .Lp_55\n"
      ".Lp_56:\n"
      "v_mfma_f32_16x16x32_bf16 v[28:31], v[236:239], v[56:59], v[28:31]\n"
      "s_waitcnt lgkmcnt(2)\n"
      "v_mfma_f32_16x16x32_bf16 v[16:19], v[120:123], v[32:35], v[16:19]\n"
      "ds_read_b32 v4, v15\n"
      "ds_read_b128 v[20:23], v9 offset:43008\n"
      "v_mfma_f32_16x16x32_bf16 v[16:19], v[140:143], v[36:39], v[16:19]\n"
      "ds_add_u32 v2, v1 offset:4192\n"
      "v_mfma_f32_16x16x32_bf16 v[16:19], v[160:163], v[40:43], v[16:19]\n"
      "buffer_load_dwordx4 v[104:107], v6, s[20:23], s49 offen offset:0 sc1\n"
      "v_mfma_f32_16x16x32_bf16 v[16:19], v[180:183], v[44:47], v[16:19]\n"
      "s_waitcnt lgkmcnt(2)\n"
      "v_cmp_gt_u32 vcc, 13, v4\n"
      "v_mfma_f32_16x16x32_bf16 v[16:19], v[200:203], v[48:51], v[16:19]\n"
      "v_cvt_pk_bf16_f32 v10, v28, v29\n"
      "v_cvt_pk_bf16_f32 v11, v30, v31\n"
      "v_mfma_f32_16x16x32_bf16 v[16:19], v[220:223], v[52:55], v[16:19]\n"
      "s_cbranch_vccnz .Lp_57\n"
      ".Lp_58:\n"
      "v_mfma_f32_16x16x32_bf16 v[16:19], v[240:243], v[56:59], v[16:19]\n"
      "buffer_load_dwordx4 v[108:111], v6, s[20:23], s49 offen offset:1024 sc1\n"
      "s_waitcnt lgkmcnt(1)\n"
      "v_mfma_f32_16x16x32_bf16 v[20:23], v[124:127], v[32:35], v[20:23]\n"
      "ds_read_b32 v4, v15\n"
      "ds_read_b128 v[24:27], v9 offset:44032\n"
      "v_mfma_f32_16x16x32_bf16 v[20:23], v[144:147], v[36:39], v[20:23]\n"
      "v_mfma_f32_16x16x32_bf16 v[20:23], v[164:167], v[40:43], v[20:23]\n"
      "v_mfma_f32_16x16x32_bf16 v[20:23], v[184:187], v[44:47], v[20:23]\n"
      "buffer_load_dwordx4 v[112:115], v7, s[20:23], s49 offen offset:0 sc1\n"
      "s_waitcnt lgkmcnt(1)\n"
      "v_cmp_gt_u32 vcc, 14, v4\n"
      "v_mfma_f32_16x16x32_bf16 v[20:23], v[204:207], v[48:51], v[20:23]\n"
      "v_cvt_pk_bf16_f32 v12, v16, v17\n"
      "v_cvt_pk_bf16_f32 v13, v18, s75\n"
      "v_mfma_f32_16x16x32_bf16 v[20:23], v[224:227], v[52:55], v[20:23]\n"
      "s_cbranch_vccnz .Lp_59\n"
      ".Lp_60:\n"
      "v_mfma_f32_16x16x32_bf16 v[20:23], v[244:247], v[56:59], v[20:23]\n"
      "buffer_store_dwordx4 v[10:13], v5, s[20:23], s28 offen offset:0 sc1\n"
      "s_waitcnt lgkmcnt(0)\n"
      "v_mfma_f32_16x16x32_bf16 v[24:27], v[128:131], v[32:35], v[24:27]\n"
      "ds_read_b32 v4, v15\n"
      "ds_read_b128 v[28:31], v9 offset:45056\n"
      "v_mfma_f32_16x16x32_bf16 v[24:27], v[148:151], v[36:39], v[24:27]\n"
      "v_mfma_f32_16x16x32_bf16 v[24:27], v[168:171], v[40:43], v[24:27]\n"
      "v_mfma_f32_16x16x32_bf16 v[24:27], v[188:191], v[44:47], v[24:27]\n"
      "s_waitcnt lgkmcnt(1)\n"
      "v_cmp_gt_u32 vcc, 15, v4\n"
      "v_mfma_f32_16x16x32_bf16 v[24:27], v[208:211], v[48:51], v[24:27]\n"
      "v_cvt_pk_bf16_f32 v10, v19, v20\n"
      "v_cvt_pk_bf16_f32 v11, v21, v22\n"
      "v_mfma_f32_16x16x32_bf16 v[24:27], v[228:231], v[52:55], v[24:27]\n"
      "s_cbranch_vccnz .Lp_61\n"
      ".Lp_62:\n"
      "v_mfma_f32_16x16x32_bf16 v[24:27], v[248:251], v[56:59], v[24:27]\n"
      "s_waitcnt lgkmcnt(0)\n"
      "v_mfma_f32_16x16x32_bf16 v[28:31], v[132:135], v[32:35], v[28:31]\n"
      "buffer_load_dwordx4 v[32:35], v5, s[20:23], s50 offen offset:0 sc1\n"
      "ds_read_b128 v[16:19], v14 offset:0\n"
      "v_mfma_f32_16x16x32_bf16 v[28:31], v[152:155], v[36:39], v[28:31]\n"
      "v_mfma_f32_16x16x32_bf16 v[28:31], v[172:175], v[40:43], v[28:31]\n"
      "ds_read_b32 v4, v2 offset:4308\n"
      "v_mfma_f32_16x16x32_bf16 v[28:31], v[192:195], v[44:47], v[28:31]\n"
      "v_mfma_f32_16x16x32_bf16 v[28:31], v[212:215], v[48:51], v[28:31]\n"
      "buffer_load_dwordx4 v[36:39], v5, s[20:23], s50 offen offset:1024 sc1\n"
      "v_cvt_pk_bf16_f32 v12, v23, v24\n"
      "v_cvt_pk_bf16_f32 v13, v25, s75\n"
      "v_mfma_f32_16x16x32_bf16 v[28:31], v[232:235], v[52:55], v[28:31]\n"
      "v_mfma_f32_16x16x32_bf16 v[28:31], v[252:255], v[56:59], v[28:31]\n"
      "buffer_store_dwordx4 v[10:13], v5, s[20:23], s28 offen offset:1024 sc1\n"
      "s_waitcnt vmcnt(15)\n"
      "s_waitcnt lgkmcnt(1)\n"
      "v_mfma_f32_16x16x32_bf16 v[16:19], v[116:119], v[60:63], v[16:19]\n"
      "ds_read_b128 v[20:23], v14 offset:64\n"
      "v_mfma_f32_16x16x32_bf16 v[16:19], v[136:139], v[64:67], v[16:19]\n"
      "buffer_load_dwordx4 v[40:43], v5, s[20:23], s50 offen offset:2048 sc1\n"
      "v_mfma_f32_16x16x32_bf16 v[16:19], v[156:159], v[68:71], v[16:19]\n"
      "s_waitcnt vmcnt(12)\n"
      "v_mfma_f32_16x16x32_bf16 v[16:19], v[176:179], v[72:75], v[16:19]\n"
      "s_waitcnt lgkmcnt(1)\n"
      "v_readfirstlane_b32 s2, v4\n"
      "s_cmp_lt_u32 s2, 4\n"
      "s_cbranch_scc1 .Lw_63\n"
      ".Lw_64:\n"
      "v_mfma_f32_16x16x32_bf16 v[16:19], v[196:199], v[76:79], v[16:19]\n"
      "v_cvt_pk_bf16_f32 v10, v26, v27\n"
      "v_cvt_pk_bf16_f32 v11, v28, v29\n"
      "v_mfma_f32_16x16x32_bf16 v[16:19], v[216:219], v[80:83], v[16:19]\n"
      "buffer_load_dwordx4 v[44:47], v5, s[20:23], s50 offen offset:3072 sc1\n"
      "v_cvt_pk_bf16_f32 v12, v30, v31\n"
      "v_cvt_pk_bf16_f32 v13, v31, s75\n"
      "v_mfma_f32_16x16x32_bf16 v[16:19], v[236:239], v[84:87], v[16:19]\n"
      "buffer_store_dwordx4 v[10:13], v5, s[20:23], s28 offen offset:2048 sc1\n"
      "s_waitcnt lgkmcnt(0)\n"
      "v_mfma_f32_16x16x32_bf16 v[20:23], v[120:123], v[60:63], v[20:23]\n"
      "ds_read_b128 v[24:27], v14 offset:128\n"
      "v_mfma_f32_16x16x32_bf16 v[20:23], v[140:143], v[64:67], v[20:23]\n"
      "v_mfma_f32_16x16x32_bf16 v[20:23], v[160:163], v[68:71], v[20:23]\n"
      "buffer_load_dwordx4 v[48:51], v6, s[20:23], s50 offen offset:0 sc1\n"
      "v_mfma_f32_16x16x32_bf16 v[20:23], v[180:183], v[72:75], v[20:23]\n"
      "v_mfma_f32_16x16x32_bf16 v[20:23], v[200:203], v[76:79], v[20:23]\n"
      "v_add_u32 v8, 4294934528, v8\n"
      "ds_write_b128 v8, v[16:19] offset:0\n"
      "v_mfma_f32_16x16x32_bf16 v[20:23], v[220:223], v[80:83], v[20:23]\n"
      "v_mfma_f32_16x16x32_bf16 v[20:23], v[240:243], v[84:87], v[20:23]\n"
      "buffer_load_dwordx4 v[52:55], v6, s[20:23], s50 offen offset:1024 sc1\n"
      "s_waitcnt lgkmcnt(1)\n"
      "v_mfma_f32_16x16x32_bf16 v[24:27], v[124:127], v[60:63], v[24:27]\n"
      "ds_read_b128 v[28:31], v14 offset:192\n"
      "v_mfma_f32_16x16x32_bf16 v[24:27], v[144:147], v[64:67], v[24:27]\n"
      "ds_add_u32 v2, v1 offset:4192\n"
      "v_mfma_f32_16x16x32_bf16 v[24:27], v[164:167], v[68:71], v[24:27]\n"
      "v_mfma_f32_16x16x32_bf16 v[24:27], v[184:187], v[72:75], v[24:27]\n"
      "buffer_load_dwordx4 v[56:59], v7, s[20:23], s50 offen offset:0 sc1\n"
      "v_mfma_f32_16x16x32_bf16 v[24:27], v[204:207], v[76:79], v[24:27]\n"
      "ds_write_b128 v8, v[20:23] offset:1024\n"
      "v_mfma_f32_16x16x32_bf16 v[24:27], v[224:227], v[80:83], v[24:27]\n"
      "v_mfma_f32_16x16x32_bf16 v[24:27], v[244:247], v[84:87], v[24:27]\n"
      "s_waitcnt lgkmcnt(2)\n"
      "v_mfma_f32_16x16x32_bf16 v[28:31], v[128:131], v[60:63], v[28:31]\n"
      "ds_read_b128 v[16:19], v14 offset:256\n"
      "v_mfma_f32_16x16x32_bf16 v[28:31], v[148:151], v[64:67], v[28:31]\n"
      "ds_add_u32 v2, v1 offset:4192\n"
      "v_mfma_f32_16x16x32_bf16 v[28:31], v[168:171], v[68:71], v[28:31]\n"
      "v_mfma_f32_16x16x32_bf16 v[28:31], v[188:191], v[72:75], v[28:31]\n"
      "v_mfma_f32_16x16x32_bf16 v[28:31], v[208:211], v[76:79], v[28:31]\n"
      "ds_write_b128 v8, v[24:27] offset:2048\n"
      "v_mfma_f32_16x16x32_bf16 v[28:31], v[228:231], v[80:83], v[28:31]\n"
      "v_mfma_f32_16x16x32_bf16 v[28:31], v[248:251], v[84:87], v[28:31]\n"
      "s_waitcnt lgkmcnt(2)\n"
      "v_mfma_f32_16x16x32_bf16 v[16:19], v[132:135], v[60:63], v[16:19]\n"
      "buffer_load_dwordx4 v[60:63], v5, s[20:23], s51 offen offset:0 sc1\n"
      "v_add_u32 v9, 4294934528, v9\n"
      "ds_read_b32 v4, v15\n"
      "ds_read_b128 v[20:23], v9 offset:0\n"
      "v_mfma_f32_16x16x32_bf16 v[16:19], v[152:155], v[64:67], v[16:19]\n"
      "ds_add_u32 v2, v1 offset:4192\n"
      "v_mfma_f32_16x16x32_bf16 v[16:19], v[172:175], v[68:71], v[16:19]\n"
      "v_mfma_f32_16x16x32_bf16 v[16:19], v[192:195], v[72:75], v[16:19]\n"
      "s_waitcnt lgkmcnt(2)\n"
      "v_cmp_gt_u32 vcc, 16, v4\n"
      "v_mfma_f32_16x16x32_bf16 v[16:19], v[212:215], v[76:79], v[16:19]\n"
      "buffer_load_dwordx4 v[64:67], v5, s[20:23], s51 offen offset:1024 sc1\n"
      "ds_write_b128 v8, v[28:31] offset:3072\n"
      "v_mfma_f32_16x16x32_bf16 v[16:19], v[232:235], v[80:83], v[16:19]\n"
      "s_cbranch_vccnz .Lp_66\n"
      ".Lp_67:\n"
      "v_mfma_f32_16x16x32_bf16 v[16:19], v[252:255], v[84:87], v[16:19]\n"
      "s_waitcnt vmcnt(16)\n"
      "s_waitcnt lgkmcnt(2)\n"
      "v_mfma_f32_16x16x32_bf16 v[20:23], v[116:119], v[88:91], v[20:23]\n"
      "ds_read_b32 v4, v15\n"
      "ds_read_b128 v[24:27], v9 offset:1024\n"
      "v_mfma_f32_16x16x32_bf16 v[20:23], v[136:139], v[92:95], v[20:23]\n"
      "buffer_load_dwordx4 v[68:71], v5, s[20:23], s51 offen offset:2048 sc1\n"
      "ds_add_u32 v2, v1 offset:4192\n"
      "v_mfma_f32_16x16x32_bf16 v[20:23], v[156:159], v[96:99], v[20:23]\n"
      "s_waitcnt vmcnt(13)\n"
      "v_mfma_f32_16x16x32_bf16 v[20:23], v[176:179], v[100:103], v[20:23]\n"
      "s_waitcnt lgkmcnt(2)\n"
      "v_cmp_gt_u32 vcc, 17, v4\n"
      "v_mfma_f32_16x16x32_bf16 v[20:23], v[196:199], v[104:107], v[20:23]\n"
      "ds_write_b128 v8, v[16:19] offset:4096\n"
      "v_mfma_f32_16x16x32_bf16 v[20:23], v[216:219], v[108:111], v[20:23]\n"
      "buffer_load_dwordx4 v[72:75], v5, s[20:23], s51 offen offset:3072 sc1\n"
      "s_cbranch_vccnz .Lp_68\n"
      ".Lp_69:\n"
      "v_mfma_f32_16x16x32_bf16 v[20:23], v[236:239], v[112:115], v[20:23]\n"
      "s_waitcnt lgkmcnt(2)\n"
      "v_mfma_f32_16x16x32_bf16 v[24:27], v[120:123], v[88:91], v[24:27]\n"
      "ds_read_b32 v4, v15\n"
      "ds_read_b128 v[28:31], v9 offset:2048\n"
      "v_mfma_f32_16x16x32_bf16 v[24:27], v[140:143], v[92:95], v[24:27]\n"
      "ds_add_u32 v2, v1 offset:4192\n"
      "v_mfma_f32_16x16x32_bf16 v[24:27], v[160:163], v[96:99], v[24:27]\n"
      "buffer_load_dwordx4 v[76:79], v6, s[20:23], s51 offen offset:0 sc1\n"
      "v_mfma_f32_16x16x32_bf16 v[24:27], v[180:183], v[100:103], v[24:27]\n"
      "s_waitcnt lgkmcnt(2)\n"
      "v_cmp_gt_u32 vcc, 18, v4\n"
      "v_mfma_f32_16x16x32_bf16 v[24:27], v[200:203], v[104:107], v[24:27]\n"
      "v_add_u32 v8, 98304, v8\n"
      "ds_write_b128 v8, v[20:23] offset:0\n"
      "v_mfma_f32_16x16x32_bf16 v[24:27], v[220:223], v[108:111], v[24:27]\n"
      "s_cbranch_vccnz .Lp_70\n"
      ".Lp_71:\n"
      "v_mfma_f32_16x16x32_bf16 v[24:27], v[240:243], v[112:115], v[24:27]\n"
      "buffer_load_dwordx4 v[80:83], v6, s[20:23], s51 offen offset:1024 sc1\n"
      "s_waitcnt lgkmcnt(2)\n"
      "v_mfma_f32_16x16x32_bf16 v[28:31], v[124:127], v[88:91], v[28:31]\n"
      "ds_read_b32 v4, v15\n"
      "ds_read_b128 v[16:19], v9 offset:3072\n"
      "v_mfma_f32_16x16x32_bf16 v[28:31], v[144:147], v[92:95], v[28:31]\n"
      "ds_add_u32 v2, v1 offset:4192\n"
      "v_mfma_f32_16x16x32_bf16 v[28:31], v[164:167], v[96:99], v[28:31]\n"
      "v_mfma_f32_16x16x32_bf16 v[28:31], v[184:187], v[100:103], v[28:31]\n"
      "buffer_load_dwordx4 v[84:87], v7, s[20:23], s51 offen offset:0 sc1\n"
      "s_waitcnt lgkmcnt(2)\n"
      "v_cmp_gt_u32 vcc, 19, v4\n"
      "v_mfma_f32_16x16x32_bf16 v[28:31], v[204:207], v[104:107], v[28:31]\n"
      "ds_write_b128 v8, v[24:27] offset:1024\n"
      "v_mfma_f32_16x16x32_bf16 v[28:31], v[224:227], v[108:111], v[28:31]\n"
      "s_cbranch_vccnz .Lp_72\n"
      ".Lp_73:\n"
      "v_mfma_f32_16x16x32_bf16 v[28:31], v[244:247], v[112:115], v[28:31]\n"
      "s_waitcnt lgkmcnt(2)\n"
      "v_mfma_f32_16x16x32_bf16 v[16:19], v[128:131], v[88:91], v[16:19]\n"
      "ds_read_b32 v4, v15\n"
      "ds_read_b128 v[20:23], v9 offset:4096\n"
      "v_mfma_f32_16x16x32_bf16 v[16:19], v[148:151], v[92:95], v[16:19]\n"
      "ds_add_u32 v2, v1 offset:4192\n"
      "v_mfma_f32_16x16x32_bf16 v[16:19], v[168:171], v[96:99], v[16:19]\n"
      "v_mfma_f32_16x16x32_bf16 v[16:19], v[188:191], v[100:103], v[16:19]\n"
      "s_waitcnt lgkmcnt(2)\n"
      "v_cmp_gt_u32 vcc, 20, v4\n"
      "v_mfma_f32_16x16x32_bf16 v[16:19], v[208:211], v[104:107], v[16:19]\n"
      "ds_write_b128 v8, v[28:31] offset:2048\n"
      "v_mfma_f32_16x16x32_bf16 v[16:19], v[228:231], v[108:111], v[16:19]\n"
      "s_cbranch_vccnz .Lp_74\n"
      ".Lp_75:\n"
      "v_mfma_f32_16x16x32_bf16 v[16:19], v[248:251], v[112:115], v[16:19]\n"
      "s_waitcnt lgkmcnt(2)\n"
      "v_mfma_f32_16x16x32_bf16 v[20:23], v[132:135], v[88:91], v[20:23]\n"
      "v_add_u32 v9, 98304, v9\n"
      "ds_read_b32 v4, v15\n"
      "ds_read_b128 v[24:27], v9 offset:0\n"
      "v_mfma_f32_16x16x32_bf16 v[20:23], v[152:155], v[92:95], v[20:23]\n"
      "ds_add_u32 v2, v1 offset:4192\n"
      "v_mfma_f32_16x16x32_bf16 v[20:23], v[172:175], v[96:99], v[20:23]\n"
      "v_mfma_f32_16x16x32_bf16 v[20:23], v[192:195], v[100:103], v[20:23]\n"
      "s_waitcnt lgkmcnt(2)\n"
      "v_cmp_gt_u32 vcc, 21, v4\n"
      "v_mfma_f32_16x16x32_bf16 v[20:23], v[212:215], v[104:107], v[20:23]\n"
      "ds_write_b128 v8, v[16:19] offset:3072\n"
      "v_mfma_f32_16x16x32_bf16 v[20:23], v[232:235], v[108:111], v[20:23]\n"
      "s_cbranch_vccnz .Lp_76\n"
      ".Lp_77:\n"
      "v_mfma_f32_16x16x32_bf16 v[20:23], v[252:255], v[112:115], v[20:23]\n"
      "s_waitcnt vmcnt(12)\n"
      "s_waitcnt lgkmcnt(2)\n"
      "v_mfma_f32_16x16x32_bf16 v[24:27], v[116:119], v[32:35], v[24:27]\n"
      "ds_read_b32 v4, v15\n"
      "ds_read_b128 v[28:31], v9 offset:1024\n"
      "v_mfma_f32_16x16x32_bf16 v[24:27], v[136:139], v[36:39], v[24:27]\n"
      "ds_add_u32 v2, v1 offset:4192\n"
      "v_mfma_f32_16x16x32_bf16 v[24:27], v[156:159], v[40:43], v[24:27]\n"
      "s_waitcnt vmcnt(7)\n"
      "v_mfma_f32_16x16x32_bf16 v[24:27], v[176:179], v[44:47], v[24:27]\n"
      "s_waitcnt lgkmcnt(2)\n"
      "v_cmp_gt_u32 vcc, 22, v4\n"
      "v_mfma_f32_16x16x32_bf16 v[24:27], v[196:199], v[48:51], v[24:27]\n"
      "ds_write_b128 v8, v[20:23] offset:4096\n"
      "v_mfma_f32_16x16x32_bf16 v[24:27], v[216:219], v[52:55], v[24:27]\n"
      "s_cbranch_vccnz .Lp_78\n"
      ".Lp_79:\n"
      "v_mfma_f32_16x16x32_bf16 v[24:27], v[236:239], v[56:59], v[24:27]\n"
      "s_waitcnt lgkmcnt(2)\n"
      "v_mfma_f32_16x16x32_bf16 v[28:31], v[120:123], v[32:35], v[28:31]\n"
      "ds_read_b32 v4, v15\n"
      "ds_read_b128 v[16:19], v9 offset:2048\n"
      "v_mfma_f32_16x16x32_bf16 v[28:31], v[140:143], v[36:39], v[28:31]\n"
      "ds_add_u32 v2, v1 offset:4192\n"
      "v_mfma_f32_16x16x32_bf16 v[28:31], v[160:163], v[40:43], v[28:31]\n"
      "v_mfma_f32_16x16x32_bf16 v[28:31], v[180:183], v[44:47], v[28:31]\n"
      "s_waitcnt lgkmcnt(2)\n"
      "v_cmp_gt_u32 vcc, 23, v4\n"
      "v_mfma_f32_16x16x32_bf16 v[28:31], v[200:203], v[48:51], v[28:31]\n"
      "v_add_u32 v8, 4294901760, v8\n"
      "ds_write_b128 v8, v[24:27] offset:0\n"
      "v_mfma_f32_16x16x32_bf16 v[28:31], v[220:223], v[52:55], v[28:31]\n"
      "s_cbranch_vccnz .Lp_80\n"
      ".Lp_81:\n"
      "v_mfma_f32_16x16x32_bf16 v[28:31], v[240:243], v[56:59], v[28:31]\n"
      "s_waitcnt lgkmcnt(2)\n"
      "v_mfma_f32_16x16x32_bf16 v[16:19], v[124:127], v[32:35], v[16:19]\n"
      "ds_read_b32 v4, v15\n"
      "ds_read_b128 v[20:23], v9 offset:3072\n"
      "v_mfma_f32_16x16x32_bf16 v[16:19], v[144:147], v[36:39], v[16:19]\n"
      "ds_add_u32 v2, v1 offset:4192\n"
      "v_mfma_f32_16x16x32_bf16 v[16:19], v[164:167], v[40:43], v[16:19]\n"
      "v_mfma_f32_16x16x32_bf16 v[16:19], v[184:187], v[44:47], v[16:19]\n"
      "s_waitcnt lgkmcnt(2)\n"
      "v_cmp_gt_u32 vcc, 24, v4\n"
      "v_mfma_f32_16x16x32_bf16 v[16:19], v[204:207], v[48:51], v[16:19]\n"
      "ds_write_b128 v8, v[28:31] offset:1024\n"
      "v_mfma_f32_16x16x32_bf16 v[16:19], v[224:227], v[52:55], v[16:19]\n"
      "s_cbranch_vccnz .Lp_82\n"
      ".Lp_83:\n"
      "v_mfma_f32_16x16x32_bf16 v[16:19], v[244:247], v[56:59], v[16:19]\n"
      "s_waitcnt lgkmcnt(2)\n"
      "v_mfma_f32_16x16x32_bf16 v[20:23], v[128:131], v[32:35], v[20:23]\n"
      "ds_read_b32 v4, v15\n"
      "ds_read_b128 v[24:27], v9 offset:4096\n"
      "v_mfma_f32_16x16x32_bf16 v[20:23], v[148:151], v[36:39], v[20:23]\n"
      "ds_add_u32 v2, v1 offset:4192\n"
      "v_mfma_f32_16x16x32_bf16 v[20:23], v[168:171], v[40:43], v[20:23]\n"
      "v_mfma_f32_16x16x32_bf16 v[20:23], v[188:191], v[44:47], v[20:23]\n"
      "s_waitcnt lgkmcnt(2)\n"
      "v_cmp_gt_u32 vcc, 25, v4\n"
      "v_mfma_f32_16x16x32_bf16 v[20:23], v[208:211], v[48:51], v[20:23]\n"
      "ds_write_b128 v8, v[16:19] offset:2048\n"
      "v_mfma_f32_16x16x32_bf16 v[20:23], v[228:231], v[52:55], v[20:23]\n"
      "s_cbranch_vccnz .Lp_84\n"
      ".Lp_85:\n"
      "v_mfma_f32_16x16x32_bf16 v[20:23], v[248:251], v[56:59], v[20:23]\n"
      "s_waitcnt lgkmcnt(2)\n"
      "v_mfma_f32_16x16x32_bf16 v[24:27], v[132:135], v[32:35], v[24:27]\n"
      "v_add_u32 v9, 4294901760, v9\n"
      "ds_read_b32 v4, v15\n"
      "ds_read_b128 v[28:31], v9 offset:0\n"
      "v_mfma_f32_16x16x32_bf16 v[24:27], v[152:155], v[36:39], v[24:27]\n"
      "ds_add_u32 v2, v1 offset:4192\n"
      "v_mfma_f32_16x16x32_bf16 v[24:27], v[172:175], v[40:43], v[24:27]\n"
      "v_mfma_f32_16x16x32_bf16 v[24:27], v[192:195], v[44:47], v[24:27]\n"
      "s_waitcnt lgkmcnt(2)\n"
      "v_cmp_gt_u32 vcc, 26, v4\n"
      "v_mfma_f32_16x16x32_bf16 v[24:27], v[212:215], v[48:51], v[24:27]\n"
      "ds_write_b128 v8, v[20:23] offset:3072\n"
      "v_mfma_f32_16x16x32_bf16 v[24:27], v[232:235], v[52:55], v[24:27]\n"
      "s_cbranch_vccnz .Lp_86\n"
      ".Lp_87:\n"
      "v_mfma_f32_16x16x32_bf16 v[24:27], v[252:255], v[56:59], v[24:27]\n"
      "s_waitcnt vmcnt(4)\n"
      "s_waitcnt lgkmcnt(2)\n"
      "v_mfma_f32_16x16x32_bf16 v[28:31], v[116:119], v[60:63], v[28:31]\n"
      "ds_read_b32 v4, v15\n"
      "ds_read_b128 v[16:19], v9 offset:1024\n"
      "v_mfma_f32_16x16x32_bf16 v[28:31], v[136:139], v[64:67], v[28:31]\n"
      "ds_add_u32 v2, v1 offset:4192\n"
      "v_mfma_f32_16x16x32_bf16 v[28:31], v[156:159], v[68:71], v[28:31]\n"
      "s_waitcnt vmcnt(0)\n"
      "v_mfma_f32_16x16x32_bf16 v[28:31], v[176:179], v[72:75], v[28:31]\n"
      "s_waitcnt lgkmcnt(2)\n"
      "v_cmp_gt_u32 vcc, 27, v4\n"
      "v_mfma_f32_16x16x32_bf16 v[28:31], v[196:199], v[76:79], v[28:31]\n"
      "ds_write_b128 v8, v[24:27] offset:4096\n"
      "v_mfma_f32_16x16x32_bf16 v[28:31], v[216:219], v[80:83], v[28:31]\n"
      "s_cbranch_vccnz .Lp_88\n"
      ".Lp_89:\n"
      "v_mfma_f32_16x16x32_bf16 v[28:31], v[236:239], v[84:87], v[28:31]\n"
      "s_waitcnt lgkmcnt(2)\n"
      "v_mfma_f32_16x16x32_bf16 v[16:19], v[120:123], v[60:63], v[16:19]\n"
      "ds_read_b32 v4, v15\n"
      "ds_read_b128 v[20:23], v9 offset:2048\n"
      "v_mfma_f32_16x16x32_bf16 v[16:19], v[140:143], v[64:67], v[16:19]\n"
      "ds_add_u32 v2, v1 offset:4192\n"
      "v_mfma_f32_16x16x32_bf16 v[16:19], v[160:163], v[68:71], v[16:19]\n"
      "v_mfma_f32_16x16x32_bf16 v[16:19], v[180:183], v[72:75], v[16:19]\n"
      "s_waitcnt lgkmcnt(2)\n"
      "v_cmp_gt_u32 vcc, 28, v4\n"
      "v_mfma_f32_16x16x32_bf16 v[16:19], v[200:203], v[76:79], v[16:19]\n"
      "ds_write_b128 v8, v[28:31] offset:20480\n"
      "v_mfma_f32_16x16x32_bf16 v[16:19], v[220:223], v[80:83], v[16:19]\n"
      "s_cbranch_vccnz .Lp_90\n"
      ".Lp_91:\n"
      "v_mfma_f32_16x16x32_bf16 v[16:19], v[240:243], v[84:87], v[16:19]\n"
      "s_waitcnt lgkmcnt(2)\n"
      "v_mfma_f32_16x16x32_bf16 v[20:23], v[124:127], v[60:63], v[20:23]\n"
      "ds_read_b32 v4, v15\n"
      "ds_read_b128 v[24:27], v9 offset:3072\n"
      "v_mfma_f32_16x16x32_bf16 v[20:23], v[144:147], v[64:67], v[20:23]\n"
      "ds_add_u32 v2, v1 offset:4192\n"
      "v_mfma_f32_16x16x32_bf16 v[20:23], v[164:167], v[68:71], v[20:23]\n"
      "v_mfma_f32_16x16x32_bf16 v[20:23], v[184:187], v[72:75], v[20:23]\n"
      "s_waitcnt lgkmcnt(2)\n"
      "v_cmp_gt_u32 vcc, 29, v4\n"
      "v_mfma_f32_16x16x32_bf16 v[20:23], v[204:207], v[76:79], v[20:23]\n"
      "ds_write_b128 v8, v[16:19] offset:21504\n"
      "v_mfma_f32_16x16x32_bf16 v[20:23], v[224:227], v[80:83], v[20:23]\n"
      "s_cbranch_vccnz .Lp_92\n"
      ".Lp_93:\n"
      "v_mfma_f32_16x16x32_bf16 v[20:23], v[244:247], v[84:87], v[20:23]\n"
      "s_waitcnt lgkmcnt(2)\n"
      "v_mfma_f32_16x16x32_bf16 v[24:27], v[128:131], v[60:63], v[24:27]\n"
      "ds_read_b32 v4, v15\n"
      "ds_read_b128 v[28:31], v9 offset:4096\n"
      "v_mfma_f32_16x16x32_bf16 v[24:27], v[148:151], v[64:67], v[24:27]\n"
      "ds_add_u32 v2, v1 offset:4192\n"
      "v_mfma_f32_16x16x32_bf16 v[24:27], v[168:171], v[68:71], v[24:27]\n"
      "v_mfma_f32_16x16x32_bf16 v[24:27], v[188:191], v[72:75], v[24:27]\n"
      "s_waitcnt lgkmcnt(2)\n"
      "v_cmp_gt_u32 vcc, 30, v4\n"
      "v_mfma_f32_16x16x32_bf16 v[24:27], v[208:211], v[76:79], v[24:27]\n"
      "ds_write_b128 v8, v[20:23] offset:22528\n"
      "v_mfma_f32_16x16x32_bf16 v[24:27], v[228:231], v[80:83], v[24:27]\n"
      "s_cbranch_vccnz .Lp_94\n"
      ".Lp_95:\n"
      "v_mfma_f32_16x16x32_bf16 v[24:27], v[248:251], v[84:87], v[24:27]\n"
      "s_waitcnt lgkmcnt(2)\n"
      "v_mfma_f32_16x16x32_bf16 v[28:31], v[132:135], v[60:63], v[28:31]\n"
      "v_mfma_f32_16x16x32_bf16 v[28:31], v[152:155], v[64:67], v[28:31]\n"
      "ds_add_u32 v2, v1 offset:4192\n"
      "v_mfma_f32_16x16x32_bf16 v[28:31], v[172:175], v[68:71], v[28:31]\n"
      "v_mfma_f32_16x16x32_bf16 v[28:31], v[192:195], v[72:75], v[28:31]\n"
      "v_mfma_f32_16x16x32_bf16 v[28:31], v[212:215], v[76:79], v[28:31]\n"
      "s_nop 0\n"
      "ds_write_b128 v8, v[24:27] offset:23552\n"
      "v_mfma_f32_16x16x32_bf16 v[28:31], v[232:235], v[80:83], v[28:31]\n"
      "v_mfma_f32_16x16x32_bf16 v[28:31], v[252:255], v[84:87], v[28:31]\n"
      "ds_add_u32 v2, v1 offset:4192\n"
      "s_nop 6\n"
      "ds_write_b128 v8, v[28:31] offset:24576\n"
      "ds_add_u32 v2, v1 offset:4192\n"
      "s_endpgm\n"
      ".LWARM:\n"
      ".Lp_30:\n"
      "s_cbranch_vccz .Lp_32\n"
      ".Lp_96:\n"
      "ds_read_b32 v4, v15\n"
      "ds_read_b128 v[20:23], v9 offset:0\n"
      "s_sub_u32 s73, s73, 1\n"
      "s_cselect_b32 s73, 0, s73\n"
      "s_waitcnt lgkmcnt(0)\n"
      "v_cmp_gt_u32 vcc, 1, v4\n"
      "s_cselect_b64 vcc, 0, vcc\n"
      "s_cbranch_vccnz .Lp_96\n"
      "s_branch .Lp_31\n"
      ".Lp_32:\n"
      "s_cbranch_vccz .Lp_34\n"
      ".Lp_97:\n"
      "ds_read_b32 v4, v15\n"
      "ds_read_b128 v[24:27], v9 offset:1024\n"
      "s_sub_u32 s73, s73, 1\n"
      "s_cselect_b32 s73, 0, s73\n"
      "s_waitcnt lgkmcnt(0)\n"
      "v_cmp_gt_u32 vcc, 2, v4\n"
      "s_cselect_b64 vcc, 0, vcc\n"
      "s_cbranch_vccnz .Lp_97\n"
      "s_branch .Lp_33\n"
      ".Lp_34:\n"
      "s_cbranch_vccz .Lp_36\n"
      ".Lp_98:\n"
      "ds_read_b32 v4, v15\n"
      "ds_read_b128 v[28:31], v9 offset:2048\n"
      "s_sub_u32 s73, s73, 1\n"
      "s_cselect_b32 s73, 0, s73\n"
      "s_waitcnt lgkmcnt(0)\n"
      "v_cmp_gt_u32 vcc, 3, v4\n"
      "s_cselect_b64 vcc, 0, vcc\n"
      "s_cbranch_vccnz .Lp_98\n"
      "s_branch .Lp_35\n"
      ".Lp_36:\n"
      "s_cbranch_vccz .Lp_38\n"
      ".Lp_99:\n"
      "ds_read_b32 v4, v15\n"
      "ds_read_b128 v[16:19], v9 offset:3072\n"
      "s_sub_u32 s73, s73, 1\n"
      "s_cselect_b32 s73, 0, s73\n"
      "s_waitcnt lgkmcnt(0)\n"
      "v_cmp_gt_u32 vcc, 4, v4\n"
      "s_cselect_b64 vcc, 0, vcc\n"
      "s_cbranch_vccnz .Lp_99\n"
      "s_branch .Lp_37\n"
      ".Lp_38:\n"
      "s_cbranch_vccz .Lp_40\n"
      ".Lp_100:\n"
      "ds_read_b32 v4, v15\n"
      "ds_read_b128 v[20:23], v9 offset:4096\n"
      "s_sub_u32 s73, s73, 1\n"
      "s_cselect_b32 s73, 0, s73\n"
      "s_waitcnt lgkmcnt(0)\n"
      "v_cmp_gt_u32 vcc, 5, v4\n"
      "s_cselect_b64 vcc, 0, vcc\n"
      "s_cbranch_vccnz .Lp_100\n"
      "s_branch .Lp_39\n"
      ".Lp_40:\n"
      "s_cbranch_vccz .Lp_42\n"
      ".Lp_101:\n"
      "ds_read_b32 v4, v15\n"
      "ds_read_b128 v[24:27], v9 offset:20480\n"
      "s_sub_u32 s73, s73, 1\n"
      "s_cselect_b32 s73, 0, s73\n"
      "s_waitcnt lgkmcnt(0)\n"
      "v_cmp_gt_u32 vcc, 6, v4\n"
      "s_cselect_b64 vcc, 0, vcc\n"
      "s_cbranch_vccnz .Lp_101\n"
      "s_branch .Lp_41\n"
      ".Lp_42:\n"
      "s_cbranch_vccz .Lp_44\n"
      ".Lp_102:\n"
      "ds_read_b32 v4, v15\n"
      "ds_read_b128 v[28:31], v9 offset:21504\n"
      "s_sub_u32 s73, s73, 1\n"
      "s_cselect_b32 s73, 0, s73\n"
      "s_waitcnt lgkmcnt(0)\n"
      "v_cmp_gt_u32 vcc, 7, v4\n"
      "s_cselect_b64 vcc, 0, vcc\n"
      "s_cbranch_vccnz .Lp_102\n"
      "s_branch .Lp_43\n"
      ".Lp_44:\n"
      "s_cbranch_vccz .Lp_49\n"
      ".Lp_103:\n"
      "ds_read_b32 v4, v15\n"
      "ds_read_b128 v[16:19], v9 offset:22528\n"
      "s_sub_u32 s73, s73, 1\n"
      "s_cselect_b32 s73, 0, s73\n"
      "s_waitcnt lgkmcnt(0)\n"
      "v_cmp_gt_u32 vcc, 8, v4\n"
      "s_cselect_b64 vcc, 0, vcc\n"
      "s_cbranch_vccnz .Lp_103\n"
      "s_branch .Lp_45\n"
      ".Lp_49:\n"
      "s_cbranch_vccz .Lp_51\n"
      ".Lp_104:\n"
      "ds_read_b32 v4, v15\n"
      "ds_read_b128 v[20:23], v9 offset:23552\n"
      "s_sub_u32 s73, s73, 1\n"
      "s_cselect_b32 s73, 0, s73\n"
      "s_waitcnt lgkmcnt(0)\n"
      "v_cmp_gt_u32 vcc, 9, v4\n"
      "s_cselect_b64 vcc, 0, vcc\n"
      "s_cbranch_vccnz .Lp_104\n"
      "s_branch .Lp_50\n"
      ".Lp_51:\n"
      "s_cbranch_vccz .Lp_53\n"
      ".Lp_105:\n"
      "ds_read_b32 v4, v15\n"
      "ds_read_b128 v[24:27], v9 offset:24576\n"
      "s_sub_u32 s73, s73, 1\n"
      "s_cselect_b32 s73, 0, s73\n"
      "s_waitcnt lgkmcnt(0)\n"
      "v_cmp_gt_u32 vcc, 10, v4\n"
      "s_cselect_b64 vcc, 0, vcc\n"
      "s_cbranch_vccnz .Lp_105\n"
      "s_branch .Lp_52\n"
      ".Lp_53:\n"
      "s_cbranch_vccz .Lp_55\n"
      ".Lp_106:\n"
      "ds_read_b32 v4, v15\n"
      "ds_read_b128 v[28:31], v9 offset:40960\n"
      "s_sub_u32 s73, s73, 1\n"
      "s_cselect_b32 s73, 0, s73\n"
      "s_waitcnt lgkmcnt(0)\n"
      "v_cmp_gt_u32 vcc, 11, v4\n"
      "s_cselect_b64 vcc, 0, vcc\n"
      "s_cbranch_vccnz .Lp_106\n"
      "s_branch .Lp_54\n"
      ".Lp_55:\n"
      "s_cbranch_vccz .Lp_57\n"
      ".Lp_107:\n"
      "ds_read_b32 v4, v15\n"
      "ds_read_b128 v[16:19], v9 offset:41984\n"
      "s_sub_u32 s73, s73, 1\n"
      "s_cselect_b32 s73, 0, s73\n"
      "s_waitcnt lgkmcnt(0)\n"
      "v_cmp_gt_u32 vcc, 12, v4\n"
      "s_cselect_b64 vcc, 0, vcc\n"
      "s_cbranch_vccnz .Lp_107\n"
      "s_branch .Lp_56\n"
      ".Lp_57:\n"
      "s_cbranch_vccz .Lp_59\n"
      ".Lp_108:\n"
      "ds_read_b32 v4, v15\n"
      "ds_read_b128 v[20:23], v9 offset:43008\n"
      "s_sub_u32 s73, s73, 1\n"
      "s_cselect_b32 s73, 0, s73\n"
      "s_waitcnt lgkmcnt(0)\n"
      "v_cmp_gt_u32 vcc, 13, v4\n"
      "s_cselect_b64 vcc, 0, vcc\n"
      "s_cbranch_vccnz .Lp_108\n"
      "s_branch .Lp_58\n"
      ".Lp_59:\n"
      "s_cbranch_vccz .Lp_61\n"
      ".Lp_109:\n"
      "ds_read_b32 v4, v15\n"
      "ds_read_b128 v[24:27], v9 offset:44032\n"
      "s_sub_u32 s73, s73, 1\n"
      "s_cselect_b32 s73, 0, s73\n"
      "s_waitcnt lgkmcnt(0)\n"
      "v_cmp_gt_u32 vcc, 14, v4\n"
      "s_cselect_b64 vcc, 0, vcc\n"
      "s_cbranch_vccnz .Lp_109\n"
      "s_branch .Lp_60\n"
      ".Lp_61:\n"
      "s_cbranch_vccz .Lp_66\n"
      ".Lp_110:\n"
      "ds_read_b32 v4, v15\n"
      "ds_read_b128 v[28:31], v9 offset:45056\n"
      "s_sub_u32 s73, s73, 1\n"
      "s_cselect_b32 s73, 0, s73\n"
      "s_waitcnt lgkmcnt(0)\n"
      "v_cmp_gt_u32 vcc, 15, v4\n"
      "s_cselect_b64 vcc, 0, vcc\n"
      "s_cbranch_vccnz .Lp_110\n"
      "s_branch .Lp_62\n"
      ".Lp_66:\n"
      "s_cbranch_vccz .Lp_68\n"
      ".Lp_111:\n"
      "ds_read_b32 v4, v15\n"
      "ds_read_b128 v[20:23], v9 offset:0\n"
      "s_sub_u32 s73, s73, 1\n"
      "s_cselect_b32 s73, 0, s73\n"
      "s_waitcnt lgkmcnt(0)\n"
      "v_cmp_gt_u32 vcc, 16, v4\n"
      "s_cselect_b64 vcc, 0, vcc\n"
      "s_cbranch_vccnz .Lp_111\n"
      "s_branch .Lp_67\n"
      ".Lp_68:\n"
      "s_cbranch_vccz .Lp_70\n"
      ".Lp_112:\n"
      "ds_read_b32 v4, v15\n"
      "ds_read_b128 v[24:27], v9 offset:1024\n"
      "s_sub_u32 s73, s73, 1\n"
      "s_cselect_b32 s73, 0, s73\n"
      "s_waitcnt lgkmcnt(0)\n"
      "v_cmp_gt_u32 vcc, 17, v4\n"
      "s_cselect_b64 vcc, 0, vcc\n"
      "s_cbranch_vccnz .Lp_112\n"
      "s_branch .Lp_69\n"
      ".Lp_70:\n"
      "s_cbranch_vccz .Lp_72\n"
      ".Lp_113:\n"
      "ds_read_b32 v4, v15\n"
      "ds_read_b128 v[28:31], v9 offset:2048\n"
      "s_sub_u32 s73, s73, 1\n"
      "s_cselect_b32 s73, 0, s73\n"
      "s_waitcnt lgkmcnt(0)\n"
      "v_cmp_gt_u32 vcc, 18, v4\n"
      "s_cselect_b64 vcc, 0, vcc\n"
      "s_cbranch_vccnz .Lp_113\n"
      "s_branch .Lp_71\n"
      ".Lp_72:\n"
      "s_cbranch_vccz .Lp_74\n"
      ".Lp_114:\n"
      "ds_read_b32 v4, v15\n"
      "ds_read_b128 v[16:19], v9 offset:3072\n"
      "s_sub_u32 s73, s73, 1\n"
      "s_cselect_b32 s73, 0, s73\n"
      "s_waitcnt lgkmcnt(0)\n"
      "v_cmp_gt_u32 vcc, 19, v4\n"
      "s_cselect_b64 vcc, 0, vcc\n"
      "s_cbranch_vccnz .Lp_114\n"
      "s_branch .Lp_73\n"
      ".Lp_74:\n"
      "s_cbranch_vccz .Lp_76\n"
      ".Lp_115:\n"
      "ds_read_b32 v4, v15\n"
      "ds_read_b128 v[20:23], v9 offset:4096\n"
      "s_sub_u32 s73, s73, 1\n"
      "s_cselect_b32 s73, 0, s73\n"
      "s_waitcnt lgkmcnt(0)\n"
      "v_cmp_gt_u32 vcc, 20, v4\n"
      "s_cselect_b64 vcc, 0, vcc\n"
      "s_cbranch_vccnz .Lp_115\n"
      "s_branch .Lp_75\n"
      ".Lp_76:\n"
      "s_cbranch_vccz .Lp_78\n"
      ".Lp_116:\n"
      "ds_read_b32 v4, v15\n"
      "ds_read_b128 v[24:27], v9 offset:0\n"
      "s_sub_u32 s73, s73, 1\n"
      "s_cselect_b32 s73, 0, s73\n"
      "s_waitcnt lgkmcnt(0)\n"
      "v_cmp_gt_u32 vcc, 21, v4\n"
      "s_cselect_b64 vcc, 0, vcc\n"
      "s_cbranch_vccnz .Lp_116\n"
      "s_branch .Lp_77\n"
      ".Lp_78:\n"
      "s_cbranch_vccz .Lp_80\n"
      ".Lp_117:\n"
      "ds_read_b32 v4, v15\n"
      "ds_read_b128 v[28:31], v9 offset:1024\n"
      "s_sub_u32 s73, s73, 1\n"
      "s_cselect_b32 s73, 0, s73\n"
      "s_waitcnt lgkmcnt(0)\n"
      "v_cmp_gt_u32 vcc, 22, v4\n"
      "s_cselect_b64 vcc, 0, vcc\n"
      "s_cbranch_vccnz .Lp_117\n"
      "s_branch .Lp_79\n"
      ".Lp_80:\n"
      "s_cbranch_vccz .Lp_82\n"
      ".Lp_118:\n"
      "ds_read_b32 v4, v15\n"
      "ds_read_b128 v[16:19], v9 offset:2048\n"
      "s_sub_u32 s73, s73, 1\n"
      "s_cselect_b32 s73, 0, s73\n"
      "s_waitcnt lgkmcnt(0)\n"
      "v_cmp_gt_u32 vcc, 23, v4\n"
      "s_cselect_b64 vcc, 0, vcc\n"
      "s_cbranch_vccnz .Lp_118\n"
      "s_branch .Lp_81\n"
      ".Lp_82:\n"
      "s_cbranch_vccz .Lp_84\n"
      ".Lp_119:\n"
      "ds_read_b32 v4, v15\n"
      "ds_read_b128 v[20:23], v9 offset:3072\n"
      "s_sub_u32 s73, s73, 1\n"
      "s_cselect_b32 s73, 0, s73\n"
      "s_waitcnt lgkmcnt(0)\n"
      "v_cmp_gt_u32 vcc, 24, v4\n"
      "s_cselect_b64 vcc, 0, vcc\n"
      "s_cbranch_vccnz .Lp_119\n"
      "s_branch .Lp_83\n"
      ".Lp_84:\n"
      "s_cbranch_vccz .Lp_86\n"
      ".Lp_120:\n"
      "ds_read_b32 v4, v15\n"
      "ds_read_b128 v[24:27], v9 offset:4096\n"
      "s_sub_u32 s73, s73, 1\n"
      "s_cselect_b32 s73, 0, s73\n"
      "s_waitcnt lgkmcnt(0)\n"
      "v_cmp_gt_u32 vcc, 25, v4\n"
      "s_cselect_b64 vcc, 0, vcc\n"
      "s_cbranch_vccnz .Lp_120\n"
      "s_branch .Lp_85\n"
      ".Lp_86:\n"
      "s_cbranch_vccz .Lp_88\n"
      ".Lp_121:\n"
      "ds_read_b32 v4, v15\n"
      "ds_read_b128 v[28:31], v9 offset:0\n"
      "s_sub_u32 s73, s73, 1\n"
      "s_cselect_b32 s73, 0, s73\n"
      "s_waitcnt lgkmcnt(0)\n"
      "v_cmp_gt_u32 vcc, 26, v4\n"
      "s_cselect_b64 vcc, 0, vcc\n"
      "s_cbranch_vccnz .Lp_121\n"
      "s_branch .Lp_87\n"
      ".Lp_88:\n"
      "s_cbranch_vccz .Lp_90\n"
      ".Lp_122:\n"
      "ds_read_b32 v4, v15\n"
      "ds_read_b128 v[16:19], v9 offset:1024\n"
      "s_sub_u32 s73, s73, 1\n"
      "s_cselect_b32 s73, 0, s73\n"
      "s_waitcnt lgkmcnt(0)\n"
      "v_cmp_gt_u32 vcc, 27, v4\n"
      "s_cselect_b64 vcc, 0, vcc\n"
      "s_cbranch_vccnz .Lp_122\n"
      "s_branch .Lp_89\n"
      ".Lp_90:\n"
      "s_cbranch_vccz .Lp_92\n"
      ".Lp_123:\n"
      "ds_read_b32 v4, v15\n"
      "ds_read_b128 v[20:23], v9 offset:2048\n"
      "s_sub_u32 s73, s73, 1\n"
      "s_cselect_b32 s73, 0, s73\n"
      "s_waitcnt lgkmcnt(0)\n"
      "v_cmp_gt_u32 vcc, 28, v4\n"
      "s_cselect_b64 vcc, 0, vcc\n"
      "s_cbranch_vccnz .Lp_123\n"
      "s_branch .Lp_91\n"
      ".Lp_92:\n"
      "s_cbranch_vccz .Lp_94\n"
      ".Lp_124:\n"
      "ds_read_b32 v4, v15\n"
      "ds_read_b128 v[24:27], v9 offset:3072\n"
      "s_sub_u32 s73, s73, 1\n"
      "s_cselect_b32 s73, 0, s73\n"
      "s_waitcnt lgkmcnt(0)\n"
      "v_cmp_gt_u32 vcc, 29, v4\n"
      "s_cselect_b64 vcc, 0, vcc\n"
      "s_cbranch_vccnz .Lp_124\n"
      "s_branch .Lp_93\n"
      ".Lp_94:\n"
      "s_cbranch_vccz .LWARME\n"
      ".Lp_125:\n"
      "ds_read_b32 v4, v15\n"
      "ds_read_b128 v[28:31], v9 offset:4096\n"
      "s_sub_u32 s73, s73, 1\n"
      "s_cselect_b32 s73, 0, s73\n"
      "s_waitcnt lgkmcnt(0)\n"
      "v_cmp_gt_u32 vcc, 30, v4\n"
      "s_cselect_b64 vcc, 0, vcc\n"
      "s_cbranch_vccnz .Lp_125\n"
      "s_branch .Lp_95\n"
      ".LM:\n"
      "s_cmp_lt_u32 s61, 28\n"
      "s_cselect_b32 s62, s57, 14\n"
      "s_cselect_b32 s63, 0, s54\n"
      "s_add_u32 s62, s62, s63\n"
      "s_lshl4_add_u32 s62, s56, s62\n"
      "s_lshl2_add_u32 s62, s62, s53\n"
      "s_mul_i32 s63, s62, 2560\n"
      "s_lshl_b32 s69, s62, 4\n"
      "v_lshrrev_b32 v5, 5, v1\n"
      "v_and_b32 v6, 31, v1\n"
      "v_mul_u32_u24 v7, 1280, v5\n"
      "v_lshl_add_u32 v18, v6, 4, v7\n"
      "v_lshl_add_u32 v19, v6, 3, v7\n"
      "v_lshlrev_b32 v11, 2, v1\n"
      "s_mul_i32 s64, s55, 160\n"
      "s_lshl2_add_u32 s65, s57, s53\n"
      "s_lshl_b32 s65, s65, 6\n"
      "v_lshlrev_b32 v12, 1, v6\n"
      "s_and_b32 s66, s2, 7\n"
      "s_mul_i32 s66, s66, 28\n"
      "s_add_u32 s66, s66, s61\n"
      "s_mul_i32 s67, s66, 205\n"
      "s_lshr_b32 s67, s67, 11\n"
      "s_mul_i32 s68, s67, 10\n"
      "s_sub_u32 s68, s66, s68\n"
      "s_lshl2_add_u32 s66, s68, s53\n"
      "s_lshl_b32 s70, s66, 7\n"
      "v_lshrrev_b32 v13, 3, v1\n"
      "v_lshlrev_b32 v14, 4, v13\n"
      "s_mov_b32 m0, 162816\n"
      "s_waitcnt lgkmcnt(0)\n"
      "buffer_load_dwordx4 v[96:99], v18, s[4:7], s63 offen\n"
      "buffer_load_dwordx4 v[100:103], v18, s[4:7], s63 offen offset:512\n"
      "buffer_load_dwordx2 v[104:105], v19, s[4:7], s63 offen offset:1024\n"
      "buffer_load_dwordx2 v[42:43], off, s[20:23], 0 sc1\n"
      "buffer_load_dword v11, s[44:47], s64 offen lds\n"
      "buffer_load_dwordx4 v[44:47], v14, s[24:27], s70 offen\n"
      "buffer_load_ushort v9, v12, s[28:31], s65 offen\n"
      "s_barrier\n"
      "s_setprio 3\n"
      "s_lshl_b32 s62, s53, 5\n"
      "s_add_u32 s62, s62, 140160\n"
      "v_and_b32 v68, 7, v1\n"
      "v_lshrrev_b32 v69, 3, v1\n"
      "v_mul_u32_u24 v192, 1040, v68\n"
      "v_lshl_add_u32 v192, v69, 2, v192\n"
      "v_add_u32 v192, s62, v192\n"
      "v_add_u32 v193, 8720, v192\n"
      "v_mul_u32_u24 v8, 8448, v69\n"
      "v_lshl_add_u32 v8, v68, 4, v8\n"
      "s_mul_i32 s84, s66, 67584\n"
      "s_lshl_b32 s63, s67, 7\n"
      "s_add_u32 s84, s84, s63\n"
      "s_add_u32 s84, s84, 32768\n"
      "s_add_u32 s85, s84, 4224\n"
      "v_lshrrev_b32 v14, 5, v1\n"
      "v_bfe_u32 v15, v1, 2, 2\n"
      "v_lshl_add_u32 v16, v14, 3, v15\n"
      "v_lshlrev_b32 v16, 8, v16\n"
      "v_bfe_u32 v17, v1, 4, 1\n"
      "v_mul_u32_u24 v29, 57344, v17\n"
      "s_lshl_b32 s64, s53, 1\n"
      "v_or_b32 v17, s64, v17\n"
      "v_lshlrev_b32 v15, 1, v15\n"
      "v_xor_b32 v17, v17, v15\n"
      "v_lshl_add_u32 v16, v17, 5, v16\n"
      "v_and_b32 v17, 3, v1\n"
      "v_lshl_add_u32 v24, v17, 3, v16\n"
      "v_and_b32 v15, 0x2f, v1\n"
      "v_lshl_add_u32 v29, v15, 4, v29\n"
      "v_and_b32 v15, 31, v1\n"
      "v_bfe_u32 v16, v1, 1, 3\n"
      "v_xor_b32 v16, v16, v14\n"
      "v_lshlrev_b32 v16, 4, v16\n"
      "v_lshl_add_u32 v4, v15, 7, v16\n"
      "v_xor_b32 v5, 32, v4\n"
      "v_xor_b32 v6, 64, v4\n"
      "v_xor_b32 v7, 96, v4\n"
      "v_lshlrev_b32 v30, 5, v15\n"
      "v_add_u32 v30, 157696, v30\n"
      "v_lshrrev_b32 v16, 4, v1\n"
      "v_xor_b32 v16, v16, v1\n"
      "v_and_b32 v16, 1, v16\n"
      "v_cmp_eq_u32 vcc, 0, v16\n"
      "v_mov_b32 v17, 0x3f803f80\n"
      "v_mov_b64 v[10:11], 0\n"
      "v_cndmask_b32 v16, 0, v17, vcc\n"
      "v_mov_b32 v17, v16\n"
      "v_mov_b32 v18, v16\n"
      "v_mov_b32 v19, v16\n"
      "v_mov_b64 v[14:15], 0\n"
      "s_lshl2_add_u32 s64, s54, s53\n"
      "s_mul_i32 s64, s64, 7\n"
      "s_add_u32 s64, s64, s55\n"
      "s_lshl_b32 s65, s64, 10\n"
      "s_mul_i32 s78, s56, 458752\n"
      "s_add_u32 s78, s78, s65\n"
      "s_add_u32 s78, s78, 7897088\n"
      "s_lshl2_add_u32 s65, s57, s53\n"
      "s_cmp_ge_u32 s65, 54\n"
      "s_cselect_b32 s78, 1073741824, s78\n"
      "s_lshl_b32 s64, s56, 6\n"
      "s_add_u32 s64, s64, s65\n"
      "s_lshl_b32 s64, s64, 8\n"
      "s_add_u32 s79, s64, 18382848\n"
      "s_xor_b32 s74, s99, 0\n"
      "s_mul_i32 s74, s74, 114688\n"
      "s_add_u32 s74, s74, s78\n"
      "s_xor_b32 s75, s99, 1\n"
      "s_mul_i32 s75, s75, 114688\n"
      "s_add_u32 s75, s75, s78\n"
      "s_xor_b32 s76, s99, 2\n"
      "s_mul_i32 s76, s76, 114688\n"
      "s_add_u32 s76, s76, s78\n"
      "s_xor_b32 s77, s99, 3\n"
      "s_mul_i32 s77, s77, 114688\n"
      "s_add_u32 s77, s77, s78\n"
      "s_mul_i32 s63, s56, 3\n"
      "s_add_u32 s63, s63, s57\n"
      "s_mul_i32 s64, s63, 205\n"
      "s_lshr_b32 s64, s64, 11\n"
      "s_mul_i32 s64, s64, 10\n"
      "s_sub_u32 s63, s63, s64\n"
      "s_sub_u32 s64, 3, s53\n"
      "s_lshr_b32 s65, s64, 1\n"
      "s_xor_b32 s65, s65, s99\n"
      "s_and_b32 s65, s65, 1\n"
      "s_mul_i32 s65, s65, 49152\n"
      "s_and_b32 s64, s64, 1\n"
      "s_lshl_b32 s64, s64, 11\n"
      "s_add_u32 s65, s65, s64\n"
      "s_mul_i32 s64, s55, 5\n"
      "s_lshr_b32 s71, s64, 2\n"
      "s_sub_u32 s71, s71, s63\n"
      "s_cselect_b32 s72, 10, 0\n"
      "s_add_u32 s71, s71, s72\n"
      "s_lshl_b32 s71, s71, 12\n"
      "s_add_u32 s71, s71, s65\n"
      "s_and_b32 s72, s64, 3\n"
      "s_lshl_b32 s72, s72, 5\n"
      "s_or_b32 s86, s71, s72\n"
      "s_add_u32 s64, s64, 1\n"
      "s_lshr_b32 s71, s64, 2\n"
      "s_sub_u32 s71, s71, s63\n"
      "s_cselect_b32 s72, 10, 0\n"
      "s_add_u32 s71, s71, s72\n"
      "s_lshl_b32 s71, s71, 12\n"
      "s_add_u32 s71, s71, s65\n"
      "s_and_b32 s72, s64, 3\n"
      "s_lshl_b32 s72, s72, 5\n"
      "s_or_b32 s87, s71, s72\n"
      "s_add_u32 s64, s64, 1\n"
      "s_lshr_b32 s71, s64, 2\n"
      "s_sub_u32 s71, s71, s63\n"
      "s_cselect_b32 s72, 10, 0\n"
      "s_add_u32 s71, s71, s72\n"
      "s_lshl_b32 s71, s71, 12\n"
      "s_add_u32 s71, s71, s65\n"
      "s_and_b32 s72, s64, 3\n"
      "s_lshl_b32 s72, s72, 5\n"
      "s_or_b32 s88, s71, s72\n"
      "s_add_u32 s64, s64, 1\n"
      "s_lshr_b32 s71, s64, 2\n"
      "s_sub_u32 s71, s71, s63\n"
      "s_cselect_b32 s72, 10, 0\n"
      "s_add_u32 s71, s71, s72\n"
      "s_lshl_b32 s71, s71, 12\n"
      "s_add_u32 s71, s71, s65\n"
      "s_and_b32 s72, s64, 3\n"
      "s_lshl_b32 s72, s72, 5\n"
      "s_or_b32 s89, s71, s72\n"
      "s_add_u32 s64, s64, 1\n"
      "s_lshr_b32 s71, s64, 2\n"
      "s_sub_u32 s71, s71, s63\n"
      "s_cselect_b32 s72, 10, 0\n"
      "s_add_u32 s71, s71, s72\n"
      "s_lshl_b32 s71, s71, 12\n"
      "s_add_u32 s71, s71, s65\n"
      "s_and_b32 s72, s64, 3\n"
      "s_lshl_b32 s72, s72, 5\n"
      "s_or_b32 s90, s71, s72\n"
      "s_add_u32 s64, s64, 1\n"
      "v_and_b32 v50, 15, v1\n"
      "v_bfe_u32 v51, v1, 1, 3\n"
      "v_lshrrev_b32 v52, 5, v1\n"
      "v_xor_b32 v51, v51, v52\n"
      "v_lshlrev_b32 v51, 4, v51\n"
      "v_lshl_add_u32 v51, v50, 7, v51\n"
      "v_bfe_u32 v52, v1, 4, 1\n"
      "v_lshl_add_u32 v28, v52, 3, v51\n"
      "s_mov_b32 s80, s92\n"
      "s_mov_b32 s81, s92\n"
      "s_mov_b32 s82, -1\n"
      "s_mov_b32 s83, 0\n"
      "s_waitcnt vmcnt(3)\n"
      "ds_swizzle_b32 v106, v96 offset:0\n"
      "v_lshlrev_b32 v116, 16, v96\n"
      "v_and_b32 v117, s92, v96\n"
      "v_lshlrev_b32 v118, 16, v97\n"
      "v_and_b32 v119, s92, v97\n"
      "v_lshlrev_b32 v120, 16, v98\n"
      "v_and_b32 v121, s92, v98\n"
      "v_lshlrev_b32 v122, 16, v99\n"
      "v_and_b32 v123, s92, v99\n"
      "v_lshlrev_b32 v124, 16, v100\n"
      "v_and_b32 v125, s92, v100\n"
      "v_lshlrev_b32 v126, 16, v101\n"
      "v_and_b32 v127, s92, v101\n"
      "v_lshlrev_b32 v128, 16, v102\n"
      "v_and_b32 v129, s92, v102\n"
      "v_lshlrev_b32 v130, 16, v103\n"
      "v_and_b32 v131, s92, v103\n"
      "v_lshlrev_b32 v132, 16, v104\n"
      "v_and_b32 v133, s92, v104\n"
      "v_lshlrev_b32 v134, 16, v105\n"
      "v_and_b32 v135, s92, v105\n"
      "v_readfirstlane_b32 s58, v42\n"
      "v_readfirstlane_b32 s59, v43\n"
      "s_waitcnt lgkmcnt(0)\n"
      "v_lshlrev_b32 v106, 16, v106\n"
      "v_mov_b32 v107, v106\n"
      "s_add_u32 s58, s58, 1\n"
      "s_addc_u32 s59, s59, 0\n"
      "v_pk_add_f32 v[116:117], v[116:117], v[106:107] neg_lo:[0,1] neg_hi:[0,1]\n"
      "v_pk_add_f32 v[118:119], v[118:119], v[106:107] neg_lo:[0,1] neg_hi:[0,1]\n"
      "v_pk_add_f32 v[120:121], v[120:121], v[106:107] neg_lo:[0,1] neg_hi:[0,1]\n"
      "v_pk_add_f32 v[122:123], v[122:123], v[106:107] neg_lo:[0,1] neg_hi:[0,1]\n"
      "v_pk_add_f32 v[124:125], v[124:125], v[106:107] neg_lo:[0,1] neg_hi:[0,1]\n"
      "v_pk_add_f32 v[126:127], v[126:127], v[106:107] neg_lo:[0,1] neg_hi:[0,1]\n"
      "v_pk_add_f32 v[128:129], v[128:129], v[106:107] neg_lo:[0,1] neg_hi:[0,1]\n"
      "v_pk_add_f32 v[130:131], v[130:131], v[106:107] neg_lo:[0,1] neg_hi:[0,1]\n"
      "v_pk_add_f32 v[132:133], v[132:133], v[106:107] neg_lo:[0,1] neg_hi:[0,1]\n"
      "v_pk_add_f32 v[134:135], v[134:135], v[106:107] neg_lo:[0,1] neg_hi:[0,1]\n"
      "v_pk_mul_f32 v[108:109], v[116:117], v[116:117]\n"
      "v_pk_mul_f32 v[110:111], v[118:119], v[118:119]\n"
      "v_pk_fma_f32 v[108:109], v[120:121], v[120:121], v[108:109]\n"
      "v_pk_add_f32 v[116:117], v[116:117], v[120:121]\n"
      "v_pk_fma_f32 v[110:111], v[122:123], v[122:123], v[110:111]\n"
      "v_pk_add_f32 v[118:119], v[118:119], v[122:123]\n"
      "v_pk_fma_f32 v[108:109], v[124:125], v[124:125], v[108:109]\n"
      "v_pk_add_f32 v[116:117], v[116:117], v[124:125]\n"
      "v_pk_fma_f32 v[110:111], v[126:127], v[126:127], v[110:111]\n"
      "v_pk_add_f32 v[118:119], v[118:119], v[126:127]\n"
      "v_pk_fma_f32 v[108:109], v[128:129], v[128:129], v[108:109]\n"
      "v_pk_add_f32 v[116:117], v[116:117], v[128:129]\n"
      "v_pk_fma_f32 v[110:111], v[130:131], v[130:131], v[110:111]\n"
      "v_pk_add_f32 v[118:119], v[118:119], v[130:131]\n"
      "v_pk_fma_f32 v[108:109], v[132:133], v[132:133], v[108:109]\n"
      "v_pk_add_f32 v[116:117], v[116:117], v[132:133]\n"
      "v_pk_fma_f32 v[110:111], v[134:135], v[134:135], v[110:111]\n"
      "v_pk_add_f32 v[118:119], v[118:119], v[134:135]\n"
      "v_pk_add_f32 v[108:109], v[108:109], v[110:111]\n"
      "v_pk_add_f32 v[116:117], v[116:117], v[118:119]\n"
      "v_add_f32 v108, v108, v109\n"
      "v_add_f32 v109, v116, v117\n"
      "s_and_b32 s93, s58, 0x1fff\n"
      "v_add_f32_dpp v108, v108, v108 quad_perm:[1,0,3,2] row_mask:0xf bank_mask:0xf\n"
      "v_add_f32_dpp v109, v109, v109 quad_perm:[1,0,3,2] row_mask:0xf bank_mask:0xf\n"
      "s_or_b32 s93, s93, 0x4000\n"
      "v_add_f32_dpp v108, v108, v108 quad_perm:[2,3,0,1] row_mask:0xf bank_mask:0xf\n"
      "v_add_f32_dpp v109, v109, v109 quad_perm:[2,3,0,1] row_mask:0xf bank_mask:0xf\n"
      "v_mov_b32 v114, 1073741824\n"
      "v_add_f32_dpp v108, v108, v108 row_half_mirror row_mask:0xf bank_mask:0xf\n"
      "v_add_f32_dpp v109, v109, v109 row_half_mirror row_mask:0xf bank_mask:0xf\n"
      "v_writelane_b32 v114, 0, 16\n"
      "v_add_f32_dpp v108, v108, v108 row_mirror row_mask:0xf bank_mask:0xf\n"
      "v_add_f32_dpp v109, v109, v109 row_mirror row_mask:0xf bank_mask:0xf\n"
      "v_writelane_b32 v114, 8, 48\n"
      "v_add_f32_dpp v108, v108, v108 row_bcast:15 row_mask:0xa bank_mask:0xf\n"
      "v_add_f32_dpp v109, v109, v109 row_bcast:15 row_mask:0xa bank_mask:0xf\n"
      "v_mul_f32 v109, 0x3acccccd, v109\n"
      "v_mul_f32 v108, 0x3acccccd, v108\n"
      "v_fma_f32 v108, -v109, v109, v108\n"
      "v_max_f32 v108, 0, v108\n"
      "v_cvt_pk_bf16_f32 v108, 0, v108\n"
      "v_add_f32 v108, 0x3727c5ac, v108\n"
      "v_cvt_pk_bf16_f32 v108, 0, v108\n"
      "v_sqrt_f32 v108, v108\n"
      "v_add_f32 v109, v106, v109\n"
      "s_add_u32 s69, s69, 8192\n"
      "v_cvt_pk_bf16_f32 v112, v109, v108\n"
      "v_mov_b32 v113, s58\n"
      "buffer_store_dwordx2 v[112:113], v114, s[20:23], s69 offen sc1\n"
      "s_cmp_lt_u32 s61, 28\n"
      "s_cbranch_scc0 .LSPARE\n"
      "v_mov_b64 v[12:13], s[58:59]\n"
      "v_cmp_eq_u32 vcc, 0, v1\n"
      "v_lshlrev_b32 v2, 2, v1\n"
      "v_add_u32 v2, 157696, v2\n"
      "v_cndmask_b32 v3, 0, 1, vcc\n"
      "s_waitcnt vmcnt(1)\n"
      "v_lshlrev_b32 v184, 16, v44\n"
      "v_lshlrev_b32 v185, 16, v46\n"
      "v_and_b32 v186, s92, v44\n"
      "v_and_b32 v187, s92, v46\n"
      "v_lshlrev_b32 v188, 16, v45\n"
      "v_lshlrev_b32 v189, 16, v47\n"
      "v_and_b32 v190, s92, v45\n"
      "v_and_b32 v191, s92, v47\n"
      "v_lshlrev_b32 v9, 16, v9\n"
      "s_barrier\n"
      "v_mov_b32 v31, 161920\n"
      "ds_write_b64 v31, v[12:13]\n"
      "ds_read_b32 v26, v2 offset:5120\n"
      "v_lshlrev_b32 v31, 3, v1\n"
      "v_add_u32 v31, 163072, v31\n"
      "s_waitcnt lgkmcnt(0)\n"
      "v_and_b32 v27, s92, v26\n"
      "v_lshlrev_b32 v26, 16, v26\n"
      "ds_write_b64 v31, v[26:27]\n"
      "ds_read_b64_tr_b16 v[96:97], v24 offset:0\n"
      "ds_read_b64_tr_b16 v[98:99], v24 offset:1024\n"
      "ds_read_b128 v[32:35], v4 offset:16384\n"
      "ds_read_b128 v[36:39], v4 offset:20480\n"
      "ds_read_b64_tr_b16 v[100:101], v24 offset:4096\n"
      "ds_read_b64_tr_b16 v[102:103], v24 offset:5120\n"
      "ds_read_b128 v[40:43], v5 offset:16384\n"
      "ds_read_b128 v[44:47], v5 offset:20480\n"
      "ds_read_b64_tr_b16 v[104:105], v24 offset:8192\n"
      "ds_read_b64_tr_b16 v[106:107], v24 offset:9216\n"
      "ds_read_b128 v[48:51], v6 offset:16384\n"
      "ds_read_b128 v[52:55], v6 offset:20480\n"
      "ds_read_b64_tr_b16 v[108:109], v24 offset:12288\n"
      "ds_read_b64_tr_b16 v[110:111], v24 offset:13312\n"
      "ds_read_b128 v[56:59], v7 offset:16384\n"
      "ds_read_b128 v[60:63], v7 offset:20480\n"
      "s_waitcnt lgkmcnt(13)\n"
      "v_mfma_f32_32x32x16_bf16 v[80:95], v[96:99], v[32:35], 0\n"
      "s_waitcnt lgkmcnt(12)\n"
      "v_mfma_f32_32x32x16_bf16 v[64:79], v[96:99], v[36:39], 0\n"
      "v_mfma_f32_16x16x32_bf16 v[20:23], v[16:19], v[96:99], 0\n"
      "s_waitcnt lgkmcnt(10)\n"
      "v_mfma_f32_16x16x32_bf16 v[20:23], v[16:19], v[100:103], v[20:23]\n"
      "s_waitcnt lgkmcnt(9)\n"
      "v_mfma_f32_32x32x16_bf16 v[80:95], v[100:103], v[40:43], v[80:95]\n"
      "s_waitcnt lgkmcnt(0)\n"
      "v_mfma_f32_32x32x16_bf16 v[64:79], v[100:103], v[44:47], v[64:79]\n"
      "s_barrier\n"
      "ds_read2_b32 v[196:197], v192 offset0:0 offset1:32\n"
      "ds_read2_b32 v[198:199], v192 offset0:64 offset1:100\n"
      "ds_read2_b32 v[200:201], v192 offset0:132 offset1:164\n"
      "ds_read2_b32 v[202:203], v192 offset0:196 offset1:228\n"
      "ds_read_b32 v204, v192 offset:1040\n"
      "ds_read_b32 v205, v192 offset:1168\n"
      "ds_read_b32 v206, v192 offset:1296\n"
      "ds_read2_b32 v[208:209], v193 offset0:0 offset1:32\n"
      "ds_read2_b32 v[210:211], v193 offset0:64 offset1:96\n"
      "ds_read2_b32 v[212:213], v193 offset0:128 offset1:160\n"
      "ds_read2_b32 v[214:215], v193 offset0:192 offset1:224\n"
      "s_waitcnt lgkmcnt(11)\n"
      "v_mfma_f32_32x32x16_bf16 v[80:95], v[104:107], v[48:51], v[80:95]\n"
      "s_waitcnt lgkmcnt(9)\n"
      "v_lshlrev_b32 v216, 16, v196\n"
      "v_and_b32 v217, s92, v196\n"
      "v_lshlrev_b32 v218, 16, v197\n"
      "v_and_b32 v219, s92, v197\n"
      "v_mfma_f32_32x32x16_bf16 v[64:79], v[104:107], v[52:55], v[64:79]\n"
      "v_lshlrev_b32 v220, 16, v198\n"
      "v_and_b32 v221, s92, v198\n"
      "v_lshlrev_b32 v222, 16, v199\n"
      "v_and_b32 v223, s92, v199\n"
      "v_mfma_f32_32x32x16_bf16 v[80:95], v[108:111], v[56:59], v[80:95]\n"
      "s_waitcnt lgkmcnt(7)\n"
      "v_lshlrev_b32 v224, 16, v200\n"
      "v_and_b32 v225, s92, v200\n"
      "v_lshlrev_b32 v226, 16, v201\n"
      "v_and_b32 v227, s92, v201\n"
      "v_mfma_f32_32x32x16_bf16 v[64:79], v[108:111], v[60:63], v[64:79]\n"
      "v_lshlrev_b32 v228, 16, v202\n"
      "v_and_b32 v229, s92, v202\n"
      "v_lshlrev_b32 v230, 16, v203\n"
      "v_and_b32 v231, s92, v203\n"
      "v_mfma_f32_16x16x32_bf16 v[20:23], v[16:19], v[104:107], v[20:23]\n"
      "s_waitcnt lgkmcnt(4)\n"
      "v_lshlrev_b32 v232, 16, v204\n"
      "v_and_b32 v233, s92, v204\n"
      "v_lshlrev_b32 v234, 16, v205\n"
      "v_and_b32 v235, s92, v205\n"
      "v_mfma_f32_16x16x32_bf16 v[20:23], v[16:19], v[108:111], v[20:23]\n"
      "v_lshlrev_b32 v236, 16, v206\n"
      "v_and_b32 v237, s92, v206\n"
      "v_pk_mul_f32 v[240:241], v[216:217], v[190:191]\n"
      "v_pk_mul_f32 v[242:243], v[218:219], v[190:191]\n"
      "v_pk_mul_f32 v[244:245], v[220:221], v[190:191]\n"
      "v_pk_mul_f32 v[246:247], v[222:223], v[190:191]\n"
      "v_pk_mul_f32 v[248:249], v[224:225], v[190:191]\n"
      "v_pk_mul_f32 v[250:251], v[226:227], v[190:191]\n"
      "v_pk_mul_f32 v[252:253], v[228:229], v[190:191]\n"
      "v_pk_mul_f32 v[254:255], v[230:231], v[190:191]\n"
      "v_pk_fma_f32 v[240:241], v[218:219], v[188:189], v[240:241]\n"
      "v_pk_fma_f32 v[242:243], v[220:221], v[188:189], v[242:243]\n"
      "v_pk_fma_f32 v[244:245], v[222:223], v[188:189], v[244:245]\n"
      "v_pk_fma_f32 v[246:247], v[224:225], v[188:189], v[246:247]\n"
      "v_pk_fma_f32 v[248:249], v[226:227], v[188:189], v[248:249]\n"
      "v_pk_fma_f32 v[250:251], v[228:229], v[188:189], v[250:251]\n"
      "v_pk_fma_f32 v[252:253], v[230:231], v[188:189], v[252:253]\n"
      "v_pk_fma_f32 v[254:255], v[232:233], v[188:189], v[254:255]\n"
      "v_pk_fma_f32 v[240:241], v[220:221], v[186:187], v[240:241]\n"
      "v_pk_fma_f32 v[242:243], v[222:223], v[186:187], v[242:243]\n"
      "v_pk_fma_f32 v[244:245], v[224:225], v[186:187], v[244:245]\n"
      "v_pk_fma_f32 v[246:247], v[226:227], v[186:187], v[246:247]\n"
      "v_pk_fma_f32 v[248:249], v[228:229], v[186:187], v[248:249]\n"
      "v_pk_fma_f32 v[250:251], v[230:231], v[186:187], v[250:251]\n"
      "v_pk_fma_f32 v[252:253], v[232:233], v[186:187], v[252:253]\n"
      "v_pk_fma_f32 v[254:255], v[234:235], v[186:187], v[254:255]\n"
      "v_pk_fma_f32 v[240:241], v[222:223], v[184:185], v[240:241]\n"
      "v_pk_fma_f32 v[242:243], v[224:225], v[184:185], v[242:243]\n"
      "v_pk_fma_f32 v[244:245], v[226:227], v[184:185], v[244:245]\n"
      "v_pk_fma_f32 v[246:247], v[228:229], v[184:185], v[246:247]\n"
      "v_pk_fma_f32 v[248:249], v[230:231], v[184:185], v[248:249]\n"
      "v_pk_fma_f32 v[250:251], v[232:233], v[184:185], v[250:251]\n"
      "v_pk_fma_f32 v[252:253], v[234:235], v[184:185], v[252:253]\n"
      "v_pk_fma_f32 v[254:255], v[236:237], v[184:185], v[254:255]\n"
      "s_waitcnt lgkmcnt(0)\n"
      "v_lshlrev_b32 v216, 16, v208\n"
      "v_and_b32 v217, s92, v208\n"
      "v_lshlrev_b32 v218, 16, v209\n"
      "v_and_b32 v219, s92, v209\n"
      "v_lshlrev_b32 v220, 16, v210\n"
      "v_and_b32 v221, s92, v210\n"
      "v_lshlrev_b32 v222, 16, v211\n"
      "v_and_b32 v223, s92, v211\n"
      "v_lshlrev_b32 v224, 16, v212\n"
      "v_and_b32 v225, s92, v212\n"
      "v_lshlrev_b32 v226, 16, v213\n"
      "v_and_b32 v227, s92, v213\n"
      "v_lshlrev_b32 v228, 16, v214\n"
      "v_and_b32 v229, s92, v214\n"
      "v_lshlrev_b32 v230, 16, v215\n"
      "v_and_b32 v231, s92, v215\n"
      "v_pk_mul_f32 v[240:241], v[240:241], v[216:217]\n"
      "v_pk_mul_f32 v[242:243], v[242:243], v[218:219]\n"
      "v_pk_mul_f32 v[244:245], v[244:245], v[220:221]\n"
      "v_pk_mul_f32 v[246:247], v[246:247], v[222:223]\n"
      "v_pk_mul_f32 v[248:249], v[248:249], v[224:225]\n"
      "v_pk_mul_f32 v[250:251], v[250:251], v[226:227]\n"
      "v_pk_mul_f32 v[252:253], v[252:253], v[228:229]\n"
      "v_pk_mul_f32 v[254:255], v[254:255], v[230:231]\n"
      "v_cvt_pk_bf16_f32 v196, v242, v240\n"
      "v_cvt_pk_bf16_f32 v197, v246, v244\n"
      "v_cvt_pk_bf16_f32 v198, v250, v248\n"
      "v_cvt_pk_bf16_f32 v199, v254, v252\n"
      "buffer_store_dwordx4 v[196:199], v8, s[20:23], s84 offen sc1\n"
      "v_cvt_pk_bf16_f32 v200, v243, v241\n"
      "v_cvt_pk_bf16_f32 v201, v247, v245\n"
      "v_cvt_pk_bf16_f32 v202, v251, v249\n"
      "v_cvt_pk_bf16_f32 v203, v255, v253\n"
      "buffer_store_dwordx4 v[200:203], v8, s[20:23], s85 offen sc1\n"
      "s_barrier\n"
      "ds_read_b64_tr_b16 v[112:113], v24 offset:24576\n"
      "ds_read_b64_tr_b16 v[114:115], v24 offset:25600\n"
      "ds_read_b128 v[32:35], v4 offset:40960\n"
      "ds_read_b128 v[36:39], v4 offset:45056\n"
      "ds_read_b64_tr_b16 v[116:117], v24 offset:28672\n"
      "ds_read_b64_tr_b16 v[118:119], v24 offset:29696\n"
      "ds_read_b128 v[40:43], v5 offset:40960\n"
      "ds_read_b128 v[44:47], v5 offset:45056\n"
      "ds_read_b64_tr_b16 v[120:121], v24 offset:32768\n"
      "ds_read_b64_tr_b16 v[122:123], v24 offset:33792\n"
      "ds_read_b128 v[48:51], v6 offset:40960\n"
      "ds_read_b128 v[52:55], v6 offset:45056\n"
      "ds_read_b64_tr_b16 v[124:125], v24 offset:36864\n"
      "ds_read_b64_tr_b16 v[126:127], v24 offset:37888\n"
      "ds_read_b128 v[56:59], v7 offset:40960\n"
      "ds_read_b128 v[60:63], v7 offset:45056\n"
      "s_waitcnt lgkmcnt(13)\n"
      "v_mfma_f32_32x32x16_bf16 v[80:95], v[112:115], v[32:35], v[80:95]\n"
      "s_waitcnt lgkmcnt(12)\n"
      "v_mfma_f32_32x32x16_bf16 v[64:79], v[112:115], v[36:39], v[64:79]\n"
      "v_mfma_f32_16x16x32_bf16 v[20:23], v[16:19], v[112:115], v[20:23]\n"
      "s_waitcnt lgkmcnt(10)\n"
      "v_mfma_f32_16x16x32_bf16 v[20:23], v[16:19], v[116:119], v[20:23]\n"
      "s_waitcnt lgkmcnt(9)\n"
      "v_mfma_f32_32x32x16_bf16 v[80:95], v[116:119], v[40:43], v[80:95]\n"
      "s_waitcnt lgkmcnt(0)\n"
      "v_add_u32 v24, 49152, v24\n"
      "v_add_u32 v4, 49152, v4\n"
      "v_add_u32 v5, 49152, v5\n"
      "v_add_u32 v6, 49152, v6\n"
      "v_add_u32 v7, 49152, v7\n"
      "v_mfma_f32_32x32x16_bf16 v[64:79], v[116:119], v[44:47], v[64:79]\n"
      "s_barrier\n"
      "v_mfma_f32_32x32x16_bf16 v[80:95], v[120:123], v[48:51], v[80:95]\n"
      "ds_read_b64_tr_b16 v[128:129], v24 offset:0\n"
      "ds_read_b64_tr_b16 v[130:131], v24 offset:1024\n"
      "ds_read_b128 v[32:35], v4 offset:16384\n"
      "ds_read_b128 v[36:39], v4 offset:20480\n"
      "v_mfma_f32_32x32x16_bf16 v[64:79], v[120:123], v[52:55], v[64:79]\n"
      "ds_read_b64_tr_b16 v[132:133], v24 offset:4096\n"
      "ds_read_b64_tr_b16 v[134:135], v24 offset:5120\n"
      "ds_read_b128 v[40:43], v5 offset:16384\n"
      "ds_read_b128 v[44:47], v5 offset:20480\n"
      "v_mfma_f32_32x32x16_bf16 v[80:95], v[124:127], v[56:59], v[80:95]\n"
      "ds_read_b64_tr_b16 v[136:137], v24 offset:8192\n"
      "ds_read_b64_tr_b16 v[138:139], v24 offset:9216\n"
      "ds_read_b128 v[48:51], v6 offset:16384\n"
      "ds_read_b128 v[52:55], v6 offset:20480\n"
      "v_mfma_f32_32x32x16_bf16 v[64:79], v[124:127], v[60:63], v[64:79]\n"
      "ds_read_b64_tr_b16 v[140:141], v24 offset:12288\n"
      "ds_read_b64_tr_b16 v[142:143], v24 offset:13312\n"
      "ds_read_b128 v[56:59], v7 offset:16384\n"
      "ds_read_b128 v[60:63], v7 offset:20480\n"
      "v_mfma_f32_16x16x32_bf16 v[20:23], v[16:19], v[120:123], v[20:23]\n"
      "v_mfma_f32_16x16x32_bf16 v[20:23], v[16:19], v[124:127], v[20:23]\n"
      "s_waitcnt lgkmcnt(13)\n"
      "v_mfma_f32_32x32x16_bf16 v[80:95], v[128:131], v[32:35], v[80:95]\n"
      "s_waitcnt lgkmcnt(12)\n"
      "v_mfma_f32_32x32x16_bf16 v[64:79], v[128:131], v[36:39], v[64:79]\n"
      "v_mfma_f32_16x16x32_bf16 v[20:23], v[16:19], v[128:131], v[20:23]\n"
      "s_waitcnt lgkmcnt(8)\n"
      "v_mfma_f32_16x16x32_bf16 v[20:23], v[16:19], v[132:135], v[20:23]\n"
      "v_mfma_f32_32x32x16_bf16 v[80:95], v[132:135], v[40:43], v[80:95]\n"
      "s_waitcnt lgkmcnt(0)\n"
      "v_mfma_f32_32x32x16_bf16 v[64:79], v[132:135], v[44:47], v[64:79]\n"
      "s_barrier\n"
      "v_mfma_f32_32x32x16_bf16 v[80:95], v[136:139], v[48:51], v[80:95]\n"
      "ds_read_b64_tr_b16 v[144:145], v24 offset:24576\n"
      "ds_read_b64_tr_b16 v[146:147], v24 offset:25600\n"
      "ds_read_b128 v[32:35], v4 offset:40960\n"
      "ds_read_b128 v[36:39], v4 offset:45056\n"
      "v_mfma_f32_32x32x16_bf16 v[64:79], v[136:139], v[52:55], v[64:79]\n"
      "ds_read_b64_tr_b16 v[148:149], v24 offset:28672\n"
      "ds_read_b64_tr_b16 v[150:151], v24 offset:29696\n"
      "ds_read_b128 v[40:43], v5 offset:40960\n"
      "ds_read_b128 v[44:47], v5 offset:45056\n"
      "v_mfma_f32_32x32x16_bf16 v[80:95], v[140:143], v[56:59], v[80:95]\n"
      "ds_read_b64_tr_b16 v[152:153], v24 offset:32768\n"
      "ds_read_b64_tr_b16 v[154:155], v24 offset:33792\n"
      "ds_read_b128 v[48:51], v6 offset:40960\n"
      "ds_read_b128 v[52:55], v6 offset:45056\n"
      "v_mfma_f32_32x32x16_bf16 v[64:79], v[140:143], v[60:63], v[64:79]\n"
      "ds_read_b64_tr_b16 v[156:157], v24 offset:36864\n"
      "ds_read_b64_tr_b16 v[158:159], v24 offset:37888\n"
      "ds_read_b128 v[56:59], v7 offset:40960\n"
      "ds_read_b128 v[60:63], v7 offset:45056\n"
      "v_mfma_f32_16x16x32_bf16 v[20:23], v[16:19], v[136:139], v[20:23]\n"
      "v_mfma_f32_16x16x32_bf16 v[20:23], v[16:19], v[140:143], v[20:23]\n"
      "s_waitcnt lgkmcnt(13)\n"
      "v_mfma_f32_32x32x16_bf16 v[80:95], v[144:147], v[32:35], v[80:95]\n"
      "s_waitcnt lgkmcnt(12)\n"
      "v_mfma_f32_32x32x16_bf16 v[64:79], v[144:147], v[36:39], v[64:79]\n"
      "v_mfma_f32_16x16x32_bf16 v[20:23], v[16:19], v[144:147], v[20:23]\n"
      "s_waitcnt lgkmcnt(8)\n"
      "v_mfma_f32_16x16x32_bf16 v[20:23], v[16:19], v[148:151], v[20:23]\n"
      "v_mfma_f32_32x32x16_bf16 v[80:95], v[148:151], v[40:43], v[80:95]\n"
      "s_waitcnt lgkmcnt(0)\n"
      "v_add_u32 v24, 49152, v24\n"
      "v_add_u32 v4, 49152, v4\n"
      "v_add_u32 v5, 49152, v5\n"
      "v_add_u32 v6, 49152, v6\n"
      "v_add_u32 v7, 49152, v7\n"
      "v_mfma_f32_32x32x16_bf16 v[64:79], v[148:151], v[44:47], v[64:79]\n"
      "s_barrier\n"
      "v_mfma_f32_32x32x16_bf16 v[80:95], v[152:155], v[48:51], v[80:95]\n"
      "ds_read_b64_tr_b16 v[160:161], v24 offset:0\n"
      "ds_read_b64_tr_b16 v[162:163], v24 offset:1024\n"
      "ds_read_b128 v[32:35], v4 offset:16384\n"
      "ds_read_b128 v[36:39], v4 offset:20480\n"
      "v_mfma_f32_32x32x16_bf16 v[64:79], v[152:155], v[52:55], v[64:79]\n"
      "ds_read_b64_tr_b16 v[164:165], v24 offset:4096\n"
      "ds_read_b64_tr_b16 v[166:167], v24 offset:5120\n"
      "ds_read_b128 v[40:43], v5 offset:16384\n"
      "ds_read_b128 v[44:47], v5 offset:20480\n"
      "v_mfma_f32_32x32x16_bf16 v[80:95], v[156:159], v[56:59], v[80:95]\n"
      "ds_read_b64_tr_b16 v[168:169], v24 offset:8192\n"
      "ds_read_b64_tr_b16 v[170:171], v24 offset:9216\n"
      "ds_read_b128 v[48:51], v6 offset:16384\n"
      "ds_read_b128 v[52:55], v6 offset:20480\n"
      "v_mfma_f32_32x32x16_bf16 v[64:79], v[156:159], v[60:63], v[64:79]\n"
      "ds_read_b64_tr_b16 v[172:173], v24 offset:12288\n"
      "ds_read_b64_tr_b16 v[174:175], v24 offset:13312\n"
      "ds_read_b128 v[56:59], v7 offset:16384\n"
      "ds_read_b128 v[60:63], v7 offset:20480\n"
      "v_mfma_f32_16x16x32_bf16 v[20:23], v[16:19], v[152:155], v[20:23]\n"
      "v_mfma_f32_16x16x32_bf16 v[20:23], v[16:19], v[156:159], v[20:23]\n"
      "s_waitcnt lgkmcnt(13)\n"
      "v_mfma_f32_32x32x16_bf16 v[80:95], v[160:163], v[32:35], v[80:95]\n"
      "s_waitcnt lgkmcnt(12)\n"
      "v_mfma_f32_32x32x16_bf16 v[64:79], v[160:163], v[36:39], v[64:79]\n"
      "v_mfma_f32_16x16x32_bf16 v[20:23], v[16:19], v[160:163], v[20:23]\n"
      "s_waitcnt lgkmcnt(8)\n"
      "v_mfma_f32_16x16x32_bf16 v[20:23], v[16:19], v[164:167], v[20:23]\n"
      "v_mfma_f32_32x32x16_bf16 v[80:95], v[164:167], v[40:43], v[80:95]\n"
      "s_waitcnt lgkmcnt(0)\n"
      "v_add_u32 v24, 4294868992, v24\n"
      "v_add_u32 v4, 4294868992, v4\n"
      "v_add_u32 v5, 4294868992, v5\n"
      "v_add_u32 v6, 4294868992, v6\n"
      "v_add_u32 v7, 4294868992, v7\n"
      "s_waitcnt vmcnt(0)\n"
      "v_mfma_f32_32x32x16_bf16 v[64:79], v[164:167], v[44:47], v[64:79]\n"
      "s_barrier\n"
      "v_mfma_f32_32x32x16_bf16 v[80:95], v[168:171], v[48:51], v[80:95]\n"
      "ds_read_b64_tr_b16 v[176:177], v24 offset:0\n"
      "ds_read_b64_tr_b16 v[178:179], v24 offset:1024\n"
      "ds_read_b128 v[32:35], v4 offset:16384\n"
      "ds_read_b128 v[36:39], v4 offset:20480\n"
      "v_mfma_f32_32x32x16_bf16 v[64:79], v[168:171], v[52:55], v[64:79]\n"
      "ds_read_b64_tr_b16 v[180:181], v24 offset:4096\n"
      "ds_read_b64_tr_b16 v[182:183], v24 offset:5120\n"
      "ds_read_b128 v[40:43], v5 offset:16384\n"
      "ds_read_b128 v[44:47], v5 offset:20480\n"
      "v_mfma_f32_32x32x16_bf16 v[80:95], v[172:175], v[56:59], v[80:95]\n"
      "ds_read_b64_tr_b16 v[184:185], v24 offset:8192\n"
      "ds_read_b64_tr_b16 v[186:187], v24 offset:9216\n"
      "ds_read_b128 v[48:51], v6 offset:16384\n"
      "ds_read_b128 v[52:55], v6 offset:20480\n"
      "v_mfma_f32_32x32x16_bf16 v[64:79], v[172:175], v[60:63], v[64:79]\n"
      "ds_read_b64_tr_b16 v[188:189], v24 offset:12288\n"
      "ds_read_b64_tr_b16 v[190:191], v24 offset:13312\n"
      "ds_read_b128 v[56:59], v7 offset:16384\n"
      "ds_read_b128 v[60:63], v7 offset:20480\n"
      "v_mfma_f32_16x16x32_bf16 v[20:23], v[16:19], v[168:171], v[20:23]\n"
      "v_mfma_f32_16x16x32_bf16 v[20:23], v[16:19], v[172:175], v[20:23]\n"
      "s_waitcnt lgkmcnt(13)\n"
      "v_mfma_f32_32x32x16_bf16 v[80:95], v[176:179], v[32:35], v[80:95]\n"
      "s_waitcnt lgkmcnt(12)\n"
      "v_mfma_f32_32x32x16_bf16 v[64:79], v[176:179], v[36:39], v[64:79]\n"
      "v_mfma_f32_16x16x32_bf16 v[20:23], v[16:19], v[176:179], v[20:23]\n"
      "s_waitcnt lgkmcnt(8)\n"
      "v_mfma_f32_16x16x32_bf16 v[20:23], v[16:19], v[180:183], v[20:23]\n"
      "v_mfma_f32_32x32x16_bf16 v[80:95], v[180:183], v[40:43], v[80:95]\n"
      "s_waitcnt lgkmcnt(0)\n"
      "v_mfma_f32_32x32x16_bf16 v[64:79], v[180:183], v[44:47], v[64:79]\n"
      "s_barrier\n"
      "v_mfma_f32_32x32x16_bf16 v[80:95], v[184:187], v[48:51], v[80:95]\n"
      "ds_read_b64_tr_b16 v[192:193], v24 offset:24576\n"
      "ds_read_b64_tr_b16 v[194:195], v24 offset:25600\n"
      "ds_read_b128 v[32:35], v4 offset:40960\n"
      "ds_read_b128 v[36:39], v4 offset:45056\n"
      "v_mfma_f32_32x32x16_bf16 v[64:79], v[184:187], v[52:55], v[64:79]\n"
      "ds_read_b64_tr_b16 v[196:197], v24 offset:28672\n"
      "ds_read_b64_tr_b16 v[198:199], v24 offset:29696\n"
      "ds_read_b128 v[40:43], v5 offset:40960\n"
      "ds_read_b128 v[44:47], v5 offset:45056\n"
      "v_mfma_f32_32x32x16_bf16 v[80:95], v[188:191], v[56:59], v[80:95]\n"
      "ds_read_b64_tr_b16 v[200:201], v24 offset:32768\n"
      "ds_read_b64_tr_b16 v[202:203], v24 offset:33792\n"
      "ds_read_b128 v[48:51], v6 offset:40960\n"
      "ds_read_b128 v[52:55], v6 offset:45056\n"
      "v_mfma_f32_32x32x16_bf16 v[64:79], v[188:191], v[60:63], v[64:79]\n"
      "ds_read_b64_tr_b16 v[204:205], v24 offset:36864\n"
      "ds_read_b64_tr_b16 v[206:207], v24 offset:37888\n"
      "ds_read_b128 v[56:59], v7 offset:40960\n"
      "ds_read_b128 v[60:63], v7 offset:45056\n"
      "v_mfma_f32_16x16x32_bf16 v[20:23], v[16:19], v[184:187], v[20:23]\n"
      "v_mfma_f32_16x16x32_bf16 v[20:23], v[16:19], v[188:191], v[20:23]\n"
      "s_waitcnt lgkmcnt(13)\n"
      "v_mfma_f32_32x32x16_bf16 v[80:95], v[192:195], v[32:35], v[80:95]\n"
      "s_waitcnt lgkmcnt(12)\n"
      "v_mfma_f32_32x32x16_bf16 v[64:79], v[192:195], v[36:39], v[64:79]\n"
      "v_mfma_f32_16x16x32_bf16 v[20:23], v[16:19], v[192:195], v[20:23]\n"
      "s_waitcnt lgkmcnt(8)\n"
      "v_mfma_f32_16x16x32_bf16 v[20:23], v[16:19], v[196:199], v[20:23]\n"
      "v_mfma_f32_32x32x16_bf16 v[80:95], v[196:199], v[40:43], v[80:95]\n"
      "s_waitcnt lgkmcnt(0)\n"
      "v_add_u32 v24, 49152, v24\n"
      "v_add_u32 v4, 49152, v4\n"
      "v_add_u32 v5, 49152, v5\n"
      "v_add_u32 v6, 49152, v6\n"
      "v_add_u32 v7, 49152, v7\n"
      "v_mfma_f32_32x32x16_bf16 v[64:79], v[196:199], v[44:47], v[64:79]\n"
      "s_barrier\n"
      "v_mfma_f32_32x32x16_bf16 v[80:95], v[200:203], v[48:51], v[80:95]\n"
      "ds_read_b64_tr_b16 v[208:209], v24 offset:0\n"
      "ds_read_b64_tr_b16 v[210:211], v24 offset:1024\n"
      "ds_read_b128 v[32:35], v4 offset:16384\n"
      "ds_read_b128 v[36:39], v4 offset:20480\n"
      "v_mfma_f32_32x32x16_bf16 v[64:79], v[200:203], v[52:55], v[64:79]\n"
      "ds_read_b64_tr_b16 v[212:213], v24 offset:4096\n"
      "ds_read_b64_tr_b16 v[214:215], v24 offset:5120\n"
      "ds_read_b128 v[40:43], v5 offset:16384\n"
      "ds_read_b128 v[44:47], v5 offset:20480\n"
      "v_mfma_f32_32x32x16_bf16 v[80:95], v[204:207], v[56:59], v[80:95]\n"
      "ds_read_b64_tr_b16 v[216:217], v24 offset:8192\n"
      "ds_read_b64_tr_b16 v[218:219], v24 offset:9216\n"
      "ds_read_b128 v[48:51], v6 offset:16384\n"
      "ds_read_b128 v[52:55], v6 offset:20480\n"
      "v_mfma_f32_32x32x16_bf16 v[64:79], v[204:207], v[60:63], v[64:79]\n"
      "ds_read_b64_tr_b16 v[220:221], v24 offset:12288\n"
      "ds_read_b64_tr_b16 v[222:223], v24 offset:13312\n"
      "ds_read_b128 v[56:59], v7 offset:16384\n"
      "ds_read_b128 v[60:63], v7 offset:20480\n"
      "v_mfma_f32_16x16x32_bf16 v[20:23], v[16:19], v[200:203], v[20:23]\n"
      "v_mfma_f32_16x16x32_bf16 v[20:23], v[16:19], v[204:207], v[20:23]\n"
      "s_waitcnt lgkmcnt(13)\n"
      "v_mfma_f32_32x32x16_bf16 v[80:95], v[208:211], v[32:35], v[80:95]\n"
      "s_waitcnt lgkmcnt(12)\n"
      "v_mfma_f32_32x32x16_bf16 v[64:79], v[208:211], v[36:39], v[64:79]\n"
      "v_mfma_f32_16x16x32_bf16 v[20:23], v[16:19], v[208:211], v[20:23]\n"
      "s_waitcnt lgkmcnt(8)\n"
      "v_mfma_f32_16x16x32_bf16 v[20:23], v[16:19], v[212:215], v[20:23]\n"
      "v_mfma_f32_32x32x16_bf16 v[80:95], v[212:215], v[40:43], v[80:95]\n"
      "s_waitcnt lgkmcnt(0)\n"
      "v_mfma_f32_32x32x16_bf16 v[64:79], v[212:215], v[44:47], v[64:79]\n"
      "s_barrier\n"
      "v_mfma_f32_32x32x16_bf16 v[80:95], v[216:219], v[48:51], v[80:95]\n"
      "ds_read_b64_tr_b16 v[224:225], v24 offset:24576\n"
      "ds_read_b64_tr_b16 v[226:227], v24 offset:25600\n"
      "ds_read_b128 v[32:35], v4 offset:40960\n"
      "ds_read_b128 v[36:39], v4 offset:45056\n"
      "v_mfma_f32_32x32x16_bf16 v[64:79], v[216:219], v[52:55], v[64:79]\n"
      "ds_read_b64_tr_b16 v[228:229], v24 offset:28672\n"
      "ds_read_b64_tr_b16 v[230:231], v24 offset:29696\n"
      "ds_read_b128 v[40:43], v5 offset:40960\n"
      "ds_read_b128 v[44:47], v5 offset:45056\n"
      "v_mfma_f32_32x32x16_bf16 v[80:95], v[220:223], v[56:59], v[80:95]\n"
      "ds_read_b64_tr_b16 v[232:233], v24 offset:32768\n"
      "ds_read_b64_tr_b16 v[234:235], v24 offset:33792\n"
      "ds_read_b128 v[48:51], v6 offset:40960\n"
      "ds_read_b128 v[52:55], v6 offset:45056\n"
      "v_mfma_f32_32x32x16_bf16 v[64:79], v[220:223], v[60:63], v[64:79]\n"
      "ds_read_b64_tr_b16 v[236:237], v24 offset:36864\n"
      "ds_read_b64_tr_b16 v[238:239], v24 offset:37888\n"
      "ds_read_b128 v[56:59], v7 offset:40960\n"
      "ds_read_b128 v[60:63], v7 offset:45056\n"
      "v_mfma_f32_16x16x32_bf16 v[20:23], v[16:19], v[216:219], v[20:23]\n"
      "v_mfma_f32_16x16x32_bf16 v[20:23], v[16:19], v[220:223], v[20:23]\n"
      "s_waitcnt lgkmcnt(13)\n"
      "v_mfma_f32_32x32x16_bf16 v[80:95], v[224:227], v[32:35], v[80:95]\n"
      "s_waitcnt lgkmcnt(12)\n"
      "v_mfma_f32_32x32x16_bf16 v[64:79], v[224:227], v[36:39], v[64:79]\n"
      "v_mfma_f32_16x16x32_bf16 v[20:23], v[16:19], v[224:227], v[20:23]\n"
      "s_waitcnt lgkmcnt(8)\n"
      "v_mfma_f32_16x16x32_bf16 v[20:23], v[16:19], v[228:231], v[20:23]\n"
      "v_mfma_f32_32x32x16_bf16 v[80:95], v[228:231], v[40:43], v[80:95]\n"
      "s_waitcnt lgkmcnt(0)\n"
      "v_add_u32 v24, 49152, v24\n"
      "v_add_u32 v4, 49152, v4\n"
      "v_add_u32 v5, 49152, v5\n"
      "v_add_u32 v6, 49152, v6\n"
      "v_add_u32 v7, 49152, v7\n"
      "v_mfma_f32_32x32x16_bf16 v[64:79], v[228:231], v[44:47], v[64:79]\n"
      "s_barrier\n"
      "v_mfma_f32_32x32x16_bf16 v[80:95], v[232:235], v[48:51], v[80:95]\n"
      "ds_read_b64_tr_b16 v[240:241], v24 offset:0\n"
      "ds_read_b64_tr_b16 v[242:243], v24 offset:1024\n"
      "ds_read_b128 v[32:35], v4 offset:16384\n"
      "ds_read_b128 v[36:39], v4 offset:20480\n"
      "v_mfma_f32_32x32x16_bf16 v[64:79], v[232:235], v[52:55], v[64:79]\n"
      "ds_read_b64_tr_b16 v[244:245], v24 offset:4096\n"
      "ds_read_b64_tr_b16 v[246:247], v24 offset:5120\n"
      "ds_read_b128 v[40:43], v5 offset:16384\n"
      "ds_read_b128 v[44:47], v5 offset:20480\n"
      "v_mfma_f32_32x32x16_bf16 v[80:95], v[236:239], v[56:59], v[80:95]\n"
      "ds_read_b64_tr_b16 v[248:249], v24 offset:8192\n"
      "ds_read_b64_tr_b16 v[250:251], v24 offset:9216\n"
      "ds_read_b128 v[48:51], v6 offset:16384\n"
      "ds_read_b128 v[52:55], v6 offset:20480\n"
      "v_mfma_f32_32x32x16_bf16 v[64:79], v[236:239], v[60:63], v[64:79]\n"
      "ds_read_b64_tr_b16 v[252:253], v24 offset:12288\n"
      "ds_read_b64_tr_b16 v[254:255], v24 offset:13312\n"
      "ds_read_b128 v[56:59], v7 offset:16384\n"
      "ds_read_b128 v[60:63], v7 offset:20480\n"
      "v_mfma_f32_16x16x32_bf16 v[20:23], v[16:19], v[232:235], v[20:23]\n"
      "v_mfma_f32_16x16x32_bf16 v[20:23], v[16:19], v[236:239], v[20:23]\n"
      "ds_read_b32 v31, v2 offset:4144\n"
      "ds_read_b64 v[12:13], v30 offset:1040\n"
      "s_waitcnt lgkmcnt(14)\n"
      "v_mfma_f32_16x16x32_bf16 v[20:23], v[16:19], v[240:243], v[20:23]\n"
      "v_subrev_u32 v4, 98304, v4\n"
      "s_waitcnt lgkmcnt(12)\n"
      "v_mfma_f32_16x16x32_bf16 v[20:23], v[16:19], v[244:247], v[20:23]\n"
      "v_subrev_u32 v5, 98304, v5\n"
      "s_waitcnt lgkmcnt(8)\n"
      "v_mfma_f32_16x16x32_bf16 v[20:23], v[16:19], v[248:251], v[20:23]\n"
      "v_subrev_u32 v6, 98304, v6\n"
      "s_waitcnt lgkmcnt(4)\n"
      "v_mfma_f32_16x16x32_bf16 v[20:23], v[16:19], v[252:255], v[20:23]\n"
      "v_subrev_u32 v7, 98304, v7\n"
      "s_waitcnt lgkmcnt(0)\n"
      "v_mfma_f32_32x32x16_bf16 v[80:95], v[240:243], v[32:35], v[80:95]\n"
      "ds_read_b64 v[16:17], v30 offset:16\n"
      "v_readlane_b32 s64, v31, 0\n"
      "s_mov_b32 vcc_hi, 0\n"
      "s_mov_b32 vcc_lo, s64\n"
      "s_nop 0\n"
      "v_mfma_f32_32x32x16_bf16 v[80:95], v[244:247], v[40:43], v[80:95]\n"
      "v_cndmask_b32 v24, v20, v21, s[80:81]\n"
      "v_cvt_pk_bf16_f32 v25, 0, v24\n"
      "v_sub_f32 v24, v24, v25\n"
      "v_cvt_pk_bf16_f32 v26, 0, v24\n"
      "v_sub_f32 v24, v24, v26\n"
      "v_mfma_f32_32x32x16_bf16 v[80:95], v[248:251], v[48:51], v[80:95]\n"
      "v_cvt_pk_bf16_f32 v25, v25, v26\n"
      "v_cvt_pk_bf16_f32 v26, v24, v9\n"
      "v_cndmask_b32 v8, 0, v25, s[82:83]\n"
      "v_cndmask_b32 v9, 0, v26, s[82:83]\n"
      "ds_read_b128 v[24:27], v30\n"
      "v_mfma_f32_32x32x16_bf16 v[80:95], v[252:255], v[56:59], v[80:95]\n"
      "s_waitcnt lgkmcnt(1)\n"
      "v_mfma_f32_32x32x16_bf16 v[80:95], v[8:11], v[16:19], v[80:95]\n"
      "v_mfma_f32_32x32x16_bf16 v[64:79], v[240:243], v[36:39], v[64:79]\n"
      "v_mfma_f32_32x32x16_bf16 v[64:79], v[244:247], v[44:47], v[64:79]\n"
      "v_mfma_f32_32x32x16_bf16 v[64:79], v[248:251], v[52:55], v[64:79]\n"
      "v_mfma_f32_32x32x16_bf16 v[64:79], v[252:255], v[60:63], v[64:79]\n"
      "v_mfma_f32_32x32x16_bf16 v[64:79], v[8:11], v[12:15], v[64:79]\n"
      "ds_read_b64 v[12:13], v30 offset:2064\n"
      "s_barrier\n"
      "s_mov_b64 s[84:85], 0\n"
      "s_mov_b32 s81, 2\n"
      "s_waitcnt lgkmcnt(1)\n"
      "s_cbranch_vccnz .Lg_126\n"
      ".Lg_127:\n"
      "v_mov_b32 v48, v24\n"
      "v_mov_b32 v49, v24\n"
      "v_mov_b32 v50, v25\n"
      "v_mov_b32 v51, v25\n"
      "v_mov_b32 v52, v26\n"
      "v_mov_b32 v53, v26\n"
      "ds_read_b128 v[24:27], v30 offset:1024\n"
      "ds_read_b128 v[32:35], v4\n"
      "ds_read_b128 v[36:39], v5\n"
      "ds_read_b128 v[40:43], v6\n"
      "ds_read_b128 v[44:47], v7\n"
      "v_pk_mul_f32 v[54:55], v[80:81], v[80:81]\n"
      "v_pk_mul_f32 v[56:57], v[82:83], v[82:83]\n"
      "v_pk_mul_f32 v[58:59], v[84:85], v[84:85]\n"
      "v_pk_mul_f32 v[60:61], v[86:87], v[86:87]\n"
      "v_pk_fma_f32 v[54:55], v[54:55], v[50:51], v[48:49]\n"
      "v_pk_fma_f32 v[56:57], v[56:57], v[50:51], v[48:49]\n"
      "v_pk_fma_f32 v[58:59], v[58:59], v[50:51], v[48:49]\n"
      "v_pk_fma_f32 v[60:61], v[60:61], v[50:51], v[48:49]\n"
      "v_pk_mul_f32 v[54:55], v[80:81], v[54:55]\n"
      "v_pk_mul_f32 v[56:57], v[82:83], v[56:57]\n"
      "v_pk_mul_f32 v[58:59], v[84:85], v[58:59]\n"
      "v_pk_mul_f32 v[60:61], v[86:87], v[60:61]\n"
      "v_exp_f32 v54, v54\n"
      "v_exp_f32 v55, v55\n"
      "v_exp_f32 v56, v56\n"
      "v_exp_f32 v57, v57\n"
      "v_exp_f32 v58, v58\n"
      "v_exp_f32 v59, v59\n"
      "v_exp_f32 v60, v60\n"
      "v_exp_f32 v61, v61\n"
      "v_pk_fma_f32 v[54:55], v[54:55], v[52:53], v[52:53]\n"
      "v_pk_fma_f32 v[56:57], v[56:57], v[52:53], v[52:53]\n"
      "v_pk_fma_f32 v[58:59], v[58:59], v[52:53], v[52:53]\n"
      "v_pk_fma_f32 v[60:61], v[60:61], v[52:53], v[52:53]\n"
      "v_rcp_f32 v54, v54\n"
      "v_rcp_f32 v55, v55\n"
      "v_rcp_f32 v56, v56\n"
      "v_rcp_f32 v57, v57\n"
      "v_rcp_f32 v58, v58\n"
      "v_rcp_f32 v59, v59\n"
      "v_rcp_f32 v60, v60\n"
      "v_rcp_f32 v61, v61\n"
      "v_pk_mul_f32 v[54:55], v[80:81], v[54:55]\n"
      "v_pk_mul_f32 v[56:57], v[82:83], v[56:57]\n"
      "v_pk_mul_f32 v[58:59], v[84:85], v[58:59]\n"
      "v_pk_mul_f32 v[60:61], v[86:87], v[60:61]\n"
      "v_cvt_pk_bf16_f32 v16, v54, v55\n"
      "v_cvt_pk_bf16_f32 v17, v56, v57\n"
      "v_cvt_pk_bf16_f32 v18, v58, v59\n"
      "v_cvt_pk_bf16_f32 v19, v60, v61\n"
      "buffer_store_dwordx4 v[16:19], v29, s[20:23], s74 offen offset:0 sc1\n"
      "v_pk_mul_f32 v[54:55], v[88:89], v[88:89]\n"
      "v_pk_mul_f32 v[56:57], v[90:91], v[90:91]\n"
      "v_pk_mul_f32 v[58:59], v[92:93], v[92:93]\n"
      "v_pk_mul_f32 v[60:61], v[94:95], v[94:95]\n"
      "v_pk_fma_f32 v[54:55], v[54:55], v[50:51], v[48:49]\n"
      "v_pk_fma_f32 v[56:57], v[56:57], v[50:51], v[48:49]\n"
      "v_pk_fma_f32 v[58:59], v[58:59], v[50:51], v[48:49]\n"
      "v_pk_fma_f32 v[60:61], v[60:61], v[50:51], v[48:49]\n"
      "v_pk_mul_f32 v[54:55], v[88:89], v[54:55]\n"
      "v_pk_mul_f32 v[56:57], v[90:91], v[56:57]\n"
      "v_pk_mul_f32 v[58:59], v[92:93], v[58:59]\n"
      "v_pk_mul_f32 v[60:61], v[94:95], v[60:61]\n"
      "v_exp_f32 v54, v54\n"
      "v_exp_f32 v55, v55\n"
      "v_exp_f32 v56, v56\n"
      "v_exp_f32 v57, v57\n"
      "v_exp_f32 v58, v58\n"
      "v_exp_f32 v59, v59\n"
      "v_exp_f32 v60, v60\n"
      "v_exp_f32 v61, v61\n"
      "v_pk_fma_f32 v[54:55], v[54:55], v[52:53], v[52:53]\n"
      "v_pk_fma_f32 v[56:57], v[56:57], v[52:53], v[52:53]\n"
      "v_pk_fma_f32 v[58:59], v[58:59], v[52:53], v[52:53]\n"
      "v_pk_fma_f32 v[60:61], v[60:61], v[52:53], v[52:53]\n"
      "v_rcp_f32 v54, v54\n"
      "v_rcp_f32 v55, v55\n"
      "v_rcp_f32 v56, v56\n"
      "v_rcp_f32 v57, v57\n"
      "v_rcp_f32 v58, v58\n"
      "v_rcp_f32 v59, v59\n"
      "v_rcp_f32 v60, v60\n"
      "v_rcp_f32 v61, v61\n"
      "v_pk_mul_f32 v[54:55], v[88:89], v[54:55]\n"
      "v_pk_mul_f32 v[56:57], v[90:91], v[56:57]\n"
      "v_pk_mul_f32 v[58:59], v[92:93], v[58:59]\n"
      "v_pk_mul_f32 v[60:61], v[94:95], v[60:61]\n"
      "v_cvt_pk_bf16_f32 v20, v54, v55\n"
      "v_cvt_pk_bf16_f32 v21, v56, v57\n"
      "v_cvt_pk_bf16_f32 v22, v58, v59\n"
      "v_cvt_pk_bf16_f32 v23, v60, v61\n"
      "buffer_store_dwordx4 v[20:23], v29, s[20:23], s74 offen offset:256 sc1\n"
      "ds_read_b128 v[48:51], v4 offset:4096\n"
      "ds_read_b128 v[52:55], v5 offset:4096\n"
      "ds_read_b128 v[56:59], v6 offset:4096\n"
      "ds_read_b128 v[60:63], v7 offset:4096\n"
      "s_waitcnt lgkmcnt(8)\n"
      "v_mfma_f32_32x32x16_bf16 v[80:95], v[8:11], v[12:15], 0\n"
      "s_cbranch_vccnz .Lg_128\n"
      ".Lg_129:\n"
      "ds_read_b64 v[12:13], v30 offset:3088\n"
      "s_nop 0\n"
      "s_waitcnt lgkmcnt(5)\n"
      "v_mfma_f32_32x32x16_bf16 v[80:95], v[96:99], v[32:35], v[80:95]\n"
      "ds_read_b128 v[32:35], v4 offset:8192\n"
      "v_mul_f32 v20, v64, v64\n"
      "v_fma_f32 v20, v20, v25, v24\n"
      "v_mul_f32 v20, v64, v20\n"
      "v_exp_f32 v20, v20\n"
      "v_mfma_f32_32x32x16_bf16 v[80:95], v[100:103], v[36:39], v[80:95]\n"
      "ds_read_b128 v[36:39], v5 offset:8192\n"
      "v_mul_f32 v21, v65, v65\n"
      "v_fma_f32 v20, v20, v26, v26\n"
      "v_fma_f32 v21, v21, v25, v24\n"
      "v_mfma_f32_32x32x16_bf16 v[80:95], v[104:107], v[40:43], v[80:95]\n"
      "ds_read_b128 v[40:43], v6 offset:8192\n"
      "v_rcp_f32 v20, v20\n"
      "v_mul_f32 v21, v65, v21\n"
      "v_mul_f32 v20, v64, v20\n"
      "v_exp_f32 v21, v21\n"
      "v_mfma_f32_32x32x16_bf16 v[80:95], v[108:111], v[44:47], v[80:95]\n"
      "ds_read_b128 v[44:47], v7 offset:8192\n"
      "v_mul_f32 v22, v66, v66\n"
      "v_fma_f32 v21, v21, v26, v26\n"
      "v_fma_f32 v22, v22, v25, v24\n"
      "s_waitcnt lgkmcnt(4)\n"
      "v_mfma_f32_32x32x16_bf16 v[80:95], v[112:115], v[48:51], v[80:95]\n"
      "ds_read_b128 v[48:51], v4 offset:12288\n"
      "v_rcp_f32 v21, v21\n"
      "v_mul_f32 v22, v66, v22\n"
      "v_mul_f32 v21, v65, v21\n"
      "v_cvt_pk_bf16_f32 v16, v20, v21\n"
      "v_mfma_f32_32x32x16_bf16 v[80:95], v[116:119], v[52:55], v[80:95]\n"
      "ds_read_b128 v[52:55], v5 offset:12288\n"
      "v_exp_f32 v22, v22\n"
      "v_mul_f32 v23, v67, v67\n"
      "v_fma_f32 v22, v22, v26, v26\n"
      "v_mfma_f32_32x32x16_bf16 v[80:95], v[120:123], v[56:59], v[80:95]\n"
      "ds_read_b128 v[56:59], v6 offset:12288\n"
      "v_fma_f32 v23, v23, v25, v24\n"
      "v_rcp_f32 v22, v22\n"
      "v_mul_f32 v23, v67, v23\n"
      "v_mul_f32 v22, v66, v22\n"
      "v_mfma_f32_32x32x16_bf16 v[80:95], v[124:127], v[60:63], v[80:95]\n"
      "ds_read_b128 v[60:63], v7 offset:12288\n"
      "v_exp_f32 v23, v23\n"
      "v_mul_f32 v20, v68, v68\n"
      "s_waitcnt lgkmcnt(4)\n"
      "v_mfma_f32_32x32x16_bf16 v[80:95], v[128:131], v[32:35], v[80:95]\n"
      "ds_read_b128 v[32:35], v4 offset:16384\n"
      "v_fma_f32 v23, v23, v26, v26\n"
      "v_fma_f32 v20, v20, v25, v24\n"
      "v_rcp_f32 v23, v23\n"
      "ds_read_b32 v31, v2 offset:2072\n"
      "v_mfma_f32_32x32x16_bf16 v[80:95], v[132:135], v[36:39], v[80:95]\n"
      "ds_read_b128 v[36:39], v5 offset:16384\n"
      "v_mul_f32 v20, v68, v20\n"
      "v_mul_f32 v23, v67, v23\n"
      "v_cvt_pk_bf16_f32 v17, v22, v23\n"
      "v_exp_f32 v20, v20\n"
      "v_mfma_f32_32x32x16_bf16 v[80:95], v[136:139], v[40:43], v[80:95]\n"
      "ds_read_b128 v[40:43], v6 offset:16384\n"
      "v_mul_f32 v21, v69, v69\n"
      "v_fma_f32 v20, v20, v26, v26\n"
      "v_fma_f32 v21, v21, v25, v24\n"
      "v_mfma_f32_32x32x16_bf16 v[80:95], v[140:143], v[44:47], v[80:95]\n"
      "ds_read_b128 v[44:47], v7 offset:16384\n"
      "v_rcp_f32 v20, v20\n"
      "v_mul_f32 v21, v69, v21\n"
      "v_mul_f32 v20, v68, v20\n"
      "s_waitcnt lgkmcnt(3)\n"
      "v_mfma_f32_32x32x16_bf16 v[80:95], v[144:147], v[48:51], v[80:95]\n"
      "v_readfirstlane_b32 s2, v31\n"
      "s_cmp_lt_u32 s2, 4\n"
      "s_cbranch_scc1 .Lw_130\n"
      ".Lw_131:\n"
      "ds_read_b128 v[48:51], v4 offset:20480\n"
      "v_exp_f32 v21, v21\n"
      "v_mfma_f32_32x32x16_bf16 v[80:95], v[148:151], v[52:55], v[80:95]\n"
      "ds_read_b128 v[52:55], v5 offset:20480\n"
      "v_mul_f32 v22, v70, v70\n"
      "v_fma_f32 v21, v21, v26, v26\n"
      "v_fma_f32 v22, v22, v25, v24\n"
      "v_mfma_f32_32x32x16_bf16 v[80:95], v[152:155], v[56:59], v[80:95]\n"
      "ds_read_b128 v[56:59], v6 offset:20480\n"
      "v_rcp_f32 v21, v21\n"
      "v_mul_f32 v22, v70, v22\n"
      "v_mul_f32 v21, v69, v21\n"
      "v_cvt_pk_bf16_f32 v18, v20, v21\n"
      "v_mfma_f32_32x32x16_bf16 v[80:95], v[156:159], v[60:63], v[80:95]\n"
      "ds_read_b128 v[60:63], v7 offset:20480\n"
      "v_exp_f32 v22, v22\n"
      "v_mul_f32 v23, v71, v71\n"
      "v_fma_f32 v22, v22, v26, v26\n"
      "s_waitcnt lgkmcnt(4)\n"
      "v_mfma_f32_32x32x16_bf16 v[80:95], v[160:163], v[32:35], v[80:95]\n"
      "ds_read_b128 v[32:35], v4 offset:24576\n"
      "v_fma_f32 v23, v23, v25, v24\n"
      "v_rcp_f32 v22, v22\n"
      "v_mul_f32 v23, v71, v23\n"
      "v_mfma_f32_32x32x16_bf16 v[80:95], v[164:167], v[36:39], v[80:95]\n"
      "ds_read_b128 v[36:39], v5 offset:24576\n"
      "v_mul_f32 v22, v70, v22\n"
      "v_exp_f32 v23, v23\n"
      "v_mul_f32 v20, v72, v72\n"
      "v_fma_f32 v23, v23, v26, v26\n"
      "v_mfma_f32_32x32x16_bf16 v[80:95], v[168:171], v[40:43], v[80:95]\n"
      "ds_read_b128 v[40:43], v6 offset:24576\n"
      "v_fma_f32 v20, v20, v25, v24\n"
      "v_rcp_f32 v23, v23\n"
      "v_mul_f32 v20, v72, v20\n"
      "v_mfma_f32_32x32x16_bf16 v[80:95], v[172:175], v[44:47], v[80:95]\n"
      "ds_read_b128 v[44:47], v7 offset:24576\n"
      "v_mul_f32 v23, v71, v23\n"
      "v_cvt_pk_bf16_f32 v19, v22, v23\n"
      "buffer_store_dwordx4 v[16:19], v29, s[20:23], s75 offen offset:0 sc1\n"
      "s_waitcnt lgkmcnt(4)\n"
      "v_mfma_f32_32x32x16_bf16 v[80:95], v[176:179], v[48:51], v[80:95]\n"
      "ds_read_b128 v[48:51], v4 offset:28672\n"
      "v_exp_f32 v20, v20\n"
      "v_mul_f32 v21, v73, v73\n"
      "v_mfma_f32_32x32x16_bf16 v[80:95], v[180:183], v[52:55], v[80:95]\n"
      "ds_read_b128 v[52:55], v5 offset:28672\n"
      "v_fma_f32 v20, v20, v26, v26\n"
      "v_fma_f32 v21, v21, v25, v24\n"
      "v_rcp_f32 v20, v20\n"
      "v_mul_f32 v21, v73, v21\n"
      "v_mfma_f32_32x32x16_bf16 v[80:95], v[184:187], v[56:59], v[80:95]\n"
      "ds_read_b128 v[56:59], v6 offset:28672\n"
      "v_mul_f32 v20, v72, v20\n"
      "v_exp_f32 v21, v21\n"
      "v_mul_f32 v22, v74, v74\n"
      "v_mfma_f32_32x32x16_bf16 v[80:95], v[188:191], v[60:63], v[80:95]\n"
      "ds_read_b128 v[60:63], v7 offset:28672\n"
      "v_fma_f32 v21, v21, v26, v26\n"
      "v_fma_f32 v22, v22, v25, v24\n"
      "v_rcp_f32 v21, v21\n"
      "s_waitcnt lgkmcnt(4)\n"
      "v_mfma_f32_32x32x16_bf16 v[80:95], v[192:195], v[32:35], v[80:95]\n"
      "ds_read_b128 v[32:35], v4 offset:32768\n"
      "v_mul_f32 v22, v74, v22\n"
      "v_mul_f32 v21, v73, v21\n"
      "v_cvt_pk_bf16_f32 v16, v20, v21\n"
      "v_exp_f32 v22, v22\n"
      "v_mfma_f32_32x32x16_bf16 v[80:95], v[196:199], v[36:39], v[80:95]\n"
      "ds_read_b128 v[36:39], v5 offset:32768\n"
      "v_mul_f32 v23, v75, v75\n"
      "v_fma_f32 v22, v22, v26, v26\n"
      "v_fma_f32 v23, v23, v25, v24\n"
      "v_mfma_f32_32x32x16_bf16 v[80:95], v[200:203], v[40:43], v[80:95]\n"
      "ds_read_b128 v[40:43], v6 offset:32768\n"
      "v_rcp_f32 v22, v22\n"
      "v_mul_f32 v23, v75, v23\n"
      "v_mul_f32 v22, v74, v22\n"
      "v_exp_f32 v23, v23\n"
      "v_mfma_f32_32x32x16_bf16 v[80:95], v[204:207], v[44:47], v[80:95]\n"
      "ds_read_b128 v[44:47], v7 offset:32768\n"
      "v_mul_f32 v20, v76, v76\n"
      "v_fma_f32 v23, v23, v26, v26\n"
      "v_fma_f32 v20, v20, v25, v24\n"
      "s_waitcnt lgkmcnt(4)\n"
      "v_mfma_f32_32x32x16_bf16 v[80:95], v[208:211], v[48:51], v[80:95]\n"
      "ds_read_b128 v[48:51], v4 offset:36864\n"
      "v_rcp_f32 v23, v23\n"
      "v_mul_f32 v20, v76, v20\n"
      "ds_read_b32 v31, v2 offset:3096\n"
      "v_mfma_f32_32x32x16_bf16 v[80:95], v[212:215], v[52:55], v[80:95]\n"
      "ds_read_b128 v[52:55], v5 offset:36864\n"
      "v_mul_f32 v23, v75, v23\n"
      "v_cvt_pk_bf16_f32 v17, v22, v23\n"
      "v_exp_f32 v20, v20\n"
      "v_mul_f32 v21, v77, v77\n"
      "v_mfma_f32_32x32x16_bf16 v[80:95], v[216:219], v[56:59], v[80:95]\n"
      "ds_read_b128 v[56:59], v6 offset:36864\n"
      "v_fma_f32 v20, v20, v26, v26\n"
      "v_fma_f32 v21, v21, v25, v24\n"
      "v_rcp_f32 v20, v20\n"
      "v_mfma_f32_32x32x16_bf16 v[80:95], v[220:223], v[60:63], v[80:95]\n"
      "ds_read_b128 v[60:63], v7 offset:36864\n"
      "v_mul_f32 v21, v77, v21\n"
      "v_mul_f32 v20, v76, v20\n"
      "v_exp_f32 v21, v21\n"
      "s_waitcnt lgkmcnt(3)\n"
      "v_mfma_f32_32x32x16_bf16 v[80:95], v[224:227], v[32:35], v[80:95]\n"
      "v_readfirstlane_b32 s2, v31\n"
      "s_cmp_lt_u32 s2, 4\n"
      "s_cbranch_scc1 .Lw_133\n"
      ".Lw_134:\n"
      "ds_read_b128 v[32:35], v4 offset:49152\n"
      "v_mul_f32 v22, v78, v78\n"
      "v_mfma_f32_32x32x16_bf16 v[80:95], v[228:231], v[36:39], v[80:95]\n"
      "ds_read_b128 v[36:39], v5 offset:49152\n"
      "v_fma_f32 v21, v21, v26, v26\n"
      "v_fma_f32 v22, v22, v25, v24\n"
      "v_rcp_f32 v21, v21\n"
      "v_mfma_f32_32x32x16_bf16 v[80:95], v[232:235], v[40:43], v[80:95]\n"
      "ds_read_b128 v[40:43], v6 offset:49152\n"
      "v_mul_f32 v22, v78, v22\n"
      "v_mul_f32 v21, v77, v21\n"
      "v_cvt_pk_bf16_f32 v18, v20, v21\n"
      "v_exp_f32 v22, v22\n"
      "v_mfma_f32_32x32x16_bf16 v[80:95], v[236:239], v[44:47], v[80:95]\n"
      "ds_read_b128 v[44:47], v7 offset:49152\n"
      "v_mul_f32 v23, v79, v79\n"
      "v_fma_f32 v22, v22, v26, v26\n"
      "v_fma_f32 v23, v23, v25, v24\n"
      "s_waitcnt lgkmcnt(4)\n"
      "v_mfma_f32_32x32x16_bf16 v[80:95], v[240:243], v[48:51], v[80:95]\n"
      "ds_read_b128 v[48:51], v4 offset:53248\n"
      "v_rcp_f32 v22, v22\n"
      "v_mul_f32 v23, v79, v23\n"
      "v_mul_f32 v22, v78, v22\n"
      "v_exp_f32 v23, v23\n"
      "v_mfma_f32_32x32x16_bf16 v[80:95], v[244:247], v[52:55], v[80:95]\n"
      "ds_read_b128 v[52:55], v5 offset:53248\n"
      "v_fma_f32 v23, v23, v26, v26\n"
      "v_rcp_f32 v23, v23\n"
      "s_nop 0\n"
      "v_mul_f32 v23, v79, v23\n"
      "v_mfma_f32_32x32x16_bf16 v[80:95], v[248:251], v[56:59], v[80:95]\n"
      "ds_read_b128 v[56:59], v6 offset:53248\n"
      "v_cvt_pk_bf16_f32 v19, v22, v23\n"
      "buffer_store_dwordx4 v[16:19], v29, s[20:23], s75 offen offset:256 sc1\n"
      "ds_read_b128 v[24:27], v30 offset:2048\n"
      "v_mfma_f32_32x32x16_bf16 v[64:79], v[252:255], v[60:63], v[80:95]\n"
      "ds_read_b128 v[60:63], v7 offset:53248\n"
      "s_nop 3\n"
      "v_mfma_f32_32x32x16_bf16 v[80:95], v[8:11], v[12:15], 0\n"
      "s_cbranch_vccnz .Lg_136\n"
      ".Lg_137:\n"
      "s_nop 1\n"
      "s_waitcnt lgkmcnt(1)\n"
      "v_mfma_f32_32x32x16_bf16 v[80:95], v[96:99], v[32:35], v[80:95]\n"
      "ds_read_b128 v[32:35], v4 offset:57344\n"
      "v_mul_f32 v20, v64, v64\n"
      "v_fma_f32 v20, v20, v25, v24\n"
      "v_mul_f32 v20, v64, v20\n"
      "v_exp_f32 v20, v20\n"
      "v_mfma_f32_32x32x16_bf16 v[80:95], v[100:103], v[36:39], v[80:95]\n"
      "ds_read_b128 v[36:39], v5 offset:57344\n"
      "v_mul_f32 v21, v65, v65\n"
      "v_fma_f32 v20, v20, v26, v26\n"
      "v_fma_f32 v21, v21, v25, v24\n"
      "v_mov_b64 v[12:13], s[58:59]\n"
      "v_mfma_f32_32x32x16_bf16 v[80:95], v[104:107], v[40:43], v[80:95]\n"
      "ds_read_b128 v[40:43], v6 offset:57344\n"
      "v_rcp_f32 v20, v20\n"
      "v_mul_f32 v21, v65, v21\n"
      "v_mul_f32 v20, v64, v20\n"
      "v_exp_f32 v21, v21\n"
      "v_mfma_f32_32x32x16_bf16 v[80:95], v[108:111], v[44:47], v[80:95]\n"
      "ds_read_b128 v[44:47], v7 offset:57344\n"
      "v_mul_f32 v22, v66, v66\n"
      "v_fma_f32 v21, v21, v26, v26\n"
      "v_fma_f32 v22, v22, v25, v24\n"
      "s_waitcnt lgkmcnt(4)\n"
      "v_mfma_f32_32x32x16_bf16 v[80:95], v[112:115], v[48:51], v[80:95]\n"
      "ds_read_b128 v[48:51], v4 offset:61440\n"
      "v_rcp_f32 v21, v21\n"
      "v_mul_f32 v22, v66, v22\n"
      "v_mul_f32 v21, v65, v21\n"
      "v_cvt_pk_bf16_f32 v16, v20, v21\n"
      "v_mfma_f32_32x32x16_bf16 v[80:95], v[116:119], v[52:55], v[80:95]\n"
      "ds_read_b128 v[52:55], v5 offset:61440\n"
      "v_exp_f32 v22, v22\n"
      "v_mul_f32 v23, v67, v67\n"
      "v_fma_f32 v22, v22, v26, v26\n"
      "v_add_u32 v4, 49152, v4\n"
      "v_mfma_f32_32x32x16_bf16 v[80:95], v[120:123], v[56:59], v[80:95]\n"
      "ds_read_b128 v[56:59], v6 offset:61440\n"
      "v_fma_f32 v23, v23, v25, v24\n"
      "v_rcp_f32 v22, v22\n"
      "v_mul_f32 v23, v67, v23\n"
      "v_add_u32 v5, 49152, v5\n"
      "v_mfma_f32_32x32x16_bf16 v[80:95], v[124:127], v[60:63], v[80:95]\n"
      "ds_read_b128 v[60:63], v7 offset:61440\n"
      "v_mul_f32 v22, v66, v22\n"
      "v_exp_f32 v23, v23\n"
      "v_add_u32 v6, 49152, v6\n"
      "s_waitcnt lgkmcnt(4)\n"
      "v_mfma_f32_32x32x16_bf16 v[80:95], v[128:131], v[32:35], v[80:95]\n"
      "ds_read_b128 v[32:35], v4 offset:16384\n"
      "v_mul_f32 v20, v68, v68\n"
      "v_fma_f32 v23, v23, v26, v26\n"
      "v_fma_f32 v20, v20, v25, v24\n"
      "v_add_u32 v7, 49152, v7\n"
      "v_mfma_f32_32x32x16_bf16 v[80:95], v[132:135], v[36:39], v[80:95]\n"
      "ds_read_b128 v[36:39], v5 offset:16384\n"
      "v_rcp_f32 v23, v23\n"
      "v_mul_f32 v20, v68, v20\n"
      "v_mul_f32 v23, v67, v23\n"
      "v_cvt_pk_bf16_f32 v17, v22, v23\n"
      "v_mfma_f32_32x32x16_bf16 v[80:95], v[136:139], v[40:43], v[80:95]\n"
      "ds_read_b128 v[40:43], v6 offset:16384\n"
      "v_exp_f32 v20, v20\n"
      "v_mul_f32 v21, v69, v69\n"
      "v_fma_f32 v20, v20, v26, v26\n"
      "v_fma_f32 v21, v21, v25, v24\n"
      "v_mfma_f32_32x32x16_bf16 v[80:95], v[140:143], v[44:47], v[80:95]\n"
      "ds_read_b128 v[44:47], v7 offset:16384\n"
      "v_rcp_f32 v20, v20\n"
      "v_mul_f32 v21, v69, v21\n"
      "v_mul_f32 v20, v68, v20\n"
      "s_waitcnt lgkmcnt(4)\n"
      "v_mfma_f32_32x32x16_bf16 v[80:95], v[144:147], v[48:51], v[80:95]\n"
      "ds_read_b128 v[48:51], v4 offset:20480\n"
      "v_exp_f32 v21, v21\n"
      "v_mul_f32 v22, v70, v70\n"
      "v_fma_f32 v21, v21, v26, v26\n"
      "v_fma_f32 v22, v22, v25, v24\n"
      "v_mfma_f32_32x32x16_bf16 v[80:95], v[148:151], v[52:55], v[80:95]\n"
      "ds_read_b128 v[52:55], v5 offset:20480\n"
      "s_getreg_b32 s2, hwreg(HW_REG_IB_STS, 0, 4)\n"
      "s_add_u32 s2, s2, s84\n"
      "s_cmp_le_u32 s2, 0\n"
      "s_cbranch_scc1 .Lh_138\n"
      ".Lh_139:\n"
      "v_mfma_f32_32x32x16_bf16 v[80:95], v[152:155], v[56:59], v[80:95]\n"
      "ds_read_b128 v[56:59], v6 offset:20480\n"
      "v_rcp_f32 v21, v21\n"
      "v_mul_f32 v22, v70, v22\n"
      "v_mul_f32 v21, v69, v21\n"
      "v_cvt_pk_bf16_f32 v18, v20, v21\n"
      "v_mfma_f32_32x32x16_bf16 v[80:95], v[156:159], v[60:63], v[80:95]\n"
      "ds_read_b128 v[60:63], v7 offset:20480\n"
      "v_exp_f32 v22, v22\n"
      "v_mul_f32 v23, v71, v71\n"
      "v_fma_f32 v22, v22, v26, v26\n"
      "s_waitcnt lgkmcnt(4)\n"
      "v_mfma_f32_32x32x16_bf16 v[80:95], v[160:163], v[32:35], v[80:95]\n"
      "ds_read_b128 v[32:35], v4 offset:24576\n"
      "v_fma_f32 v23, v23, v25, v24\n"
      "v_rcp_f32 v22, v22\n"
      "v_mul_f32 v23, v71, v23\n"
      "v_mul_f32 v22, v70, v22\n"
      "v_mfma_f32_32x32x16_bf16 v[80:95], v[164:167], v[36:39], v[80:95]\n"
      "ds_read_b128 v[36:39], v5 offset:24576\n"
      "v_exp_f32 v23, v23\n"
      "v_mul_f32 v20, v72, v72\n"
      "v_fma_f32 v23, v23, v26, v26\n"
      "v_fma_f32 v20, v20, v25, v24\n"
      "v_mfma_f32_32x32x16_bf16 v[80:95], v[168:171], v[40:43], v[80:95]\n"
      "ds_read_b128 v[40:43], v6 offset:24576\n"
      "v_rcp_f32 v23, v23\n"
      "v_mul_f32 v20, v72, v20\n"
      "v_mul_f32 v23, v71, v23\n"
      "v_cvt_pk_bf16_f32 v19, v22, v23\n"
      "v_mfma_f32_32x32x16_bf16 v[80:95], v[172:175], v[44:47], v[80:95]\n"
      "ds_read_b128 v[44:47], v7 offset:24576\n"
      "buffer_store_dwordx4 v[16:19], v29, s[20:23], s76 offen offset:0 sc1\n"
      "s_waitcnt lgkmcnt(4)\n"
      "v_mfma_f32_32x32x16_bf16 v[80:95], v[176:179], v[48:51], v[80:95]\n"
      "ds_read_b128 v[48:51], v4 offset:28672\n"
      "v_exp_f32 v20, v20\n"
      "v_mul_f32 v21, v73, v73\n"
      "v_fma_f32 v20, v20, v26, v26\n"
      "v_fma_f32 v21, v21, v25, v24\n"
      "v_mfma_f32_32x32x16_bf16 v[80:95], v[180:183], v[52:55], v[80:95]\n"
      "ds_read_b128 v[52:55], v5 offset:28672\n"
      "s_getreg_b32 s2, hwreg(HW_REG_IB_STS, 0, 4)\n"
      "s_add_u32 s2, s2, s84\n"
      "s_cmp_le_u32 s2, 1\n"
      "s_cbranch_scc1 .Lh_140\n"
      ".Lh_141:\n"
      "v_mfma_f32_32x32x16_bf16 v[80:95], v[184:187], v[56:59], v[80:95]\n"
      "ds_read_b128 v[56:59], v6 offset:28672\n"
      "v_rcp_f32 v20, v20\n"
      "v_mul_f32 v21, v73, v21\n"
      "v_mul_f32 v20, v72, v20\n"
      "v_exp_f32 v21, v21\n"
      "v_mfma_f32_32x32x16_bf16 v[80:95], v[188:191], v[60:63], v[80:95]\n"
      "ds_read_b128 v[60:63], v7 offset:28672\n"
      "v_mul_f32 v22, v74, v74\n"
      "v_fma_f32 v21, v21, v26, v26\n"
      "v_fma_f32 v22, v22, v25, v24\n"
      "s_waitcnt lgkmcnt(4)\n"
      "v_mfma_f32_32x32x16_bf16 v[80:95], v[192:195], v[32:35], v[80:95]\n"
      "ds_read_b128 v[32:35], v4 offset:32768\n"
      "v_rcp_f32 v21, v21\n"
      "v_mul_f32 v22, v74, v22\n"
      "v_mul_f32 v21, v73, v21\n"
      "v_cvt_pk_bf16_f32 v16, v20, v21\n"
      "v_mfma_f32_32x32x16_bf16 v[80:95], v[196:199], v[36:39], v[80:95]\n"
      "ds_read_b128 v[36:39], v5 offset:32768\n"
      "v_exp_f32 v22, v22\n"
      "v_mul_f32 v23, v75, v75\n"
      "v_fma_f32 v22, v22, v26, v26\n"
      "v_fma_f32 v23, v23, v25, v24\n"
      "v_mfma_f32_32x32x16_bf16 v[80:95], v[200:203], v[40:43], v[80:95]\n"
      "ds_read_b128 v[40:43], v6 offset:32768\n"
      "v_rcp_f32 v22, v22\n"
      "v_mul_f32 v23, v75, v23\n"
      "v_mul_f32 v22, v74, v22\n"
      "v_mfma_f32_32x32x16_bf16 v[80:95], v[204:207], v[44:47], v[80:95]\n"
      "ds_read_b128 v[44:47], v7 offset:32768\n"
      "v_exp_f32 v23, v23\n"
      "v_mul_f32 v20, v76, v76\n"
      "v_fma_f32 v23, v23, v26, v26\n"
      "s_waitcnt lgkmcnt(4)\n"
      "v_mfma_f32_32x32x16_bf16 v[80:95], v[208:211], v[48:51], v[80:95]\n"
      "ds_read_b128 v[48:51], v4 offset:36864\n"
      "v_fma_f32 v20, v20, v25, v24\n"
      "v_rcp_f32 v23, v23\n"
      "v_mul_f32 v20, v76, v20\n"
      "v_mul_f32 v23, v75, v23\n"
      "v_mfma_f32_32x32x16_bf16 v[80:95], v[212:215], v[52:55], v[80:95]\n"
      "ds_read_b128 v[52:55], v5 offset:36864\n"
      "s_getreg_b32 s2, hwreg(HW_REG_IB_STS, 0, 4)\n"
      "s_add_u32 s2, s2, s84\n"
      "s_cmp_le_u32 s2, 1\n"
      "s_cbranch_scc1 .Lh_142\n"
      ".Lh_143:\n"
      "v_mfma_f32_32x32x16_bf16 v[80:95], v[216:219], v[56:59], v[80:95]\n"
      "ds_read_b128 v[56:59], v6 offset:36864\n"
      "v_cvt_pk_bf16_f32 v17, v22, v23\n"
      "v_exp_f32 v20, v20\n"
      "v_mul_f32 v21, v77, v77\n"
      "v_fma_f32 v20, v20, v26, v26\n"
      "v_mfma_f32_32x32x16_bf16 v[80:95], v[220:223], v[60:63], v[80:95]\n"
      "ds_read_b128 v[60:63], v7 offset:36864\n"
      "v_fma_f32 v21, v21, v25, v24\n"
      "v_rcp_f32 v20, v20\n"
      "v_mul_f32 v21, v77, v21\n"
      "s_waitcnt lgkmcnt(4)\n"
      "v_mfma_f32_32x32x16_bf16 v[80:95], v[224:227], v[32:35], v[80:95]\n"
      "v_mul_f32 v20, v76, v20\n"
      "v_exp_f32 v21, v21\n"
      "v_mul_f32 v22, v78, v78\n"
      "v_fma_f32 v21, v21, v26, v26\n"
      "v_fma_f32 v22, v22, v25, v24\n"
      "v_mfma_f32_32x32x16_bf16 v[80:95], v[228:231], v[36:39], v[80:95]\n"
      "v_rcp_f32 v21, v21\n"
      "v_mul_f32 v22, v78, v22\n"
      "v_mul_f32 v21, v77, v21\n"
      "v_xor_b32 v31, s86, v28\n"
      "ds_read_b64 v[218:219], v31\n"
      "v_mfma_f32_32x32x16_bf16 v[80:95], v[232:235], v[40:43], v[80:95]\n"
      "v_cvt_pk_bf16_f32 v18, v20, v21\n"
      "v_exp_f32 v22, v22\n"
      "v_mul_f32 v23, v79, v79\n"
      "v_xor_b32 v31, s87, v28\n"
      "ds_read_b64 v[220:221], v31\n"
      "v_mfma_f32_32x32x16_bf16 v[80:95], v[236:239], v[44:47], v[80:95]\n"
      "v_fma_f32 v22, v22, v26, v26\n"
      "v_fma_f32 v23, v23, v25, v24\n"
      "v_xor_b32 v31, s88, v28\n"
      "ds_read_b64 v[222:223], v31\n"
      "s_waitcnt lgkmcnt(3)\n"
      "v_mfma_f32_32x32x16_bf16 v[80:95], v[240:243], v[48:51], v[80:95]\n"
      "v_rcp_f32 v22, v22\n"
      "v_mul_f32 v23, v79, v23\n"
      "v_mul_f32 v22, v78, v22\n"
      "v_xor_b32 v31, s89, v28\n"
      "ds_read_b64 v[224:225], v31\n"
      "v_mfma_f32_32x32x16_bf16 v[80:95], v[244:247], v[52:55], v[80:95]\n"
      "v_exp_f32 v23, v23\n"
      "s_nop 0\n"
      "v_fma_f32 v23, v23, v26, v26\n"
      "v_rcp_f32 v23, v23\n"
      "v_xor_b32 v31, s90, v28\n"
      "ds_read_b64 v[226:227], v31\n"
      "v_mfma_f32_32x32x16_bf16 v[80:95], v[248:251], v[56:59], v[80:95]\n"
      "v_mul_f32 v23, v79, v23\n"
      "v_cvt_pk_bf16_f32 v19, v22, v23\n"
      "buffer_store_dwordx4 v[16:19], v29, s[20:23], s76 offen offset:256 sc1\n"
      "ds_read_b128 v[24:27], v30 offset:3072\n"
      "v_mfma_f32_32x32x16_bf16 v[64:79], v[252:255], v[60:63], v[80:95]\n"
      "ds_add_u32 v2, v3 offset:4276\n"
      "s_getreg_b32 s2, hwreg(HW_REG_IB_STS, 0, 4)\n"
      "s_add_u32 s2, s2, s84\n"
      "s_cmp_le_u32 s2, 2\n"
      "s_cbranch_scc1 .Lh_144\n"
      ".Lh_145:\n"
      "s_waitcnt lgkmcnt(0)\n"
      "s_cbranch_vccnz .Lg_146\n"
      ".Lg_147:\n"
      "v_mov_b32 v32, v24\n"
      "v_mov_b32 v33, v24\n"
      "v_mov_b32 v34, v25\n"
      "v_mov_b32 v35, v25\n"
      "v_mov_b32 v36, v26\n"
      "v_mov_b32 v37, v26\n"
      "v_pk_mul_f32 v[38:39], v[64:65], v[64:65]\n"
      "v_pk_mul_f32 v[40:41], v[66:67], v[66:67]\n"
      "v_pk_mul_f32 v[42:43], v[68:69], v[68:69]\n"
      "v_pk_mul_f32 v[44:45], v[70:71], v[70:71]\n"
      "v_pk_fma_f32 v[38:39], v[38:39], v[34:35], v[32:33]\n"
      "v_pk_fma_f32 v[40:41], v[40:41], v[34:35], v[32:33]\n"
      "v_pk_fma_f32 v[42:43], v[42:43], v[34:35], v[32:33]\n"
      "v_pk_fma_f32 v[44:45], v[44:45], v[34:35], v[32:33]\n"
      "v_pk_mul_f32 v[38:39], v[64:65], v[38:39]\n"
      "v_pk_mul_f32 v[40:41], v[66:67], v[40:41]\n"
      "v_pk_mul_f32 v[42:43], v[68:69], v[42:43]\n"
      "v_pk_mul_f32 v[44:45], v[70:71], v[44:45]\n"
      "v_exp_f32 v38, v38\n"
      "v_exp_f32 v39, v39\n"
      "v_exp_f32 v40, v40\n"
      "v_exp_f32 v41, v41\n"
      "v_exp_f32 v42, v42\n"
      "v_exp_f32 v43, v43\n"
      "v_exp_f32 v44, v44\n"
      "v_exp_f32 v45, v45\n"
      "v_pk_fma_f32 v[38:39], v[38:39], v[36:37], v[36:37]\n"
      "v_pk_fma_f32 v[40:41], v[40:41], v[36:37], v[36:37]\n"
      "v_pk_fma_f32 v[42:43], v[42:43], v[36:37], v[36:37]\n"
      "v_pk_fma_f32 v[44:45], v[44:45], v[36:37], v[36:37]\n"
      "v_rcp_f32 v38, v38\n"
      "v_rcp_f32 v39, v39\n"
      "v_rcp_f32 v40, v40\n"
      "v_rcp_f32 v41, v41\n"
      "v_rcp_f32 v42, v42\n"
      "v_rcp_f32 v43, v43\n"
      "v_rcp_f32 v44, v44\n"
      "v_rcp_f32 v45, v45\n"
      "v_pk_mul_f32 v[38:39], v[64:65], v[38:39]\n"
      "v_pk_mul_f32 v[40:41], v[66:67], v[40:41]\n"
      "v_pk_mul_f32 v[42:43], v[68:69], v[42:43]\n"
      "v_pk_mul_f32 v[44:45], v[70:71], v[44:45]\n"
      "v_cvt_pk_bf16_f32 v16, v38, v39\n"
      "v_cvt_pk_bf16_f32 v17, v40, v41\n"
      "v_cvt_pk_bf16_f32 v18, v42, v43\n"
      "v_cvt_pk_bf16_f32 v19, v44, v45\n"
      "buffer_store_dwordx4 v[16:19], v29, s[20:23], s77 offen offset:0 sc1\n"
      "v_pk_mul_f32 v[38:39], v[72:73], v[72:73]\n"
      "v_pk_mul_f32 v[40:41], v[74:75], v[74:75]\n"
      "v_pk_mul_f32 v[42:43], v[76:77], v[76:77]\n"
      "v_pk_mul_f32 v[44:45], v[78:79], v[78:79]\n"
      "v_pk_fma_f32 v[38:39], v[38:39], v[34:35], v[32:33]\n"
      "v_pk_fma_f32 v[40:41], v[40:41], v[34:35], v[32:33]\n"
      "v_pk_fma_f32 v[42:43], v[42:43], v[34:35], v[32:33]\n"
      "v_pk_fma_f32 v[44:45], v[44:45], v[34:35], v[32:33]\n"
      "v_pk_mul_f32 v[38:39], v[72:73], v[38:39]\n"
      "v_pk_mul_f32 v[40:41], v[74:75], v[40:41]\n"
      "v_pk_mul_f32 v[42:43], v[76:77], v[42:43]\n"
      "v_pk_mul_f32 v[44:45], v[78:79], v[44:45]\n"
      "v_exp_f32 v38, v38\n"
      "v_exp_f32 v39, v39\n"
      "v_exp_f32 v40, v40\n"
      "v_exp_f32 v41, v41\n"
      "v_exp_f32 v42, v42\n"
      "v_exp_f32 v43, v43\n"
      "v_exp_f32 v44, v44\n"
      "v_exp_f32 v45, v45\n"
      "v_pk_fma_f32 v[38:39], v[38:39], v[36:37], v[36:37]\n"
      "v_pk_fma_f32 v[40:41], v[40:41], v[36:37], v[36:37]\n"
      "v_pk_fma_f32 v[42:43], v[42:43], v[36:37], v[36:37]\n"
      "v_pk_fma_f32 v[44:45], v[44:45], v[36:37], v[36:37]\n"
      "ds_read_b32 v31, v2 offset:4304\n"
      "v_rcp_f32 v38, v38\n"
      "v_rcp_f32 v39, v39\n"
      "v_rcp_f32 v40, v40\n"
      "v_rcp_f32 v41, v41\n"
      "v_rcp_f32 v42, v42\n"
      "v_rcp_f32 v43, v43\n"
      "v_rcp_f32 v44, v44\n"
      "v_rcp_f32 v45, v45\n"
      "v_pk_mul_f32 v[38:39], v[72:73], v[38:39]\n"
      "v_pk_mul_f32 v[40:41], v[74:75], v[40:41]\n"
      "v_pk_mul_f32 v[42:43], v[76:77], v[42:43]\n"
      "v_pk_mul_f32 v[44:45], v[78:79], v[44:45]\n"
      "v_cvt_pk_bf16_f32 v20, v38, v39\n"
      "v_cvt_pk_bf16_f32 v21, v40, v41\n"
      "v_cvt_pk_bf16_f32 v22, v42, v43\n"
      "v_cvt_pk_bf16_f32 v23, v44, v45\n"
      "buffer_store_dwordx4 v[20:23], v29, s[20:23], s77 offen offset:256 sc1\n"
      "s_getreg_b32 s2, hwreg(HW_REG_IB_STS, 0, 4)\n"
      "s_add_u32 s2, s2, s84\n"
      "s_cmp_le_u32 s2, 4\n"
      "s_cbranch_scc1 .Lh_148\n"
      ".Lh_149:\n"
      ".LCONV:\n"
      "v_and_b32 v10, 1, v1\n"
      "v_xor_b32 v10, 1, v10\n"
      "v_mul_u32_u24 v10, 4160, v10\n"
      "v_and_b32 v11, 14, v1\n"
      "v_lshl_add_u32 v10, v11, 1, v10\n"
      "v_and_b32 v11, 48, v1\n"
      "v_sub_u32 v10, v10, v11\n"
      "v_and_b32 v21, 15, v1\n"
      "v_lshl_add_u32 v11, v21, 5, v11\n"
      "s_lshr_b32 s64, s53, 1\n"
      "s_and_b32 s65, s53, 1\n"
      "s_lshl_b32 s66, s65, 8\n"
      "s_mul_i32 s67, s81, 26112\n"
      "s_cmp_eq_u32 s81, 2\n"
      "s_cselect_b32 s67, 122880, s67\n"
      "s_mul_i32 s62, s64, 8320\n"
      "s_add_u32 s62, s62, s66\n"
      "s_add_u32 s62, s62, s67\n"
      "s_add_u32 s62, s62, 84\n"
      "v_add_u32 v212, s62, v10\n"
      "s_mul_i32 s62, s64, 4608\n"
      "s_sub_u32 s63, s62, s66\n"
      "s_add_u32 s63, s63, s67\n"
      "s_add_u32 s63, s63, 16928\n"
      "v_add_u32 v217, s63, v11\n"
      "s_lshl_b32 s62, s81, 5\n"
      "v_add_u32 v30, s62, v2\n"
      "s_waitcnt lgkmcnt(0)\n"
      "v_readfirstlane_b32 s2, v31\n"
      "s_cmp_lt_u32 s2, 4\n"
      "s_cbranch_scc1 .Lw_150\n"
      ".Lw_151:\n"
      "v_add_u32 v213, 1024, v212\n"
      "v_add_u32 v214, 2048, v212\n"
      "v_add_u32 v215, 3072, v212\n"
      "ds_read2_b32 v[144:145], v212 offset0:3 offset1:2\n"
      "ds_read2_b32 v[146:147], v212 offset0:1 offset1:0\n"
      "ds_read_b128 v[188:191], v217 offset:1728\n"
      "ds_read_b128 v[184:187], v217 offset:1216\n"
      "ds_read_b128 v[180:183], v217 offset:704\n"
      "ds_read_b128 v[176:179], v217 offset:192\n"
      "ds_read2_b32 v[148:149], v212 offset0:131 offset1:130\n"
      "ds_read2_b32 v[150:151], v212 offset0:129 offset1:128\n"
      "ds_read2_b32 v[152:153], v213 offset0:3 offset1:2\n"
      "ds_read2_b32 v[154:155], v213 offset0:1 offset1:0\n"
      "s_waitcnt lgkmcnt(7)\n"
      "v_mfma_f32_16x16x32_bf16 v[108:111], v[144:147], v[188:191], 0\n"
      "ds_read2_b32 v[156:157], v213 offset0:131 offset1:130\n"
      "s_waitcnt lgkmcnt(7)\n"
      "v_mfma_f32_16x16x32_bf16 v[104:107], v[144:147], v[184:187], 0\n"
      "ds_read2_b32 v[158:159], v213 offset0:129 offset1:128\n"
      "s_waitcnt lgkmcnt(7)\n"
      "v_mfma_f32_16x16x32_bf16 v[100:103], v[144:147], v[180:183], 0\n"
      "ds_read2_b32 v[160:161], v214 offset0:3 offset1:2\n"
      "v_lshlrev_b32 v9, 4, v1\n"
      "s_waitcnt lgkmcnt(7)\n"
      "v_mfma_f32_16x16x32_bf16 v[96:99], v[144:147], v[176:179], 0\n"
      "ds_read2_b32 v[162:163], v214 offset0:1 offset1:0\n"
      "s_and_b32 s63, s81, 1\n"
      "s_waitcnt lgkmcnt(6)\n"
      "v_mfma_f32_16x16x32_bf16 v[112:115], v[148:151], v[188:191], 0\n"
      "ds_read2_b32 v[164:165], v214 offset0:131 offset1:130\n"
      "s_mul_i32 s63, s63, 45056\n"
      "v_mfma_f32_16x16x32_bf16 v[108:111], v[148:151], v[184:187], v[108:111]\n"
      "ds_read2_b32 v[166:167], v214 offset0:129 offset1:128\n"
      "s_sub_u32 s63, 98304, s63\n"
      "v_mfma_f32_16x16x32_bf16 v[104:107], v[148:151], v[180:183], v[104:107]\n"
      "ds_read2_b32 v[168:169], v215 offset0:3 offset1:2\n"
      "s_lshl_b32 s62, s53, 12\n"
      "v_mfma_f32_16x16x32_bf16 v[100:103], v[148:151], v[176:179], v[100:103]\n"
      "ds_read2_b32 v[170:171], v215 offset0:1 offset1:0\n"
      "s_add_u32 s67, s62, s63\n"
      "s_waitcnt lgkmcnt(7)\n"
      "v_mfma_f32_16x16x32_bf16 v[116:119], v[152:155], v[188:191], 0\n"
      "ds_read2_b32 v[172:173], v215 offset0:131 offset1:130\n"
      "v_add_u32 v14, s67, v9\n"
      "v_mfma_f32_16x16x32_bf16 v[112:115], v[152:155], v[184:187], v[112:115]\n"
      "ds_read2_b32 v[174:175], v215 offset0:129 offset1:128\n"
      "s_xor_b32 s62, s62, 4096\n"
      "v_mfma_f32_16x16x32_bf16 v[108:111], v[152:155], v[180:183], v[108:111]\n"
      "ds_read2_b32 v[144:145], v212 offset0:19 offset1:18\n"
      "s_add_u32 s67, s62, s63\n"
      "v_mfma_f32_16x16x32_bf16 v[104:107], v[152:155], v[176:179], v[104:107]\n"
      "ds_read2_b32 v[146:147], v212 offset0:17 offset1:16\n"
      "v_add_u32 v15, s67, v9\n"
      "s_waitcnt lgkmcnt(7)\n"
      "v_mfma_f32_16x16x32_bf16 v[120:123], v[156:159], v[188:191], 0\n"
      "ds_read_b128 v[204:207], v217 offset:1664\n"
      "s_lshl_b32 s62, s53, 2\n"
      "v_mfma_f32_16x16x32_bf16 v[116:119], v[156:159], v[184:187], v[116:119]\n"
      "ds_read_b128 v[200:203], v217 offset:1152\n"
      "v_add_u32 v19, s62, v30\n"
      "v_mfma_f32_16x16x32_bf16 v[112:115], v[156:159], v[180:183], v[112:115]\n"
      "ds_read_b128 v[196:199], v217 offset:640\n"
      "s_xor_b32 s62, s62, 4\n"
      "v_mfma_f32_16x16x32_bf16 v[108:111], v[156:159], v[176:179], v[108:111]\n"
      "ds_read_b128 v[192:195], v217 offset:128\n"
      "v_add_u32 v20, s62, v30\n"
      "v_mfma_f32_16x16x32_bf16 v[124:127], v[160:163], v[188:191], 0\n"
      "ds_read2_b32 v[148:149], v212 offset0:147 offset1:146\n"
      "v_and_b32 v16, 15, v1\n"
      "v_mfma_f32_16x16x32_bf16 v[120:123], v[160:163], v[184:187], v[120:123]\n"
      "ds_read2_b32 v[150:151], v212 offset0:145 offset1:144\n"
      "v_lshrrev_b32 v17, 4, v1\n"
      "v_mfma_f32_16x16x32_bf16 v[116:119], v[160:163], v[180:183], v[116:119]\n"
      "ds_read2_b32 v[152:153], v213 offset0:19 offset1:18\n"
      "v_lshrrev_b32 v21, 5, v1\n"
      "v_mfma_f32_16x16x32_bf16 v[112:115], v[160:163], v[176:179], v[112:115]\n"
      "ds_read2_b32 v[154:155], v213 offset0:17 offset1:16\n"
      "v_lshlrev_b32 v21, 4, v21\n"
      "s_waitcnt lgkmcnt(7)\n"
      "v_mfma_f32_16x16x32_bf16 v[124:127], v[164:167], v[184:187], v[124:127]\n"
      "s_getreg_b32 s2, hwreg(HW_REG_IB_STS, 0, 4)\n"
      "s_add_u32 s2, s2, s84\n"
      "s_cmp_le_u32 s2, 4\n"
      "s_cbranch_scc1 .Lh_153\n"
      ".Lh_154:\n"
      "v_mfma_f32_16x16x32_bf16 v[120:123], v[164:167], v[180:183], v[120:123]\n"
      "ds_read2_b32 v[156:157], v213 offset0:147 offset1:146\n"
      "v_lshl_add_u32 v21, v16, 5, v21\n"
      "v_mfma_f32_16x16x32_bf16 v[116:119], v[164:167], v[176:179], v[116:119]\n"
      "ds_read2_b32 v[158:159], v213 offset0:145 offset1:144\n"
      "v_and_b32 v22, 1, v17\n"
      "v_mfma_f32_16x16x32_bf16 v[124:127], v[168:171], v[180:183], v[124:127]\n"
      "ds_read2_b32 v[160:161], v214 offset0:19 offset1:18\n"
      "v_lshl_add_u32 v5, v22, 9, v21\n"
      "v_mfma_f32_16x16x32_bf16 v[120:123], v[168:171], v[176:179], v[120:123]\n"
      "ds_read2_b32 v[162:163], v214 offset0:17 offset1:16\n"
      "s_lshl1_add_u32 s63, s81, s98\n"
      "v_mfma_f32_16x16x32_bf16 v[124:127], v[172:175], v[176:179], v[124:127]\n"
      "ds_read2_b32 v[164:165], v214 offset0:147 offset1:146\n"
      "s_add_u32 s63, s63, s64\n"
      "v_mfma_f32_16x16x32_bf16 v[108:111], v[144:147], v[204:207], v[108:111]\n"
      "ds_read2_b32 v[166:167], v214 offset0:145 offset1:144\n"
      "s_mul_i32 s63, s63, 4224\n"
      "s_waitcnt lgkmcnt(6)\n"
      "v_mfma_f32_16x16x32_bf16 v[104:107], v[144:147], v[200:203], v[104:107]\n"
      "ds_read2_b32 v[168:169], v215 offset0:19 offset1:18\n"
      "s_lshl_b32 s67, s65, 11\n"
      "v_mfma_f32_16x16x32_bf16 v[100:103], v[144:147], v[196:199], v[100:103]\n"
      "ds_read2_b32 v[170:171], v215 offset0:17 offset1:16\n"
      "s_add_u32 s63, s63, s67\n"
      "v_mfma_f32_16x16x32_bf16 v[96:99], v[144:147], v[192:195], v[96:99]\n"
      "ds_read2_b32 v[172:173], v215 offset0:147 offset1:146\n"
      "s_add_u32 s80, s63, 2801152\n"
      "v_mfma_f32_16x16x32_bf16 v[112:115], v[148:151], v[204:207], v[112:115]\n"
      "s_getreg_b32 s2, hwreg(HW_REG_IB_STS, 0, 4)\n"
      "s_add_u32 s2, s2, s85\n"
      "s_cmp_le_u32 s2, 0\n"
      "s_cbranch_scc1 .Lh_155\n"
      ".Lh_156:\n"
      "v_mfma_f32_16x16x32_bf16 v[108:111], v[148:151], v[200:203], v[108:111]\n"
      "ds_read2_b32 v[174:175], v215 offset0:145 offset1:144\n"
      "s_sub_u32 s82, 3, s53\n"
      "v_mfma_f32_16x16x32_bf16 v[104:107], v[148:151], v[196:199], v[104:107]\n"
      "ds_read2_b32 v[144:145], v212 offset0:35 offset1:34\n"
      "s_lshr_b32 s63, s82, 1\n"
      "v_mfma_f32_16x16x32_bf16 v[100:103], v[148:151], v[192:195], v[100:103]\n"
      "ds_read2_b32 v[146:147], v212 offset0:33 offset1:32\n"
      "s_lshl1_add_u32 s63, s54, s63\n"
      "v_mfma_f32_16x16x32_bf16 v[116:119], v[152:155], v[204:207], v[116:119]\n"
      "ds_read_b128 v[188:191], v217 offset:1600\n"
      "s_xor_b32 s63, s63, s99\n"
      "v_mfma_f32_16x16x32_bf16 v[112:115], v[152:155], v[200:203], v[112:115]\n"
      "ds_read_b128 v[184:187], v217 offset:1088\n"
      "s_lshl_b32 s63, s63, 10\n"
      "v_mfma_f32_16x16x32_bf16 v[108:111], v[152:155], v[196:199], v[108:111]\n"
      "ds_read_b128 v[180:183], v217 offset:576\n"
      "s_and_b32 s67, s82, 1\n"
      "v_mfma_f32_16x16x32_bf16 v[104:107], v[152:155], v[192:195], v[104:107]\n"
      "ds_read_b128 v[176:179], v217 offset:64\n"
      "s_lshl_b32 s67, s67, 9\n"
      "s_waitcnt lgkmcnt(7)\n"
      "v_mfma_f32_16x16x32_bf16 v[120:123], v[156:159], v[204:207], v[120:123]\n"
      "ds_read2_b32 v[148:149], v212 offset0:163 offset1:162\n"
      "s_add_u32 s63, s63, s67\n"
      "v_mfma_f32_16x16x32_bf16 v[116:119], v[156:159], v[200:203], v[116:119]\n"
      "ds_read2_b32 v[150:151], v212 offset0:161 offset1:160\n"
      "s_add_u32 s63, s63, 157716\n"
      "v_mfma_f32_16x16x32_bf16 v[112:115], v[156:159], v[196:199], v[112:115]\n"
      "ds_read2_b32 v[152:153], v213 offset0:35 offset1:34\n"
      "v_lshl_add_u32 v21, v16, 5, s63\n"
      "v_mfma_f32_16x16x32_bf16 v[108:111], v[156:159], v[192:195], v[108:111]\n"
      "ds_read2_b32 v[154:155], v213 offset0:33 offset1:32\n"
      "ds_read_b32 v23, v21\n"
      "v_mfma_f32_16x16x32_bf16 v[124:127], v[160:163], v[204:207], v[124:127]\n"
      "s_cmp_eq_u32 s84, 0\n"
      "s_cbranch_scc1 .Lh_157\n"
      ".Lh_158:\n"
      "v_mfma_f32_16x16x32_bf16 v[120:123], v[160:163], v[200:203], v[120:123]\n"
      "ds_read2_b32 v[156:157], v213 offset0:163 offset1:162\n"
      "s_lshl1_add_u32 s62, s56, s54\n"
      "v_mfma_f32_16x16x32_bf16 v[116:119], v[160:163], v[196:199], v[116:119]\n"
      "ds_read2_b32 v[158:159], v213 offset0:161 offset1:160\n"
      "s_lshl2_add_u32 s63, s62, s82\n"
      "v_mfma_f32_16x16x32_bf16 v[112:115], v[160:163], v[192:195], v[112:115]\n"
      "ds_read2_b32 v[160:161], v214 offset0:35 offset1:34\n"
      "s_mul_i32 s70, s63, 20480\n"
      "v_mfma_f32_16x16x32_bf16 v[124:127], v[164:167], v[200:203], v[124:127]\n"
      "s_getreg_b32 s2, hwreg(HW_REG_IB_STS, 0, 4)\n"
      "s_add_u32 s2, s2, s85\n"
      "s_cmp_le_u32 s2, 0\n"
      "s_cbranch_scc1 .Lh_159\n"
      ".Lh_160:\n"
      "v_mfma_f32_16x16x32_bf16 v[120:123], v[164:167], v[196:199], v[120:123]\n"
      "ds_read2_b32 v[162:163], v214 offset0:33 offset1:32\n"
      "s_mul_i32 s63, s55, 160\n"
      "v_mfma_f32_16x16x32_bf16 v[116:119], v[164:167], v[192:195], v[116:119]\n"
      "ds_read2_b32 v[164:165], v214 offset0:163 offset1:162\n"
      "s_add_u32 s70, s70, s63\n"
      "v_mfma_f32_16x16x32_bf16 v[124:127], v[168:171], v[196:199], v[124:127]\n"
      "ds_read2_b32 v[166:167], v214 offset0:161 offset1:160\n"
      "v_mul_u32_u24 v4, 1280, v16\n"
      "v_mfma_f32_16x16x32_bf16 v[120:123], v[168:171], v[192:195], v[120:123]\n"
      "v_lshl_add_u32 v4, v17, 3, v4\n"
      "s_mul_i32 s63, s53, 5120\n"
      "s_waitcnt lgkmcnt(4)\n"
      "v_mfma_f32_16x16x32_bf16 v[124:127], v[172:175], v[192:195], v[124:127]\n"
      "ds_read2_b32 v[168:169], v215 offset0:35 offset1:34\n"
      "s_lshl2_add_u32 s72, s60, s53\n"
      "v_mfma_f32_16x16x32_bf16 v[108:111], v[144:147], v[188:191], v[108:111]\n"
      "ds_read2_b32 v[170:171], v215 offset0:33 offset1:32\n"
      "s_mul_i32 s72, s72, 3072\n"
      "v_mfma_f32_16x16x32_bf16 v[104:107], v[144:147], v[184:187], v[104:107]\n"
      "ds_read2_b32 v[172:173], v215 offset0:163 offset1:162\n"
      "s_add_u32 s72, s72, 15237120\n"
      "v_mfma_f32_16x16x32_bf16 v[100:103], v[144:147], v[180:183], v[100:103]\n"
      "ds_read2_b32 v[174:175], v215 offset0:161 offset1:160\n"
      "s_add_u32 s63, s63, 53248\n"
      "v_mfma_f32_16x16x32_bf16 v[96:99], v[144:147], v[176:179], v[96:99]\n"
      "ds_read2_b32 v[144:145], v212 offset0:51 offset1:50\n"
      "v_add_u32 v8, s63, v9\n"
      "v_mfma_f32_16x16x32_bf16 v[112:115], v[148:151], v[188:191], v[112:115]\n"
      "ds_read2_b32 v[146:147], v212 offset0:49 offset1:48\n"
      "s_lshl_b32 s74, s93, 16\n"
      "v_mfma_f32_16x16x32_bf16 v[108:111], v[148:151], v[184:187], v[108:111]\n"
      "ds_read_b128 v[204:207], v217 offset:1536\n"
      "s_lshl2_add_u32 s63, s55, s53\n"
      "v_mfma_f32_16x16x32_bf16 v[104:107], v[148:151], v[180:183], v[104:107]\n"
      "ds_read_b128 v[200:203], v217 offset:1024\n"
      "s_mul_i32 s73, s63, 84480\n"
      "v_mfma_f32_16x16x32_bf16 v[100:103], v[148:151], v[176:179], v[100:103]\n"
      "ds_read_b128 v[196:199], v217 offset:512\n"
      "s_lshl_b32 s62, s62, 7\n"
      "v_mfma_f32_16x16x32_bf16 v[116:119], v[152:155], v[188:191], v[116:119]\n"
      "ds_read_b128 v[192:195], v217 offset:0\n"
      "s_add_u32 s73, s73, s62\n"
      "v_mfma_f32_16x16x32_bf16 v[112:115], v[152:155], v[184:187], v[112:115]\n"
      "ds_read2_b32 v[148:149], v212 offset0:179 offset1:178\n"
      "s_add_u32 s73, s73, 2818048\n"
      "v_mfma_f32_16x16x32_bf16 v[108:111], v[152:155], v[180:183], v[108:111]\n"
      "ds_read2_b32 v[150:151], v212 offset0:177 offset1:176\n"
      "s_mul_i32 s63, s63, 10\n"
      "v_mfma_f32_16x16x32_bf16 v[104:107], v[152:155], v[176:179], v[104:107]\n"
      "ds_read2_b32 v[152:153], v213 offset0:51 offset1:50\n"
      "v_min_u32 v21, 9, v1\n"
      "v_mfma_f32_16x16x32_bf16 v[120:123], v[156:159], v[188:191], v[120:123]\n"
      "ds_read2_b32 v[154:155], v213 offset0:49 offset1:48\n"
      "v_add_u32 v21, s63, v21\n"
      "v_mfma_f32_16x16x32_bf16 v[116:119], v[156:159], v[184:187], v[116:119]\n"
      "v_add_u32 v22, 448, v21\n"
      "v_mul_u32_u24 v22, 21846, v22\n"
      "v_mfma_f32_16x16x32_bf16 v[112:115], v[156:159], v[180:183], v[112:115]\n"
      "s_getreg_b32 s2, hwreg(HW_REG_IB_STS, 0, 4)\n"
      "s_add_u32 s2, s2, s85\n"
      "s_cmp_le_u32 s2, 0\n"
      "s_cbranch_scc1 .Lh_161\n"
      ".Lh_162:\n"
      "v_mfma_f32_16x16x32_bf16 v[108:111], v[156:159], v[176:179], v[108:111]\n"
      "ds_read2_b32 v[156:157], v213 offset0:179 offset1:178\n"
      "v_lshrrev_b32 v22, 16, v22\n"
      "s_waitcnt lgkmcnt(4)\n"
      "v_mfma_f32_16x16x32_bf16 v[124:127], v[160:163], v[188:191], v[124:127]\n"
      "ds_read2_b32 v[158:159], v213 offset0:177 offset1:176\n"
      "v_min_u32 v21, v21, v22\n"
      "v_mfma_f32_16x16x32_bf16 v[120:123], v[160:163], v[184:187], v[120:123]\n"
      "s_mov_b32 s63, 18864128\n"
      "v_lshl_add_u32 v25, v21, 7, s63\n"
      "v_mfma_f32_16x16x32_bf16 v[116:119], v[160:163], v[180:183], v[116:119]\n"
      "v_and_b32 v21, 7, v1\n"
      "v_lshrrev_b32 v22, 3, v1\n"
      "v_mfma_f32_16x16x32_bf16 v[112:115], v[160:163], v[176:179], v[112:115]\n"
      "ds_read2_b32 v[160:161], v214 offset0:51 offset1:50\n"
      "v_mul_u32_u24 v6, 4224, v22\n"
      "v_mfma_f32_16x16x32_bf16 v[124:127], v[164:167], v[184:187], v[124:127]\n"
      "ds_read2_b32 v[162:163], v214 offset0:49 offset1:48\n"
      "v_lshl_add_u32 v6, v21, 4, v6\n"
      "v_mfma_f32_16x16x32_bf16 v[120:123], v[164:167], v[180:183], v[120:123]\n"
      "s_lshl_b32 s62, s82, 5\n"
      "s_add_u32 s62, s62, 139520\n"
      "v_mfma_f32_16x16x32_bf16 v[116:119], v[164:167], v[176:179], v[116:119]\n"
      "ds_read2_b32 v[164:165], v214 offset0:179 offset1:178\n"
      "v_lshrrev_b32 v21, 2, v1\n"
      "v_mfma_f32_16x16x32_bf16 v[124:127], v[168:171], v[180:183], v[124:127]\n"
      "ds_read2_b32 v[166:167], v214 offset0:177 offset1:176\n"
      "v_lshl_add_u32 v21, v21, 7, s62\n"
      "v_mfma_f32_16x16x32_bf16 v[120:123], v[168:171], v[176:179], v[120:123]\n"
      "v_and_b32 v22, 3, v1\n"
      "v_lshl_add_u32 v7, v22, 3, v21\n"
      "v_mfma_f32_16x16x32_bf16 v[124:127], v[172:175], v[176:179], v[124:127]\n"
      "ds_read2_b32 v[168:169], v215 offset0:51 offset1:50\n"
      "s_mov_b32 s76, 0xbfb8aa3b\n"
      "v_mfma_f32_16x16x32_bf16 v[108:111], v[144:147], v[204:207], v[108:111]\n"
      "ds_read2_b32 v[170:171], v215 offset0:49 offset1:48\n"
      "s_mov_b32 s77, 0xbf317218\n"
      "v_mfma_f32_16x16x32_bf16 v[104:107], v[144:147], v[200:203], v[104:107]\n"
      "ds_read2_b32 v[172:173], v215 offset0:179 offset1:178\n"
      "v_and_b32 v21, s92, v23\n"
      "v_mfma_f32_16x16x32_bf16 v[100:103], v[144:147], v[196:199], v[100:103]\n"
      "ds_read2_b32 v[174:175], v215 offset0:177 offset1:176\n"
      "v_mfma_f32_16x16x32_bf16 v[96:99], v[144:147], v[192:195], v[96:99]\n"
      "ds_read2_b32 v[144:145], v212 offset0:3 offset1:2\n"
      "s_waitcnt lgkmcnt(6)\n"
      "v_mfma_f32_16x16x32_bf16 v[112:115], v[148:151], v[204:207], v[112:115]\n"
      "ds_read2_b32 v[146:147], v212 offset0:1 offset1:0\n"
      "v_mfma_f32_16x16x32_bf16 v[108:111], v[148:151], v[200:203], v[108:111]\n"
      "v_rcp_f32 v21, v21\n"
      "v_mfma_f32_16x16x32_bf16 v[104:107], v[148:151], v[196:199], v[104:107]\n"
      "s_getreg_b32 s2, hwreg(HW_REG_IB_STS, 0, 4)\n"
      "s_add_u32 s2, s2, s85\n"
      "s_cmp_le_u32 s2, 0\n"
      "s_cbranch_scc1 .Lh_163\n"
      ".Lh_164:\n"
      "v_mfma_f32_16x16x32_bf16 v[100:103], v[148:151], v[192:195], v[100:103]\n"
      "ds_read2_b32 v[148:149], v212 offset0:131 offset1:130\n"
      "v_lshlrev_b32 v22, 16, v23\n"
      "v_mfma_f32_16x16x32_bf16 v[116:119], v[152:155], v[204:207], v[116:119]\n"
      "ds_read_b32 v31, v2 offset:4280\n"
      "v_mul_f32 v21, s77, v21\n"
      "v_mfma_f32_16x16x32_bf16 v[112:115], v[152:155], v[200:203], v[112:115]\n"
      "ds_read2_b32 v[150:151], v212 offset0:129 offset1:128\n"
      "v_mul_f32 v22, v22, v21\n"
      "v_mfma_f32_16x16x32_bf16 v[108:111], v[152:155], v[196:199], v[108:111]\n"
      "v_lshlrev_b32 v32, 16, v218\n"
      "v_and_b32 v33, s92, v218\n"
      "v_mfma_f32_16x16x32_bf16 v[104:107], v[152:155], v[192:195], v[104:107]\n"
      "ds_read2_b32 v[152:153], v213 offset0:3 offset1:2\n"
      "v_lshlrev_b32 v34, 16, v219\n"
      "v_mfma_f32_16x16x32_bf16 v[120:123], v[156:159], v[204:207], v[120:123]\n"
      "ds_read2_b32 v[154:155], v213 offset0:1 offset1:0\n"
      "v_and_b32 v35, s92, v219\n"
      "v_mfma_f32_16x16x32_bf16 v[116:119], v[156:159], v[200:203], v[116:119]\n"
      "v_lshlrev_b32 v36, 16, v220\n"
      "v_and_b32 v37, s92, v220\n"
      "v_mfma_f32_16x16x32_bf16 v[112:115], v[156:159], v[196:199], v[112:115]\n"
      "s_waitcnt lgkmcnt(3)\n"
      "v_readfirstlane_b32 s2, v31\n"
      "s_cmp_lt_u32 s2, 4\n"
      "s_cbranch_scc1 .Lw_165\n"
      ".Lw_166:\n"
      "v_mfma_f32_16x16x32_bf16 v[108:111], v[156:159], v[192:195], v[108:111]\n"
      "ds_read2_b32 v[156:157], v213 offset0:131 offset1:130\n"
      "v_lshlrev_b32 v38, 16, v221\n"
      "v_mfma_f32_16x16x32_bf16 v[124:127], v[160:163], v[204:207], v[124:127]\n"
      "ds_read_b128 v[204:207], v217 offset:3776\n"
      "v_and_b32 v39, s92, v221\n"
      "v_mfma_f32_16x16x32_bf16 v[120:123], v[160:163], v[200:203], v[120:123]\n"
      "ds_read2_b32 v[158:159], v213 offset0:129 offset1:128\n"
      "v_lshlrev_b32 v40, 16, v222\n"
      "v_mfma_f32_16x16x32_bf16 v[116:119], v[160:163], v[196:199], v[116:119]\n"
      "ds_read_b128 v[188:191], v217 offset:3712\n"
      "v_and_b32 v41, s92, v222\n"
      "v_mfma_f32_16x16x32_bf16 v[112:115], v[160:163], v[192:195], v[112:115]\n"
      "ds_read2_b32 v[160:161], v212 offset0:19 offset1:18\n"
      "v_lshlrev_b32 v42, 16, v223\n"
      "v_mfma_f32_16x16x32_bf16 v[124:127], v[164:167], v[200:203], v[124:127]\n"
      "ds_read_b128 v[200:203], v217 offset:3264\n"
      "v_and_b32 v43, s92, v223\n"
      "v_mfma_f32_16x16x32_bf16 v[120:123], v[164:167], v[196:199], v[120:123]\n"
      "ds_read2_b32 v[162:163], v212 offset0:17 offset1:16\n"
      "v_lshlrev_b32 v44, 16, v224\n"
      "v_mfma_f32_16x16x32_bf16 v[116:119], v[164:167], v[192:195], v[116:119]\n"
      "ds_read_b128 v[184:187], v217 offset:3200\n"
      "v_and_b32 v45, s92, v224\n"
      "v_mfma_f32_16x16x32_bf16 v[124:127], v[168:171], v[196:199], v[124:127]\n"
      "ds_read_b128 v[196:199], v217 offset:2752\n"
      "v_lshlrev_b32 v46, 16, v225\n"
      "v_mfma_f32_16x16x32_bf16 v[120:123], v[168:171], v[192:195], v[120:123]\n"
      "s_cmp_eq_u32 s85, 0\n"
      "s_cbranch_scc1 .Lh_168\n"
      ".Lh_169:\n"
      "v_mfma_f32_16x16x32_bf16 v[124:127], v[172:175], v[192:195], v[124:127]\n"
      "ds_read_b128 v[192:195], v217 offset:2240\n"
      "v_and_b32 v47, s92, v225\n"
      "s_waitcnt lgkmcnt(6)\n"
      "v_mfma_f32_16x16x32_bf16 v[124:127], v[144:147], v[204:207], v[124:127]\n"
      "ds_read_b128 v[180:183], v217 offset:2688\n"
      "v_lshlrev_b32 v48, 16, v226\n"
      "s_waitcnt lgkmcnt(5)\n"
      "v_mfma_f32_16x16x32_bf16 v[120:123], v[144:147], v[200:203], v[120:123]\n"
      "ds_read_b128 v[176:179], v217 offset:2176\n"
      "v_and_b32 v49, s92, v226\n"
      "s_waitcnt lgkmcnt(3)\n"
      "v_mfma_f32_16x16x32_bf16 v[116:119], v[144:147], v[196:199], v[116:119]\n"
      "ds_read2_b32 v[164:165], v212 offset0:147 offset1:146\n"
      "v_lshlrev_b32 v50, 16, v227\n"
      "s_waitcnt lgkmcnt(3)\n"
      "v_mfma_f32_16x16x32_bf16 v[112:115], v[144:147], v[192:195], v[112:115]\n"
      "ds_read2_b32 v[166:167], v212 offset0:145 offset1:144\n"
      "v_and_b32 v51, s92, v227\n"
      "v_mfma_f32_16x16x32_bf16 v[124:127], v[148:151], v[200:203], v[124:127]\n"
      "ds_read2_b32 v[168:169], v213 offset0:19 offset1:18\n"
      "v_fma_f32 v52, v32, v21, v22\n"
      "v_mfma_f32_16x16x32_bf16 v[120:123], v[148:151], v[196:199], v[120:123]\n"
      "ds_read2_b32 v[170:171], v213 offset0:17 offset1:16\n"
      "v_fma_f32 v53, v33, v21, v22\n"
      "v_mfma_f32_16x16x32_bf16 v[116:119], v[148:151], v[192:195], v[116:119]\n"
      "ds_read2_b32 v[172:173], v213 offset0:147 offset1:146\n"
      "v_fma_f32 v54, v34, v21, v22\n"
      "v_mfma_f32_16x16x32_bf16 v[124:127], v[152:155], v[196:199], v[124:127]\n"
      "ds_read2_b32 v[174:175], v213 offset0:145 offset1:144\n"
      "v_fma_f32 v55, v35, v21, v22\n"
      "v_mfma_f32_16x16x32_bf16 v[120:123], v[152:155], v[192:195], v[120:123]\n"
      "ds_read_b32 v31, v2 offset:4276\n"
      "v_fma_f32 v56, v36, v21, v22\n"
      "v_mfma_f32_16x16x32_bf16 v[124:127], v[156:159], v[192:195], v[124:127]\n"
      "ds_read2_b32 v[144:145], v212 offset0:35 offset1:34\n"
      "ds_read2_b32 v[146:147], v212 offset0:33 offset1:32\n"
      "v_mfma_f32_16x16x32_bf16 v[124:127], v[160:163], v[188:191], v[124:127]\n"
      "ds_read_b128 v[204:207], v217 offset:3648\n"
      "ds_read_b128 v[200:203], v217 offset:3136\n"
      "v_mfma_f32_16x16x32_bf16 v[120:123], v[160:163], v[184:187], v[120:123]\n"
      "ds_read_b128 v[196:199], v217 offset:2624\n"
      "v_fma_f32 v57, v37, v21, v22\n"
      "s_waitcnt lgkmcnt(9)\n"
      "v_mfma_f32_16x16x32_bf16 v[116:119], v[160:163], v[180:183], v[116:119]\n"
      "ds_read_b128 v[192:195], v217 offset:2112\n"
      "v_fma_f32 v58, v38, v21, v22\n"
      "v_mfma_f32_16x16x32_bf16 v[112:115], v[160:163], v[176:179], v[112:115]\n"
      "ds_read2_b32 v[148:149], v212 offset0:163 offset1:162\n"
      "ds_read2_b32 v[150:151], v212 offset0:161 offset1:160\n"
      "v_mfma_f32_16x16x32_bf16 v[124:127], v[164:167], v[184:187], v[124:127]\n"
      "ds_read2_b32 v[152:153], v213 offset0:35 offset1:34\n"
      "v_fma_f32 v59, v39, v21, v22\n"
      "v_mfma_f32_16x16x32_bf16 v[120:123], v[164:167], v[180:183], v[120:123]\n"
      "ds_read2_b32 v[154:155], v213 offset0:33 offset1:32\n"
      "v_fma_f32 v60, v40, v21, v22\n"
      "v_mfma_f32_16x16x32_bf16 v[116:119], v[164:167], v[176:179], v[116:119]\n"
      "s_waitcnt lgkmcnt(8)\n"
      "v_readfirstlane_b32 s2, v31\n"
      "s_cmp_lt_u32 s2, 4\n"
      "s_cbranch_scc1 .Lw_170\n"
      ".Lw_171:\n"
      "v_mfma_f32_16x16x32_bf16 v[124:127], v[168:171], v[180:183], v[124:127]\n"
      "ds_read2_b32 v[156:157], v213 offset0:163 offset1:162\n"
      "v_fma_f32 v61, v41, v21, v22\n"
      "v_mfma_f32_16x16x32_bf16 v[120:123], v[168:171], v[176:179], v[120:123]\n"
      "ds_read2_b32 v[158:159], v213 offset0:161 offset1:160\n"
      "v_fma_f32 v62, v42, v21, v22\n"
      "v_mfma_f32_16x16x32_bf16 v[124:127], v[172:175], v[176:179], v[124:127]\n"
      "ds_read2_b32 v[160:161], v212 offset0:51 offset1:50\n"
      "ds_read2_b32 v[162:163], v212 offset0:49 offset1:48\n"
      "s_waitcnt lgkmcnt(7)\n"
      "v_mfma_f32_16x16x32_bf16 v[124:127], v[144:147], v[204:207], v[124:127]\n"
      "ds_read_b128 v[188:191], v217 offset:3584\n"
      "ds_read_b128 v[184:187], v217 offset:3072\n"
      "v_mfma_f32_16x16x32_bf16 v[120:123], v[144:147], v[200:203], v[120:123]\n"
      "ds_read_b128 v[180:183], v217 offset:2560\n"
      "v_fma_f32 v63, v43, v21, v22\n"
      "v_mfma_f32_16x16x32_bf16 v[116:119], v[144:147], v[196:199], v[116:119]\n"
      "ds_read_b128 v[176:179], v217 offset:2048\n"
      "v_fma_f32 v64, v44, v21, v22\n"
      "v_mfma_f32_16x16x32_bf16 v[112:115], v[144:147], v[192:195], v[112:115]\n"
      "ds_read2_b32 v[164:165], v212 offset0:179 offset1:178\n"
      "ds_read2_b32 v[166:167], v212 offset0:177 offset1:176\n"
      "s_waitcnt lgkmcnt(10)\n"
      "v_mfma_f32_16x16x32_bf16 v[124:127], v[148:151], v[200:203], v[124:127]\n"
      "ds_read2_b32 v[168:169], v213 offset0:51 offset1:50\n"
      "v_fma_f32 v65, v45, v21, v22\n"
      "v_mfma_f32_16x16x32_bf16 v[120:123], v[148:151], v[196:199], v[120:123]\n"
      "ds_read2_b32 v[170:171], v213 offset0:49 offset1:48\n"
      "v_fma_f32 v66, v46, v21, v22\n"
      "v_mfma_f32_16x16x32_bf16 v[116:119], v[148:151], v[192:195], v[116:119]\n"
      "ds_read2_b32 v[172:173], v213 offset0:179 offset1:178\n"
      "v_fma_f32 v67, v47, v21, v22\n"
      "v_mfma_f32_16x16x32_bf16 v[124:127], v[152:155], v[196:199], v[124:127]\n"
      "ds_read2_b32 v[174:175], v213 offset0:177 offset1:176\n"
      "v_fma_f32 v68, v48, v21, v22\n"
      "v_mfma_f32_16x16x32_bf16 v[120:123], v[152:155], v[192:195], v[120:123]\n"
      "v_fma_f32 v69, v49, v21, v22\n"
      "v_fma_f32 v70, v50, v21, v22\n"
      "s_waitcnt lgkmcnt(7)\n"
      "v_mfma_f32_16x16x32_bf16 v[124:127], v[156:159], v[192:195], v[124:127]\n"
      "v_fma_f32 v71, v51, v21, v22\n"
      "v_mfma_f32_16x16x32_bf16 v[124:127], v[160:163], v[188:191], v[124:127]\n"
      "v_mfma_f32_16x16x32_bf16 v[120:123], v[160:163], v[184:187], v[120:123]\n"
      "v_mfma_f32_16x16x32_bf16 v[116:119], v[160:163], v[180:183], v[116:119]\n"
      "s_waitcnt lgkmcnt(2)\n"
      "v_mfma_f32_16x16x32_bf16 v[112:115], v[160:163], v[176:179], v[112:115]\n"
      "v_mfma_f32_16x16x32_bf16 v[124:127], v[164:167], v[184:187], v[124:127]\n"
      "v_mfma_f32_16x16x32_bf16 v[120:123], v[164:167], v[180:183], v[120:123]\n"
      "v_mfma_f32_16x16x32_bf16 v[116:119], v[164:167], v[176:179], v[116:119]\n"
      "v_mfma_f32_16x16x32_bf16 v[124:127], v[168:171], v[180:183], v[124:127]\n"
      "v_mfma_f32_16x16x32_bf16 v[120:123], v[168:171], v[176:179], v[120:123]\n"
      "s_waitcnt lgkmcnt(0)\n"
      "v_mfma_f32_16x16x32_bf16 v[124:127], v[172:175], v[176:179], v[124:127]\n"
      "s_setprio 3\n"
      "s_cmp_eq_u32 s65, 0\n"
      "s_cbranch_scc1 .Lx_173\n"
      "ds_write_b128 v14, v[96:99] offset:0\n"
      "ds_write_b128 v14, v[100:103] offset:1024\n"
      "ds_write_b128 v14, v[104:107] offset:2048\n"
      "ds_write_b128 v14, v[108:111] offset:3072\n"
      "ds_add_u32 v19, v3 offset:4248\n"
      "v_mov_b64 v[96:97], v[112:113]\n"
      "v_mov_b64 v[98:99], v[114:115]\n"
      "v_mov_b64 v[100:101], v[116:117]\n"
      "v_mov_b64 v[102:103], v[118:119]\n"
      "v_mov_b64 v[104:105], v[120:121]\n"
      "v_mov_b64 v[106:107], v[122:123]\n"
      "v_mov_b64 v[108:109], v[124:125]\n"
      "v_mov_b64 v[110:111], v[126:127]\n"
      "s_branch .Lx_174\n"
      ".Lx_173:\n"
      "ds_write_b128 v14, v[112:115] offset:0\n"
      "ds_write_b128 v14, v[116:119] offset:1024\n"
      "s_nop 0\n"
      "ds_write_b128 v14, v[120:123] offset:2048\n"
      "s_nop 0\n"
      "ds_write_b128 v14, v[124:127] offset:3072\n"
      "ds_add_u32 v19, v3 offset:4248\n"
      ".Lx_174:\n"
      "s_mov_b32 s3, 0x80000\n"
      ".Lp_175:\n"
      "ds_read_b32 v31, v20 offset:4248\n"
      "s_sub_u32 s3, s3, 1\n"
      "s_waitcnt lgkmcnt(0)\n"
      "v_readlane_b32 s2, v31, 0\n"
      "s_cselect_b32 s2, -1, s2\n"
      "s_cmp_lt_u32 s2, 1\n"
      "s_cbranch_scc1 .Lp_175\n"
      "ds_read_b128 v[72:75], v15 offset:0\n"
      "ds_read_b128 v[76:79], v15 offset:1024\n"
      "ds_read_b128 v[80:83], v15 offset:2048\n"
      "ds_read_b128 v[84:87], v15 offset:3072\n"
      "ds_add_u32 v30, v3 offset:4244\n"
      "s_waitcnt lgkmcnt(3)\n"
      "v_add_f32 v96, v96, v72\n"
      "v_add_f32 v97, v97, v73\n"
      "v_add_f32 v98, v98, v74\n"
      "v_add_f32 v99, v99, v75\n"
      "v_add_f32 v100, v100, v76\n"
      "v_add_f32 v101, v101, v77\n"
      "v_add_f32 v102, v102, v78\n"
      "v_add_f32 v103, v103, v79\n"
      "v_cvt_pk_bf16_f32 v88, v96, v97\n"
      "v_cvt_pk_bf16_f32 v90, v100, v101\n"
      "v_cvt_pk_bf16_f32 v89, v98, v99\n"
      "v_cvt_pk_bf16_f32 v91, v102, v103\n"
      "v_permlane16_swap_b32 v88, v90\n"
      "s_nop 0\n"
      "v_permlane16_swap_b32 v89, v91\n"
      "buffer_store_dwordx4 v[88:91], v5, s[20:23], s80 offen offset:0 sc1\n"
      "s_waitcnt lgkmcnt(1)\n"
      "v_add_f32 v104, v104, v80\n"
      "v_add_f32 v105, v105, v81\n"
      "v_add_f32 v106, v106, v82\n"
      "v_add_f32 v107, v107, v83\n"
      "v_add_f32 v108, v108, v84\n"
      "v_add_f32 v109, v109, v85\n"
      "v_add_f32 v110, v110, v86\n"
      "v_add_f32 v111, v111, v87\n"
      "v_cvt_pk_bf16_f32 v92, v104, v105\n"
      "v_cvt_pk_bf16_f32 v94, v108, v109\n"
      "v_cvt_pk_bf16_f32 v93, v106, v107\n"
      "v_cvt_pk_bf16_f32 v95, v110, v111\n"
      "v_permlane16_swap_b32 v92, v94\n"
      "s_nop 0\n"
      "v_permlane16_swap_b32 v93, v95\n"
      "buffer_store_dwordx4 v[92:95], v5, s[20:23], s80 offen offset:1024 sc1\n"
      "s_lshl_b32 s62, s60, 7\n"
      "s_add_u32 s62, s62, 18864128\n"
      "s_waitcnt vmcnt(0)\n"
      "ds_add_rtn_u32 v31, v2, v3 offset:4172\n"
      "s_waitcnt lgkmcnt(0)\n"
      "v_readfirstlane_b32 s2, v31\n"
      "s_cmp_eq_u32 s2, 3\n"
      "s_cselect_b32 s62, s62, 1073741824\n"
      "s_mov_b64 exec, 255\n"
      "buffer_store_dwordx4 v[12:15], v9, s[20:23], s62 offen sc1\n"
      "s_mov_b64 exec, -1\n"
      "ds_read_b32 v31, v2 offset:4308\n"
      "s_waitcnt lgkmcnt(0)\n"
      "v_readfirstlane_b32 s2, v31\n"
      "s_cmp_lt_u32 s2, 4\n"
      "s_cbranch_scc1 .Lw_176\n"
      ".Lw_177:\n"
      "v_and_b32 v24, 1, v17\n"
      "v_mul_u32_u24 v24, 24, v24\n"
      "v_add_u32 v24, v24, v4\n"
      "s_mul_i32 s62, s53, 2560\n"
      "s_add_u32 s63, s73, 32768\n"
      "s_add_u32 s64, s73, 65536\n"
      "s_add_u32 m0, s62, 139520\n"
      "s_mov_b32 s3, 0x80000\n"
      ".Lp_179:\n"
      "s_sleep 1\n"
      "ds_read_b32 v31, v2 offset:4192\n"
      "s_sub_u32 s3, s3, 1\n"
      "s_waitcnt lgkmcnt(0)\n"
      "v_readlane_b32 s2, v31, s53\n"
      "s_cselect_b32 s2, -1, s2\n"
      "s_cmp_lt_u32 s2, 16\n"
      "s_cbranch_scc1 .Lp_179\n"
      "buffer_load_dwordx2 v[26:27], v25, s[20:23], 0 offen sc1\n"
      "s_waitcnt vmcnt(0)\n"
      "v_cmp_ne_u64 vcc, s[58:59], v[26:27]\n"
      "s_cbranch_vccnz .Lf_180\n"
      ".Lf_181:\n"
      "buffer_load_dwordx4 v6, s[20:23], s73 offen sc1 lds\n"
      "buffer_load_dwordx4 v6, s[20:23], s63 offen offset:1024 sc1 lds\n"
      "s_mov_b32 exec_hi, 0\n"
      "buffer_load_dwordx4 v6, s[20:23], s64 offen offset:2048 sc1 lds\n"
      "s_mov_b32 exec_hi, -1\n"
      "s_mov_b32 s3, 0x80000\n"
      ".Lp_183:\n"
      "s_sleep 1\n"
      "ds_read_b32 v31, v2 offset:4192\n"
      "s_sub_u32 s3, s3, 1\n"
      "s_waitcnt lgkmcnt(0)\n"
      "v_readlane_b32 s2, v31, s53\n"
      "s_cselect_b32 s2, -1, s2\n"
      "s_cmp_lt_u32 s2, 17\n"
      "s_cbranch_scc1 .Lp_183\n"
      "buffer_load_dwordx4 v[164:167], v9, s[20:23], s72 offen offset:0 sc1\n"
      "buffer_load_dwordx4 v[168:171], v9, s[20:23], s72 offen offset:1024 sc1\n"
      "buffer_load_dwordx4 v[172:175], v9, s[20:23], s72 offen offset:2048 sc1\n"
      "s_waitcnt vmcnt(3)\n"
      "ds_add_u32 v2, v3 offset:4140\n"
      "s_mov_b32 s3, 0x80000\n"
      ".Lp_184:\n"
      "ds_read_b32 v31, v2 offset:4140\n"
      "s_sub_u32 s3, s3, 1\n"
      "s_waitcnt lgkmcnt(0)\n"
      "v_readlane_b32 s2, v31, 0\n"
      "s_cselect_b32 s2, -1, s2\n"
      "s_cmp_lt_u32 s2, 4\n"
      "s_cbranch_scc1 .Lp_184\n"
      "ds_read_b64_tr_b16 v[218:219], v7 offset:0\n"
      "ds_read_b64_tr_b16 v[220:221], v7 offset:2048\n"
      "ds_read_b64_tr_b16 v[222:223], v7 offset:4096\n"
      "ds_read_b64_tr_b16 v[224:225], v7 offset:6144\n"
      "ds_read_b64_tr_b16 v[226:227], v7 offset:8192\n"
      "s_waitcnt lgkmcnt(3)\n"
      "v_lshlrev_b32 v21, 16, v218\n"
      "v_and_b32 v22, s92, v218\n"
      "v_mul_f32 v52, v52, v21\n"
      "v_mul_f32 v53, v53, v22\n"
      "v_lshlrev_b32 v21, 16, v219\n"
      "v_and_b32 v22, s92, v219\n"
      "v_mul_f32 v54, v54, v21\n"
      "v_mul_f32 v55, v55, v22\n"
      "v_lshlrev_b32 v21, 16, v220\n"
      "v_and_b32 v22, s92, v220\n"
      "v_mul_f32 v56, v56, v21\n"
      "v_mul_f32 v57, v57, v22\n"
      "v_lshlrev_b32 v21, 16, v221\n"
      "v_and_b32 v22, s92, v221\n"
      "v_mul_f32 v58, v58, v21\n"
      "v_mul_f32 v59, v59, v22\n"
      "s_waitcnt lgkmcnt(0)\n"
      "v_lshlrev_b32 v21, 16, v222\n"
      "v_and_b32 v22, s92, v222\n"
      "v_mul_f32 v60, v60, v21\n"
      "v_mul_f32 v61, v61, v22\n"
      "v_lshlrev_b32 v21, 16, v223\n"
      "v_and_b32 v22, s92, v223\n"
      "v_mul_f32 v62, v62, v21\n"
      "v_mul_f32 v63, v63, v22\n"
      "v_lshlrev_b32 v21, 16, v224\n"
      "v_and_b32 v22, s92, v224\n"
      "v_mul_f32 v64, v64, v21\n"
      "v_mul_f32 v65, v65, v22\n"
      "v_lshlrev_b32 v21, 16, v225\n"
      "v_and_b32 v22, s92, v225\n"
      "v_mul_f32 v66, v66, v21\n"
      "v_mul_f32 v67, v67, v22\n"
      "v_lshlrev_b32 v21, 16, v226\n"
      "v_and_b32 v22, s92, v226\n"
      "v_mul_f32 v68, v68, v21\n"
      "v_mul_f32 v69, v69, v22\n"
      "v_lshlrev_b32 v21, 16, v227\n"
      "v_and_b32 v22, s92, v227\n"
      "v_mul_f32 v70, v70, v21\n"
      "v_mul_f32 v71, v71, v22\n"
      "s_waitcnt vmcnt(0)\n"
      "s_waitcnt vmcnt(63)\n"
      "v_xor_b32 v21, s74, v167\n"
      "v_bitop3_b32 v21, v21, v171, s74 bitop3:0xf6\n"
      "v_bitop3_b32 v21, v21, v175, s74 bitop3:0xf6\n"
      "v_cmp_lt_u32 vcc, 0xffff, v21\n"
      "s_cbranch_vccnz .Ly_185\n"
      ".Ly_186:\n"
      "v_lshlrev_b32 v144, 16, v164\n"
      "v_and_b32 v145, s92, v164\n"
      "v_lshlrev_b32 v146, 16, v165\n"
      "v_and_b32 v147, s92, v165\n"
      "v_lshlrev_b32 v148, 16, v166\n"
      "v_and_b32 v149, s92, v166\n"
      "v_lshlrev_b32 v150, 16, v167\n"
      "v_lshlrev_b32 v151, 16, v168\n"
      "v_and_b32 v152, s92, v168\n"
      "v_lshlrev_b32 v153, 16, v169\n"
      "v_and_b32 v154, s92, v169\n"
      "v_lshlrev_b32 v155, 16, v170\n"
      "v_and_b32 v156, s92, v170\n"
      "v_lshlrev_b32 v157, 16, v171\n"
      "v_lshlrev_b32 v158, 16, v172\n"
      "v_and_b32 v159, s92, v172\n"
      "v_lshlrev_b32 v160, 16, v173\n"
      "v_and_b32 v161, s92, v173\n"
      "v_lshlrev_b32 v162, 16, v174\n"
      "v_and_b32 v163, s92, v174\n"
      "s_mov_b32 s77, s76\n"
      "v_pk_mul_f32 v[144:145], v[144:145], s[76:77]\n"
      "v_pk_mul_f32 v[146:147], v[146:147], s[76:77]\n"
      "v_pk_mul_f32 v[148:149], v[148:149], s[76:77]\n"
      "v_pk_mul_f32 v[150:151], v[150:151], s[76:77]\n"
      "v_pk_mul_f32 v[152:153], v[152:153], s[76:77]\n"
      "v_pk_mul_f32 v[154:155], v[154:155], s[76:77]\n"
      "v_pk_mul_f32 v[156:157], v[156:157], s[76:77]\n"
      "v_pk_mul_f32 v[158:159], v[158:159], s[76:77]\n"
      "v_pk_mul_f32 v[160:161], v[160:161], s[76:77]\n"
      "v_pk_mul_f32 v[162:163], v[162:163], s[76:77]\n"
      "v_mov_b32 v22, 1.0\n"
      "v_mov_b32 v23, 1.0\n"
      "s_mov_b32 s3, 0x80000\n"
      ".Lp_188:\n"
      "ds_read_b32 v31, v2 offset:4192\n"
      "s_sub_u32 s3, s3, 1\n"
      "s_waitcnt lgkmcnt(0)\n"
      "v_readlane_b32 s2, v31, s53\n"
      "s_cselect_b32 s2, -1, s2\n"
      "s_cmp_lt_u32 s2, 32\n"
      "s_cbranch_scc1 .Lp_188\n"
      "ds_read_b128 v[176:179], v8 offset:0\n"
      "ds_read_b128 v[180:183], v8 offset:1024\n"
      "s_waitcnt lgkmcnt(1)\n"
      "v_pk_fma_f32 v[176:177], v[176:177], s[76:77], v[144:145]\n"
      "v_pk_fma_f32 v[178:179], v[178:179], s[76:77], v[146:147]\n"
      "s_waitcnt lgkmcnt(0)\n"
      "v_pk_fma_f32 v[180:181], v[180:181], s[76:77], v[148:149]\n"
      "v_pk_fma_f32 v[182:183], v[182:183], s[76:77], v[150:151]\n"
      "v_exp_f32 v196, v176\n"
      "v_exp_f32 v197, v177\n"
      "v_exp_f32 v198, v178\n"
      "v_exp_f32 v199, v179\n"
      "v_exp_f32 v200, v180\n"
      "v_exp_f32 v201, v181\n"
      "v_exp_f32 v202, v182\n"
      "v_exp_f32 v203, v183\n"
      "v_pk_mul_f32 v[52:53], v[52:53], v[176:177]\n"
      "v_pk_mul_f32 v[54:55], v[54:55], v[178:179]\n"
      "v_pk_mul_f32 v[56:57], v[56:57], v[180:181]\n"
      "v_pk_mul_f32 v[58:59], v[58:59], v[182:183]\n"
      "v_pk_add_f32 v[196:197], v[196:197], v[22:23]\n"
      "v_pk_add_f32 v[198:199], v[198:199], v[22:23]\n"
      "v_pk_add_f32 v[200:201], v[200:201], v[22:23]\n"
      "v_pk_add_f32 v[202:203], v[202:203], v[22:23]\n"
      "v_rcp_f32 v196, v196\n"
      "v_rcp_f32 v197, v197\n"
      "v_rcp_f32 v198, v198\n"
      "v_rcp_f32 v199, v199\n"
      "v_rcp_f32 v200, v200\n"
      "v_rcp_f32 v201, v201\n"
      "v_rcp_f32 v202, v202\n"
      "v_rcp_f32 v203, v203\n"
      "v_pk_fma_f32 v[52:53], v[52:53], v[196:197], v[32:33]\n"
      "v_pk_fma_f32 v[54:55], v[54:55], v[198:199], v[34:35]\n"
      "v_pk_fma_f32 v[56:57], v[56:57], v[200:201], v[36:37]\n"
      "v_pk_fma_f32 v[58:59], v[58:59], v[202:203], v[38:39]\n"
      "v_cvt_pk_bf16_f32 v218, v52, v53\n"
      "v_cvt_pk_bf16_f32 v219, v54, v55\n"
      "v_cvt_pk_bf16_f32 v220, v56, v57\n"
      "v_cvt_pk_bf16_f32 v221, v58, v59\n"
      "s_nop 0\n"
      "v_permlane16_swap_b32 v218, v220\n"
      "v_permlane16_swap_b32 v219, v221\n"
      "buffer_store_dwordx4 v[218:221], v24, s[48:51], s70 offen offset:0\n"
      "s_mov_b32 s3, 0x80000\n"
      ".Lp_189:\n"
      "ds_read_b32 v31, v2 offset:4192\n"
      "s_sub_u32 s3, s3, 1\n"
      "s_waitcnt lgkmcnt(0)\n"
      "v_readlane_b32 s2, v31, s53\n"
      "s_cselect_b32 s2, -1, s2\n"
      "s_cmp_lt_u32 s2, 34\n"
      "s_cbranch_scc1 .Lp_189\n"
      "ds_read_b128 v[184:187], v8 offset:2048\n"
      "ds_read_b128 v[188:191], v8 offset:3072\n"
      "s_waitcnt lgkmcnt(1)\n"
      "v_pk_fma_f32 v[184:185], v[184:185], s[76:77], v[152:153]\n"
      "v_pk_fma_f32 v[186:187], v[186:187], s[76:77], v[154:155]\n"
      "s_waitcnt lgkmcnt(0)\n"
      "v_pk_fma_f32 v[188:189], v[188:189], s[76:77], v[156:157]\n"
      "v_pk_fma_f32 v[190:191], v[190:191], s[76:77], v[158:159]\n"
      "v_exp_f32 v204, v184\n"
      "v_exp_f32 v205, v185\n"
      "v_exp_f32 v206, v186\n"
      "v_exp_f32 v207, v187\n"
      "v_exp_f32 v208, v188\n"
      "v_exp_f32 v209, v189\n"
      "v_exp_f32 v210, v190\n"
      "v_exp_f32 v211, v191\n"
      "v_pk_mul_f32 v[60:61], v[60:61], v[184:185]\n"
      "v_pk_mul_f32 v[62:63], v[62:63], v[186:187]\n"
      "v_pk_mul_f32 v[64:65], v[64:65], v[188:189]\n"
      "v_pk_mul_f32 v[66:67], v[66:67], v[190:191]\n"
      "v_pk_add_f32 v[204:205], v[204:205], v[22:23]\n"
      "v_pk_add_f32 v[206:207], v[206:207], v[22:23]\n"
      "v_pk_add_f32 v[208:209], v[208:209], v[22:23]\n"
      "v_pk_add_f32 v[210:211], v[210:211], v[22:23]\n"
      "v_rcp_f32 v204, v204\n"
      "v_rcp_f32 v205, v205\n"
      "v_rcp_f32 v206, v206\n"
      "v_rcp_f32 v207, v207\n"
      "v_rcp_f32 v208, v208\n"
      "v_rcp_f32 v209, v209\n"
      "v_rcp_f32 v210, v210\n"
      "v_rcp_f32 v211, v211\n"
      "v_pk_fma_f32 v[60:61], v[60:61], v[204:205], v[40:41]\n"
      "v_pk_fma_f32 v[62:63], v[62:63], v[206:207], v[42:43]\n"
      "v_pk_fma_f32 v[64:65], v[64:65], v[208:209], v[44:45]\n"
      "v_pk_fma_f32 v[66:67], v[66:67], v[210:211], v[46:47]\n"
      "v_cvt_pk_bf16_f32 v222, v60, v61\n"
      "v_cvt_pk_bf16_f32 v223, v62, v63\n"
      "v_cvt_pk_bf16_f32 v224, v64, v65\n"
      "v_cvt_pk_bf16_f32 v225, v66, v67\n"
      "s_nop 0\n"
      "v_permlane16_swap_b32 v222, v224\n"
      "v_permlane16_swap_b32 v223, v225\n"
      "buffer_store_dwordx4 v[222:225], v24, s[48:51], s70 offen offset:64\n"
      "s_mov_b32 s3, 0x80000\n"
      ".Lp_190:\n"
      "ds_read_b32 v31, v2 offset:4192\n"
      "s_sub_u32 s3, s3, 1\n"
      "s_waitcnt lgkmcnt(0)\n"
      "v_readlane_b32 s2, v31, s53\n"
      "s_cselect_b32 s2, -1, s2\n"
      "s_cmp_lt_u32 s2, 35\n"
      "s_cbranch_scc1 .Lp_190\n"
      "ds_read_b128 v[192:195], v8 offset:4096\n"
      "s_waitcnt lgkmcnt(0)\n"
      "v_pk_fma_f32 v[192:193], v[192:193], s[76:77], v[160:161]\n"
      "v_pk_fma_f32 v[194:195], v[194:195], s[76:77], v[162:163]\n"
      "v_exp_f32 v212, v192\n"
      "v_exp_f32 v213, v193\n"
      "v_exp_f32 v214, v194\n"
      "v_exp_f32 v215, v195\n"
      "v_pk_mul_f32 v[68:69], v[68:69], v[192:193]\n"
      "v_pk_mul_f32 v[70:71], v[70:71], v[194:195]\n"
      "v_pk_add_f32 v[212:213], v[212:213], v[22:23]\n"
      "v_pk_add_f32 v[214:215], v[214:215], v[22:23]\n"
      "v_rcp_f32 v212, v212\n"
      "v_rcp_f32 v213, v213\n"
      "v_rcp_f32 v214, v214\n"
      "v_rcp_f32 v215, v215\n"
      "v_pk_fma_f32 v[68:69], v[68:69], v[212:213], v[48:49]\n"
      "v_pk_fma_f32 v[70:71], v[70:71], v[214:215], v[50:51]\n"
      "v_cvt_pk_bf16_f32 v226, v68, v69\n"
      "v_cvt_pk_bf16_f32 v227, v70, v71\n"
      "buffer_store_dwordx2 v[226:227], v4, s[48:51], s70 offen offset:128\n"
      "s_endpgm\n"
      ".LSPARE:\n"
      "s_and_b32 s70, s60, 7\n"
      "s_lshl2_add_u32 s70, s70, s61\n"
      "s_sub_u32 s70, s70, 28\n"
      "s_cmp_lt_u32 s52, 4\n"
      "s_cbranch_scc1 .LSPM\n"
      "s_lshl_b32 s71, s53, 5\n"
      "s_add_u32 s71, s71, s70\n"
      "s_add_u32 s71, s71, 224\n"
      "v_lshrrev_b32 v20, 4, v1\n"
      "v_mul_u32_u24 v20, 1280, v20\n"
      "v_and_b32 v21, 15, v1\n"
      "v_lshl_add_u32 v20, v21, 4, v20\n"
      "v_add_u32 v21, 4080, v20\n"
      "v_add_u32 v22, 8160, v20\n"
      "v_add_u32 v23, 12240, v20\n"
      "s_mul_i32 s72, s71, 205\n"
      "s_lshr_b32 s72, s72, 10\n"
      "s_mul_i32 s73, s72, 5\n"
      "s_sub_u32 s73, s71, s73\n"
      "s_mul_i32 s74, s53, 8320\n"
      "s_add_u32 s74, s74, 53248\n"
      "s_mul_i32 s62, s72, 40960\n"
      "s_lshl_b32 s63, s73, 8\n"
      "s_add_u32 s62, s62, s63\n"
      "s_mov_b32 m0, s74\n"
      "s_add_u32 s63, s62, 20480\n"
      "s_lshl_b32 s94, s53, 12\n"
      "s_lshl_b32 s95, s53, 10\n"
      "s_cmp_lt_u32 s71, 270\n"
      "s_waitcnt lgkmcnt(0)\n"
      "s_barrier\n"
      "buffer_load_dwordx2 v[46:47], off, s[20:23], 0 sc1\n"
      "s_cbranch_scc0 .Ls_191\n"
      "buffer_load_dwordx4 v20, s[40:43], s62 offen offset:0 nt lds\n"
      "buffer_load_dwordx4 v21, s[40:43], s62 offen offset:1040 nt lds\n"
      "buffer_load_dwordx4 v22, s[40:43], s62 offen offset:2080 nt lds\n"
      "buffer_load_dwordx4 v23, s[40:43], s62 offen offset:3120 nt lds\n"
      "s_add_u32 m0, s74, 4160\n"
      "s_nop 0\n"
      "buffer_load_dwordx4 v20, s[40:43], s63 offen offset:0 nt lds\n"
      "buffer_load_dwordx4 v21, s[40:43], s63 offen offset:1040 nt lds\n"
      "buffer_load_dwordx4 v22, s[40:43], s63 offen offset:2080 nt lds\n"
      "buffer_load_dwordx4 v23, s[40:43], s63 offen offset:3120 nt lds\n"
      ".Ls_191:\n"
      "v_mov_b64 v[36:37], 0\n"
      "v_mov_b64 v[38:39], 0\n"
      "s_lshl_b32 s63, s53, 10\n"
      "s_add_u32 s63, s63, 157696\n"
      "v_lshl_add_u32 v40, v1, 4, s63\n"
      "ds_write_b128 v40, v[36:39]\n"
      "v_lshlrev_b32 v40, 2, v1\n"
      "v_add_u32 v40, 161792, v40\n"
      "ds_write_b32 v40, v36\n"
      "v_lshlrev_b32 v5, 4, v1\n"
      "v_add_u32 v6, 4096, v5\n"
      "s_lshl_b32 s24, s98, 12\n"
      "s_add_u32 s24, s24, s95\n"
      "s_add_u32 s25, s24, 4096\n"
      "v_mov_b32 v32, s25\n"
      "v_writelane_b32 v32, s24, 0\n"
      "s_cmp_eq_u32 s53, 0\n"
      "s_cselect_b32 s63, 1073741824, -4\n"
      "v_add_u32 v32, s63, v32\n"
      "v_lshrrev_b32 v33, 11, v32\n"
      "v_xor_b32 v33, 524288, v33\n"
      "s_sub_u32 s91, s98, 4\n"
      "s_lshr_b32 s91, s91, 4\n"
      "s_lshr_b32 s63, s98, 4\n"
      "s_sub_u32 s63, s63, s91\n"
      "s_lshl_b32 s63, s63, 9\n"
      "v_and_b32 v72, 8, v1\n"
      "v_mul_u32_u24 v72, s63, v72\n"
      "v_and_b32 v73, 7, v1\n"
      "v_lshl_add_u32 v30, v73, 7, v72\n"
      "s_lshl_b32 s91, s91, 12\n"
      "s_add_u32 s91, s91, s95\n"
      "s_add_u32 s91, s91, 18644992\n"
      "v_add_u32 v30, s91, v30\n"
      "s_and_b32 s63, s53, 1\n"
      "s_mul_i32 s63, s63, 4608\n"
      "s_add_u32 s63, s63, 139520\n"
      "v_lshl_add_u32 v34, v1, 3, s63\n"
      "v_lshrrev_b32 v72, 1, v1\n"
      "v_min_u32 v72, 6, v72\n"
      "s_mul_i32 s62, s54, 28\n"
      "s_add_u32 s62, s62, s53\n"
      "v_lshl_add_u32 v72, v72, 2, s62\n"
      "v_min_u32 v72, 53, v72\n"
      "s_lshl_b32 s63, s56, 14\n"
      "s_add_u32 s63, s63, 18382848\n"
      "v_lshl_add_u32 v13, v72, 8, s63\n"
      "s_mul_i32 s63, s55, 80\n"
      "s_add_u32 s91, s63, 79\n"
      "s_lshr_b32 s63, s63, 7\n"
      "s_lshr_b32 s91, s91, 7\n"
      "s_sub_u32 s91, s91, s63\n"
      "v_and_b32 v73, s91, v1\n"
      "v_lshl_add_u32 v72, v72, 3, v73\n"
      "s_lshl_b32 s63, s63, 7\n"
      "s_add_u32 s63, s63, 18808832\n"
      "v_lshl_add_u32 v31, v72, 7, s63\n"
      "v_lshrrev_b32 v72, 3, v1\n"
      "v_and_b32 v72, 3, v72\n"
      "v_mul_u32_u24 v73, 4160, v72\n"
      "v_add_u32 v73, 122880, v73\n"
      "v_and_b32 v72, 7, v1\n"
      "v_lshl_add_u32 v35, v72, 3, v73\n"
      "s_add_u32 s62, s95, 122944\n"
      "v_add_u32 v40, s62, v5\n"
      "v_and_b32 v72, 31, v1\n"
      "v_lshlrev_b32 v42, 3, v72\n"
      "s_add_u32 s62, s95, 157696\n"
      "v_lshl_add_u32 v43, v72, 5, s62\n"
      "s_xor_b32 s31, s99, s53\n"
      "s_lshl2_add_u32 s31, s56, s31\n"
      "s_lshl_b32 s31, s31, 8\n"
      "s_add_u32 s31, s31, 8192\n"
      "s_lshl2_add_u32 s62, s54, s53\n"
      "s_cmp_ge_u32 s62, 6\n"
      "s_cselect_b32 s63, 1073741824, 6144\n"
      "v_add_u32 v7, s63, v5\n"
      "s_mul_i32 s62, s62, 7168\n"
      "s_mul_i32 s63, s55, 286720\n"
      "s_add_u32 s63, s63, s62\n"
      "s_add_u32 s27, s63, 5603328\n"
      "s_mul_i32 s63, s56, 458752\n"
      "s_add_u32 s63, s63, s62\n"
      "s_add_u32 s63, s63, 7897088\n"
      "s_mul_i32 s62, s54, 229376\n"
      "s_add_u32 s91, s63, s62\n"
      "s_sub_u32 s62, 229376, s62\n"
      "s_add_u32 s93, s63, s62\n"
      "s_sub_u32 s62, 4, s53\n"
      "s_and_b32 s62, s62, 3\n"
      "s_mul_i32 s62, s62, 57344\n"
      "s_add_u32 s44, s93, s62\n"
      "s_add_u32 s48, s91, s62\n"
      "s_sub_u32 s62, 5, s53\n"
      "s_and_b32 s62, s62, 3\n"
      "s_mul_i32 s62, s62, 57344\n"
      "s_add_u32 s45, s93, s62\n"
      "s_add_u32 s49, s91, s62\n"
      "s_sub_u32 s62, 6, s53\n"
      "s_and_b32 s62, s62, 3\n"
      "s_mul_i32 s62, s62, 57344\n"
      "s_add_u32 s46, s93, s62\n"
      "s_add_u32 s50, s91, s62\n"
      "s_sub_u32 s62, 7, s53\n"
      "s_and_b32 s62, s62, 3\n"
      "s_mul_i32 s62, s62, 57344\n"
      "s_add_u32 s47, s93, s62\n"
      "s_add_u32 s51, s91, s62\n"
      "s_add_u32 s91, s53, 3\n"
      "s_and_b32 s91, s91, 3\n"
      "s_lshl2_add_u32 s62, s91, 161888\n"
      "v_mov_b32 v15, s62\n"
      "s_mul_i32 s62, s53, 5120\n"
      "s_add_u32 s63, s62, 32768\n"
      "v_add_u32 v8, s63, v5\n"
      "s_mul_i32 s63, s91, 5120\n"
      "s_add_u32 s63, s63, 32768\n"
      "v_add_u32 v9, s63, v5\n"
      "s_xor_b32 s63, s60, 1\n"
      "s_lshl2_add_u32 s63, s63, s53\n"
      "s_mul_i32 s28, s63, 3072\n"
      "s_add_u32 s28, s28, 15237120\n"
      "v_cmp_eq_u32 vcc, s53, v1\n"
      "v_and_b32 v14, 48, v1\n"
      "v_add_u32 v14, 163072, v14\n"
      "v_cndmask_b32 v55, 0, 1, vcc\n"
      "s_sub_u32 s62, s24, 16384\n"
      "buffer_load_dwordx4 v[140:143], v5, s[36:39], s62 offen nt\n"
      "s_sub_u32 s62, s25, 16384\n"
      "buffer_load_dwordx4 v[144:147], v5, s[36:39], s62 offen nt\n"
      "v_subrev_u32 v72, 16384, v32\n"
      "v_subrev_u32 v73, 8, v33\n"
      "buffer_load_dword v132, v72, s[36:39], 0 offen nt\n"
      "buffer_load_ushort v133, v73, s[32:35], 0 offen\n"
      "s_sub_u32 s62, s24, 8192\n"
      "buffer_load_dwordx4 v[148:151], v5, s[36:39], s62 offen nt\n"
      "s_sub_u32 s62, s25, 8192\n"
      "buffer_load_dwordx4 v[152:155], v5, s[36:39], s62 offen nt\n"
      "v_subrev_u32 v72, 8192, v32\n"
      "v_subrev_u32 v73, 4, v33\n"
      "buffer_load_dword v134, v72, s[36:39], 0 offen nt\n"
      "buffer_load_ushort v135, v73, s[32:35], 0 offen\n"
      "v_lshrrev_b32 v20, 4, v1\n"
      "v_and_b32 v21, 1, v20\n"
      "v_lshrrev_b32 v20, 1, v20\n"
      "v_lshl_add_u32 v20, v21, 2, v20\n"
      "v_mul_u32_u24 v20, 1040, v20\n"
      "v_bfe_u32 v21, v1, 2, 2\n"
      "v_lshl_add_u32 v20, v21, 8, v20\n"
      "v_and_b32 v21, 3, v1\n"
      "v_lshl_add_u32 v20, v21, 3, v20\n"
      "s_lshl3_add_u32 s62, s72, s73\n"
      "s_lshl_b32 s62, s62, 7\n"
      "s_add_u32 s62, s62, 18808832\n"
      "v_mov_b32 v23, 1073741824\n"
      "s_mov_b32 exec_lo, 0xffff\n"
      "s_mov_b32 exec_hi, 0x0\n"
      "v_lshl_add_u32 v23, v1, 3, s62\n"
      "s_mov_b64 exec, -1\n"
      "v_add_u32 v21, s74, v20\n"
      "s_lshr_b32 s63, s72, 2\n"
      "s_and_b32 s75, s72, 3\n"
      "s_mul_i32 s75, s75, 7\n"
      "s_add_u32 s75, s75, s63\n"
      "s_cmp_ge_u32 s63, 7\n"
      "s_cselect_b32 s63, 21, 0\n"
      "s_add_u32 s75, s75, s63\n"
      "s_lshl_b32 s75, s75, 10\n"
      "s_mul_i32 s63, s73, 458752\n"
      "s_add_u32 s75, s75, s63\n"
      "s_add_u32 s75, s75, 5603328\n"
      "v_subrev_u32 v136, 52736, v40\n"
      "v_subrev_u32 v137, 52736, v34\n"
      "v_subrev_u32 v138, 52736, v35\n"
      "s_waitcnt vmcnt(8) lgkmcnt(0)\n"
      "v_cmp_eq_u32 vcc, 0, v1\n"
      "v_lshlrev_b32 v2, 2, v1\n"
      "v_add_u32 v2, 157696, v2\n"
      "v_cndmask_b32 v3, 0, 1, vcc\n"
      "v_add_co_u32 v46, vcc, 1, v46\n"
      "s_nop 1\n"
      "v_addc_co_u32 v47, vcc, 0, v47, vcc\n"
      "s_lshr_b32 s62, s53, 1\n"
      "s_lshl_b32 s62, s62, 5\n"
      "v_add_u32 v139, s62, v2\n"
      "v_readfirstlane_b32 s58, v46\n"
      "v_readfirstlane_b32 s59, v47\n"
      "v_mov_b32 v1, v55\n"
      "s_barrier\n"
      "s_cmp_lt_u32 s71, 270\n"
      "s_cbranch_scc0 .Ls_192\n"
      "ds_read_b64_tr_b16 v[160:161], v21 offset:0\n"
      "ds_read_b64_tr_b16 v[162:163], v21 offset:2080\n"
      "ds_read_b64_tr_b16 v[164:165], v21 offset:32\n"
      "ds_read_b64_tr_b16 v[166:167], v21 offset:2112\n"
      "ds_read_b64_tr_b16 v[168:169], v21 offset:64\n"
      "ds_read_b64_tr_b16 v[170:171], v21 offset:2144\n"
      "ds_read_b64_tr_b16 v[172:173], v21 offset:96\n"
      "ds_read_b64_tr_b16 v[174:175], v21 offset:2176\n"
      "s_waitcnt lgkmcnt(6)\n"
      "ds_read_b64_tr_b16 v[176:177], v21 offset:128\n"
      "ds_read_b64_tr_b16 v[178:179], v21 offset:2208\n"
      "ds_read_b64_tr_b16 v[180:181], v21 offset:160\n"
      "ds_read_b64_tr_b16 v[182:183], v21 offset:2240\n"
      "ds_read_b64_tr_b16 v[184:185], v21 offset:192\n"
      "ds_read_b64_tr_b16 v[186:187], v21 offset:2272\n"
      "ds_read_b64_tr_b16 v[188:189], v21 offset:224\n"
      "ds_read_b64_tr_b16 v[190:191], v21 offset:2304\n"
      "s_waitcnt lgkmcnt(14)\n"
      "buffer_store_dwordx4 v[160:163], v5, s[20:23], s75 offen sc1\n"
      "s_add_u32 s75, s75, 57344\n"
      "s_waitcnt lgkmcnt(12)\n"
      "buffer_store_dwordx4 v[164:167], v5, s[20:23], s75 offen sc1\n"
      "s_add_u32 s75, s75, 57344\n"
      "s_waitcnt lgkmcnt(10)\n"
      "buffer_store_dwordx4 v[168:171], v5, s[20:23], s75 offen sc1\n"
      "s_add_u32 s75, s75, 57344\n"
      "s_waitcnt lgkmcnt(8)\n"
      "buffer_store_dwordx4 v[172:175], v5, s[20:23], s75 offen sc1\n"
      "s_add_u32 s75, s75, 57344\n"
      "s_waitcnt lgkmcnt(6)\n"
      "buffer_store_dwordx4 v[176:179], v5, s[20:23], s75 offen sc1\n"
      "s_add_u32 s75, s75, 57344\n"
      "s_waitcnt lgkmcnt(4)\n"
      "buffer_store_dwordx4 v[180:183], v5, s[20:23], s75 offen sc1\n"
      "s_add_u32 s75, s75, 57344\n"
      "s_waitcnt lgkmcnt(2)\n"
      "buffer_store_dwordx4 v[184:187], v5, s[20:23], s75 offen sc1\n"
      "s_add_u32 s75, s75, 57344\n"
      "s_waitcnt lgkmcnt(0)\n"
      "buffer_store_dwordx4 v[188:191], v5, s[20:23], s75 offen sc1\n"
      "s_waitcnt vmcnt(8)\n"
      "s_branch .Ls_193\n"
      ".Ls_192:\n"
      "v_mov_b32 v23, 1073741824\n"
      "s_waitcnt vmcnt(0)\n"
      ".Ls_193:\n"
      "ds_write_b64 v137, v[36:37] offset:0\n"
      "ds_write_b64 v138, v[36:37] offset:0\n"
      "s_cmp_eq_u32 s53, 0\n"
      "s_cbranch_scc0 .Ld_194\n"
      "v_readlane_b32 s62, v133, 0\n"
      "v_readlane_b32 s63, v133, 1\n"
      "s_mov_b64 exec, 1\n"
      "v_lshlrev_b32 v72, 16, v140\n"
      "s_lshl_b32 s62, s62, 16\n"
      "v_and_b32 v73, s92, v140\n"
      "v_add_f32 v72, s62, v72\n"
      "v_cvt_pk_bf16_f32 v140, v72, v73\n"
      "v_lshlrev_b32 v72, 16, v144\n"
      "s_lshl_b32 s63, s63, 16\n"
      "v_and_b32 v73, s92, v144\n"
      "v_add_f32 v72, s63, v72\n"
      "v_cvt_pk_bf16_f32 v144, v72, v73\n"
      "s_mov_b64 exec, -1\n"
      ".Ld_194:\n"
      "v_readlane_b32 s62, v132, 1\n"
      "ds_write_b128 v136, v[144:147] offset:8320\n"
      "v_alignbyte_b32 v103, v147, v146, 2\n"
      "v_writelane_b32 v72, s62, 0\n"
      "v_alignbyte_b32 v101, v145, v144, 2\n"
      "v_alignbyte_b32 v102, v146, v145, 2\n"
      "v_mov_b32_dpp v72, v147 wave_shr:1 row_mask:0xf bank_mask:0xf\n"
      "v_alignbyte_b32 v100, v144, v72, 2\n"
      "ds_write_b128 v136, v[100:103] offset:12480\n"
      "ds_write_b128 v136, v[140:143] offset:0\n"
      "v_alignbyte_b32 v99, v143, v142, 2\n"
      "v_alignbyte_b32 v97, v141, v140, 2\n"
      "v_alignbyte_b32 v98, v142, v141, 2\n"
      "v_mov_b32_dpp v132, v143 wave_shr:1 row_mask:0xf bank_mask:0xf\n"
      "v_alignbyte_b32 v96, v140, v132, 2\n"
      "ds_write_b128 v136, v[96:99] offset:4160\n"
      "ds_write_b64 v137, v[36:37] offset:26112\n"
      "ds_write_b64 v138, v[36:37] offset:26112\n"
      "s_cmp_eq_u32 s53, 0\n"
      "s_cbranch_scc0 .Ld_195\n"
      "v_readlane_b32 s62, v135, 0\n"
      "v_readlane_b32 s63, v135, 1\n"
      "s_mov_b64 exec, 1\n"
      "v_lshlrev_b32 v72, 16, v148\n"
      "s_lshl_b32 s62, s62, 16\n"
      "v_and_b32 v73, s92, v148\n"
      "v_add_f32 v72, s62, v72\n"
      "v_cvt_pk_bf16_f32 v148, v72, v73\n"
      "v_lshlrev_b32 v72, 16, v152\n"
      "s_lshl_b32 s63, s63, 16\n"
      "v_and_b32 v73, s92, v152\n"
      "v_add_f32 v72, s63, v72\n"
      "v_cvt_pk_bf16_f32 v152, v72, v73\n"
      "s_mov_b64 exec, -1\n"
      ".Ld_195:\n"
      "v_readlane_b32 s62, v134, 1\n"
      "ds_write_b128 v136, v[152:155] offset:34432\n"
      "v_alignbyte_b32 v103, v155, v154, 2\n"
      "v_writelane_b32 v72, s62, 0\n"
      "v_alignbyte_b32 v101, v153, v152, 2\n"
      "v_alignbyte_b32 v102, v154, v153, 2\n"
      "v_mov_b32_dpp v72, v155 wave_shr:1 row_mask:0xf bank_mask:0xf\n"
      "v_alignbyte_b32 v100, v152, v72, 2\n"
      "ds_write_b128 v136, v[100:103] offset:38592\n"
      "ds_write_b128 v136, v[148:151] offset:26112\n"
      "v_alignbyte_b32 v99, v151, v150, 2\n"
      "v_alignbyte_b32 v97, v149, v148, 2\n"
      "v_alignbyte_b32 v98, v150, v149, 2\n"
      "v_mov_b32_dpp v134, v151 wave_shr:1 row_mask:0xf bank_mask:0xf\n"
      "v_alignbyte_b32 v96, v148, v134, 2\n"
      "ds_write_b128 v136, v[96:99] offset:30272\n"
      "s_waitcnt vmcnt(0)\n"
      "buffer_store_dwordx2 v[46:47], v23, s[20:23], 0 offen sc1\n"
      "s_mov_b32 s3, 0x80000\n"
      ".Lp_196:\n"
      "s_sleep 2\n"
      "ds_read_b32 v4, v2 offset:4104\n"
      "s_sub_u32 s3, s3, 1\n"
      "s_waitcnt lgkmcnt(0)\n"
      "v_readfirstlane_b32 s2, v4\n"
      "s_cselect_b32 s2, -1, s2\n"
      "s_cmp_lt_u32 s2, 4\n"
      "s_cbranch_scc1 .Lp_196\n"
      "buffer_load_dwordx2 v[76:77], v42, s[20:23], s31 offen sc1\n"
      "buffer_load_dwordx4 v[56:59], v5, s[36:39], s24 offen nt\n"
      "buffer_load_dwordx4 v[60:63], v5, s[36:39], s25 offen nt\n"
      "buffer_load_dword v52, v32, s[36:39], 0 offen nt\n"
      "buffer_load_ushort v53, v33, s[32:35], 0 offen\n"
      "s_waitcnt vmcnt(4)\n"
      "v_cmp_ne_u32 vcc, s58, v77\n"
      "s_cbranch_vccnz .Lt_197\n"
      ".Lt_198:\n"
      "v_and_b32 v82, s92, v76\n"
      "v_rcp_f32 v77, v82\n"
      "v_xor_b32 v85, 0x8000, v76\n"
      "s_mov_b32 s62, 0x01000100\n"
      "v_mul_f32 v80, 0xc0135761, v77\n"
      "v_mul_f32 v81, v77, v77\n"
      "v_cmp_nle_f32 vcc, 0x3089705f, v77\n"
      "v_mul_f32 v81, 0x3d372713, v81\n"
      "v_perm_b32 v84, v85, v85, s62\n"
      "v_mul_f32 v81, v81, v80\n"
      "v_mov_b32 v83, 1.0\n"
      "ds_write_b64 v43, v[84:85] offset:16\n"
      "s_cbranch_vccnz .Lb_200\n"
      ".Lb_201:\n"
      "ds_write_b128 v43, v[80:83]\n"
      "ds_add_u32 v2, v3 offset:4240\n"
      "s_cmp_lt_u32 s53, 2\n"
      "s_cbranch_scc1 .Ls_202\n"
      "s_mov_b32 s3, 0x80000\n"
      ".Lp_203:\n"
      "s_sleep 2\n"
      "ds_read_b32 v4, v2 offset:4108\n"
      "s_sub_u32 s3, s3, 1\n"
      "s_waitcnt lgkmcnt(0)\n"
      "v_readfirstlane_b32 s2, v4\n"
      "s_cselect_b32 s2, -1, s2\n"
      "s_cmp_lt_u32 s2, 4\n"
      "s_cbranch_scc1 .Lp_203\n"
      "s_sleep 24\n"
      ".Ls_202:\n"
      "buffer_load_dwordx2 v[48:49], v30, s[20:23], 0 offen sc1\n"
      "s_waitcnt vmcnt(0)\n"
      "v_cmp_ne_u64 vcc, s[58:59], v[48:49]\n"
      "s_cbranch_vccnz .Lf_204\n"
      ".Lf_205:\n"
      "s_mul_i32 s62, s98, 4224\n"
      "s_add_u32 s62, s62, s95\n"
      "s_add_u32 m0, s95, 87296\n"
      "s_add_u32 s63, s62, 15872\n"
      "buffer_load_dwordx4 v5, s[20:23], s63 offen sc1 lds\n"
      "s_add_u32 m0, s95, 91904\n"
      "s_add_u32 s63, s62, 20096\n"
      "buffer_load_dwordx4 v5, s[20:23], s63 offen sc1 lds\n"
      "s_mul_i32 s62, s98, 4224\n"
      "s_add_u32 s62, s62, s95\n"
      "s_add_u32 m0, s95, 113408\n"
      "s_add_u32 s63, s62, 24320\n"
      "buffer_load_dwordx4 v5, s[20:23], s63 offen sc1 lds\n"
      "s_add_u32 m0, s95, 118016\n"
      "s_add_u32 s63, s62, 28544\n"
      "buffer_load_dwordx4 v5, s[20:23], s63 offen sc1 lds\n"
      "buffer_load_dwordx2 v[50:51], v31, s[20:23], 0 offen sc1\n"
      "s_waitcnt vmcnt(1)\n"
      "ds_add_u32 v139, v3 offset:4240\n"
      "s_waitcnt vmcnt(0)\n"
      "s_branch .LS3\n"
      ".LSPM:\n"
      "v_lshrrev_b32 v24, 3, v1\n"
      "v_mul_u32_u24 v25, 1280, v24\n"
      "v_and_b32 v26, 7, v1\n"
      "v_lshl_add_u32 v25, v26, 4, v25\n"
      "v_cmp_gt_u32 vcc, 40, v1\n"
      "v_mov_b32 v26, 1073741824\n"
      "v_lshlrev_b32 v24, 4, v24\n"
      "v_cndmask_b32 v26, v25, v26, vcc\n"
      "v_add_u32 v27, 9200, v25\n"
      "s_mul_i32 s62, s70, 3\n"
      "s_add_u32 s62, s62, 224\n"
      "s_mul_i32 s64, s53, 2080\n"
      "s_mul_i32 s65, s53, 20480\n"
      "s_mul_i32 s80, s62, 205\n"
      "s_lshr_b32 s80, s80, 11\n"
      "s_mul_i32 s81, s80, 10\n"
      "s_sub_u32 s81, s62, s81\n"
      "s_mul_i32 s66, s80, 81920\n"
      "s_lshl_b32 s63, s81, 7\n"
      "s_add_u32 s66, s66, s63\n"
      "s_lshl2_add_u32 s81, s81, s53\n"
      "s_lshl_b32 s63, s81, 7\n"
      "buffer_load_dwordx4 v[168:171], v24, s[24:27], s63 offen\n"
      "s_mov_b32 m0, 0\n"
      "s_sub_u32 s63, s66, 10240\n"
      "buffer_load_dwordx4 v26, s[12:15], s63 offen nt lds\n"
      "s_add_u32 s63, s66, s65\n"
      "s_add_u32 m0, s64, 1040\n"
      "s_nop 0\n"
      "buffer_load_dwordx4 v25, s[12:15], s63 offen nt lds\n"
      "buffer_load_dwordx4 v27, s[12:15], s63 offen offset:1040 nt lds\n"
      "s_add_u32 m0, s64, 9360\n"
      "s_nop 0\n"
      "buffer_load_dwordx4 v25, s[16:19], s63 offen nt lds\n"
      "buffer_load_dwordx4 v27, s[16:19], s63 offen offset:1040 nt lds\n"
      "s_add_u32 s62, s62, 1\n"
      "v_cmp_eq_u32 vcc, 0, v1\n"
      "v_lshlrev_b32 v2, 2, v1\n"
      "v_add_u32 v2, 157696, v2\n"
      "v_cndmask_b32 v3, 0, 1, vcc\n"
      "s_barrier\n"
      "s_mul_i32 s82, s62, 205\n"
      "s_lshr_b32 s82, s82, 11\n"
      "s_mul_i32 s83, s82, 10\n"
      "s_sub_u32 s83, s62, s83\n"
      "s_mul_i32 s66, s82, 81920\n"
      "s_lshl_b32 s63, s83, 7\n"
      "s_add_u32 s66, s66, s63\n"
      "s_lshl2_add_u32 s83, s83, s53\n"
      "s_lshl_b32 s63, s83, 7\n"
      "buffer_load_dwordx4 v[172:175], v24, s[24:27], s63 offen\n"
      "s_mov_b32 m0, 17680\n"
      "s_sub_u32 s63, s66, 10240\n"
      "buffer_load_dwordx4 v26, s[12:15], s63 offen nt lds\n"
      "s_add_u32 s63, s66, s65\n"
      "s_add_u32 m0, s64, 18720\n"
      "s_nop 0\n"
      "buffer_load_dwordx4 v25, s[12:15], s63 offen nt lds\n"
      "buffer_load_dwordx4 v27, s[12:15], s63 offen offset:1040 nt lds\n"
      "s_add_u32 m0, s64, 27040\n"
      "s_nop 0\n"
      "buffer_load_dwordx4 v25, s[16:19], s63 offen nt lds\n"
      "buffer_load_dwordx4 v27, s[16:19], s63 offen offset:1040 nt lds\n"
      "s_add_u32 s62, s62, 1\n"
      "s_mul_i32 s84, s62, 205\n"
      "s_lshr_b32 s84, s84, 11\n"
      "s_mul_i32 s85, s84, 10\n"
      "s_sub_u32 s85, s62, s85\n"
      "s_mul_i32 s66, s84, 81920\n"
      "s_lshl_b32 s63, s85, 7\n"
      "s_add_u32 s66, s66, s63\n"
      "s_lshl2_add_u32 s85, s85, s53\n"
      "s_lshl_b32 s63, s85, 7\n"
      "buffer_load_dwordx4 v[176:179], v24, s[24:27], s63 offen\n"
      "s_mov_b32 m0, 35360\n"
      "s_sub_u32 s63, s66, 10240\n"
      "buffer_load_dwordx4 v26, s[12:15], s63 offen nt lds\n"
      "s_add_u32 s63, s66, s65\n"
      "s_add_u32 m0, s64, 36400\n"
      "s_nop 0\n"
      "buffer_load_dwordx4 v25, s[12:15], s63 offen nt lds\n"
      "buffer_load_dwordx4 v27, s[12:15], s63 offen offset:1040 nt lds\n"
      "s_add_u32 m0, s64, 44720\n"
      "s_nop 0\n"
      "buffer_load_dwordx4 v25, s[16:19], s63 offen nt lds\n"
      "buffer_load_dwordx4 v27, s[16:19], s63 offen offset:1040 nt lds\n"
      "s_add_u32 s62, s62, 1\n"
      "v_mov_b64 v[12:13], s[58:59]\n"
      "s_lshl_b32 s62, s53, 5\n"
      "s_add_u32 s62, s62, 640\n"
      "v_and_b32 v68, 7, v1\n"
      "v_lshrrev_b32 v69, 3, v1\n"
      "v_mul_u32_u24 v192, 1040, v68\n"
      "v_lshl_add_u32 v192, v69, 2, v192\n"
      "v_add_u32 v192, s62, v192\n"
      "v_add_u32 v193, 8720, v192\n"
      "v_mul_u32_u24 v8, 8448, v69\n"
      "v_lshl_add_u32 v8, v68, 4, v8\n"
      "v_mov_b32 v23, 1073741824\n"
      "s_lshl3_add_u32 s65, s60, 4096\n"
      "s_cmp_eq_u32 s52, 0\n"
      "s_cselect_b32 s65, s65, 1073741824\n"
      "v_writelane_b32 v23, s65, 48\n"
      "s_waitcnt vmcnt(12)\n"
      "ds_add_u32 v2, v3 offset:4096\n"
      "v_lshlrev_b32 v184, 16, v168\n"
      "v_lshlrev_b32 v185, 16, v170\n"
      "v_and_b32 v186, s92, v168\n"
      "v_and_b32 v187, s92, v170\n"
      "v_lshlrev_b32 v188, 16, v169\n"
      "v_lshlrev_b32 v189, 16, v171\n"
      "v_and_b32 v190, s92, v169\n"
      "v_and_b32 v191, s92, v171\n"
      "s_mul_i32 s62, s81, 67584\n"
      "s_lshl_b32 s63, s80, 7\n"
      "s_add_u32 s62, s62, s63\n"
      "s_add_u32 s62, s62, 32768\n"
      "s_add_u32 s63, s62, 4224\n"
      "s_lshl_b32 s64, s81, 5\n"
      "s_add_u32 s64, s64, s80\n"
      "s_lshl_b32 s64, s64, 7\n"
      "s_add_u32 s64, s64, 18644992\n"
      "s_mov_b32 exec_lo, 0xffff\n"
      "s_mov_b32 exec_hi, 0x0\n"
      "v_lshl_add_u32 v23, v1, 3, s64\n"
      "s_mov_b64 exec, -1\n"
      "s_mov_b32 s3, 0x80000\n"
      ".Lp_207:\n"
      "ds_read_b32 v31, v2 offset:4096\n"
      "s_sub_u32 s3, s3, 1\n"
      "s_waitcnt lgkmcnt(0)\n"
      "v_readlane_b32 s2, v31, 0\n"
      "s_cselect_b32 s2, -1, s2\n"
      "s_cmp_lt_u32 s2, 4\n"
      "s_cbranch_scc1 .Lp_207\n"
      "ds_read2_b32 v[196:197], v192 offset0:0 offset1:32\n"
      "ds_read2_b32 v[198:199], v192 offset0:64 offset1:100\n"
      "ds_read2_b32 v[200:201], v192 offset0:132 offset1:164\n"
      "ds_read2_b32 v[202:203], v192 offset0:196 offset1:228\n"
      "ds_read_b32 v204, v192 offset:1040\n"
      "ds_read_b32 v205, v192 offset:1168\n"
      "ds_read_b32 v206, v192 offset:1296\n"
      "ds_read2_b32 v[208:209], v193 offset0:0 offset1:32\n"
      "ds_read2_b32 v[210:211], v193 offset0:64 offset1:96\n"
      "ds_read2_b32 v[212:213], v193 offset0:128 offset1:160\n"
      "ds_read2_b32 v[214:215], v193 offset0:192 offset1:224\n"
      "s_waitcnt lgkmcnt(9)\n"
      "v_lshlrev_b32 v216, 16, v196\n"
      "v_and_b32 v217, s92, v196\n"
      "v_lshlrev_b32 v218, 16, v197\n"
      "v_and_b32 v219, s92, v197\n"
      "v_lshlrev_b32 v220, 16, v198\n"
      "v_and_b32 v221, s92, v198\n"
      "v_lshlrev_b32 v222, 16, v199\n"
      "v_and_b32 v223, s92, v199\n"
      "s_waitcnt lgkmcnt(7)\n"
      "v_lshlrev_b32 v224, 16, v200\n"
      "v_and_b32 v225, s92, v200\n"
      "v_lshlrev_b32 v226, 16, v201\n"
      "v_and_b32 v227, s92, v201\n"
      "v_lshlrev_b32 v228, 16, v202\n"
      "v_and_b32 v229, s92, v202\n"
      "v_lshlrev_b32 v230, 16, v203\n"
      "v_and_b32 v231, s92, v203\n"
      "s_waitcnt lgkmcnt(4)\n"
      "v_lshlrev_b32 v232, 16, v204\n"
      "v_and_b32 v233, s92, v204\n"
      "v_lshlrev_b32 v234, 16, v205\n"
      "v_and_b32 v235, s92, v205\n"
      "v_lshlrev_b32 v236, 16, v206\n"
      "v_and_b32 v237, s92, v206\n"
      "v_pk_mul_f32 v[240:241], v[216:217], v[190:191]\n"
      "v_pk_mul_f32 v[242:243], v[218:219], v[190:191]\n"
      "v_pk_mul_f32 v[244:245], v[220:221], v[190:191]\n"
      "v_pk_mul_f32 v[246:247], v[222:223], v[190:191]\n"
      "v_pk_mul_f32 v[248:249], v[224:225], v[190:191]\n"
      "v_pk_mul_f32 v[250:251], v[226:227], v[190:191]\n"
      "v_pk_mul_f32 v[252:253], v[228:229], v[190:191]\n"
      "v_pk_mul_f32 v[254:255], v[230:231], v[190:191]\n"
      "v_pk_fma_f32 v[240:241], v[218:219], v[188:189], v[240:241]\n"
      "v_pk_fma_f32 v[242:243], v[220:221], v[188:189], v[242:243]\n"
      "v_pk_fma_f32 v[244:245], v[222:223], v[188:189], v[244:245]\n"
      "v_pk_fma_f32 v[246:247], v[224:225], v[188:189], v[246:247]\n"
      "v_pk_fma_f32 v[248:249], v[226:227], v[188:189], v[248:249]\n"
      "v_pk_fma_f32 v[250:251], v[228:229], v[188:189], v[250:251]\n"
      "v_pk_fma_f32 v[252:253], v[230:231], v[188:189], v[252:253]\n"
      "v_pk_fma_f32 v[254:255], v[232:233], v[188:189], v[254:255]\n"
      "v_pk_fma_f32 v[240:241], v[220:221], v[186:187], v[240:241]\n"
      "v_pk_fma_f32 v[242:243], v[222:223], v[186:187], v[242:243]\n"
      "v_pk_fma_f32 v[244:245], v[224:225], v[186:187], v[244:245]\n"
      "v_pk_fma_f32 v[246:247], v[226:227], v[186:187], v[246:247]\n"
      "v_pk_fma_f32 v[248:249], v[228:229], v[186:187], v[248:249]\n"
      "v_pk_fma_f32 v[250:251], v[230:231], v[186:187], v[250:251]\n"
      "v_pk_fma_f32 v[252:253], v[232:233], v[186:187], v[252:253]\n"
      "v_pk_fma_f32 v[254:255], v[234:235], v[186:187], v[254:255]\n"
      "v_pk_fma_f32 v[240:241], v[222:223], v[184:185], v[240:241]\n"
      "v_pk_fma_f32 v[242:243], v[224:225], v[184:185], v[242:243]\n"
      "v_pk_fma_f32 v[244:245], v[226:227], v[184:185], v[244:245]\n"
      "v_pk_fma_f32 v[246:247], v[228:229], v[184:185], v[246:247]\n"
      "v_pk_fma_f32 v[248:249], v[230:231], v[184:185], v[248:249]\n"
      "v_pk_fma_f32 v[250:251], v[232:233], v[184:185], v[250:251]\n"
      "v_pk_fma_f32 v[252:253], v[234:235], v[184:185], v[252:253]\n"
      "v_pk_fma_f32 v[254:255], v[236:237], v[184:185], v[254:255]\n"
      "s_waitcnt lgkmcnt(0)\n"
      "v_lshlrev_b32 v216, 16, v208\n"
      "v_and_b32 v217, s92, v208\n"
      "v_lshlrev_b32 v218, 16, v209\n"
      "v_and_b32 v219, s92, v209\n"
      "v_lshlrev_b32 v220, 16, v210\n"
      "v_and_b32 v221, s92, v210\n"
      "v_lshlrev_b32 v222, 16, v211\n"
      "v_and_b32 v223, s92, v211\n"
      "v_lshlrev_b32 v224, 16, v212\n"
      "v_and_b32 v225, s92, v212\n"
      "v_lshlrev_b32 v226, 16, v213\n"
      "v_and_b32 v227, s92, v213\n"
      "v_lshlrev_b32 v228, 16, v214\n"
      "v_and_b32 v229, s92, v214\n"
      "v_lshlrev_b32 v230, 16, v215\n"
      "v_and_b32 v231, s92, v215\n"
      "v_pk_mul_f32 v[240:241], v[240:241], v[216:217]\n"
      "v_pk_mul_f32 v[242:243], v[242:243], v[218:219]\n"
      "v_pk_mul_f32 v[244:245], v[244:245], v[220:221]\n"
      "v_pk_mul_f32 v[246:247], v[246:247], v[222:223]\n"
      "v_pk_mul_f32 v[248:249], v[248:249], v[224:225]\n"
      "v_pk_mul_f32 v[250:251], v[250:251], v[226:227]\n"
      "v_pk_mul_f32 v[252:253], v[252:253], v[228:229]\n"
      "v_pk_mul_f32 v[254:255], v[254:255], v[230:231]\n"
      "v_cvt_pk_bf16_f32 v196, v242, v240\n"
      "v_cvt_pk_bf16_f32 v197, v246, v244\n"
      "v_cvt_pk_bf16_f32 v198, v250, v248\n"
      "v_cvt_pk_bf16_f32 v199, v254, v252\n"
      "buffer_store_dwordx4 v[196:199], v8, s[20:23], s62 offen sc1\n"
      "v_cvt_pk_bf16_f32 v200, v243, v241\n"
      "v_cvt_pk_bf16_f32 v201, v247, v245\n"
      "v_cvt_pk_bf16_f32 v202, v251, v249\n"
      "v_cvt_pk_bf16_f32 v203, v255, v253\n"
      "buffer_store_dwordx4 v[200:203], v8, s[20:23], s63 offen sc1\n"
      "v_add_u32 v192, 17680, v192\n"
      "v_add_u32 v193, 17680, v193\n"
      "s_waitcnt vmcnt(8)\n"
      "ds_add_u32 v2, v3 offset:4100\n"
      "v_lshlrev_b32 v184, 16, v172\n"
      "v_lshlrev_b32 v185, 16, v174\n"
      "v_and_b32 v186, s92, v172\n"
      "v_and_b32 v187, s92, v174\n"
      "v_lshlrev_b32 v188, 16, v173\n"
      "v_lshlrev_b32 v189, 16, v175\n"
      "v_and_b32 v190, s92, v173\n"
      "v_and_b32 v191, s92, v175\n"
      "s_mul_i32 s62, s83, 67584\n"
      "s_lshl_b32 s63, s82, 7\n"
      "s_add_u32 s62, s62, s63\n"
      "s_add_u32 s62, s62, 32768\n"
      "s_add_u32 s63, s62, 4224\n"
      "s_lshl_b32 s64, s83, 5\n"
      "s_add_u32 s64, s64, s82\n"
      "s_lshl_b32 s64, s64, 7\n"
      "s_add_u32 s64, s64, 18644992\n"
      "s_mov_b32 exec_lo, 0xffff0000\n"
      "s_mov_b32 exec_hi, 0x0\n"
      "s_sub_u32 s64, s64, 128\n"
      "v_lshl_add_u32 v23, v1, 3, s64\n"
      "s_mov_b64 exec, -1\n"
      "s_mov_b32 s3, 0x80000\n"
      ".Lp_208:\n"
      "ds_read_b32 v31, v2 offset:4100\n"
      "s_sub_u32 s3, s3, 1\n"
      "s_waitcnt lgkmcnt(0)\n"
      "v_readlane_b32 s2, v31, 0\n"
      "s_cselect_b32 s2, -1, s2\n"
      "s_cmp_lt_u32 s2, 4\n"
      "s_cbranch_scc1 .Lp_208\n"
      "ds_read2_b32 v[196:197], v192 offset0:0 offset1:32\n"
      "ds_read2_b32 v[198:199], v192 offset0:64 offset1:100\n"
      "ds_read2_b32 v[200:201], v192 offset0:132 offset1:164\n"
      "ds_read2_b32 v[202:203], v192 offset0:196 offset1:228\n"
      "ds_read_b32 v204, v192 offset:1040\n"
      "ds_read_b32 v205, v192 offset:1168\n"
      "ds_read_b32 v206, v192 offset:1296\n"
      "ds_read2_b32 v[208:209], v193 offset0:0 offset1:32\n"
      "ds_read2_b32 v[210:211], v193 offset0:64 offset1:96\n"
      "ds_read2_b32 v[212:213], v193 offset0:128 offset1:160\n"
      "ds_read2_b32 v[214:215], v193 offset0:192 offset1:224\n"
      "s_waitcnt lgkmcnt(9)\n"
      "v_lshlrev_b32 v216, 16, v196\n"
      "v_and_b32 v217, s92, v196\n"
      "v_lshlrev_b32 v218, 16, v197\n"
      "v_and_b32 v219, s92, v197\n"
      "v_lshlrev_b32 v220, 16, v198\n"
      "v_and_b32 v221, s92, v198\n"
      "v_lshlrev_b32 v222, 16, v199\n"
      "v_and_b32 v223, s92, v199\n"
      "s_waitcnt lgkmcnt(7)\n"
      "v_lshlrev_b32 v224, 16, v200\n"
      "v_and_b32 v225, s92, v200\n"
      "v_lshlrev_b32 v226, 16, v201\n"
      "v_and_b32 v227, s92, v201\n"
      "v_lshlrev_b32 v228, 16, v202\n"
      "v_and_b32 v229, s92, v202\n"
      "v_lshlrev_b32 v230, 16, v203\n"
      "v_and_b32 v231, s92, v203\n"
      "s_waitcnt lgkmcnt(4)\n"
      "v_lshlrev_b32 v232, 16, v204\n"
      "v_and_b32 v233, s92, v204\n"
      "v_lshlrev_b32 v234, 16, v205\n"
      "v_and_b32 v235, s92, v205\n"
      "v_lshlrev_b32 v236, 16, v206\n"
      "v_and_b32 v237, s92, v206\n"
      "v_pk_mul_f32 v[240:241], v[216:217], v[190:191]\n"
      "v_pk_mul_f32 v[242:243], v[218:219], v[190:191]\n"
      "v_pk_mul_f32 v[244:245], v[220:221], v[190:191]\n"
      "v_pk_mul_f32 v[246:247], v[222:223], v[190:191]\n"
      "v_pk_mul_f32 v[248:249], v[224:225], v[190:191]\n"
      "v_pk_mul_f32 v[250:251], v[226:227], v[190:191]\n"
      "v_pk_mul_f32 v[252:253], v[228:229], v[190:191]\n"
      "v_pk_mul_f32 v[254:255], v[230:231], v[190:191]\n"
      "v_pk_fma_f32 v[240:241], v[218:219], v[188:189], v[240:241]\n"
      "v_pk_fma_f32 v[242:243], v[220:221], v[188:189], v[242:243]\n"
      "v_pk_fma_f32 v[244:245], v[222:223], v[188:189], v[244:245]\n"
      "v_pk_fma_f32 v[246:247], v[224:225], v[188:189], v[246:247]\n"
      "v_pk_fma_f32 v[248:249], v[226:227], v[188:189], v[248:249]\n"
      "v_pk_fma_f32 v[250:251], v[228:229], v[188:189], v[250:251]\n"
      "v_pk_fma_f32 v[252:253], v[230:231], v[188:189], v[252:253]\n"
      "v_pk_fma_f32 v[254:255], v[232:233], v[188:189], v[254:255]\n"
      "v_pk_fma_f32 v[240:241], v[220:221], v[186:187], v[240:241]\n"
      "v_pk_fma_f32 v[242:243], v[222:223], v[186:187], v[242:243]\n"
      "v_pk_fma_f32 v[244:245], v[224:225], v[186:187], v[244:245]\n"
      "v_pk_fma_f32 v[246:247], v[226:227], v[186:187], v[246:247]\n"
      "v_pk_fma_f32 v[248:249], v[228:229], v[186:187], v[248:249]\n"
      "v_pk_fma_f32 v[250:251], v[230:231], v[186:187], v[250:251]\n"
      "v_pk_fma_f32 v[252:253], v[232:233], v[186:187], v[252:253]\n"
      "v_pk_fma_f32 v[254:255], v[234:235], v[186:187], v[254:255]\n"
      "v_pk_fma_f32 v[240:241], v[222:223], v[184:185], v[240:241]\n"
      "v_pk_fma_f32 v[242:243], v[224:225], v[184:185], v[242:243]\n"
      "v_pk_fma_f32 v[244:245], v[226:227], v[184:185], v[244:245]\n"
      "v_pk_fma_f32 v[246:247], v[228:229], v[184:185], v[246:247]\n"
      "v_pk_fma_f32 v[248:249], v[230:231], v[184:185], v[248:249]\n"
      "v_pk_fma_f32 v[250:251], v[232:233], v[184:185], v[250:251]\n"
      "v_pk_fma_f32 v[252:253], v[234:235], v[184:185], v[252:253]\n"
      "v_pk_fma_f32 v[254:255], v[236:237], v[184:185], v[254:255]\n"
      "s_waitcnt lgkmcnt(0)\n"
      "v_lshlrev_b32 v216, 16, v208\n"
      "v_and_b32 v217, s92, v208\n"
      "v_lshlrev_b32 v218, 16, v209\n"
      "v_and_b32 v219, s92, v209\n"
      "v_lshlrev_b32 v220, 16, v210\n"
      "v_and_b32 v221, s92, v210\n"
      "v_lshlrev_b32 v222, 16, v211\n"
      "v_and_b32 v223, s92, v211\n"
      "v_lshlrev_b32 v224, 16, v212\n"
      "v_and_b32 v225, s92, v212\n"
      "v_lshlrev_b32 v226, 16, v213\n"
      "v_and_b32 v227, s92, v213\n"
      "v_lshlrev_b32 v228, 16, v214\n"
      "v_and_b32 v229, s92, v214\n"
      "v_lshlrev_b32 v230, 16, v215\n"
      "v_and_b32 v231, s92, v215\n"
      "v_pk_mul_f32 v[240:241], v[240:241], v[216:217]\n"
      "v_pk_mul_f32 v[242:243], v[242:243], v[218:219]\n"
      "v_pk_mul_f32 v[244:245], v[244:245], v[220:221]\n"
      "v_pk_mul_f32 v[246:247], v[246:247], v[222:223]\n"
      "v_pk_mul_f32 v[248:249], v[248:249], v[224:225]\n"
      "v_pk_mul_f32 v[250:251], v[250:251], v[226:227]\n"
      "v_pk_mul_f32 v[252:253], v[252:253], v[228:229]\n"
      "v_pk_mul_f32 v[254:255], v[254:255], v[230:231]\n"
      "v_cvt_pk_bf16_f32 v196, v242, v240\n"
      "v_cvt_pk_bf16_f32 v197, v246, v244\n"
      "v_cvt_pk_bf16_f32 v198, v250, v248\n"
      "v_cvt_pk_bf16_f32 v199, v254, v252\n"
      "buffer_store_dwordx4 v[196:199], v8, s[20:23], s62 offen sc1\n"
      "v_cvt_pk_bf16_f32 v200, v243, v241\n"
      "v_cvt_pk_bf16_f32 v201, v247, v245\n"
      "v_cvt_pk_bf16_f32 v202, v251, v249\n"
      "v_cvt_pk_bf16_f32 v203, v255, v253\n"
      "buffer_store_dwordx4 v[200:203], v8, s[20:23], s63 offen sc1\n"
      "v_add_u32 v192, 17680, v192\n"
      "v_add_u32 v193, 17680, v193\n"
      "s_waitcnt vmcnt(4)\n"
      "ds_add_u32 v2, v3 offset:4104\n"
      "v_lshlrev_b32 v184, 16, v176\n"
      "v_lshlrev_b32 v185, 16, v178\n"
      "v_and_b32 v186, s92, v176\n"
      "v_and_b32 v187, s92, v178\n"
      "v_lshlrev_b32 v188, 16, v177\n"
      "v_lshlrev_b32 v189, 16, v179\n"
      "v_and_b32 v190, s92, v177\n"
      "v_and_b32 v191, s92, v179\n"
      "s_mul_i32 s62, s85, 67584\n"
      "s_lshl_b32 s63, s84, 7\n"
      "s_add_u32 s62, s62, s63\n"
      "s_add_u32 s62, s62, 32768\n"
      "s_add_u32 s63, s62, 4224\n"
      "s_lshl_b32 s64, s85, 5\n"
      "s_add_u32 s64, s64, s84\n"
      "s_lshl_b32 s64, s64, 7\n"
      "s_add_u32 s64, s64, 18644992\n"
      "s_mov_b32 exec_lo, 0x0\n"
      "s_mov_b32 exec_hi, 0xffff\n"
      "s_sub_u32 s64, s64, 256\n"
      "v_lshl_add_u32 v23, v1, 3, s64\n"
      "s_mov_b64 exec, -1\n"
      "s_mov_b32 s3, 0x80000\n"
      ".Lp_209:\n"
      "ds_read_b32 v31, v2 offset:4104\n"
      "s_sub_u32 s3, s3, 1\n"
      "s_waitcnt lgkmcnt(0)\n"
      "v_readlane_b32 s2, v31, 0\n"
      "s_cselect_b32 s2, -1, s2\n"
      "s_cmp_lt_u32 s2, 4\n"
      "s_cbranch_scc1 .Lp_209\n"
      "ds_read2_b32 v[196:197], v192 offset0:0 offset1:32\n"
      "ds_read2_b32 v[198:199], v192 offset0:64 offset1:100\n"
      "ds_read2_b32 v[200:201], v192 offset0:132 offset1:164\n"
      "ds_read2_b32 v[202:203], v192 offset0:196 offset1:228\n"
      "ds_read_b32 v204, v192 offset:1040\n"
      "ds_read_b32 v205, v192 offset:1168\n"
      "ds_read_b32 v206, v192 offset:1296\n"
      "ds_read2_b32 v[208:209], v193 offset0:0 offset1:32\n"
      "ds_read2_b32 v[210:211], v193 offset0:64 offset1:96\n"
      "ds_read2_b32 v[212:213], v193 offset0:128 offset1:160\n"
      "ds_read2_b32 v[214:215], v193 offset0:192 offset1:224\n"
      "s_waitcnt lgkmcnt(9)\n"
      "v_lshlrev_b32 v216, 16, v196\n"
      "v_and_b32 v217, s92, v196\n"
      "v_lshlrev_b32 v218, 16, v197\n"
      "v_and_b32 v219, s92, v197\n"
      "v_lshlrev_b32 v220, 16, v198\n"
      "v_and_b32 v221, s92, v198\n"
      "v_lshlrev_b32 v222, 16, v199\n"
      "v_and_b32 v223, s92, v199\n"
      "s_waitcnt lgkmcnt(7)\n"
      "v_lshlrev_b32 v224, 16, v200\n"
      "v_and_b32 v225, s92, v200\n"
      "v_lshlrev_b32 v226, 16, v201\n"
      "v_and_b32 v227, s92, v201\n"
      "v_lshlrev_b32 v228, 16, v202\n"
      "v_and_b32 v229, s92, v202\n"
      "v_lshlrev_b32 v230, 16, v203\n"
      "v_and_b32 v231, s92, v203\n"
      "s_waitcnt lgkmcnt(4)\n"
      "v_lshlrev_b32 v232, 16, v204\n"
      "v_and_b32 v233, s92, v204\n"
      "v_lshlrev_b32 v234, 16, v205\n"
      "v_and_b32 v235, s92, v205\n"
      "v_lshlrev_b32 v236, 16, v206\n"
      "v_and_b32 v237, s92, v206\n"
      "v_pk_mul_f32 v[240:241], v[216:217], v[190:191]\n"
      "v_pk_mul_f32 v[242:243], v[218:219], v[190:191]\n"
      "v_pk_mul_f32 v[244:245], v[220:221], v[190:191]\n"
      "v_pk_mul_f32 v[246:247], v[222:223], v[190:191]\n"
      "v_pk_mul_f32 v[248:249], v[224:225], v[190:191]\n"
      "v_pk_mul_f32 v[250:251], v[226:227], v[190:191]\n"
      "v_pk_mul_f32 v[252:253], v[228:229], v[190:191]\n"
      "v_pk_mul_f32 v[254:255], v[230:231], v[190:191]\n"
      "v_pk_fma_f32 v[240:241], v[218:219], v[188:189], v[240:241]\n"
      "v_pk_fma_f32 v[242:243], v[220:221], v[188:189], v[242:243]\n"
      "v_pk_fma_f32 v[244:245], v[222:223], v[188:189], v[244:245]\n"
      "v_pk_fma_f32 v[246:247], v[224:225], v[188:189], v[246:247]\n"
      "v_pk_fma_f32 v[248:249], v[226:227], v[188:189], v[248:249]\n"
      "v_pk_fma_f32 v[250:251], v[228:229], v[188:189], v[250:251]\n"
      "v_pk_fma_f32 v[252:253], v[230:231], v[188:189], v[252:253]\n"
      "v_pk_fma_f32 v[254:255], v[232:233], v[188:189], v[254:255]\n"
      "v_pk_fma_f32 v[240:241], v[220:221], v[186:187], v[240:241]\n"
      "v_pk_fma_f32 v[242:243], v[222:223], v[186:187], v[242:243]\n"
      "v_pk_fma_f32 v[244:245], v[224:225], v[186:187], v[244:245]\n"
      "v_pk_fma_f32 v[246:247], v[226:227], v[186:187], v[246:247]\n"
      "v_pk_fma_f32 v[248:249], v[228:229], v[186:187], v[248:249]\n"
      "v_pk_fma_f32 v[250:251], v[230:231], v[186:187], v[250:251]\n"
      "v_pk_fma_f32 v[252:253], v[232:233], v[186:187], v[252:253]\n"
      "v_pk_fma_f32 v[254:255], v[234:235], v[186:187], v[254:255]\n"
      "v_pk_fma_f32 v[240:241], v[222:223], v[184:185], v[240:241]\n"
      "v_pk_fma_f32 v[242:243], v[224:225], v[184:185], v[242:243]\n"
      "v_pk_fma_f32 v[244:245], v[226:227], v[184:185], v[244:245]\n"
      "v_pk_fma_f32 v[246:247], v[228:229], v[184:185], v[246:247]\n"
      "v_pk_fma_f32 v[248:249], v[230:231], v[184:185], v[248:249]\n"
      "v_pk_fma_f32 v[250:251], v[232:233], v[184:185], v[250:251]\n"
      "v_pk_fma_f32 v[252:253], v[234:235], v[184:185], v[252:253]\n"
      "v_pk_fma_f32 v[254:255], v[236:237], v[184:185], v[254:255]\n"
      "s_waitcnt lgkmcnt(0)\n"
      "v_lshlrev_b32 v216, 16, v208\n"
      "v_and_b32 v217, s92, v208\n"
      "v_lshlrev_b32 v218, 16, v209\n"
      "v_and_b32 v219, s92, v209\n"
      "v_lshlrev_b32 v220, 16, v210\n"
      "v_and_b32 v221, s92, v210\n"
      "v_lshlrev_b32 v222, 16, v211\n"
      "v_and_b32 v223, s92, v211\n"
      "v_lshlrev_b32 v224, 16, v212\n"
      "v_and_b32 v225, s92, v212\n"
      "v_lshlrev_b32 v226, 16, v213\n"
      "v_and_b32 v227, s92, v213\n"
      "v_lshlrev_b32 v228, 16, v214\n"
      "v_and_b32 v229, s92, v214\n"
      "v_lshlrev_b32 v230, 16, v215\n"
      "v_and_b32 v231, s92, v215\n"
      "v_pk_mul_f32 v[240:241], v[240:241], v[216:217]\n"
      "v_pk_mul_f32 v[242:243], v[242:243], v[218:219]\n"
      "v_pk_mul_f32 v[244:245], v[244:245], v[220:221]\n"
      "v_pk_mul_f32 v[246:247], v[246:247], v[222:223]\n"
      "v_pk_mul_f32 v[248:249], v[248:249], v[224:225]\n"
      "v_pk_mul_f32 v[250:251], v[250:251], v[226:227]\n"
      "v_pk_mul_f32 v[252:253], v[252:253], v[228:229]\n"
      "v_pk_mul_f32 v[254:255], v[254:255], v[230:231]\n"
      "v_cvt_pk_bf16_f32 v196, v242, v240\n"
      "v_cvt_pk_bf16_f32 v197, v246, v244\n"
      "v_cvt_pk_bf16_f32 v198, v250, v248\n"
      "v_cvt_pk_bf16_f32 v199, v254, v252\n"
      "buffer_store_dwordx4 v[196:199], v8, s[20:23], s62 offen sc1\n"
      "v_cvt_pk_bf16_f32 v200, v243, v241\n"
      "v_cvt_pk_bf16_f32 v201, v247, v245\n"
      "v_cvt_pk_bf16_f32 v202, v251, v249\n"
      "v_cvt_pk_bf16_f32 v203, v255, v253\n"
      "buffer_store_dwordx4 v[200:203], v8, s[20:23], s63 offen sc1\n"
      "s_waitcnt vmcnt(0)\n"
      "buffer_store_dwordx2 v[12:13], v23, s[20:23], 0 offen sc1\n"
      "ds_add_u32 v2, v3 offset:4108\n"
      "ds_read_b32 v26, v2 offset:5120\n"
      "v_lshlrev_b32 v31, 3, v1\n"
      "v_add_u32 v31, 163072, v31\n"
      "s_waitcnt lgkmcnt(0)\n"
      "v_and_b32 v27, s92, v26\n"
      "v_lshlrev_b32 v26, 16, v26\n"
      "ds_write_b64 v31, v[26:27]\n"
      "ds_add_u32 v2, v3 offset:4168\n"
      "s_mov_b32 s81, 2\n"
      "s_lshl3_add_u32 s64, s60, s52\n"
      "s_cmp_eq_u32 s64, 2040\n"
      "s_cbranch_scc0 .Le_210\n"
      "v_lshlrev_b32 v25, 3, v1\n"
      "s_mov_b32 s64, 4096\n"
      "buffer_load_dwordx2 v[26:27], v25, s[20:23], s64 offen sc1\n"
      "s_waitcnt vmcnt(0)\n"
      "v_cmp_ne_u64 vcc, s[58:59], v[26:27]\n"
      "s_cbranch_vccnz .Lf_211\n"
      ".Lf_212:\n"
      "s_mov_b32 s64, 4608\n"
      "buffer_load_dwordx2 v[26:27], v25, s[20:23], s64 offen sc1\n"
      "s_waitcnt vmcnt(0)\n"
      "v_cmp_ne_u64 vcc, s[58:59], v[26:27]\n"
      "s_cbranch_vccnz .Lf_214\n"
      ".Lf_215:\n"
      "s_mov_b32 s64, 5120\n"
      "buffer_load_dwordx2 v[26:27], v25, s[20:23], s64 offen sc1\n"
      "s_waitcnt vmcnt(0)\n"
      "v_cmp_ne_u64 vcc, s[58:59], v[26:27]\n"
      "s_cbranch_vccnz .Lf_217\n"
      ".Lf_218:\n"
      "s_mov_b32 s64, 5632\n"
      "buffer_load_dwordx2 v[26:27], v25, s[20:23], s64 offen sc1\n"
      "s_waitcnt vmcnt(0)\n"
      "v_cmp_ne_u64 vcc, s[58:59], v[26:27]\n"
      "s_cbranch_vccnz .Lf_220\n"
      ".Lf_221:\n"
      "s_mov_b64 exec, 1\n"
      "buffer_store_dwordx2 v[12:13], off, s[20:23], 0 sc1\n"
      "s_mov_b64 exec, -1\n"
      ".Le_210:\n"
      "s_sub_u32 s66, 3, s53\n"
      "s_lshl1_add_u32 s62, s56, s54\n"
      "s_lshl2_add_u32 s63, s62, s66\n"
      "s_mul_i32 s62, s63, 20480\n"
      "s_mul_i32 s63, s55, 160\n"
      "s_add_u32 s62, s62, s63\n"
      "v_and_b32 v21, 15, v1\n"
      "v_mul_u32_u24 v21, 1280, v21\n"
      "v_lshrrev_b32 v22, 4, v1\n"
      "v_lshl_add_u32 v21, v22, 3, v21\n"
      "buffer_load_dwordx2 v[218:219], v21, s[4:7], s62 offen offset:0\n"
      "buffer_load_dwordx2 v[220:221], v21, s[4:7], s62 offen offset:32\n"
      "buffer_load_dwordx2 v[222:223], v21, s[4:7], s62 offen offset:64\n"
      "buffer_load_dwordx2 v[224:225], v21, s[4:7], s62 offen offset:96\n"
      "buffer_load_dwordx2 v[226:227], v21, s[4:7], s62 offen offset:128\n"
      "s_mov_b32 s79, 1073741824\n"
      "s_mov_b32 s77, 1073741824\n"
      "s_mov_b32 s84, 64\n"
      "s_mov_b32 s85, 64\n"
      "s_setprio 3\n"
      "v_and_b32 v10, 1, v1\n"
      "v_xor_b32 v10, 1, v10\n"
      "v_mul_u32_u24 v10, 4160, v10\n"
      "v_and_b32 v11, 14, v1\n"
      "v_lshl_add_u32 v10, v11, 1, v10\n"
      "v_and_b32 v11, 48, v1\n"
      "v_sub_u32 v10, v10, v11\n"
      "v_and_b32 v21, 15, v1\n"
      "v_lshl_add_u32 v11, v21, 5, v11\n"
      "s_lshr_b32 s64, s53, 1\n"
      "s_and_b32 s65, s53, 1\n"
      "s_mul_i32 s67, s64, 26112\n"
      "s_add_u32 s67, s67, 70144\n"
      "s_mul_i32 s62, s65, 8320\n"
      "s_add_u32 s62, s62, s67\n"
      "s_add_u32 s62, s62, 84\n"
      "v_add_u32 v212, s62, v10\n"
      "s_mul_i32 s62, s65, 4608\n"
      "s_add_u32 s62, s62, s67\n"
      "s_add_u32 s62, s62, 16672\n"
      "v_add_u32 v217, s62, v11\n"
      "v_lshrrev_b32 v22, 5, v1\n"
      "v_lshlrev_b32 v22, 4, v22\n"
      "v_lshl_add_u32 v22, v21, 5, v22\n"
      "v_bfe_u32 v21, v1, 4, 1\n"
      "v_lshl_add_u32 v5, v21, 9, v22\n"
      "s_add_u32 s63, s98, s53\n"
      "s_mul_i32 s63, s63, 4224\n"
      "s_add_u32 s63, s63, 2801152\n"
      "s_mov_b32 s3, 0x80000\n"
      ".Lp_223:\n"
      "s_sleep 1\n"
      "ds_read_b32 v31, v2 offset:4240\n"
      "s_sub_u32 s3, s3, 1\n"
      "s_waitcnt lgkmcnt(0)\n"
      "v_readlane_b32 s2, v31, 0\n"
      "s_cselect_b32 s2, -1, s2\n"
      "s_cmp_lt_u32 s2, 6\n"
      "s_cbranch_scc1 .Lp_223\n"
      "v_add_u32 v213, 1024, v212\n"
      "v_add_u32 v214, 2048, v212\n"
      "v_add_u32 v215, 3072, v212\n"
      "ds_read2_b32 v[144:145], v212 offset0:3 offset1:2\n"
      "ds_read2_b32 v[146:147], v212 offset0:1 offset1:0\n"
      "ds_read_b128 v[188:191], v217 offset:1984\n"
      "ds_read_b128 v[184:187], v217 offset:1472\n"
      "ds_read_b128 v[180:183], v217 offset:960\n"
      "ds_read_b128 v[176:179], v217 offset:448\n"
      "ds_read2_b32 v[148:149], v212 offset0:131 offset1:130\n"
      "ds_read2_b32 v[150:151], v212 offset0:129 offset1:128\n"
      "ds_read2_b32 v[152:153], v213 offset0:3 offset1:2\n"
      "ds_read2_b32 v[154:155], v213 offset0:1 offset1:0\n"
      "s_waitcnt lgkmcnt(7)\n"
      "v_mfma_f32_16x16x32_bf16 v[108:111], v[144:147], v[188:191], 0\n"
      "ds_read2_b32 v[156:157], v213 offset0:131 offset1:130\n"
      "s_waitcnt lgkmcnt(7)\n"
      "v_mfma_f32_16x16x32_bf16 v[104:107], v[144:147], v[184:187], 0\n"
      "ds_read2_b32 v[158:159], v213 offset0:129 offset1:128\n"
      "s_waitcnt lgkmcnt(7)\n"
      "v_mfma_f32_16x16x32_bf16 v[100:103], v[144:147], v[180:183], 0\n"
      "ds_read2_b32 v[160:161], v214 offset0:3 offset1:2\n"
      "s_waitcnt lgkmcnt(7)\n"
      "v_mfma_f32_16x16x32_bf16 v[96:99], v[144:147], v[176:179], 0\n"
      "ds_read2_b32 v[162:163], v214 offset0:1 offset1:0\n"
      "s_waitcnt lgkmcnt(6)\n"
      "v_mfma_f32_16x16x32_bf16 v[112:115], v[148:151], v[188:191], 0\n"
      "ds_read2_b32 v[164:165], v214 offset0:131 offset1:130\n"
      "v_mfma_f32_16x16x32_bf16 v[108:111], v[148:151], v[184:187], v[108:111]\n"
      "ds_read2_b32 v[166:167], v214 offset0:129 offset1:128\n"
      "v_mfma_f32_16x16x32_bf16 v[104:107], v[148:151], v[180:183], v[104:107]\n"
      "ds_read2_b32 v[168:169], v215 offset0:3 offset1:2\n"
      "v_mfma_f32_16x16x32_bf16 v[100:103], v[148:151], v[176:179], v[100:103]\n"
      "ds_read2_b32 v[170:171], v215 offset0:1 offset1:0\n"
      "s_waitcnt lgkmcnt(7)\n"
      "v_mfma_f32_16x16x32_bf16 v[116:119], v[152:155], v[188:191], 0\n"
      "ds_read2_b32 v[172:173], v215 offset0:131 offset1:130\n"
      "v_mfma_f32_16x16x32_bf16 v[112:115], v[152:155], v[184:187], v[112:115]\n"
      "ds_read2_b32 v[174:175], v215 offset0:129 offset1:128\n"
      "v_mfma_f32_16x16x32_bf16 v[108:111], v[152:155], v[180:183], v[108:111]\n"
      "ds_read2_b32 v[144:145], v212 offset0:19 offset1:18\n"
      "v_mfma_f32_16x16x32_bf16 v[104:107], v[152:155], v[176:179], v[104:107]\n"
      "ds_read2_b32 v[146:147], v212 offset0:17 offset1:16\n"
      "s_waitcnt lgkmcnt(7)\n"
      "v_mfma_f32_16x16x32_bf16 v[120:123], v[156:159], v[188:191], 0\n"
      "ds_read_b128 v[204:207], v217 offset:1920\n"
      "v_mfma_f32_16x16x32_bf16 v[116:119], v[156:159], v[184:187], v[116:119]\n"
      "ds_read_b128 v[200:203], v217 offset:1408\n"
      "v_mfma_f32_16x16x32_bf16 v[112:115], v[156:159], v[180:183], v[112:115]\n"
      "ds_read_b128 v[196:199], v217 offset:896\n"
      "v_mfma_f32_16x16x32_bf16 v[108:111], v[156:159], v[176:179], v[108:111]\n"
      "ds_read_b128 v[192:195], v217 offset:384\n"
      "v_mfma_f32_16x16x32_bf16 v[124:127], v[160:163], v[188:191], 0\n"
      "ds_read2_b32 v[148:149], v212 offset0:147 offset1:146\n"
      "v_mfma_f32_16x16x32_bf16 v[120:123], v[160:163], v[184:187], v[120:123]\n"
      "ds_read2_b32 v[150:151], v212 offset0:145 offset1:144\n"
      "v_mfma_f32_16x16x32_bf16 v[116:119], v[160:163], v[180:183], v[116:119]\n"
      "ds_read2_b32 v[152:153], v213 offset0:19 offset1:18\n"
      "v_mfma_f32_16x16x32_bf16 v[112:115], v[160:163], v[176:179], v[112:115]\n"
      "ds_read2_b32 v[154:155], v213 offset0:17 offset1:16\n"
      "s_waitcnt lgkmcnt(7)\n"
      "v_mfma_f32_16x16x32_bf16 v[124:127], v[164:167], v[184:187], v[124:127]\n"
      "ds_read2_b32 v[156:157], v213 offset0:147 offset1:146\n"
      "v_mfma_f32_16x16x32_bf16 v[120:123], v[164:167], v[180:183], v[120:123]\n"
      "ds_read2_b32 v[158:159], v213 offset0:145 offset1:144\n"
      "v_mfma_f32_16x16x32_bf16 v[116:119], v[164:167], v[176:179], v[116:119]\n"
      "ds_read2_b32 v[160:161], v214 offset0:19 offset1:18\n"
      "v_mfma_f32_16x16x32_bf16 v[124:127], v[168:171], v[180:183], v[124:127]\n"
      "ds_read2_b32 v[162:163], v214 offset0:17 offset1:16\n"
      "v_mfma_f32_16x16x32_bf16 v[120:123], v[168:171], v[176:179], v[120:123]\n"
      "ds_read2_b32 v[164:165], v214 offset0:147 offset1:146\n"
      "v_mfma_f32_16x16x32_bf16 v[124:127], v[172:175], v[176:179], v[124:127]\n"
      "ds_read2_b32 v[166:167], v214 offset0:145 offset1:144\n"
      "v_mfma_f32_16x16x32_bf16 v[108:111], v[144:147], v[204:207], v[108:111]\n"
      "ds_read2_b32 v[168:169], v215 offset0:19 offset1:18\n"
      "s_waitcnt lgkmcnt(7)\n"
      "v_mfma_f32_16x16x32_bf16 v[104:107], v[144:147], v[200:203], v[104:107]\n"
      "ds_read2_b32 v[170:171], v215 offset0:17 offset1:16\n"
      "v_mfma_f32_16x16x32_bf16 v[100:103], v[144:147], v[196:199], v[100:103]\n"
      "ds_read2_b32 v[172:173], v215 offset0:147 offset1:146\n"
      "v_mfma_f32_16x16x32_bf16 v[96:99], v[144:147], v[192:195], v[96:99]\n"
      "ds_read2_b32 v[174:175], v215 offset0:145 offset1:144\n"
      "v_mfma_f32_16x16x32_bf16 v[112:115], v[148:151], v[204:207], v[112:115]\n"
      "ds_read2_b32 v[144:145], v212 offset0:35 offset1:34\n"
      "v_mfma_f32_16x16x32_bf16 v[108:111], v[148:151], v[200:203], v[108:111]\n"
      "ds_read2_b32 v[146:147], v212 offset0:33 offset1:32\n"
      "v_mfma_f32_16x16x32_bf16 v[104:107], v[148:151], v[196:199], v[104:107]\n"
      "ds_read_b128 v[188:191], v217 offset:1856\n"
      "v_mfma_f32_16x16x32_bf16 v[100:103], v[148:151], v[192:195], v[100:103]\n"
      "ds_read_b128 v[184:187], v217 offset:1344\n"
      "v_mfma_f32_16x16x32_bf16 v[116:119], v[152:155], v[204:207], v[116:119]\n"
      "ds_read_b128 v[180:183], v217 offset:832\n"
      "v_mfma_f32_16x16x32_bf16 v[112:115], v[152:155], v[200:203], v[112:115]\n"
      "ds_read_b128 v[176:179], v217 offset:320\n"
      "v_mfma_f32_16x16x32_bf16 v[108:111], v[152:155], v[196:199], v[108:111]\n"
      "ds_read2_b32 v[148:149], v212 offset0:163 offset1:162\n"
      "v_mfma_f32_16x16x32_bf16 v[104:107], v[152:155], v[192:195], v[104:107]\n"
      "ds_read2_b32 v[150:151], v212 offset0:161 offset1:160\n"
      "s_waitcnt lgkmcnt(7)\n"
      "v_mfma_f32_16x16x32_bf16 v[120:123], v[156:159], v[204:207], v[120:123]\n"
      "ds_read2_b32 v[152:153], v213 offset0:35 offset1:34\n"
      "v_mfma_f32_16x16x32_bf16 v[116:119], v[156:159], v[200:203], v[116:119]\n"
      "ds_read2_b32 v[154:155], v213 offset0:33 offset1:32\n"
      "v_mfma_f32_16x16x32_bf16 v[112:115], v[156:159], v[196:199], v[112:115]\n"
      "v_mfma_f32_16x16x32_bf16 v[108:111], v[156:159], v[192:195], v[108:111]\n"
      "ds_read2_b32 v[156:157], v213 offset0:163 offset1:162\n"
      "v_mfma_f32_16x16x32_bf16 v[124:127], v[160:163], v[204:207], v[124:127]\n"
      "ds_read2_b32 v[158:159], v213 offset0:161 offset1:160\n"
      "v_mfma_f32_16x16x32_bf16 v[120:123], v[160:163], v[200:203], v[120:123]\n"
      "v_mfma_f32_16x16x32_bf16 v[116:119], v[160:163], v[196:199], v[116:119]\n"
      "v_mfma_f32_16x16x32_bf16 v[112:115], v[160:163], v[192:195], v[112:115]\n"
      "ds_read2_b32 v[160:161], v214 offset0:35 offset1:34\n"
      "v_mfma_f32_16x16x32_bf16 v[124:127], v[164:167], v[200:203], v[124:127]\n"
      "ds_read2_b32 v[162:163], v214 offset0:33 offset1:32\n"
      "v_mfma_f32_16x16x32_bf16 v[120:123], v[164:167], v[196:199], v[120:123]\n"
      "v_mfma_f32_16x16x32_bf16 v[116:119], v[164:167], v[192:195], v[116:119]\n"
      "ds_read2_b32 v[164:165], v214 offset0:163 offset1:162\n"
      "v_mfma_f32_16x16x32_bf16 v[124:127], v[168:171], v[196:199], v[124:127]\n"
      "ds_read2_b32 v[166:167], v214 offset0:161 offset1:160\n"
      "v_mfma_f32_16x16x32_bf16 v[120:123], v[168:171], v[192:195], v[120:123]\n"
      "v_mfma_f32_16x16x32_bf16 v[124:127], v[172:175], v[192:195], v[124:127]\n"
      "ds_read2_b32 v[168:169], v215 offset0:35 offset1:34\n"
      "s_waitcnt lgkmcnt(5)\n"
      "v_mfma_f32_16x16x32_bf16 v[108:111], v[144:147], v[188:191], v[108:111]\n"
      "ds_read2_b32 v[170:171], v215 offset0:33 offset1:32\n"
      "v_mfma_f32_16x16x32_bf16 v[104:107], v[144:147], v[184:187], v[104:107]\n"
      "ds_read2_b32 v[172:173], v215 offset0:163 offset1:162\n"
      "v_mfma_f32_16x16x32_bf16 v[100:103], v[144:147], v[180:183], v[100:103]\n"
      "ds_read2_b32 v[174:175], v215 offset0:161 offset1:160\n"
      "v_mfma_f32_16x16x32_bf16 v[96:99], v[144:147], v[176:179], v[96:99]\n"
      "ds_read2_b32 v[144:145], v212 offset0:51 offset1:50\n"
      "v_mfma_f32_16x16x32_bf16 v[112:115], v[148:151], v[188:191], v[112:115]\n"
      "ds_read2_b32 v[146:147], v212 offset0:49 offset1:48\n"
      "v_mfma_f32_16x16x32_bf16 v[108:111], v[148:151], v[184:187], v[108:111]\n"
      "ds_read_b128 v[204:207], v217 offset:1792\n"
      "v_mfma_f32_16x16x32_bf16 v[104:107], v[148:151], v[180:183], v[104:107]\n"
      "ds_read_b128 v[200:203], v217 offset:1280\n"
      "v_mfma_f32_16x16x32_bf16 v[100:103], v[148:151], v[176:179], v[100:103]\n"
      "ds_read_b128 v[196:199], v217 offset:768\n"
      "v_mfma_f32_16x16x32_bf16 v[116:119], v[152:155], v[188:191], v[116:119]\n"
      "ds_read_b128 v[192:195], v217 offset:256\n"
      "v_mfma_f32_16x16x32_bf16 v[112:115], v[152:155], v[184:187], v[112:115]\n"
      "ds_read2_b32 v[148:149], v212 offset0:179 offset1:178\n"
      "v_mfma_f32_16x16x32_bf16 v[108:111], v[152:155], v[180:183], v[108:111]\n"
      "ds_read2_b32 v[150:151], v212 offset0:177 offset1:176\n"
      "v_mfma_f32_16x16x32_bf16 v[104:107], v[152:155], v[176:179], v[104:107]\n"
      "ds_read2_b32 v[152:153], v213 offset0:51 offset1:50\n"
      "v_mfma_f32_16x16x32_bf16 v[120:123], v[156:159], v[188:191], v[120:123]\n"
      "ds_read2_b32 v[154:155], v213 offset0:49 offset1:48\n"
      "v_mfma_f32_16x16x32_bf16 v[116:119], v[156:159], v[184:187], v[116:119]\n"
      "v_mfma_f32_16x16x32_bf16 v[112:115], v[156:159], v[180:183], v[112:115]\n"
      "v_mfma_f32_16x16x32_bf16 v[108:111], v[156:159], v[176:179], v[108:111]\n"
      "ds_read2_b32 v[156:157], v213 offset0:179 offset1:178\n"
      "s_waitcnt lgkmcnt(5)\n"
      "v_mfma_f32_16x16x32_bf16 v[124:127], v[160:163], v[188:191], v[124:127]\n"
      "ds_read2_b32 v[158:159], v213 offset0:177 offset1:176\n"
      "v_mfma_f32_16x16x32_bf16 v[120:123], v[160:163], v[184:187], v[120:123]\n"
      "v_mfma_f32_16x16x32_bf16 v[116:119], v[160:163], v[180:183], v[116:119]\n"
      "v_mfma_f32_16x16x32_bf16 v[112:115], v[160:163], v[176:179], v[112:115]\n"
      "ds_read2_b32 v[160:161], v214 offset0:51 offset1:50\n"
      "v_mfma_f32_16x16x32_bf16 v[124:127], v[164:167], v[184:187], v[124:127]\n"
      "ds_read2_b32 v[162:163], v214 offset0:49 offset1:48\n"
      "v_mfma_f32_16x16x32_bf16 v[120:123], v[164:167], v[180:183], v[120:123]\n"
      "v_mfma_f32_16x16x32_bf16 v[116:119], v[164:167], v[176:179], v[116:119]\n"
      "ds_read2_b32 v[164:165], v214 offset0:179 offset1:178\n"
      "v_mfma_f32_16x16x32_bf16 v[124:127], v[168:171], v[180:183], v[124:127]\n"
      "ds_read2_b32 v[166:167], v214 offset0:177 offset1:176\n"
      "v_mfma_f32_16x16x32_bf16 v[120:123], v[168:171], v[176:179], v[120:123]\n"
      "v_mfma_f32_16x16x32_bf16 v[124:127], v[172:175], v[176:179], v[124:127]\n"
      "ds_read2_b32 v[168:169], v215 offset0:51 offset1:50\n"
      "v_mfma_f32_16x16x32_bf16 v[108:111], v[144:147], v[204:207], v[108:111]\n"
      "ds_read2_b32 v[170:171], v215 offset0:49 offset1:48\n"
      "v_mfma_f32_16x16x32_bf16 v[104:107], v[144:147], v[200:203], v[104:107]\n"
      "ds_read2_b32 v[172:173], v215 offset0:179 offset1:178\n"
      "v_mfma_f32_16x16x32_bf16 v[100:103], v[144:147], v[196:199], v[100:103]\n"
      "ds_read2_b32 v[174:175], v215 offset0:177 offset1:176\n"
      "v_mfma_f32_16x16x32_bf16 v[96:99], v[144:147], v[192:195], v[96:99]\n"
      "ds_read2_b32 v[144:145], v212 offset0:67 offset1:66\n"
      "s_waitcnt lgkmcnt(6)\n"
      "v_mfma_f32_16x16x32_bf16 v[112:115], v[148:151], v[204:207], v[112:115]\n"
      "ds_read2_b32 v[146:147], v212 offset0:65 offset1:64\n"
      "v_mfma_f32_16x16x32_bf16 v[108:111], v[148:151], v[200:203], v[108:111]\n"
      "ds_read_b128 v[188:191], v217 offset:1728\n"
      "v_mfma_f32_16x16x32_bf16 v[104:107], v[148:151], v[196:199], v[104:107]\n"
      "ds_read_b128 v[184:187], v217 offset:1216\n"
      "v_mfma_f32_16x16x32_bf16 v[100:103], v[148:151], v[192:195], v[100:103]\n"
      "ds_read_b128 v[180:183], v217 offset:704\n"
      "v_mfma_f32_16x16x32_bf16 v[116:119], v[152:155], v[204:207], v[116:119]\n"
      "ds_read_b128 v[176:179], v217 offset:192\n"
      "v_mfma_f32_16x16x32_bf16 v[112:115], v[152:155], v[200:203], v[112:115]\n"
      "ds_read2_b32 v[148:149], v212 offset0:195 offset1:194\n"
      "v_mfma_f32_16x16x32_bf16 v[108:111], v[152:155], v[196:199], v[108:111]\n"
      "ds_read2_b32 v[150:151], v212 offset0:193 offset1:192\n"
      "v_mfma_f32_16x16x32_bf16 v[104:107], v[152:155], v[192:195], v[104:107]\n"
      "ds_read2_b32 v[152:153], v213 offset0:67 offset1:66\n"
      "v_mfma_f32_16x16x32_bf16 v[120:123], v[156:159], v[204:207], v[120:123]\n"
      "ds_read2_b32 v[154:155], v213 offset0:65 offset1:64\n"
      "v_mfma_f32_16x16x32_bf16 v[116:119], v[156:159], v[200:203], v[116:119]\n"
      "v_mfma_f32_16x16x32_bf16 v[112:115], v[156:159], v[196:199], v[112:115]\n"
      "v_mfma_f32_16x16x32_bf16 v[108:111], v[156:159], v[192:195], v[108:111]\n"
      "ds_read2_b32 v[156:157], v213 offset0:195 offset1:194\n"
      "v_mfma_f32_16x16x32_bf16 v[124:127], v[160:163], v[204:207], v[124:127]\n"
      "ds_read2_b32 v[158:159], v213 offset0:193 offset1:192\n"
      "v_mfma_f32_16x16x32_bf16 v[120:123], v[160:163], v[200:203], v[120:123]\n"
      "v_mfma_f32_16x16x32_bf16 v[116:119], v[160:163], v[196:199], v[116:119]\n"
      "v_mfma_f32_16x16x32_bf16 v[112:115], v[160:163], v[192:195], v[112:115]\n"
      "ds_read2_b32 v[160:161], v214 offset0:67 offset1:66\n"
      "s_waitcnt lgkmcnt(3)\n"
      "v_mfma_f32_16x16x32_bf16 v[124:127], v[164:167], v[200:203], v[124:127]\n"
      "ds_read2_b32 v[162:163], v214 offset0:65 offset1:64\n"
      "v_mfma_f32_16x16x32_bf16 v[120:123], v[164:167], v[196:199], v[120:123]\n"
      "v_mfma_f32_16x16x32_bf16 v[116:119], v[164:167], v[192:195], v[116:119]\n"
      "ds_read2_b32 v[164:165], v214 offset0:195 offset1:194\n"
      "v_mfma_f32_16x16x32_bf16 v[124:127], v[168:171], v[196:199], v[124:127]\n"
      "ds_read2_b32 v[166:167], v214 offset0:193 offset1:192\n"
      "v_mfma_f32_16x16x32_bf16 v[120:123], v[168:171], v[192:195], v[120:123]\n"
      "v_mfma_f32_16x16x32_bf16 v[124:127], v[172:175], v[192:195], v[124:127]\n"
      "ds_read2_b32 v[168:169], v215 offset0:67 offset1:66\n"
      "v_mfma_f32_16x16x32_bf16 v[108:111], v[144:147], v[188:191], v[108:111]\n"
      "ds_read2_b32 v[170:171], v215 offset0:65 offset1:64\n"
      "v_mfma_f32_16x16x32_bf16 v[104:107], v[144:147], v[184:187], v[104:107]\n"
      "ds_read2_b32 v[172:173], v215 offset0:195 offset1:194\n"
      "v_mfma_f32_16x16x32_bf16 v[100:103], v[144:147], v[180:183], v[100:103]\n"
      "ds_read2_b32 v[174:175], v215 offset0:193 offset1:192\n"
      "v_mfma_f32_16x16x32_bf16 v[96:99], v[144:147], v[176:179], v[96:99]\n"
      "ds_read2_b32 v[144:145], v212 offset0:83 offset1:82\n"
      "v_mfma_f32_16x16x32_bf16 v[112:115], v[148:151], v[188:191], v[112:115]\n"
      "ds_read2_b32 v[146:147], v212 offset0:81 offset1:80\n"
      "v_mfma_f32_16x16x32_bf16 v[108:111], v[148:151], v[184:187], v[108:111]\n"
      "ds_read_b128 v[204:207], v217 offset:1664\n"
      "v_mfma_f32_16x16x32_bf16 v[104:107], v[148:151], v[180:183], v[104:107]\n"
      "ds_read_b128 v[200:203], v217 offset:1152\n"
      "v_mfma_f32_16x16x32_bf16 v[100:103], v[148:151], v[176:179], v[100:103]\n"
      "ds_read_b128 v[196:199], v217 offset:640\n"
      "v_mfma_f32_16x16x32_bf16 v[116:119], v[152:155], v[188:191], v[116:119]\n"
      "ds_read_b128 v[192:195], v217 offset:128\n"
      "v_mfma_f32_16x16x32_bf16 v[112:115], v[152:155], v[184:187], v[112:115]\n"
      "ds_read2_b32 v[148:149], v212 offset0:211 offset1:210\n"
      "v_mfma_f32_16x16x32_bf16 v[108:111], v[152:155], v[180:183], v[108:111]\n"
      "ds_read2_b32 v[150:151], v212 offset0:209 offset1:208\n"
      "v_mfma_f32_16x16x32_bf16 v[104:107], v[152:155], v[176:179], v[104:107]\n"
      "ds_read2_b32 v[152:153], v213 offset0:83 offset1:82\n"
      "s_waitcnt lgkmcnt(7)\n"
      "v_mfma_f32_16x16x32_bf16 v[120:123], v[156:159], v[188:191], v[120:123]\n"
      "ds_read2_b32 v[154:155], v213 offset0:81 offset1:80\n"
      "v_mfma_f32_16x16x32_bf16 v[116:119], v[156:159], v[184:187], v[116:119]\n"
      "v_mfma_f32_16x16x32_bf16 v[112:115], v[156:159], v[180:183], v[112:115]\n"
      "v_mfma_f32_16x16x32_bf16 v[108:111], v[156:159], v[176:179], v[108:111]\n"
      "ds_read2_b32 v[156:157], v213 offset0:211 offset1:210\n"
      "v_mfma_f32_16x16x32_bf16 v[124:127], v[160:163], v[188:191], v[124:127]\n"
      "ds_read2_b32 v[158:159], v213 offset0:209 offset1:208\n"
      "v_mfma_f32_16x16x32_bf16 v[120:123], v[160:163], v[184:187], v[120:123]\n"
      "v_mfma_f32_16x16x32_bf16 v[116:119], v[160:163], v[180:183], v[116:119]\n"
      "v_mfma_f32_16x16x32_bf16 v[112:115], v[160:163], v[176:179], v[112:115]\n"
      "ds_read2_b32 v[160:161], v214 offset0:83 offset1:82\n"
      "v_mfma_f32_16x16x32_bf16 v[124:127], v[164:167], v[184:187], v[124:127]\n"
      "ds_read2_b32 v[162:163], v214 offset0:81 offset1:80\n"
      "v_mfma_f32_16x16x32_bf16 v[120:123], v[164:167], v[180:183], v[120:123]\n"
      "v_mfma_f32_16x16x32_bf16 v[116:119], v[164:167], v[176:179], v[116:119]\n"
      "ds_read2_b32 v[164:165], v214 offset0:211 offset1:210\n"
      "v_mfma_f32_16x16x32_bf16 v[124:127], v[168:171], v[180:183], v[124:127]\n"
      "ds_read2_b32 v[166:167], v214 offset0:209 offset1:208\n"
      "v_mfma_f32_16x16x32_bf16 v[120:123], v[168:171], v[176:179], v[120:123]\n"
      "v_mfma_f32_16x16x32_bf16 v[124:127], v[172:175], v[176:179], v[124:127]\n"
      "ds_read2_b32 v[168:169], v215 offset0:83 offset1:82\n"
      "s_waitcnt lgkmcnt(5)\n"
      "v_mfma_f32_16x16x32_bf16 v[108:111], v[144:147], v[204:207], v[108:111]\n"
      "ds_read2_b32 v[170:171], v215 offset0:81 offset1:80\n"
      "v_mfma_f32_16x16x32_bf16 v[104:107], v[144:147], v[200:203], v[104:107]\n"
      "ds_read2_b32 v[172:173], v215 offset0:211 offset1:210\n"
      "v_mfma_f32_16x16x32_bf16 v[100:103], v[144:147], v[196:199], v[100:103]\n"
      "ds_read2_b32 v[174:175], v215 offset0:209 offset1:208\n"
      "v_mfma_f32_16x16x32_bf16 v[96:99], v[144:147], v[192:195], v[96:99]\n"
      "ds_read2_b32 v[144:145], v212 offset0:99 offset1:98\n"
      "v_mfma_f32_16x16x32_bf16 v[112:115], v[148:151], v[204:207], v[112:115]\n"
      "ds_read2_b32 v[146:147], v212 offset0:97 offset1:96\n"
      "v_mfma_f32_16x16x32_bf16 v[108:111], v[148:151], v[200:203], v[108:111]\n"
      "ds_read_b128 v[188:191], v217 offset:1600\n"
      "v_mfma_f32_16x16x32_bf16 v[104:107], v[148:151], v[196:199], v[104:107]\n"
      "ds_read_b128 v[184:187], v217 offset:1088\n"
      "v_mfma_f32_16x16x32_bf16 v[100:103], v[148:151], v[192:195], v[100:103]\n"
      "ds_read_b128 v[180:183], v217 offset:576\n"
      "v_mfma_f32_16x16x32_bf16 v[116:119], v[152:155], v[204:207], v[116:119]\n"
      "ds_read_b128 v[176:179], v217 offset:64\n"
      "v_mfma_f32_16x16x32_bf16 v[112:115], v[152:155], v[200:203], v[112:115]\n"
      "ds_read2_b32 v[148:149], v212 offset0:227 offset1:226\n"
      "v_mfma_f32_16x16x32_bf16 v[108:111], v[152:155], v[196:199], v[108:111]\n"
      "ds_read2_b32 v[150:151], v212 offset0:225 offset1:224\n"
      "v_mfma_f32_16x16x32_bf16 v[104:107], v[152:155], v[192:195], v[104:107]\n"
      "ds_read2_b32 v[152:153], v213 offset0:99 offset1:98\n"
      "v_mfma_f32_16x16x32_bf16 v[120:123], v[156:159], v[204:207], v[120:123]\n"
      "ds_read2_b32 v[154:155], v213 offset0:97 offset1:96\n"
      "v_mfma_f32_16x16x32_bf16 v[116:119], v[156:159], v[200:203], v[116:119]\n"
      "v_mfma_f32_16x16x32_bf16 v[112:115], v[156:159], v[196:199], v[112:115]\n"
      "v_mfma_f32_16x16x32_bf16 v[108:111], v[156:159], v[192:195], v[108:111]\n"
      "ds_read2_b32 v[156:157], v213 offset0:227 offset1:226\n"
      "s_waitcnt lgkmcnt(5)\n"
      "v_mfma_f32_16x16x32_bf16 v[124:127], v[160:163], v[204:207], v[124:127]\n"
      "ds_read2_b32 v[158:159], v213 offset0:225 offset1:224\n"
      "v_mfma_f32_16x16x32_bf16 v[120:123], v[160:163], v[200:203], v[120:123]\n"
      "v_mfma_f32_16x16x32_bf16 v[116:119], v[160:163], v[196:199], v[116:119]\n"
      "v_mfma_f32_16x16x32_bf16 v[112:115], v[160:163], v[192:195], v[112:115]\n"
      "ds_read2_b32 v[160:161], v214 offset0:99 offset1:98\n"
      "v_mfma_f32_16x16x32_bf16 v[124:127], v[164:167], v[200:203], v[124:127]\n"
      "ds_read2_b32 v[162:163], v214 offset0:97 offset1:96\n"
      "v_mfma_f32_16x16x32_bf16 v[120:123], v[164:167], v[196:199], v[120:123]\n"
      "v_mfma_f32_16x16x32_bf16 v[116:119], v[164:167], v[192:195], v[116:119]\n"
      "ds_read2_b32 v[164:165], v214 offset0:227 offset1:226\n"
      "v_mfma_f32_16x16x32_bf16 v[124:127], v[168:171], v[196:199], v[124:127]\n"
      "ds_read2_b32 v[166:167], v214 offset0:225 offset1:224\n"
      "v_mfma_f32_16x16x32_bf16 v[120:123], v[168:171], v[192:195], v[120:123]\n"
      "v_mfma_f32_16x16x32_bf16 v[124:127], v[172:175], v[192:195], v[124:127]\n"
      "ds_read2_b32 v[168:169], v215 offset0:99 offset1:98\n"
      "v_mfma_f32_16x16x32_bf16 v[108:111], v[144:147], v[188:191], v[108:111]\n"
      "ds_read2_b32 v[170:171], v215 offset0:97 offset1:96\n"
      "v_mfma_f32_16x16x32_bf16 v[104:107], v[144:147], v[184:187], v[104:107]\n"
      "ds_read2_b32 v[172:173], v215 offset0:227 offset1:226\n"
      "v_mfma_f32_16x16x32_bf16 v[100:103], v[144:147], v[180:183], v[100:103]\n"
      "ds_read2_b32 v[174:175], v215 offset0:225 offset1:224\n"
      "v_mfma_f32_16x16x32_bf16 v[96:99], v[144:147], v[176:179], v[96:99]\n"
      "ds_read2_b32 v[144:145], v212 offset0:115 offset1:114\n"
      "s_waitcnt lgkmcnt(6)\n"
      "v_mfma_f32_16x16x32_bf16 v[112:115], v[148:151], v[188:191], v[112:115]\n"
      "ds_read2_b32 v[146:147], v212 offset0:113 offset1:112\n"
      "v_mfma_f32_16x16x32_bf16 v[108:111], v[148:151], v[184:187], v[108:111]\n"
      "ds_read_b128 v[204:207], v217 offset:1536\n"
      "v_mfma_f32_16x16x32_bf16 v[104:107], v[148:151], v[180:183], v[104:107]\n"
      "ds_read_b128 v[200:203], v217 offset:1024\n"
      "v_mfma_f32_16x16x32_bf16 v[100:103], v[148:151], v[176:179], v[100:103]\n"
      "ds_read_b128 v[196:199], v217 offset:512\n"
      "v_mfma_f32_16x16x32_bf16 v[116:119], v[152:155], v[188:191], v[116:119]\n"
      "ds_read_b128 v[192:195], v217 offset:0\n"
      "v_mfma_f32_16x16x32_bf16 v[112:115], v[152:155], v[184:187], v[112:115]\n"
      "ds_read2_b32 v[148:149], v212 offset0:243 offset1:242\n"
      "v_mfma_f32_16x16x32_bf16 v[108:111], v[152:155], v[180:183], v[108:111]\n"
      "ds_read2_b32 v[150:151], v212 offset0:241 offset1:240\n"
      "v_mfma_f32_16x16x32_bf16 v[104:107], v[152:155], v[176:179], v[104:107]\n"
      "ds_read2_b32 v[152:153], v213 offset0:115 offset1:114\n"
      "v_mfma_f32_16x16x32_bf16 v[120:123], v[156:159], v[188:191], v[120:123]\n"
      "ds_read2_b32 v[154:155], v213 offset0:113 offset1:112\n"
      "v_mfma_f32_16x16x32_bf16 v[116:119], v[156:159], v[184:187], v[116:119]\n"
      "v_mfma_f32_16x16x32_bf16 v[112:115], v[156:159], v[180:183], v[112:115]\n"
      "v_mfma_f32_16x16x32_bf16 v[108:111], v[156:159], v[176:179], v[108:111]\n"
      "ds_read2_b32 v[156:157], v213 offset0:243 offset1:242\n"
      "v_mfma_f32_16x16x32_bf16 v[124:127], v[160:163], v[188:191], v[124:127]\n"
      "ds_read2_b32 v[158:159], v213 offset0:241 offset1:240\n"
      "v_mfma_f32_16x16x32_bf16 v[120:123], v[160:163], v[184:187], v[120:123]\n"
      "v_mfma_f32_16x16x32_bf16 v[116:119], v[160:163], v[180:183], v[116:119]\n"
      "v_mfma_f32_16x16x32_bf16 v[112:115], v[160:163], v[176:179], v[112:115]\n"
      "ds_read2_b32 v[160:161], v214 offset0:115 offset1:114\n"
      "s_waitcnt lgkmcnt(3)\n"
      "v_mfma_f32_16x16x32_bf16 v[124:127], v[164:167], v[184:187], v[124:127]\n"
      "ds_read2_b32 v[162:163], v214 offset0:113 offset1:112\n"
      "v_mfma_f32_16x16x32_bf16 v[120:123], v[164:167], v[180:183], v[120:123]\n"
      "v_mfma_f32_16x16x32_bf16 v[116:119], v[164:167], v[176:179], v[116:119]\n"
      "ds_read2_b32 v[164:165], v214 offset0:243 offset1:242\n"
      "v_mfma_f32_16x16x32_bf16 v[124:127], v[168:171], v[180:183], v[124:127]\n"
      "ds_read2_b32 v[166:167], v214 offset0:241 offset1:240\n"
      "v_mfma_f32_16x16x32_bf16 v[120:123], v[168:171], v[176:179], v[120:123]\n"
      "v_mfma_f32_16x16x32_bf16 v[124:127], v[172:175], v[176:179], v[124:127]\n"
      "ds_read2_b32 v[168:169], v215 offset0:115 offset1:114\n"
      "v_mfma_f32_16x16x32_bf16 v[108:111], v[144:147], v[204:207], v[108:111]\n"
      "ds_read2_b32 v[170:171], v215 offset0:113 offset1:112\n"
      "v_mfma_f32_16x16x32_bf16 v[104:107], v[144:147], v[200:203], v[104:107]\n"
      "ds_read2_b32 v[172:173], v215 offset0:243 offset1:242\n"
      "v_mfma_f32_16x16x32_bf16 v[100:103], v[144:147], v[196:199], v[100:103]\n"
      "ds_read2_b32 v[174:175], v215 offset0:241 offset1:240\n"
      "v_mfma_f32_16x16x32_bf16 v[96:99], v[144:147], v[192:195], v[96:99]\n"
      "ds_read2_b32 v[144:145], v212 offset0:3 offset1:2\n"
      "v_mfma_f32_16x16x32_bf16 v[112:115], v[148:151], v[204:207], v[112:115]\n"
      "ds_read2_b32 v[146:147], v212 offset0:1 offset1:0\n"
      "v_mfma_f32_16x16x32_bf16 v[108:111], v[148:151], v[200:203], v[108:111]\n"
      "v_mfma_f32_16x16x32_bf16 v[104:107], v[148:151], v[196:199], v[104:107]\n"
      "v_mfma_f32_16x16x32_bf16 v[100:103], v[148:151], v[192:195], v[100:103]\n"
      "ds_read2_b32 v[148:149], v212 offset0:131 offset1:130\n"
      "v_mfma_f32_16x16x32_bf16 v[116:119], v[152:155], v[204:207], v[116:119]\n"
      "ds_read_b32 v31, v2 offset:4272\n"
      "v_mfma_f32_16x16x32_bf16 v[112:115], v[152:155], v[200:203], v[112:115]\n"
      "ds_read2_b32 v[150:151], v212 offset0:129 offset1:128\n"
      "v_mfma_f32_16x16x32_bf16 v[108:111], v[152:155], v[196:199], v[108:111]\n"
      "v_mfma_f32_16x16x32_bf16 v[104:107], v[152:155], v[192:195], v[104:107]\n"
      "ds_read2_b32 v[152:153], v213 offset0:3 offset1:2\n"
      "s_waitcnt lgkmcnt(4)\n"
      "v_mfma_f32_16x16x32_bf16 v[120:123], v[156:159], v[204:207], v[120:123]\n"
      "ds_read2_b32 v[154:155], v213 offset0:1 offset1:0\n"
      "v_mfma_f32_16x16x32_bf16 v[116:119], v[156:159], v[200:203], v[116:119]\n"
      "v_mfma_f32_16x16x32_bf16 v[112:115], v[156:159], v[196:199], v[112:115]\n"
      "s_waitcnt lgkmcnt(3)\n"
      "v_readfirstlane_b32 s2, v31\n"
      "s_cmp_lt_u32 s2, 2\n"
      "s_cbranch_scc1 .Lw_224\n"
      ".Lw_225:\n"
      "v_mfma_f32_16x16x32_bf16 v[108:111], v[156:159], v[192:195], v[108:111]\n"
      "ds_read2_b32 v[156:157], v213 offset0:131 offset1:130\n"
      "v_mfma_f32_16x16x32_bf16 v[124:127], v[160:163], v[204:207], v[124:127]\n"
      "ds_read_b128 v[204:207], v217 offset:4032\n"
      "v_mfma_f32_16x16x32_bf16 v[120:123], v[160:163], v[200:203], v[120:123]\n"
      "ds_read2_b32 v[158:159], v213 offset0:129 offset1:128\n"
      "v_mfma_f32_16x16x32_bf16 v[116:119], v[160:163], v[196:199], v[116:119]\n"
      "ds_read_b128 v[188:191], v217 offset:3968\n"
      "v_mfma_f32_16x16x32_bf16 v[112:115], v[160:163], v[192:195], v[112:115]\n"
      "ds_read2_b32 v[160:161], v212 offset0:19 offset1:18\n"
      "v_mfma_f32_16x16x32_bf16 v[124:127], v[164:167], v[200:203], v[124:127]\n"
      "ds_read_b128 v[200:203], v217 offset:3520\n"
      "v_mfma_f32_16x16x32_bf16 v[120:123], v[164:167], v[196:199], v[120:123]\n"
      "ds_read2_b32 v[162:163], v212 offset0:17 offset1:16\n"
      "v_mfma_f32_16x16x32_bf16 v[116:119], v[164:167], v[192:195], v[116:119]\n"
      "ds_read_b128 v[184:187], v217 offset:3456\n"
      "v_mfma_f32_16x16x32_bf16 v[124:127], v[168:171], v[196:199], v[124:127]\n"
      "ds_read_b128 v[196:199], v217 offset:3008\n"
      "v_mfma_f32_16x16x32_bf16 v[120:123], v[168:171], v[192:195], v[120:123]\n"
      "ds_read_b128 v[180:183], v217 offset:2944\n"
      "v_mfma_f32_16x16x32_bf16 v[124:127], v[172:175], v[192:195], v[124:127]\n"
      "ds_read_b128 v[192:195], v217 offset:2496\n"
      "s_waitcnt lgkmcnt(7)\n"
      "v_mfma_f32_16x16x32_bf16 v[124:127], v[144:147], v[204:207], v[124:127]\n"
      "ds_read_b128 v[176:179], v217 offset:2432\n"
      "s_waitcnt lgkmcnt(6)\n"
      "v_mfma_f32_16x16x32_bf16 v[120:123], v[144:147], v[200:203], v[120:123]\n"
      "ds_read2_b32 v[164:165], v212 offset0:147 offset1:146\n"
      "s_waitcnt lgkmcnt(4)\n"
      "v_mfma_f32_16x16x32_bf16 v[116:119], v[144:147], v[196:199], v[116:119]\n"
      "ds_read2_b32 v[166:167], v212 offset0:145 offset1:144\n"
      "s_waitcnt lgkmcnt(3)\n"
      "v_mfma_f32_16x16x32_bf16 v[112:115], v[144:147], v[192:195], v[112:115]\n"
      "ds_read2_b32 v[168:169], v213 offset0:19 offset1:18\n"
      "v_mfma_f32_16x16x32_bf16 v[124:127], v[148:151], v[200:203], v[124:127]\n"
      "ds_read2_b32 v[170:171], v213 offset0:17 offset1:16\n"
      "v_mfma_f32_16x16x32_bf16 v[120:123], v[148:151], v[196:199], v[120:123]\n"
      "ds_read2_b32 v[172:173], v213 offset0:147 offset1:146\n"
      "v_mfma_f32_16x16x32_bf16 v[116:119], v[148:151], v[192:195], v[116:119]\n"
      "ds_read2_b32 v[174:175], v213 offset0:145 offset1:144\n"
      "v_mfma_f32_16x16x32_bf16 v[124:127], v[152:155], v[196:199], v[124:127]\n"
      "ds_read2_b32 v[144:145], v212 offset0:35 offset1:34\n"
      "v_mfma_f32_16x16x32_bf16 v[120:123], v[152:155], v[192:195], v[120:123]\n"
      "ds_read2_b32 v[146:147], v212 offset0:33 offset1:32\n"
      "v_mfma_f32_16x16x32_bf16 v[124:127], v[156:159], v[192:195], v[124:127]\n"
      "ds_read_b128 v[204:207], v217 offset:3904\n"
      "v_mfma_f32_16x16x32_bf16 v[124:127], v[160:163], v[188:191], v[124:127]\n"
      "ds_read_b128 v[200:203], v217 offset:3392\n"
      "v_mfma_f32_16x16x32_bf16 v[120:123], v[160:163], v[184:187], v[120:123]\n"
      "ds_read_b128 v[196:199], v217 offset:2880\n"
      "v_mfma_f32_16x16x32_bf16 v[116:119], v[160:163], v[180:183], v[116:119]\n"
      "ds_read_b128 v[192:195], v217 offset:2368\n"
      "s_waitcnt lgkmcnt(7)\n"
      "v_mfma_f32_16x16x32_bf16 v[112:115], v[160:163], v[176:179], v[112:115]\n"
      "ds_read2_b32 v[148:149], v212 offset0:163 offset1:162\n"
      "ds_read2_b32 v[150:151], v212 offset0:161 offset1:160\n"
      "v_mfma_f32_16x16x32_bf16 v[124:127], v[164:167], v[184:187], v[124:127]\n"
      "ds_read2_b32 v[152:153], v213 offset0:35 offset1:34\n"
      "v_mfma_f32_16x16x32_bf16 v[120:123], v[164:167], v[180:183], v[120:123]\n"
      "ds_read2_b32 v[154:155], v213 offset0:33 offset1:32\n"
      "v_mfma_f32_16x16x32_bf16 v[116:119], v[164:167], v[176:179], v[116:119]\n"
      "ds_read2_b32 v[156:157], v213 offset0:163 offset1:162\n"
      "v_mfma_f32_16x16x32_bf16 v[124:127], v[168:171], v[180:183], v[124:127]\n"
      "ds_read2_b32 v[158:159], v213 offset0:161 offset1:160\n"
      "v_mfma_f32_16x16x32_bf16 v[120:123], v[168:171], v[176:179], v[120:123]\n"
      "ds_read2_b32 v[160:161], v212 offset0:51 offset1:50\n"
      "s_waitcnt lgkmcnt(8)\n"
      "v_mfma_f32_16x16x32_bf16 v[124:127], v[172:175], v[176:179], v[124:127]\n"
      "ds_read2_b32 v[162:163], v212 offset0:49 offset1:48\n"
      "ds_read_b128 v[188:191], v217 offset:3840\n"
      "v_mfma_f32_16x16x32_bf16 v[124:127], v[144:147], v[204:207], v[124:127]\n"
      "ds_read_b128 v[184:187], v217 offset:3328\n"
      "v_mfma_f32_16x16x32_bf16 v[120:123], v[144:147], v[200:203], v[120:123]\n"
      "ds_read_b128 v[180:183], v217 offset:2816\n"
      "v_mfma_f32_16x16x32_bf16 v[116:119], v[144:147], v[196:199], v[116:119]\n"
      "ds_read_b128 v[176:179], v217 offset:2304\n"
      "s_waitcnt lgkmcnt(8)\n"
      "v_mfma_f32_16x16x32_bf16 v[112:115], v[144:147], v[192:195], v[112:115]\n"
      "ds_read2_b32 v[164:165], v212 offset0:179 offset1:178\n"
      "ds_read2_b32 v[166:167], v212 offset0:177 offset1:176\n"
      "v_mfma_f32_16x16x32_bf16 v[124:127], v[148:151], v[200:203], v[124:127]\n"
      "ds_read2_b32 v[168:169], v213 offset0:51 offset1:50\n"
      "v_mfma_f32_16x16x32_bf16 v[120:123], v[148:151], v[196:199], v[120:123]\n"
      "ds_read2_b32 v[170:171], v213 offset0:49 offset1:48\n"
      "v_mfma_f32_16x16x32_bf16 v[116:119], v[148:151], v[192:195], v[116:119]\n"
      "ds_read2_b32 v[172:173], v213 offset0:179 offset1:178\n"
      "v_mfma_f32_16x16x32_bf16 v[124:127], v[152:155], v[196:199], v[124:127]\n"
      "ds_read2_b32 v[174:175], v213 offset0:177 offset1:176\n"
      "v_mfma_f32_16x16x32_bf16 v[120:123], v[152:155], v[192:195], v[120:123]\n"
      "ds_read2_b32 v[144:145], v212 offset0:67 offset1:66\n"
      "s_waitcnt lgkmcnt(8)\n"
      "v_mfma_f32_16x16x32_bf16 v[124:127], v[156:159], v[192:195], v[124:127]\n"
      "ds_read2_b32 v[146:147], v212 offset0:65 offset1:64\n"
      "ds_read_b128 v[204:207], v217 offset:3776\n"
      "v_mfma_f32_16x16x32_bf16 v[124:127], v[160:163], v[188:191], v[124:127]\n"
      "ds_read_b128 v[200:203], v217 offset:3264\n"
      "v_mfma_f32_16x16x32_bf16 v[120:123], v[160:163], v[184:187], v[120:123]\n"
      "ds_read_b128 v[196:199], v217 offset:2752\n"
      "v_mfma_f32_16x16x32_bf16 v[116:119], v[160:163], v[180:183], v[116:119]\n"
      "ds_read_b128 v[192:195], v217 offset:2240\n"
      "s_waitcnt lgkmcnt(8)\n"
      "v_mfma_f32_16x16x32_bf16 v[112:115], v[160:163], v[176:179], v[112:115]\n"
      "ds_read2_b32 v[148:149], v212 offset0:195 offset1:194\n"
      "ds_read2_b32 v[150:151], v212 offset0:193 offset1:192\n"
      "v_mfma_f32_16x16x32_bf16 v[124:127], v[164:167], v[184:187], v[124:127]\n"
      "ds_read2_b32 v[152:153], v213 offset0:67 offset1:66\n"
      "v_mfma_f32_16x16x32_bf16 v[120:123], v[164:167], v[180:183], v[120:123]\n"
      "ds_read2_b32 v[154:155], v213 offset0:65 offset1:64\n"
      "v_mfma_f32_16x16x32_bf16 v[116:119], v[164:167], v[176:179], v[116:119]\n"
      "ds_read2_b32 v[156:157], v213 offset0:195 offset1:194\n"
      "v_mfma_f32_16x16x32_bf16 v[124:127], v[168:171], v[180:183], v[124:127]\n"
      "ds_read2_b32 v[158:159], v213 offset0:193 offset1:192\n"
      "v_mfma_f32_16x16x32_bf16 v[120:123], v[168:171], v[176:179], v[120:123]\n"
      "ds_read2_b32 v[160:161], v212 offset0:83 offset1:82\n"
      "s_waitcnt lgkmcnt(8)\n"
      "v_mfma_f32_16x16x32_bf16 v[124:127], v[172:175], v[176:179], v[124:127]\n"
      "ds_read2_b32 v[162:163], v212 offset0:81 offset1:80\n"
      "ds_read_b128 v[188:191], v217 offset:3712\n"
      "v_mfma_f32_16x16x32_bf16 v[124:127], v[144:147], v[204:207], v[124:127]\n"
      "ds_read_b128 v[184:187], v217 offset:3200\n"
      "v_mfma_f32_16x16x32_bf16 v[120:123], v[144:147], v[200:203], v[120:123]\n"
      "ds_read_b128 v[180:183], v217 offset:2688\n"
      "v_mfma_f32_16x16x32_bf16 v[116:119], v[144:147], v[196:199], v[116:119]\n"
      "ds_read_b128 v[176:179], v217 offset:2176\n"
      "s_waitcnt lgkmcnt(8)\n"
      "v_mfma_f32_16x16x32_bf16 v[112:115], v[144:147], v[192:195], v[112:115]\n"
      "ds_read2_b32 v[164:165], v212 offset0:211 offset1:210\n"
      "ds_read2_b32 v[166:167], v212 offset0:209 offset1:208\n"
      "v_mfma_f32_16x16x32_bf16 v[124:127], v[148:151], v[200:203], v[124:127]\n"
      "ds_read2_b32 v[168:169], v213 offset0:83 offset1:82\n"
      "v_mfma_f32_16x16x32_bf16 v[120:123], v[148:151], v[196:199], v[120:123]\n"
      "ds_read2_b32 v[170:171], v213 offset0:81 offset1:80\n"
      "v_mfma_f32_16x16x32_bf16 v[116:119], v[148:151], v[192:195], v[116:119]\n"
      "ds_read2_b32 v[172:173], v213 offset0:211 offset1:210\n"
      "v_mfma_f32_16x16x32_bf16 v[124:127], v[152:155], v[196:199], v[124:127]\n"
      "ds_read2_b32 v[174:175], v213 offset0:209 offset1:208\n"
      "v_mfma_f32_16x16x32_bf16 v[120:123], v[152:155], v[192:195], v[120:123]\n"
      "ds_read2_b32 v[144:145], v212 offset0:99 offset1:98\n"
      "s_waitcnt lgkmcnt(8)\n"
      "v_mfma_f32_16x16x32_bf16 v[124:127], v[156:159], v[192:195], v[124:127]\n"
      "ds_read2_b32 v[146:147], v212 offset0:97 offset1:96\n"
      "ds_read_b128 v[204:207], v217 offset:3648\n"
      "v_mfma_f32_16x16x32_bf16 v[124:127], v[160:163], v[188:191], v[124:127]\n"
      "ds_read_b128 v[200:203], v217 offset:3136\n"
      "v_mfma_f32_16x16x32_bf16 v[120:123], v[160:163], v[184:187], v[120:123]\n"
      "ds_read_b128 v[196:199], v217 offset:2624\n"
      "v_mfma_f32_16x16x32_bf16 v[116:119], v[160:163], v[180:183], v[116:119]\n"
      "ds_read_b128 v[192:195], v217 offset:2112\n"
      "s_waitcnt lgkmcnt(8)\n"
      "v_mfma_f32_16x16x32_bf16 v[112:115], v[160:163], v[176:179], v[112:115]\n"
      "ds_read2_b32 v[148:149], v212 offset0:227 offset1:226\n"
      "ds_read2_b32 v[150:151], v212 offset0:225 offset1:224\n"
      "v_mfma_f32_16x16x32_bf16 v[124:127], v[164:167], v[184:187], v[124:127]\n"
      "ds_read2_b32 v[152:153], v213 offset0:99 offset1:98\n"
      "v_mfma_f32_16x16x32_bf16 v[120:123], v[164:167], v[180:183], v[120:123]\n"
      "ds_read2_b32 v[154:155], v213 offset0:97 offset1:96\n"
      "v_mfma_f32_16x16x32_bf16 v[116:119], v[164:167], v[176:179], v[116:119]\n"
      "ds_read2_b32 v[156:157], v213 offset0:227 offset1:226\n"
      "v_mfma_f32_16x16x32_bf16 v[124:127], v[168:171], v[180:183], v[124:127]\n"
      "ds_read2_b32 v[158:159], v213 offset0:225 offset1:224\n"
      "v_mfma_f32_16x16x32_bf16 v[120:123], v[168:171], v[176:179], v[120:123]\n"
      "ds_read2_b32 v[160:161], v212 offset0:115 offset1:114\n"
      "s_waitcnt lgkmcnt(8)\n"
      "v_mfma_f32_16x16x32_bf16 v[124:127], v[172:175], v[176:179], v[124:127]\n"
      "ds_read2_b32 v[162:163], v212 offset0:113 offset1:112\n"
      "ds_read_b128 v[188:191], v217 offset:3584\n"
      "v_mfma_f32_16x16x32_bf16 v[124:127], v[144:147], v[204:207], v[124:127]\n"
      "ds_read_b128 v[184:187], v217 offset:3072\n"
      "v_mfma_f32_16x16x32_bf16 v[120:123], v[144:147], v[200:203], v[120:123]\n"
      "ds_read_b128 v[180:183], v217 offset:2560\n"
      "v_mfma_f32_16x16x32_bf16 v[116:119], v[144:147], v[196:199], v[116:119]\n"
      "ds_read_b128 v[176:179], v217 offset:2048\n"
      "s_waitcnt lgkmcnt(8)\n"
      "v_mfma_f32_16x16x32_bf16 v[112:115], v[144:147], v[192:195], v[112:115]\n"
      "ds_read2_b32 v[164:165], v212 offset0:243 offset1:242\n"
      "ds_read2_b32 v[166:167], v212 offset0:241 offset1:240\n"
      "v_mfma_f32_16x16x32_bf16 v[124:127], v[148:151], v[200:203], v[124:127]\n"
      "ds_read2_b32 v[168:169], v213 offset0:115 offset1:114\n"
      "v_mfma_f32_16x16x32_bf16 v[120:123], v[148:151], v[196:199], v[120:123]\n"
      "ds_read2_b32 v[170:171], v213 offset0:113 offset1:112\n"
      "v_mfma_f32_16x16x32_bf16 v[116:119], v[148:151], v[192:195], v[116:119]\n"
      "ds_read2_b32 v[172:173], v213 offset0:243 offset1:242\n"
      "v_mfma_f32_16x16x32_bf16 v[124:127], v[152:155], v[196:199], v[124:127]\n"
      "ds_read2_b32 v[174:175], v213 offset0:241 offset1:240\n"
      "v_mfma_f32_16x16x32_bf16 v[120:123], v[152:155], v[192:195], v[120:123]\n"
      "s_waitcnt lgkmcnt(7)\n"
      "v_mfma_f32_16x16x32_bf16 v[124:127], v[156:159], v[192:195], v[124:127]\n"
      "v_mfma_f32_16x16x32_bf16 v[124:127], v[160:163], v[188:191], v[124:127]\n"
      "v_mfma_f32_16x16x32_bf16 v[120:123], v[160:163], v[184:187], v[120:123]\n"
      "v_mfma_f32_16x16x32_bf16 v[116:119], v[160:163], v[180:183], v[116:119]\n"
      "s_waitcnt lgkmcnt(2)\n"
      "v_mfma_f32_16x16x32_bf16 v[112:115], v[160:163], v[176:179], v[112:115]\n"
      "v_mfma_f32_16x16x32_bf16 v[124:127], v[164:167], v[184:187], v[124:127]\n"
      "v_mfma_f32_16x16x32_bf16 v[120:123], v[164:167], v[180:183], v[120:123]\n"
      "v_mfma_f32_16x16x32_bf16 v[116:119], v[164:167], v[176:179], v[116:119]\n"
      "v_mfma_f32_16x16x32_bf16 v[124:127], v[168:171], v[180:183], v[124:127]\n"
      "v_mfma_f32_16x16x32_bf16 v[120:123], v[168:171], v[176:179], v[120:123]\n"
      "s_waitcnt lgkmcnt(0)\n"
      "v_mfma_f32_16x16x32_bf16 v[124:127], v[172:175], v[176:179], v[124:127]\n"
      "ds_add_u32 v2, v3 offset:4276\n"
      "s_waitcnt vmcnt(0)\n"
      "ds_read_b32 v31, v2 offset:4304\n"
      "v_cvt_pk_bf16_f32 v72, v96, v97\n"
      "v_cvt_pk_bf16_f32 v74, v100, v101\n"
      "v_cvt_pk_bf16_f32 v73, v98, v99\n"
      "v_cvt_pk_bf16_f32 v75, v102, v103\n"
      "v_permlane16_swap_b32 v72, v74\n"
      "s_nop 0\n"
      "v_permlane16_swap_b32 v73, v75\n"
      "buffer_store_dwordx4 v[72:75], v5, s[20:23], s63 offen offset:0 sc1\n"
      "v_cvt_pk_bf16_f32 v76, v104, v105\n"
      "v_cvt_pk_bf16_f32 v78, v108, v109\n"
      "v_cvt_pk_bf16_f32 v77, v106, v107\n"
      "v_cvt_pk_bf16_f32 v79, v110, v111\n"
      "v_permlane16_swap_b32 v76, v78\n"
      "s_nop 0\n"
      "v_permlane16_swap_b32 v77, v79\n"
      "buffer_store_dwordx4 v[76:79], v5, s[20:23], s63 offen offset:1024 sc1\n"
      "v_cvt_pk_bf16_f32 v80, v112, v113\n"
      "v_cvt_pk_bf16_f32 v82, v116, v117\n"
      "v_cvt_pk_bf16_f32 v81, v114, v115\n"
      "v_cvt_pk_bf16_f32 v83, v118, v119\n"
      "v_permlane16_swap_b32 v80, v82\n"
      "s_nop 0\n"
      "v_permlane16_swap_b32 v81, v83\n"
      "buffer_store_dwordx4 v[80:83], v5, s[20:23], s63 offen offset:2048 sc1\n"
      "v_cvt_pk_bf16_f32 v84, v120, v121\n"
      "v_cvt_pk_bf16_f32 v86, v124, v125\n"
      "v_cvt_pk_bf16_f32 v85, v122, v123\n"
      "v_cvt_pk_bf16_f32 v87, v126, v127\n"
      "v_permlane16_swap_b32 v84, v86\n"
      "s_nop 0\n"
      "v_permlane16_swap_b32 v85, v87\n"
      "buffer_store_dwordx4 v[84:87], v5, s[20:23], s63 offen offset:3072 sc1\n"
      "s_branch .LCONV\n"
      ".Lt_1:\n"
      "s_mov_b32 s3, 0x10000\n"
      ".Lt_3:\n"
      "s_sleep 4\n"
      "buffer_load_dwordx2 v[76:77], v42, s[20:23], s31 offen sc1\n"
      "s_waitcnt vmcnt(0)\n"
      "v_cmp_ne_u32 vcc, s58, v77\n"
      "s_sub_u32 s3, s3, 1\n"
      "s_cselect_b64 vcc, 0, vcc\n"
      "s_cbranch_vccnz .Lt_3\n"
      "s_branch .Lt_2\n"
      ".Lb_4:\n"
      "s_mov_b64 exec, vcc\n"
      "v_mov_b32 v80, 0xc0135761\n"
      "v_mov_b32 v81, 0xbdd2d3e8\n"
      "v_mov_b32 v82, 1.0\n"
      "v_mov_b32 v83, v77\n"
      "s_mov_b64 exec, -1\n"
      "ds_add_u32 v2, v3 offset:4144\n"
      "s_branch .Lb_5\n"
      ".Lw_6:\n"
      "s_mov_b32 s3, 0x80000\n"
      ".Lw_8:\n"
      "ds_read_b32 v4, v2 offset:4152\n"
      "s_waitcnt lgkmcnt(0)\n"
      "v_readfirstlane_b32 s2, v4\n"
      "s_sub_u32 s3, s3, 1\n"
      "s_cselect_b32 s2, -1, s2\n"
      "s_cmp_lt_u32 s2, 4\n"
      "s_cbranch_scc1 .Lw_8\n"
      "s_branch .Lw_7\n"
      ".Lf_9:\n"
      "s_mov_b32 s3, 0x10000\n"
      ".Lf_11:\n"
      "s_sleep 4\n"
      "buffer_load_dwordx2 v[50:51], v31, s[20:23], 0 offen sc1\n"
      "s_waitcnt vmcnt(0)\n"
      "v_cmp_ne_u64 vcc, s[58:59], v[50:51]\n"
      "s_sub_u32 s3, s3, 1\n"
      "s_cselect_b64 vcc, 0, vcc\n"
      "s_cbranch_vccnz .Lf_11\n"
      "s_branch .Lf_10\n"
      ".Lf_13:\n"
      "s_mov_b32 s3, 0x10000\n"
      ".Lf_15:\n"
      "s_sleep 4\n"
      "buffer_load_dwordx2 v[48:49], v30, s[20:23], 0 offen sc1\n"
      "s_waitcnt vmcnt(0)\n"
      "v_cmp_ne_u64 vcc, s[58:59], v[48:49]\n"
      "s_sub_u32 s3, s3, 1\n"
      "s_cselect_b64 vcc, 0, vcc\n"
      "s_cbranch_vccnz .Lf_15\n"
      "s_branch .Lf_14\n"
      ".Lf_18:\n"
      "s_mov_b32 s3, 0x10000\n"
      ".Lf_20:\n"
      "s_sleep 4\n"
      "buffer_load_dwordx2 v[48:49], v30, s[20:23], 0 offen sc1\n"
      "s_waitcnt vmcnt(0)\n"
      "v_cmp_ne_u64 vcc, s[58:59], v[48:49]\n"
      "s_sub_u32 s3, s3, 1\n"
      "s_cselect_b64 vcc, 0, vcc\n"
      "s_cbranch_vccnz .Lf_20\n"
      "s_branch .Lf_19\n"
      ".Lw_21:\n"
      "s_mov_b32 s3, 0x80000\n"
      ".Lw_23:\n"
      "ds_read_b32 v4, v2 offset:4168\n"
      "s_waitcnt lgkmcnt(0)\n"
      "v_readfirstlane_b32 s2, v4\n"
      "s_sub_u32 s3, s3, 1\n"
      "s_cselect_b32 s2, -1, s2\n"
      "s_cmp_lt_u32 s2, 4\n"
      "s_cbranch_scc1 .Lw_23\n"
      "s_branch .Lw_22\n"
      ".Lf_24:\n"
      "s_mov_b32 s3, 0x10000\n"
      ".Lf_26:\n"
      "s_sleep 4\n"
      "buffer_load_dwordx2 v[10:11], v13, s[20:23], 0 offen sc1\n"
      "s_waitcnt vmcnt(0)\n"
      "v_cmp_ne_u64 vcc, s[58:59], v[10:11]\n"
      "s_sub_u32 s3, s3, 1\n"
      "s_cselect_b64 vcc, 0, vcc\n"
      "s_cbranch_vccnz .Lf_26\n"
      "s_branch .Lf_25\n"
      ".Lw_27:\n"
      "s_mov_b32 s3, 0x80000\n"
      ".Lw_29:\n"
      "ds_read_b32 v4, v2 offset:4276\n"
      "s_waitcnt lgkmcnt(0)\n"
      "v_readfirstlane_b32 s2, v4\n"
      "s_sub_u32 s3, s3, 1\n"
      "s_cselect_b32 s2, -1, s2\n"
      "s_cmp_lt_u32 s2, 4\n"
      "s_cbranch_scc1 .Lw_29\n"
      "s_branch .Lw_28\n"
      ".Lf_46:\n"
      "s_mov_b32 s3, 0x10000\n"
      ".Lf_48:\n"
      "s_sleep 4\n"
      "buffer_load_dwordx2 v[10:11], v13, s[20:23], s74 offen sc1\n"
      "s_waitcnt vmcnt(0)\n"
      "v_cmp_ne_u64 vcc, s[58:59], v[10:11]\n"
      "s_sub_u32 s3, s3, 1\n"
      "s_cselect_b64 vcc, 0, vcc\n"
      "s_cbranch_vccnz .Lf_48\n"
      "s_branch .Lf_47\n"
      ".Lw_63:\n"
      "s_mov_b32 s3, 0x80000\n"
      ".Lw_65:\n"
      "ds_read_b32 v4, v2 offset:4308\n"
      "s_waitcnt lgkmcnt(0)\n"
      "v_readfirstlane_b32 s2, v4\n"
      "s_sub_u32 s3, s3, 1\n"
      "s_cselect_b32 s2, -1, s2\n"
      "s_cmp_lt_u32 s2, 4\n"
      "s_cbranch_scc1 .Lw_65\n"
      "s_branch .Lw_64\n"
      ".Lg_126:\n"
      "s_waitcnt lgkmcnt(0)\n"
      "s_nop 11\n"
      "v_mul_f32 v80, v80, v27\n"
      "v_mul_f32 v81, v81, v27\n"
      "v_mul_f32 v82, v82, v27\n"
      "v_mul_f32 v83, v83, v27\n"
      "v_mul_f32 v84, v84, v27\n"
      "v_mul_f32 v85, v85, v27\n"
      "v_mul_f32 v86, v86, v27\n"
      "v_mul_f32 v87, v87, v27\n"
      "v_mul_f32 v88, v88, v27\n"
      "v_mul_f32 v89, v89, v27\n"
      "v_mul_f32 v90, v90, v27\n"
      "v_mul_f32 v91, v91, v27\n"
      "v_mul_f32 v92, v92, v27\n"
      "v_mul_f32 v93, v93, v27\n"
      "v_mul_f32 v94, v94, v27\n"
      "v_mul_f32 v95, v95, v27\n"
      "s_branch .Lg_127\n"
      ".Lg_128:\n"
      "s_waitcnt lgkmcnt(0)\n"
      "s_nop 11\n"
      "v_mul_f32 v64, v64, v27\n"
      "v_mul_f32 v65, v65, v27\n"
      "v_mul_f32 v66, v66, v27\n"
      "v_mul_f32 v67, v67, v27\n"
      "v_mul_f32 v68, v68, v27\n"
      "v_mul_f32 v69, v69, v27\n"
      "v_mul_f32 v70, v70, v27\n"
      "v_mul_f32 v71, v71, v27\n"
      "v_mul_f32 v72, v72, v27\n"
      "v_mul_f32 v73, v73, v27\n"
      "v_mul_f32 v74, v74, v27\n"
      "v_mul_f32 v75, v75, v27\n"
      "v_mul_f32 v76, v76, v27\n"
      "v_mul_f32 v77, v77, v27\n"
      "v_mul_f32 v78, v78, v27\n"
      "v_mul_f32 v79, v79, v27\n"
      "s_branch .Lg_129\n"
      ".Lw_130:\n"
      "s_mov_b32 s3, 0x80000\n"
      ".Lw_132:\n"
      "ds_read_b32 v31, v2 offset:2072\n"
      "s_waitcnt lgkmcnt(0)\n"
      "v_readfirstlane_b32 s2, v31\n"
      "s_sub_u32 s3, s3, 1\n"
      "s_cselect_b32 s2, -1, s2\n"
      "s_cmp_lt_u32 s2, 4\n"
      "s_cbranch_scc1 .Lw_132\n"
      "s_branch .Lw_131\n"
      ".Lw_133:\n"
      "s_mov_b32 s3, 0x80000\n"
      ".Lw_135:\n"
      "ds_read_b32 v31, v2 offset:3096\n"
      "s_waitcnt lgkmcnt(0)\n"
      "v_readfirstlane_b32 s2, v31\n"
      "s_sub_u32 s3, s3, 1\n"
      "s_cselect_b32 s2, -1, s2\n"
      "s_cmp_lt_u32 s2, 4\n"
      "s_cbranch_scc1 .Lw_135\n"
      "s_branch .Lw_134\n"
      ".Lg_136:\n"
      "s_waitcnt lgkmcnt(0)\n"
      "s_nop 11\n"
      "v_mul_f32 v64, v64, v27\n"
      "v_mul_f32 v65, v65, v27\n"
      "v_mul_f32 v66, v66, v27\n"
      "v_mul_f32 v67, v67, v27\n"
      "v_mul_f32 v68, v68, v27\n"
      "v_mul_f32 v69, v69, v27\n"
      "v_mul_f32 v70, v70, v27\n"
      "v_mul_f32 v71, v71, v27\n"
      "v_mul_f32 v72, v72, v27\n"
      "v_mul_f32 v73, v73, v27\n"
      "v_mul_f32 v74, v74, v27\n"
      "v_mul_f32 v75, v75, v27\n"
      "v_mul_f32 v76, v76, v27\n"
      "v_mul_f32 v77, v77, v27\n"
      "v_mul_f32 v78, v78, v27\n"
      "v_mul_f32 v79, v79, v27\n"
      "s_branch .Lg_137\n"
      ".Lh_138:\n"
      "s_waitcnt vmcnt(0)\n"
      "s_mov_b64 exec, 255\n"
      "buffer_store_dwordx4 v[12:15], v29, s[20:23], s79 offen offset:0 sc1\n"
      "s_mov_b64 exec, -1\n"
      "s_mov_b32 s84, 64\n"
      "ds_add_u32 v2, v3 offset:4168\n"
      "s_branch .Lh_139\n"
      ".Lh_140:\n"
      "s_waitcnt vmcnt(1)\n"
      "s_mov_b64 exec, 255\n"
      "buffer_store_dwordx4 v[12:15], v29, s[20:23], s79 offen offset:0 sc1\n"
      "s_mov_b64 exec, -1\n"
      "s_mov_b32 s84, 64\n"
      "ds_add_u32 v2, v3 offset:4168\n"
      "s_branch .Lh_141\n"
      ".Lh_142:\n"
      "s_waitcnt vmcnt(1)\n"
      "s_mov_b64 exec, 255\n"
      "buffer_store_dwordx4 v[12:15], v29, s[20:23], s79 offen offset:0 sc1\n"
      "s_mov_b64 exec, -1\n"
      "s_mov_b32 s84, 64\n"
      "ds_add_u32 v2, v3 offset:4168\n"
      "s_branch .Lh_143\n"
      ".Lh_144:\n"
      "s_waitcnt vmcnt(2)\n"
      "s_mov_b64 exec, 255\n"
      "buffer_store_dwordx4 v[12:15], v29, s[20:23], s79 offen offset:0 sc1\n"
      "s_mov_b64 exec, -1\n"
      "s_mov_b32 s84, 64\n"
      "ds_add_u32 v2, v3 offset:4168\n"
      "s_branch .Lh_145\n"
      ".Lg_146:\n"
      "s_waitcnt lgkmcnt(0)\n"
      "s_nop 11\n"
      "v_mul_f32 v64, v64, v27\n"
      "v_mul_f32 v65, v65, v27\n"
      "v_mul_f32 v66, v66, v27\n"
      "v_mul_f32 v67, v67, v27\n"
      "v_mul_f32 v68, v68, v27\n"
      "v_mul_f32 v69, v69, v27\n"
      "v_mul_f32 v70, v70, v27\n"
      "v_mul_f32 v71, v71, v27\n"
      "v_mul_f32 v72, v72, v27\n"
      "v_mul_f32 v73, v73, v27\n"
      "v_mul_f32 v74, v74, v27\n"
      "v_mul_f32 v75, v75, v27\n"
      "v_mul_f32 v76, v76, v27\n"
      "v_mul_f32 v77, v77, v27\n"
      "v_mul_f32 v78, v78, v27\n"
      "v_mul_f32 v79, v79, v27\n"
      "s_branch .Lg_147\n"
      ".Lh_148:\n"
      "s_waitcnt vmcnt(4)\n"
      "s_mov_b64 exec, 255\n"
      "buffer_store_dwordx4 v[12:15], v29, s[20:23], s79 offen offset:0 sc1\n"
      "s_mov_b64 exec, -1\n"
      "s_mov_b32 s84, 64\n"
      "ds_add_u32 v2, v3 offset:4168\n"
      "s_branch .Lh_149\n"
      ".Lw_150:\n"
      "s_mov_b32 s3, 0x80000\n"
      ".Lw_152:\n"
      "ds_read_b32 v31, v30 offset:4240\n"
      "s_waitcnt lgkmcnt(0)\n"
      "v_readfirstlane_b32 s2, v31\n"
      "s_sub_u32 s3, s3, 1\n"
      "s_cselect_b32 s2, -1, s2\n"
      "s_cmp_lt_u32 s2, 4\n"
      "s_cbranch_scc1 .Lw_152\n"
      "s_branch .Lw_151\n"
      ".Lh_153:\n"
      "s_waitcnt vmcnt(4)\n"
      "s_mov_b64 exec, 255\n"
      "buffer_store_dwordx4 v[12:15], v29, s[20:23], s79 offen offset:0 sc1\n"
      "s_mov_b64 exec, -1\n"
      "s_mov_b32 s84, 64\n"
      "ds_add_u32 v2, v3 offset:4168\n"
      "s_branch .Lh_154\n"
      ".Lh_155:\n"
      "s_waitcnt vmcnt(0)\n"
      "s_mov_b64 exec, 255\n"
      "buffer_store_dwordx4 v[12:15], v29, s[20:23], s79 offen offset:128 sc1\n"
      "s_mov_b64 exec, -1\n"
      "s_mov_b32 s85, 64\n"
      "s_branch .Lh_156\n"
      ".Lh_157:\n"
      "s_waitcnt vmcnt(4)\n"
      "s_mov_b64 exec, 255\n"
      "buffer_store_dwordx4 v[12:15], v29, s[20:23], s79 offen offset:0 sc1\n"
      "s_mov_b64 exec, -1\n"
      "s_mov_b32 s84, 64\n"
      "ds_add_u32 v2, v3 offset:4168\n"
      "s_branch .Lh_158\n"
      ".Lh_159:\n"
      "s_waitcnt vmcnt(0)\n"
      "s_mov_b64 exec, 255\n"
      "buffer_store_dwordx4 v[12:15], v29, s[20:23], s79 offen offset:128 sc1\n"
      "s_mov_b64 exec, -1\n"
      "s_mov_b32 s85, 64\n"
      "s_branch .Lh_160\n"
      ".Lh_161:\n"
      "s_waitcnt vmcnt(0)\n"
      "s_mov_b64 exec, 255\n"
      "buffer_store_dwordx4 v[12:15], v29, s[20:23], s79 offen offset:128 sc1\n"
      "s_mov_b64 exec, -1\n"
      "s_mov_b32 s85, 64\n"
      "s_branch .Lh_162\n"
      ".Lh_163:\n"
      "s_waitcnt vmcnt(0)\n"
      "s_mov_b64 exec, 255\n"
      "buffer_store_dwordx4 v[12:15], v29, s[20:23], s79 offen offset:128 sc1\n"
      "s_mov_b64 exec, -1\n"
      "s_mov_b32 s85, 64\n"
      "s_branch .Lh_164\n"
      ".Lw_165:\n"
      "s_mov_b32 s3, 0x80000\n"
      ".Lw_167:\n"
      "ds_read_b32 v31, v2 offset:4280\n"
      "s_waitcnt lgkmcnt(0)\n"
      "v_readfirstlane_b32 s2, v31\n"
      "s_sub_u32 s3, s3, 1\n"
      "s_cselect_b32 s2, -1, s2\n"
      "s_cmp_lt_u32 s2, 4\n"
      "s_cbranch_scc1 .Lw_167\n"
      "s_branch .Lw_166\n"
      ".Lh_168:\n"
      "s_waitcnt vmcnt(0)\n"
      "s_mov_b64 exec, 255\n"
      "buffer_store_dwordx4 v[12:15], v29, s[20:23], s79 offen offset:128 sc1\n"
      "s_mov_b64 exec, -1\n"
      "s_mov_b32 s85, 64\n"
      "s_branch .Lh_169\n"
      ".Lw_170:\n"
      "s_mov_b32 s3, 0x80000\n"
      ".Lw_172:\n"
      "ds_read_b32 v31, v2 offset:4276\n"
      "s_waitcnt lgkmcnt(0)\n"
      "v_readfirstlane_b32 s2, v31\n"
      "s_sub_u32 s3, s3, 1\n"
      "s_cselect_b32 s2, -1, s2\n"
      "s_cmp_lt_u32 s2, 4\n"
      "s_cbranch_scc1 .Lw_172\n"
      "s_branch .Lw_171\n"
      ".Lw_176:\n"
      "s_mov_b32 s3, 0x80000\n"
      ".Lw_178:\n"
      "ds_read_b32 v31, v2 offset:4308\n"
      "s_waitcnt lgkmcnt(0)\n"
      "v_readfirstlane_b32 s2, v31\n"
      "s_sub_u32 s3, s3, 1\n"
      "s_cselect_b32 s2, -1, s2\n"
      "s_cmp_lt_u32 s2, 4\n"
      "s_cbranch_scc1 .Lw_178\n"
      "s_branch .Lw_177\n"
      ".Lf_180:\n"
      "s_mov_b32 s3, 0x10000\n"
      ".Lf_182:\n"
      "s_sleep 4\n"
      "buffer_load_dwordx2 v[26:27], v25, s[20:23], 0 offen sc1\n"
      "s_waitcnt vmcnt(0)\n"
      "v_cmp_ne_u64 vcc, s[58:59], v[26:27]\n"
      "s_sub_u32 s3, s3, 1\n"
      "s_cselect_b64 vcc, 0, vcc\n"
      "s_cbranch_vccnz .Lf_182\n"
      "s_branch .Lf_181\n"
      ".Ly_185:\n"
      "s_mov_b32 s3, 0x10000\n"
      ".Ly_187:\n"
      "s_sleep 4\n"
      "buffer_load_dwordx4 v[164:167], v9, s[20:23], s72 offen offset:0 sc1\n"
      "buffer_load_dwordx4 v[168:171], v9, s[20:23], s72 offen offset:1024 sc1\n"
      "buffer_load_dwordx4 v[172:175], v9, s[20:23], s72 offen offset:2048 sc1\n"
      "s_waitcnt vmcnt(0)\n"
      "v_xor_b32 v21, s74, v167\n"
      "v_bitop3_b32 v21, v21, v171, s74 bitop3:0xf6\n"
      "v_bitop3_b32 v21, v21, v175, s74 bitop3:0xf6\n"
      "v_cmp_lt_u32 vcc, 0xffff, v21\n"
      "s_sub_u32 s3, s3, 1\n"
      "s_cselect_b64 vcc, 0, vcc\n"
      "s_cbranch_vccnz .Ly_187\n"
      "s_branch .Ly_186\n"
      ".Lt_197:\n"
      "s_mov_b32 s3, 0x10000\n"
      ".Lt_199:\n"
      "s_sleep 4\n"
      "buffer_load_dwordx2 v[76:77], v42, s[20:23], s31 offen sc1\n"
      "s_waitcnt vmcnt(0)\n"
      "v_cmp_ne_u32 vcc, s58, v77\n"
      "s_sub_u32 s3, s3, 1\n"
      "s_cselect_b64 vcc, 0, vcc\n"
      "s_cbranch_vccnz .Lt_199\n"
      "s_branch .Lt_198\n"
      ".Lb_200:\n"
      "s_mov_b64 exec, vcc\n"
      "v_mov_b32 v80, 0xc0135761\n"
      "v_mov_b32 v81, 0xbdd2d3e8\n"
      "v_mov_b32 v82, 1.0\n"
      "v_mov_b32 v83, v77\n"
      "s_mov_b64 exec, -1\n"
      "ds_add_u32 v2, v3 offset:4144\n"
      "s_branch .Lb_201\n"
      ".Lf_204:\n"
      "s_mov_b32 s3, 0x10000\n"
      ".Lf_206:\n"
      "s_sleep 4\n"
      "buffer_load_dwordx2 v[48:49], v30, s[20:23], 0 offen sc1\n"
      "s_waitcnt vmcnt(0)\n"
      "v_cmp_ne_u64 vcc, s[58:59], v[48:49]\n"
      "s_sub_u32 s3, s3, 1\n"
      "s_cselect_b64 vcc, 0, vcc\n"
      "s_cbranch_vccnz .Lf_206\n"
      "s_branch .Lf_205\n"
      ".Lf_211:\n"
      "s_mov_b32 s3, 0x10000\n"
      ".Lf_213:\n"
      "s_sleep 4\n"
      "buffer_load_dwordx2 v[26:27], v25, s[20:23], s64 offen sc1\n"
      "s_waitcnt vmcnt(0)\n"
      "v_cmp_ne_u64 vcc, s[58:59], v[26:27]\n"
      "s_sub_u32 s3, s3, 1\n"
      "s_cselect_b64 vcc, 0, vcc\n"
      "s_cbranch_vccnz .Lf_213\n"
      "s_branch .Lf_212\n"
      ".Lf_214:\n"
      "s_mov_b32 s3, 0x10000\n"
      ".Lf_216:\n"
      "s_sleep 4\n"
      "buffer_load_dwordx2 v[26:27], v25, s[20:23], s64 offen sc1\n"
      "s_waitcnt vmcnt(0)\n"
      "v_cmp_ne_u64 vcc, s[58:59], v[26:27]\n"
      "s_sub_u32 s3, s3, 1\n"
      "s_cselect_b64 vcc, 0, vcc\n"
      "s_cbranch_vccnz .Lf_216\n"
      "s_branch .Lf_215\n"
      ".Lf_217:\n"
      "s_mov_b32 s3, 0x10000\n"
      ".Lf_219:\n"
      "s_sleep 4\n"
      "buffer_load_dwordx2 v[26:27], v25, s[20:23], s64 offen sc1\n"
      "s_waitcnt vmcnt(0)\n"
      "v_cmp_ne_u64 vcc, s[58:59], v[26:27]\n"
      "s_sub_u32 s3, s3, 1\n"
      "s_cselect_b64 vcc, 0, vcc\n"
      "s_cbranch_vccnz .Lf_219\n"
      "s_branch .Lf_218\n"
      ".Lf_220:\n"
      "s_mov_b32 s3, 0x10000\n"
      ".Lf_222:\n"
      "s_sleep 4\n"
      "buffer_load_dwordx2 v[26:27], v25, s[20:23], s64 offen sc1\n"
      "s_waitcnt vmcnt(0)\n"
      "v_cmp_ne_u64 vcc, s[58:59], v[26:27]\n"
      "s_sub_u32 s3, s3, 1\n"
      "s_cselect_b64 vcc, 0, vcc\n"
      "s_cbranch_vccnz .Lf_222\n"
      "s_branch .Lf_221\n"
      ".Lw_224:\n"
      "s_mov_b32 s3, 0x80000\n"
      ".Lw_226:\n"
      "ds_read_b32 v31, v2 offset:4272\n"
      "s_waitcnt lgkmcnt(0)\n"
      "v_readfirstlane_b32 s2, v31\n"
      "s_sub_u32 s3, s3, 1\n"
      "s_cselect_b32 s2, -1, s2\n"
      "s_cmp_lt_u32 s2, 2\n"
      "s_cbranch_scc1 .Lw_226\n"
      "s_branch .Lw_225\n"
      ".Lfz_end:\n"
      ".fill 320, 4, 0xBF800000\n"
      : "+{v0}"(tid) : "{s[0:1]}"(__builtin_amdgcn_kernarg_segment_ptr()), "{s2}"(blockIdx.x) : "v1", "v2", "v3", "v4", "v5", "v6", "v7", "v8", "v9", "v10", "v11", "v12", "v13", "v14", "v15", "v16", "v17", "v18", "v19", "v20", "v21", "v22", "v23", "v24", "v25", "v26", "v27", "v28", "v29", "v30", "v31", "v32", "v33", "v34", "v35", "v36", "v37", "v38", "v39", "v40", "v41", "v42", "v43", "v44", "v45", "v46", "v47", "v48", "v49", "v50", "v51", "v52", "v53", "v54", "v55", "v56", "v57", "v58", "v59", "v60", "v61", "v62", "v63", "v64", "v65", "v66", "v67", "v68", "v69", "v70", "v71", "v72", "v73", "v74", "v75", "v76", "v77", "v78", "v79", "v80", "v81", "v82", "v83", "v84", "v85", "v86", "v87", "v88", "v89", "v90", "v91", "v92", "v93", "v94", "v95", "v96", "v97", "v98", "v99", "v100", "v101", "v102", "v103", "v104", "v105", "v106", "v107", "v108", "v109", "v110", "v111", "v112", "v113", "v114", "v115", "v116", "v117", "v118", "v119", "v120", "v121", "v122", "v123", "v124", "v125", "v126", "v127", "v128", "v129", "v130", "v131", "v132", "v133", "v134", "v135", "v136", "v137", "v138", "v139", "v140", "v141", "v142", "v143", "v144", "v145", "v146", "v147", "v148", "v149", "v150", "v151", "v152", "v153", "v154", "v155", "v156", "v157", "v158", "v159", "v160", "v161", "v162", "v163", "v164", "v165", "v166", "v167", "v168", "v169", "v170", "v171", "v172", "v173", "v174", "v175", "v176", "v177", "v178", "v179", "v180", "v181", "v182", "v183", "v184", "v185", "v186", "v187", "v188", "v189", "v190", "v191", "v192", "v193", "v194", "v195", "v196", "v197", "v198", "v199", "v200", "v201", "v202", "v203", "v204", "v205", "v206", "v207", "v208", "v209", "v210", "v211", "v212", "v213", "v214", "v215", "v216", "v217", "v218", "v219", "v220", "v221", "v222", "v223", "v224", "v225", "v226", "v227", "v228", "v229", "v230", "v231", "v232", "v233", "v234", "v235", "v236", "v237", "v238", "v239", "v240", "v241", "v242", "v243", "v244", "v245", "v246", "v247", "v248", "v249", "v250", "v251", "v252", "v253", "v254", "v255", "s3", "s4", "s5", "s6", "s7", "s8", "s9", "s10", "s11", "s12", "s13", "s14", "s15", "s16", "s17", "s18", "s19", "s20", "s21", "s22", "s23", "s24", "s25", "s26", "s27", "s28", "s29", "s30", "s31", "s33", "s34", "s35", "s36", "s37", "s38", "s39", "s40", "s41", "s42", "s43", "s44", "s45", "s46", "s47", "s48", "s49", "s50", "s51", "s52", "s53", "s54", "s55", "s56", "s57", "s58", "s59", "s60", "s61", "s62", "s63", "s64", "s65", "s66", "s67", "s68", "s69", "s70", "s71", "s72", "s73", "s74", "s75", "s76", "s77", "s78", "s79", "s80", "s81", "s82", "s83", "s84", "s85", "s86", "s87", "s88", "s89", "s90", "s91", "s92", "s93", "s94", "s95", "s96", "s97", "s98", "s99", "vcc", "scc", "memory");
#endif
}
}

extern "C" void kernel_launch(void* const* d_in, const int* in_sizes, int n_in, void* d_out, int out_size, void* d_ws,
                              size_t ws_size, hipStream_t stream) {
  (void)hipFuncSetAttribute((const void*)fused_kernel, hipFuncAttributeMaxDynamicSharedMemorySize, 163840);
  fused_kernel<<<256, 512, 163840, stream>>>(d_in[0], 2621440u, 0x20000u, d_in[7], 2211840u, 0x20000u, d_in[1], 2621440u, 0x20000u, d_in[2], 2621440u, 0x20000u, d_ws, 18896896u, 0x20000u, d_in[3], 5120u, 0x20000u, d_in[8], 3456u, 0x20000u, d_in[5], 1280u, 0x20000u, d_in[4], 2621440u, 0x20000u, d_in[9], 2211840u, 0x20000u, d_in[10], 1280u, 0x20000u, d_out, 2621440u, 0x20000u);
}
```

```cpp
#include <hip/hip_runtime.h>
#include <stdint.h>
namespace {
constexpr unsigned WS_STATS = 8192, WS_KV = 32768, WS_Y2 = 32768, WS_W2T = 2818048, WS_HT = 5111808, WS_EX = 12451840, WS_END = 16111616;
constexpr unsigned KVPI = 4224, Y2PI = 4224;
#define BUF const void*, unsigned, unsigned
__global__ __launch_bounds__(512, 2) void fused_kernel(BUF, BUF, BUF, BUF, BUF, BUF, BUF, BUF, BUF, BUF, BUF, BUF) {
#if defined(__HIP_DEVICE_COMPILE__)
  unsigned tid = threadIdx.x;
  asm volatile(
      "s_load_dwordx16 s[4:19], s[0:1], 0x0\n"
      "s_load_dwordx16 s[20:35], s[0:1], 0x40\n"
      "s_load_dwordx16 s[36:51], s[0:1], 0x80\n"
      "s_getpc_b64 s[64:65]\n"
      ".Lfz_pc:\n"
      "v_lshrrev_b32 v2, 6, v0\n"
      "v_and_b32 v1, 63, v0\n"
      "s_mov_b32 s60, s2\n"
      "v_readfirstlane_b32 s52, v2\n"
      "s_lshr_b32 s61, s2, 3\n"
      "s_mov_b32 s66, .Lfz_end-.Lfz_pc\n"
      "s_mov_b32 s67, 0x20000\n"
      "s_lshl3_add_u32 s62, s61, s52\n"
      "s_lshl_b32 s62, s62, 7\n"
      "v_and_b32 v2, 1, v0\n"
      "v_lshlrev_b32 v2, 15, v2\n"
      "buffer_load_dword v3, v2, s[64:67], s62 offen\n"
      "s_and_b32 s53, s52, 3\n"
      "s_and_b32 s54, s2, 1\n"
      "s_mul_i32 s62, s61, 37\n"
      "s_lshr_b32 s62, s62, 8\n"
      "s_mul_i32 s63, s62, 7\n"
      "s_sub_u32 s63, s61, s63\n"
      "s_cmp_lt_u32 s61, 28\n"
      "s_cselect_b32 s62, s62, s63\n"
      "s_cselect_b32 s55, s63, 7\n"
      "s_bfe_u32 s63, s2, 0x20001\n"
      "s_lshl2_add_u32 s56, s63, s62\n"
      "s_mul_i32 s63, s54, 7\n"
      "s_add_u32 s57, s63, s55\n"
      "s_mov_b32 s92, 0xffff0000\n"
      "s_mul_i32 s62, s2, 6\n"
      "s_sub_u32 s62, s62, 892\n"
      "s_lshl_b32 s63, s2, 1\n"
      "s_max_i32 s98, s62, s63\n"
      "s_and_b32 s62, s57, 1\n"
      "s_xor_b32 s63, s54, 1\n"
      "s_lshl1_add_u32 s99, s63, s62\n"
      "s_cmp_lt_u32 s52, 4\n"
      "s_cbranch_scc1 .LM\n"
      "s_cmp_lt_u32 s61, 28\n"
      "s_cbranch_scc0 .LSPARE\n"
      "v_lshrrev_b32 v72, 3, v1\n"
      "v_and_b32 v73, 7, v1\n"
      "v_mul_u32_u24 v74, 1280, v72\n"
      "v_lshl_add_u32 v24, v73, 4, v74\n"
      "v_lshrrev_b32 v75, 4, v1\n"
      "s_and_b32 s62, s53, 1\n"
      "s_lshl_b32 s62, s62, 2\n"
      "v_xor_b32 v73, v73, v75\n"
      "v_xor_b32 v73, s62, v73\n"
      "v_lshl_add_u32 v74, v73, 4, v74\n"
      "s_mul_i32 s62, s99, 40960\n"
      "v_add_u32 v20, s62, v74\n"
      "s_xor_b32 s62, s99, 1\n"
      "s_mul_i32 s62, s62, 40960\n"
      "v_add_u32 v21, s62, v74\n"
      "v_and_b32 v73, 15, v1\n"
      "v_lshlrev_b32 v76, 2, v75\n"
      "v_xor_b32 v73, v73, v76\n"
      "s_cmp_eq_u32 s57, 13\n"
      "s_cselect_b32 s63, 7, 15\n"
      "v_and_b32 v73, s63, v73\n"
      "v_mul_u32_u24 v75, 3456, v75\n"
      "v_lshl_add_u32 v16, v73, 4, v75\n"
      "v_add_u32 v17, 12800, v16\n"
      "v_add_u32 v18, 25600, v16\n"
      "v_add_u32 v19, 38400, v16\n"
      "s_mul_i32 s63, s56, 3\n"
      "s_add_u32 s63, s63, s57\n"
      "s_mul_i32 s64, s63, 205\n"
      "s_lshr_b32 s64, s64, 11\n"
      "s_mul_i32 s64, s64, 10\n"
      "s_sub_u32 s63, s63, s64\n"
      "s_lshl_b32 s64, s56, 4\n"
      "s_add_u32 s64, s64, s53\n"
      "s_mul_i32 s68, s64, 10240\n"
      "s_mul_i32 s69, s53, 55296\n"
      "s_lshl_b32 s64, s57, 8\n"
      "s_add_u32 s69, s69, s64\n"
      "s_lshl_b32 s64, s63, 7\n"
      "s_add_u32 s70, s68, s64\n"
      "s_mul_i32 s64, s63, 221184\n"
      "s_add_u32 s80, s69, s64\n"
      "s_add_u32 s64, s68, 1280\n"
      "s_and_b32 s91, s2, 7\n"
      "s_mul_i32 s91, s91, 28\n"
      "s_add_u32 s91, s91, s61\n"
      "s_mul_i32 s93, s91, 205\n"
      "s_lshr_b32 s90, s93, 10\n"
      "s_lshr_b32 s93, s93, 11\n"
      "s_mul_i32 s65, s93, 80640\n"
      "s_lshl_b32 s67, s91, 7\n"
      "s_add_u32 s65, s65, s67\n"
      "s_sub_u32 s66, s65, 10240\n"
      "s_cselect_b32 s66, 1073741824, s66\n"
      "s_mul_i32 s67, s53, 20480\n"
      "s_add_u32 s65, s65, s67\n"
      "s_lshr_b32 s67, s67, 1\n"
      "s_mul_i32 s90, s90, 39680\n"
      "s_add_u32 s90, s90, s67\n"
      "s_lshl_b32 s67, s91, 8\n"
      "s_add_u32 s90, s90, s67\n"
      "v_add_u32 v25, 9200, v24\n"
      "v_cmp_gt_u32 vcc, 40, v1\n"
      "v_mov_b32 v72, 1073741824\n"
      "s_mul_i32 s67, s53, 2080\n"
      "v_cndmask_b32 v26, v24, v72, vcc\n"
      "v_lshrrev_b32 v72, 4, v1\n"
      "v_mul_u32_u24 v72, 1280, v72\n"
      "v_and_b32 v73, 15, v1\n"
      "v_lshl_add_u32 v27, v73, 4, v72\n"
      "v_add_u32 v28, 4080, v27\n"
      "s_add_u32 s71, s70, 128\n"
      "s_add_u32 s81, s80, 221184\n"
      "s_cmp_eq_u32 s71, s64\n"
      "s_cselect_b32 s71, s68, s71\n"
      "s_cselect_b32 s81, s69, s81\n"
      "s_lshl_b32 s94, s53, 12\n"
      "s_lshl_b32 s95, s53, 10\n"
      "s_setprio 2\n"
      "s_waitcnt lgkmcnt(0)\n"
      "s_barrier\n"
      "s_add_u32 m0, s94, 0\n"
      "s_nop 0\n"
      "buffer_load_dwordx4 v16, s[8:11], s80 offen offset:0 lds\n"
      "buffer_load_dwordx4 v17, s[8:11], s80 offen offset:1024 lds\n"
      "buffer_load_dwordx4 v18, s[8:11], s80 offen offset:2048 lds\n"
      "buffer_load_dwordx4 v19, s[8:11], s80 offen offset:3072 lds\n"
      "s_add_u32 m0, s95, 16384\n"
      "s_nop 0\n"
      "buffer_load_dwordx4 v20, s[4:7], s70 offen lds\n"
      "s_add_u32 m0, s95, 20480\n"
      "s_nop 0\n"
      "buffer_load_dwordx4 v21, s[4:7], s70 offen lds\n"
      "s_mov_b32 m0, 139520\n"
      "s_nop 0\n"
      "buffer_load_dwordx4 v26, s[12:15], s66 offen nt lds\n"
      "s_add_u32 m0, s67, 140560\n"
      "s_nop 0\n"
      "buffer_load_dwordx4 v24, s[12:15], s65 offen nt lds\n"
      "buffer_load_dwordx4 v25, s[12:15], s65 offen offset:1040 nt lds\n"
      "s_add_u32 m0, s67, 148880\n"
      "s_nop 0\n"
      "buffer_load_dwordx4 v24, s[16:19], s65 offen nt lds\n"
      "buffer_load_dwordx4 v25, s[16:19], s65 offen offset:1040 nt lds\n"
      "s_add_u32 m0, s67, 122880\n"
      "s_nop 0\n"
      "buffer_load_dwordx4 v27, s[40:43], s90 offen nt lds\n"
      "buffer_load_dwordx4 v28, s[40:43], s90 offen offset:1040 nt lds\n"
      "s_add_u32 s72, s71, 128\n"
      "s_add_u32 s82, s81, 221184\n"
      "s_cmp_eq_u32 s72, s64\n"
      "s_cselect_b32 s72, s68, s72\n"
      "s_cselect_b32 s82, s69, s82\n"
      "s_add_u32 s73, s72, 128\n"
      "s_add_u32 s83, s82, 221184\n"
      "s_cmp_eq_u32 s73, s64\n"
      "s_cselect_b32 s73, s68, s73\n"
      "s_cselect_b32 s83, s69, s83\n"
      "s_add_u32 s74, s73, 128\n"
      "s_add_u32 s84, s83, 221184\n"
      "s_cmp_eq_u32 s74, s64\n"
      "s_cselect_b32 s74, s68, s74\n"
      "s_cselect_b32 s84, s69, s84\n"
      "s_add_u32 s75, s74, 128\n"
      "s_add_u32 s85, s84, 221184\n"
      "s_cmp_eq_u32 s75, s64\n"
      "s_cselect_b32 s75, s68, s75\n"
      "s_cselect_b32 s85, s69, s85\n"
      "s_add_u32 s76, s75, 128\n"
      "s_add_u32 s86, s85, 221184\n"
      "s_cmp_eq_u32 s76, s64\n"
      "s_cselect_b32 s76, s68, s76\n"
      "s_cselect_b32 s86, s69, s86\n"
      "s_add_u32 s77, s76, 128\n"
      "s_add_u32 s87, s86, 221184\n"
      "s_cmp_eq_u32 s77, s64\n"
      "s_cselect_b32 s77, s68, s77\n"
      "s_cselect_b32 s87, s69, s87\n"
      "s_add_u32 s78, s77, 128\n"
      "s_add_u32 s88, s87, 221184\n"
      "s_cmp_eq_u32 s78, s64\n"
      "s_cselect_b32 s78, s68, s78\n"
      "s_cselect_b32 s88, s69, s88\n"
      "s_add_u32 s79, s78, 128\n"
      "s_add_u32 s89, s88, 221184\n"
      "s_cmp_eq_u32 s79, s64\n"
      "s_cselect_b32 s79, s68, s79\n"
      "s_cselect_b32 s89, s69, s89\n"
      "s_xor_b32 s62, s99, 2\n"
      "s_mul_i32 s62, s62, 40960\n"
      "v_add_u32 v22, s62, v74\n"
      "s_xor_b32 s62, s99, 3\n"
      "s_mul_i32 s62, s62, 40960\n"
      "v_add_u32 v23, s62, v74\n"
      "s_mul_i32 s62, s93, 10\n"
      "s_sub_u32 s62, s91, s62\n"
      "s_lshl2_add_u32 s62, s62, s53\n"
      "s_lshl_b32 s62, s62, 5\n"
      "s_add_u32 s62, s62, s93\n"
      "s_lshl_b32 s62, s62, 7\n"
      "s_add_u32 s62, s62, 15859712\n"
      "v_mov_b32 v29, 1073741824\n"
      "s_mov_b32 exec_lo, 0xffff\n"
      "s_mov_b32 exec_hi, 0x0\n"
      "v_lshl_add_u32 v29, v1, 3, s62\n"
      "s_mov_b64 exec, -1\n"
      "s_mul_i32 s93, s91, 205\n"
      "s_lshr_b32 s93, s93, 10\n"
      "s_mul_i32 s62, s93, 5\n"
      "s_sub_u32 s91, s91, s62\n"
      "s_lshr_b32 s30, s93, 2\n"
      "s_and_b32 s29, s93, 3\n"
      "s_mul_i32 s29, s29, 7\n"
      "s_add_u32 s29, s29, s30\n"
      "s_cmp_ge_u32 s30, 7\n"
      "s_cselect_b32 s30, 21, 0\n"
      "s_add_u32 s29, s29, s30\n"
      "s_lshl_b32 s29, s29, 10\n"
      "s_mul_i32 s30, s91, 458752\n"
      "s_add_u32 s29, s29, s30\n"
      "s_add_u32 s29, s29, 2818048\n"
      "s_mul_i32 s30, s53, 114688\n"
      "s_add_u32 s29, s29, s30\n"
      "s_add_u32 s30, s29, 57344\n"
      "s_lshl3_add_u32 s62, s93, s91\n"
      "s_lshl_b32 s62, s62, 7\n"
      "s_add_u32 s62, s62, 16023552\n"
      "s_lshl3_add_u32 s63, s60, 4096\n"
      "s_cmp_eq_u32 s53, 0\n"
      "s_cselect_b32 s62, s62, 1073741824\n"
      "s_cselect_b32 s63, s63, 1073741824\n"
      "s_mov_b32 exec_lo, 0xffff0000\n"
      "s_mov_b32 exec_hi, 0x0\n"
      "s_sub_u32 s62, s62, 128\n"
      "v_lshl_add_u32 v29, v1, 3, s62\n"
      "s_mov_b64 exec, -1\n"
      "v_writelane_b32 v29, s63, 32\n"
      "v_lshrrev_b32 v44, 4, v1\n"
      "v_and_b32 v72, 1, v44\n"
      "v_lshrrev_b32 v44, 1, v44\n"
      "v_lshl_add_u32 v44, v72, 2, v44\n"
      "v_mul_u32_u24 v44, 1040, v44\n"
      "v_bfe_u32 v72, v1, 2, 2\n"
      "v_lshl_add_u32 v44, v72, 8, v44\n"
      "v_and_b32 v72, 3, v1\n"
      "v_lshl_add_u32 v44, v72, 3, v44\n"
      "s_lshl_b32 s64, s53, 6\n"
      "s_add_u32 s64, s64, 122880\n"
      "v_add_u32 v44, s64, v44\n"
      "v_mov_b64 v[36:37], 0\n"
      "v_mov_b64 v[38:39], 0\n"
      "s_lshl_b32 s63, s53, 10\n"
      "s_add_u32 s63, s63, 157696\n"
      "v_lshl_add_u32 v40, v1, 4, s63\n"
      "ds_write_b128 v40, v[36:39]\n"
      "v_lshlrev_b32 v40, 2, v1\n"
      "v_add_u32 v40, 161792, v40\n"
      "ds_write_b32 v40, v36\n"
      "v_lshlrev_b32 v5, 4, v1\n"
      "v_add_u32 v6, 4096, v5\n"
      "s_lshl_b32 s24, s98, 12\n"
      "s_add_u32 s24, s24, s95\n"
      "s_add_u32 s25, s24, 4096\n"
      "v_mov_b32 v32, s25\n"
      "v_writelane_b32 v32, s24, 0\n"
      "s_cmp_eq_u32 s53, 0\n"
      "s_cselect_b32 s63, 1073741824, -4\n"
      "v_add_u32 v32, s63, v32\n"
      "v_lshrrev_b32 v33, 11, v32\n"
      "v_xor_b32 v33, 524288, v33\n"
      "s_sub_u32 s91, s98, 0\n"
      "s_lshr_b32 s91, s91, 4\n"
      "s_lshr_b32 s63, s98, 4\n"
      "s_sub_u32 s63, s63, s91\n"
      "s_lshl_b32 s63, s63, 9\n"
      "v_and_b32 v72, 8, v1\n"
      "v_mul_u32_u24 v72, s63, v72\n"
      "v_and_b32 v73, 7, v1\n"
      "v_lshl_add_u32 v30, v73, 7, v72\n"
      "s_lshl_b32 s91, s91, 12\n"
      "s_add_u32 s91, s91, s95\n"
      "s_add_u32 s91, s91, 15859712\n"
      "v_add_u32 v30, s91, v30\n"
      "s_and_b32 s63, s53, 1\n"
      "s_mul_i32 s63, s63, 4608\n"
      "s_add_u32 s63, s63, 139520\n"
      "v_lshl_add_u32 v34, v1, 3, s63\n"
      "v_lshrrev_b32 v72, 1, v1\n"
      "v_min_u32 v72, 6, v72\n"
      "s_mul_i32 s62, s54, 28\n"
      "s_add_u32 s62, s62, s53\n"
      "v_lshl_add_u32 v72, v72, 2, s62\n"
      "v_min_u32 v72, 53, v72\n"
      "s_lshl_b32 s63, s56, 14\n"
      "s_add_u32 s63, s63, 15597568\n"
      "v_lshl_add_u32 v13, v72, 8, s63\n"
      "s_mul_i32 s63, s55, 80\n"
      "s_add_u32 s91, s63, 79\n"
      "s_lshr_b32 s63, s63, 7\n"
      "s_lshr_b32 s91, s91, 7\n"
      "s_sub_u32 s91, s91, s63\n"
      "v_and_b32 v73, s91, v1\n"
      "v_lshl_add_u32 v72, v72, 3, v73\n"
      "s_lshl_b32 s63, s63, 7\n"
      "s_add_u32 s63, s63, 16023552\n"
      "v_lshl_add_u32 v31, v72, 7, s63\n"
      "v_lshrrev_b32 v72, 3, v1\n"
      "v_and_b32 v72, 3, v72\n"
      "v_mul_u32_u24 v73, 4160, v72\n"
      "v_add_u32 v73, 122880, v73\n"
      "v_and_b32 v72, 7, v1\n"
      "v_lshl_add_u32 v35, v72, 3, v73\n"
      "s_add_u32 s62, s95, 122944\n"
      "v_add_u32 v40, s62, v5\n"
      "v_and_b32 v72, 31, v1\n"
      "v_lshlrev_b32 v42, 3, v72\n"
      "s_add_u32 s62, s95, 157696\n"
      "v_lshl_add_u32 v43, v72, 5, s62\n"
      "s_xor_b32 s31, s99, s53\n"
      "s_lshl2_add_u32 s31, s56, s31\n"
      "s_lshl_b32 s31, s31, 8\n"
      "s_add_u32 s31, s31, 8192\n"
      "s_lshl2_add_u32 s62, s54, s53\n"
      "s_cmp_ge_u32 s62, 6\n"
      "s_cselect_b32 s63, 1073741824, 6144\n"
      "v_add_u32 v7, s63, v5\n"
      "s_mul_i32 s62, s62, 7168\n"
      "s_mul_i32 s63, s55, 286720\n"
      "s_add_u32 s63, s63, s62\n"
      "s_add_u32 s27, s63, 2818048\n"
      "s_mul_i32 s63, s56, 458752\n"
      "s_add_u32 s63, s63, s62\n"
      "s_add_u32 s63, s63, 5111808\n"
      "s_mul_i32 s62, s54, 229376\n"
      "s_add_u32 s91, s63, s62\n"
      "s_sub_u32 s62, 229376, s62\n"
      "s_add_u32 s93, s63, s62\n"
      "s_sub_u32 s62, 4, s53\n"
      "s_and_b32 s62, s62, 3\n"
      "s_mul_i32 s62, s62, 57344\n"
      "s_add_u32 s44, s93, s62\n"
      "s_add_u32 s48, s91, s62\n"
      "s_sub_u32 s62, 5, s53\n"
      "s_and_b32 s62, s62, 3\n"
      "s_mul_i32 s62, s62, 57344\n"
      "s_add_u32 s45, s93, s62\n"
      "s_add_u32 s49, s91, s62\n"
      "s_sub_u32 s62, 6, s53\n"
      "s_and_b32 s62, s62, 3\n"
      "s_mul_i32 s62, s62, 57344\n"
      "s_add_u32 s46, s93, s62\n"
      "s_add_u32 s50, s91, s62\n"
      "s_sub_u32 s62, 7, s53\n"
      "s_and_b32 s62, s62, 3\n"
      "s_mul_i32 s62, s62, 57344\n"
      "s_add_u32 s47, s93, s62\n"
      "s_add_u32 s51, s91, s62\n"
      "s_add_u32 s91, s53, 3\n"
      "s_and_b32 s91, s91, 3\n"
      "s_lshl2_add_u32 s62, s91, 161888\n"
      "v_mov_b32 v15, s62\n"
      "s_mul_i32 s62, s53, 5120\n"
      "s_add_u32 s63, s62, 32768\n"
      "v_add_u32 v8, s63, v5\n"
      "s_mul_i32 s63, s91, 5120\n"
      "s_add_u32 s63, s63, 32768\n"
      "v_add_u32 v9, s63, v5\n"
      "s_xor_b32 s63, s60, 1\n"
      "s_lshl2_add_u32 s63, s63, s53\n"
      "s_mul_i32 s28, s63, 3072\n"
      "s_add_u32 s28, s28, 12451840\n"
      "v_cmp_eq_u32 vcc, s53, v1\n"
      "v_and_b32 v14, 48, v1\n"
      "v_add_u32 v14, 163072, v14\n"
      "v_cndmask_b32 v55, 0, 1, vcc\n"
      ".LWARME:\n"
      "v_mov_b32 v45, 161920\n"
      "s_add_u32 m0, s94, 24576\n"
      "s_nop 0\n"
      "buffer_load_dwordx4 v16, s[8:11], s81 offen offset:0 lds\n"
      "buffer_load_dwordx4 v17, s[8:11], s81 offen offset:1024 lds\n"
      "buffer_load_dwordx4 v18, s[8:11], s81 offen offset:2048 lds\n"
      "buffer_load_dwordx4 v19, s[8:11], s81 offen offset:3072 lds\n"
      "s_add_u32 m0, s95, 40960\n"
      "s_nop 0\n"
      "buffer_load_dwordx4 v20, s[4:7], s71 offen lds\n"
      "s_add_u32 m0, s95, 45056\n"
      "s_nop 0\n"
      "buffer_load_dwordx4 v21, s[4:7], s71 offen lds\n"
      "s_waitcnt vmcnt(13) lgkmcnt(0)\n"
      "v_cmp_eq_u32 vcc, 0, v1\n"
      "v_lshlrev_b32 v2, 2, v1\n"
      "v_add_u32 v2, 157696, v2\n"
      "v_cndmask_b32 v3, 0, 1, vcc\n"
      "s_barrier\n"
      "s_add_u32 m0, s94, 49152\n"
      "s_nop 0\n"
      "buffer_load_dwordx4 v16, s[8:11], s82 offen offset:0 lds\n"
      "buffer_load_dwordx4 v17, s[8:11], s82 offen offset:1024 lds\n"
      "buffer_load_dwordx4 v18, s[8:11], s82 offen offset:2048 lds\n"
      "buffer_load_dwordx4 v19, s[8:11], s82 offen offset:3072 lds\n"
      "s_add_u32 m0, s95, 65536\n"
      "s_nop 0\n"
      "buffer_load_dwordx4 v20, s[4:7], s72 offen lds\n"
      "s_add_u32 m0, s95, 69632\n"
      "s_nop 0\n"
      "buffer_load_dwordx4 v21, s[4:7], s72 offen lds\n"
      "s_waitcnt vmcnt(12)\n"
      "s_barrier\n"
      "ds_read_b64_tr_b16 v[88:89], v44 offset:0\n"
      "ds_read_b64_tr_b16 v[90:91], v44 offset:2080\n"
      "ds_read_b64_tr_b16 v[92:93], v44 offset:32\n"
      "ds_read_b64_tr_b16 v[94:95], v44 offset:2112\n"
      "ds_read_b64 v[46:47], v45\n"
      "s_waitcnt lgkmcnt(0)\n"
      "buffer_store_dwordx4 v[88:91], v5, s[20:23], s29 offen sc1\n"
      "buffer_store_dwordx4 v[92:95], v5, s[20:23], s30 offen sc1\n"
      "s_waitcnt lgkmcnt(0)\n"
      "v_readfirstlane_b32 s58, v46\n"
      "v_readfirstlane_b32 s59, v47\n"
      "v_mov_b32 v1, v55\n"
      "s_add_u32 m0, s94, 73728\n"
      "s_nop 0\n"
      "buffer_load_dwordx4 v16, s[8:11], s83 offen offset:0 lds\n"
      "buffer_load_dwordx4 v17, s[8:11], s83 offen offset:1024 lds\n"
      "buffer_load_dwordx4 v18, s[8:11], s83 offen offset:2048 lds\n"
      "buffer_load_dwordx4 v19, s[8:11], s83 offen offset:3072 lds\n"
      "s_add_u32 m0, s95, 90112\n"
      "s_nop 0\n"
      "buffer_load_dwordx4 v20, s[4:7], s73 offen lds\n"
      "s_add_u32 m0, s95, 94208\n"
      "s_nop 0\n"
      "buffer_load_dwordx4 v21, s[4:7], s73 offen lds\n"
      "s_waitcnt vmcnt(14)\n"
      "s_barrier\n"
      "s_add_u32 m0, s94, 98304\n"
      "s_nop 0\n"
      "buffer_load_dwordx4 v16, s[8:11], s84 offen offset:0 lds\n"
      "buffer_load_dwordx4 v17, s[8:11], s84 offen offset:1024 lds\n"
      "buffer_load_dwordx4 v18, s[8:11], s84 offen offset:2048 lds\n"
      "buffer_load_dwordx4 v19, s[8:11], s84 offen offset:3072 lds\n"
      "s_add_u32 m0, s95, 114688\n"
      "s_nop 0\n"
      "buffer_load_dwordx4 v20, s[4:7], s74 offen lds\n"
      "s_add_u32 m0, s95, 118784\n"
      "s_nop 0\n"
      "buffer_load_dwordx4 v21, s[4:7], s74 offen lds\n"
      "s_waitcnt vmcnt(14)\n"
      "s_barrier\n"
      "s_add_u32 m0, s94, 0\n"
      "s_nop 0\n"
      "buffer_load_dwordx4 v16, s[8:11], s85 offen offset:0 lds\n"
      "buffer_load_dwordx4 v17, s[8:11], s85 offen offset:1024 lds\n"
      "buffer_load_dwordx4 v18, s[8:11], s85 offen offset:2048 lds\n"
      "buffer_load_dwordx4 v19, s[8:11], s85 offen offset:3072 lds\n"
      "s_add_u32 m0, s95, 16384\n"
      "s_nop 0\n"
      "buffer_load_dwordx4 v20, s[4:7], s75 offen lds\n"
      "s_add_u32 m0, s95, 20480\n"
      "s_nop 0\n"
      "buffer_load_dwordx4 v21, s[4:7], s75 offen lds\n"
      "buffer_load_dwordx2 v[76:77], v42, s[20:23], s31 offen sc1\n"
      "s_waitcnt vmcnt(13)\n"
      "s_barrier\n"
      "s_add_u32 m0, s94, 24576\n"
      "s_nop 0\n"
      "buffer_load_dwordx4 v16, s[8:11], s86 offen offset:0 lds\n"
      "buffer_load_dwordx4 v17, s[8:11], s86 offen offset:1024 lds\n"
      "buffer_load_dwordx4 v18, s[8:11], s86 offen offset:2048 lds\n"
      "buffer_load_dwordx4 v19, s[8:11], s86 offen offset:3072 lds\n"
      "s_add_u32 m0, s95, 40960\n"
      "s_nop 0\n"
      "buffer_load_dwordx4 v20, s[4:7], s76 offen lds\n"
      "s_add_u32 m0, s95, 45056\n"
      "s_nop 0\n"
      "buffer_load_dwordx4 v21, s[4:7], s76 offen lds\n"
      "s_waitcnt vmcnt(13)\n"
      "s_barrier\n"
      "s_add_u32 m0, s94, 49152\n"
      "s_nop 0\n"
      "buffer_load_dwordx4 v16, s[8:11], s87 offen offset:0 lds\n"
      "buffer_load_dwordx4 v17, s[8:11], s87 offen offset:1024 lds\n"
      "buffer_load_dwordx4 v18, s[8:11], s87 offen offset:2048 lds\n"
      "buffer_load_dwordx4 v19, s[8:11], s87 offen offset:3072 lds\n"
      "s_add_u32 m0, s95, 65536\n"
      "s_nop 0\n"
      "buffer_load_dwordx4 v20, s[4:7], s77 offen lds\n"
      "s_add_u32 m0, s95, 69632\n"
      "s_nop 0\n"
      "buffer_load_dwordx4 v21, s[4:7], s77 offen lds\n"
      "s_waitcnt vmcnt(12)\n"
      "s_barrier\n"
      "buffer_store_dwordx2 v[46:47], v29, s[20:23], 0 offen sc1\n"
      "v_cmp_ne_u32 vcc, s58, v77\n"
      "s_cbranch_vccnz .Lt_1\n"
      ".Lt_2:\n"
      "v_and_b32 v82, s92, v76\n"
      "v_rcp_f32 v77, v82\n"
      "v_xor_b32 v85, 0x8000, v76\n"
      "s_mov_b32 s62, 0x01000100\n"
      "v_mul_f32 v80, 0xc0135761, v77\n"
      "v_mul_f32 v81, v77, v77\n"
      "v_cmp_nle_f32 vcc, 0x3089705f, v77\n"
      "v_mul_f32 v81, 0x3d372713, v81\n"
      "v_perm_b32 v84, v85, v85, s62\n"
      "v_mul_f32 v81, v81, v80\n"
      "v_mov_b32 v83, 1.0\n"
      "ds_write_b64 v43, v[84:85] offset:16\n"
      "s_cbranch_vccnz .Lb_4\n"
      ".Lb_5:\n"
      "ds_write_b128 v43, v[80:83]\n"
      "s_add_u32 m0, s94, 73728\n"
      "s_nop 0\n"
      "buffer_load_dwordx4 v16, s[8:11], s88 offen offset:0 lds\n"
      "buffer_load_dwordx4 v17, s[8:11], s88 offen offset:1024 lds\n"
      "buffer_load_dwordx4 v18, s[8:11], s88 offen offset:2048 lds\n"
      "buffer_load_dwordx4 v19, s[8:11], s88 offen offset:3072 lds\n"
      "s_add_u32 m0, s95, 90112\n"
      "s_nop 0\n"
      "buffer_load_dwordx4 v20, s[4:7], s78 offen lds\n"
      "s_add_u32 m0, s95, 94208\n"
      "s_nop 0\n"
      "buffer_load_dwordx4 v21, s[4:7], s78 offen lds\n"
      "s_waitcnt vmcnt(13)\n"
      "s_barrier\n"
      "s_add_u32 m0, s94, 98304\n"
      "s_nop 0\n"
      "buffer_load_dwordx4 v16, s[8:11], s89 offen offset:0 lds\n"
      "buffer_load_dwordx4 v17, s[8:11], s89 offen offset:1024 lds\n"
      "buffer_load_dwordx4 v18, s[8:11], s89 offen offset:2048 lds\n"
      "buffer_load_dwordx4 v19, s[8:11], s89 offen offset:3072 lds\n"
      "s_add_u32 m0, s95, 114688\n"
      "s_nop 0\n"
      "buffer_load_dwordx4 v20, s[4:7], s79 offen lds\n"
      "s_add_u32 m0, s95, 118784\n"
      "s_nop 0\n"
      "buffer_load_dwordx4 v21, s[4:7], s79 offen lds\n"
      "s_waitcnt vmcnt(13)\n"
      "s_barrier\n"
      "s_add_u32 m0, s95, 0\n"
      "s_nop 0\n"
      "buffer_load_dwordx4 v22, s[4:7], s70 offen lds\n"
      "s_add_u32 m0, s95, 4096\n"
      "s_nop 0\n"
      "buffer_load_dwordx4 v22, s[4:7], s71 offen lds\n"
      "s_add_u32 m0, s95, 8192\n"
      "s_nop 0\n"
      "buffer_load_dwordx4 v22, s[4:7], s72 offen lds\n"
      "s_add_u32 m0, s95, 12288\n"
      "s_nop 0\n"
      "buffer_load_dwordx4 v22, s[4:7], s73 offen lds\n"
      "s_add_u32 m0, s95, 16384\n"
      "s_nop 0\n"
      "buffer_load_dwordx4 v22, s[4:7], s74 offen lds\n"
      "s_waitcnt vmcnt(11)\n"
      "s_barrier\n"
      "s_add_u32 m0, s95, 20480\n"
      "s_nop 0\n"
      "buffer_load_dwordx4 v22, s[4:7], s75 offen lds\n"
      "s_add_u32 m0, s95, 24576\n"
      "s_nop 0\n"
      "buffer_load_dwordx4 v22, s[4:7], s76 offen lds\n"
      "s_add_u32 m0, s95, 28672\n"
      "s_nop 0\n"
      "buffer_load_dwordx4 v22, s[4:7], s77 offen lds\n"
      "s_add_u32 m0, s95, 32768\n"
      "s_nop 0\n"
      "buffer_load_dwordx4 v22, s[4:7], s78 offen lds\n"
      "s_add_u32 m0, s95, 36864\n"
      "s_nop 0\n"
      "buffer_load_dwordx4 v22, s[4:7], s79 offen lds\n"
      "s_waitcnt vmcnt(10) lgkmcnt(0)\n"
      "s_barrier\n"
      "s_add_u32 m0, s95, 49152\n"
      "s_nop 0\n"
      "buffer_load_dwordx4 v23, s[4:7], s70 offen lds\n"
      "s_add_u32 m0, s95, 53248\n"
      "s_nop 0\n"
      "buffer_load_dwordx4 v23, s[4:7], s71 offen lds\n"
      "s_add_u32 m0, s95, 57344\n"
      "s_nop 0\n"
      "buffer_load_dwordx4 v23, s[4:7], s72 offen lds\n"
      "s_add_u32 m0, s95, 61440\n"
      "s_nop 0\n"
      "buffer_load_dwordx4 v23, s[4:7], s73 offen lds\n"
      "s_add_u32 m0, s95, 65536\n"
      "s_nop 0\n"
      "buffer_load_dwordx4 v23, s[4:7], s74 offen lds\n"
      "buffer_load_dwordx2 v[50:51], v31, s[20:23], 0 offen sc1\n"
      "s_cmp_lt_u32 s53, 2\n"
      "s_cselect_b32 s62, 0, 1073741824\n"
      "buffer_load_dwordx2 v[48:49], v30, s[20:23], s62 offen sc1\n"
      "s_waitcnt vmcnt(12)\n"
      "s_barrier\n"
      "s_add_u32 m0, s95, 69632\n"
      "s_nop 0\n"
      "buffer_load_dwordx4 v23, s[4:7], s75 offen lds\n"
      "s_add_u32 m0, s95, 73728\n"
      "s_nop 0\n"
      "buffer_load_dwordx4 v23, s[4:7], s76 offen lds\n"
      "s_add_u32 m0, s95, 77824\n"
      "s_nop 0\n"
      "buffer_load_dwordx4 v23, s[4:7], s77 offen lds\n"
      "s_add_u32 m0, s95, 81920\n"
      "s_nop 0\n"
      "buffer_load_dwordx4 v23, s[4:7], s78 offen lds\n"
      "s_add_u32 m0, s95, 86016\n"
      "s_nop 0\n"
      "buffer_load_dwordx4 v23, s[4:7], s79 offen lds\n"
      "ds_add_u32 v2, v3 offset:4152\n"
      "s_waitcnt vmcnt(12)\n"
      "ds_add_u32 v2, v3 offset:2072\n"
      "ds_read_b32 v4, v2 offset:4152\n"
      "s_waitcnt lgkmcnt(0)\n"
      "v_readfirstlane_b32 s2, v4\n"
      "s_cmp_lt_u32 s2, 4\n"
      "s_cbranch_scc1 .Lw_6\n"
      ".Lw_7:\n"
      ".LS2:\n"
      "buffer_load_dwordx4 v[56:59], v5, s[36:39], s24 offen nt\n"
      "buffer_load_dwordx4 v[60:63], v5, s[36:39], s25 offen nt\n"
      "buffer_load_dword v52, v32, s[36:39], 0 offen nt\n"
      "buffer_load_ushort v53, v33, s[32:35], 0 offen\n"
      ".LS3:\n"
      "ds_write_b64 v34, v[36:37]\n"
      "ds_write_b64 v35, v[36:37]\n"
      "s_waitcnt vmcnt(4)\n"
      "ds_add_u32 v2, v3 offset:3096\n"
      "s_waitcnt vmcnt(10)\n"
      "v_cmp_ne_u64 vcc, s[58:59], v[50:51]\n"
      "s_cbranch_vccnz .Lf_9\n"
      ".Lf_10:\n"
      "s_add_u32 s66, s27, 57344\n"
      "s_add_u32 s67, s27, 114688\n"
      "s_add_u32 s68, s27, 172032\n"
      "s_add_u32 s69, s27, 229376\n"
      "s_cmp_lt_u32 s53, 2\n"
      "s_cbranch_scc0 .Lk_12\n"
      "s_waitcnt vmcnt(9)\n"
      "v_cmp_ne_u64 vcc, s[58:59], v[48:49]\n"
      "s_cbranch_vccnz .Lf_13\n"
      ".Lf_14:\n"
      "s_mul_i32 s62, s98, 4224\n"
      "s_add_u32 s62, s62, s95\n"
      "s_add_u32 m0, s95, 140032\n"
      "s_add_u32 s63, s62, 32768\n"
      "buffer_load_dwordx4 v5, s[20:23], s63 offen sc1 lds\n"
      "s_add_u32 m0, s95, 144640\n"
      "s_add_u32 s63, s62, 36992\n"
      "buffer_load_dwordx4 v5, s[20:23], s63 offen sc1 lds\n"
      ".Lk_12:\n"
      "s_waitcnt vmcnt(6)\n"
      "buffer_load_dwordx4 v[116:119], v5, s[20:23], s27 offen offset:0 sc1\n"
      "buffer_load_dwordx4 v[136:139], v5, s[20:23], s27 offen offset:1024 sc1\n"
      "s_waitcnt vmcnt(6)\n"
      "buffer_load_dwordx4 v[156:159], v5, s[20:23], s27 offen offset:2048 sc1\n"
      "buffer_load_dwordx4 v[176:179], v5, s[20:23], s27 offen offset:3072 sc1\n"
      "s_waitcnt vmcnt(4)\n"
      "s_cmp_eq_u32 s53, 0\n"
      "s_cbranch_scc0 .Ld_16\n"
      "v_readlane_b32 s62, v53, 0\n"
      "v_readlane_b32 s63, v53, 1\n"
      "s_mov_b64 exec, 1\n"
      "v_lshlrev_b32 v72, 16, v56\n"
      "s_lshl_b32 s62, s62, 16\n"
      "v_and_b32 v73, s92, v56\n"
      "v_add_f32 v72, s62, v72\n"
      "v_cvt_pk_bf16_f32 v56, v72, v73\n"
      "v_lshlrev_b32 v72, 16, v60\n"
      "s_lshl_b32 s63, s63, 16\n"
      "v_and_b32 v73, s92, v60\n"
      "v_add_f32 v72, s63, v72\n"
      "v_cvt_pk_bf16_f32 v60, v72, v73\n"
      "s_mov_b64 exec, -1\n"
      ".Ld_16:\n"
      "v_readlane_b32 s62, v52, 1\n"
      "ds_write_b128 v40, v[60:63] offset:8320\n"
      "v_alignbyte_b32 v103, v63, v62, 2\n"
      "v_writelane_b32 v72, s62, 0\n"
      "v_alignbyte_b32 v101, v61, v60, 2\n"
      "v_alignbyte_b32 v102, v62, v61, 2\n"
      "v_mov_b32_dpp v72, v63 wave_shr:1 row_mask:0xf bank_mask:0xf\n"
      "v_alignbyte_b32 v100, v60, v72, 2\n"
      "ds_write_b128 v40, v[100:103] offset:12480\n"
      "s_waitcnt vmcnt(6)\n"
      "buffer_load_dwordx4 v[196:199], v6, s[20:23], s27 offen offset:0 sc1\n"
      "buffer_load_dwordx4 v[216:219], v6, s[20:23], s27 offen offset:1024 sc1\n"
      "ds_write_b128 v40, v[56:59] offset:0\n"
      "v_alignbyte_b32 v99, v59, v58, 2\n"
      "v_alignbyte_b32 v97, v57, v56, 2\n"
      "v_alignbyte_b32 v98, v58, v57, 2\n"
      "v_mov_b32_dpp v52, v59 wave_shr:1 row_mask:0xf bank_mask:0xf\n"
      "v_alignbyte_b32 v96, v56, v52, 2\n"
      "ds_write_b128 v40, v[96:99] offset:4160\n"
      "s_waitcnt vmcnt(6)\n"
      "buffer_load_dwordx4 v[236:239], v7, s[20:23], s27 offen offset:0 sc1\n"
      "buffer_load_dwordx4 v[120:123], v5, s[20:23], s66 offen offset:0 sc1\n"
      "s_cmp_lt_u32 s53, 2\n"
      "s_cselect_b32 s62, 1073741824, 0\n"
      "buffer_load_dwordx2 v[48:49], v30, s[20:23], s62 offen sc1\n"
      "s_waitcnt vmcnt(9)\n"
      "ds_add_u32 v2, v3 offset:4304\n"
      "s_waitcnt vmcnt(6)\n"
      "buffer_load_dwordx4 v[140:143], v5, s[20:23], s66 offen offset:1024 sc1\n"
      "buffer_load_dwordx4 v[160:163], v5, s[20:23], s66 offen offset:2048 sc1\n"
      "s_waitcnt vmcnt(6)\n"
      "buffer_load_dwordx4 v[180:183], v5, s[20:23], s66 offen offset:3072 sc1\n"
      "buffer_load_dwordx4 v[200:203], v6, s[20:23], s66 offen offset:0 sc1\n"
      "s_waitcnt vmcnt(6)\n"
      "buffer_load_dwordx4 v[220:223], v6, s[20:23], s66 offen offset:1024 sc1\n"
      "buffer_load_dwordx4 v[240:243], v7, s[20:23], s66 offen offset:0 sc1\n"
      "s_cmp_lt_u32 s53, 2\n"
      "s_cbranch_scc1 .Lk_17\n"
      "s_waitcnt vmcnt(6)\n"
      "v_cmp_ne_u64 vcc, s[58:59], v[48:49]\n"
      "s_cbranch_vccnz .Lf_18\n"
      ".Lf_19:\n"
      "s_mul_i32 s62, s98, 4224\n"
      "s_add_u32 s62, s62, s95\n"
      "s_add_u32 m0, s95, 140032\n"
      "s_add_u32 s63, s62, 32768\n"
      "buffer_load_dwordx4 v5, s[20:23], s63 offen sc1 lds\n"
      "s_add_u32 m0, s95, 144640\n"
      "s_add_u32 s63, s62, 36992\n"
      "buffer_load_dwordx4 v5, s[20:23], s63 offen sc1 lds\n"
      ".Lk_17:\n"
      "s_waitcnt vmcnt(6)\n"
      "buffer_load_dwordx4 v[124:127], v5, s[20:23], s67 offen offset:0 sc1\n"
      "buffer_load_dwordx4 v[144:147], v5, s[20:23], s67 offen offset:1024 sc1\n"
      "s_waitcnt vmcnt(6)\n"
      "buffer_load_dwordx4 v[164:167], v5, s[20:23], s67 offen offset:2048 sc1\n"
      "buffer_load_dwordx4 v[184:187], v5, s[20:23], s67 offen offset:3072 sc1\n"
      "s_waitcnt vmcnt(6)\n"
      "buffer_load_dwordx4 v[204:207], v6, s[20:23], s67 offen offset:0 sc1\n"
      "buffer_load_dwordx4 v[224:227], v6, s[20:23], s67 offen offset:1024 sc1\n"
      "s_waitcnt vmcnt(6)\n"
      "ds_add_u32 v2, v3 offset:4280\n"
      "s_waitcnt vmcnt(6)\n"
      "buffer_load_dwordx4 v[244:247], v7, s[20:23], s67 offen offset:0 sc1\n"
      "buffer_load_dwordx4 v[128:131], v5, s[20:23], s68 offen offset:0 sc1\n"
      "s_waitcnt vmcnt(6)\n"
      "buffer_load_dwordx4 v[148:151], v5, s[20:23], s68 offen offset:1024 sc1\n"
      "buffer_load_dwordx4 v[168:171], v5, s[20:23], s68 offen offset:2048 sc1\n"
      "s_waitcnt vmcnt(6)\n"
      "buffer_load_dwordx4 v[188:191], v5, s[20:23], s68 offen offset:3072 sc1\n"
      "buffer_load_dwordx4 v[208:211], v6, s[20:23], s68 offen offset:0 sc1\n"
      "s_waitcnt vmcnt(6)\n"
      "buffer_load_dwordx4 v[228:231], v6, s[20:23], s68 offen offset:1024 sc1\n"
      "buffer_load_dwordx4 v[248:251], v7, s[20:23], s68 offen offset:0 sc1\n"
      "s_waitcnt vmcnt(6)\n"
      "buffer_load_dwordx4 v[132:135], v5, s[20:23], s69 offen offset:0 sc1\n"
      "buffer_load_dwordx4 v[152:155], v5, s[20:23], s69 offen offset:1024 sc1\n"
      "buffer_load_dwordx2 v[10:11], v13, s[20:23], 0 offen sc1\n"
      "s_waitcnt vmcnt(6)\n"
      "buffer_load_dwordx4 v[172:175], v5, s[20:23], s69 offen offset:2048 sc1\n"
      "buffer_load_dwordx4 v[192:195], v5, s[20:23], s69 offen offset:3072 sc1\n"
      "s_waitcnt vmcnt(6)\n"
      "buffer_load_dwordx4 v[212:215], v6, s[20:23], s69 offen offset:0 sc1\n"
      "buffer_load_dwordx4 v[232:235], v6, s[20:23], s69 offen offset:1024 sc1\n"
      "s_waitcnt vmcnt(6)\n"
      "buffer_load_dwordx4 v[252:255], v7, s[20:23], s69 offen offset:0 sc1\n"
      "s_and_b32 s75, s58, 0x1fff\n"
      "s_or_b32 s75, s75, 0x4000\n"
      "s_lshl_b32 s75, s75, 16\n"
      "s_mov_b32 s74, 128\n"
      "s_mov_b32 s73, 0x100000\n"
      "s_waitcnt vmcnt(5)\n"
      "v_cmp_ne_u64 vcc, s[58:59], v[10:11]\n"
      "s_cbranch_vccnz .Lf_21\n"
      ".Lf_22:\n"
      "buffer_load_dwordx4 v[32:35], v5, s[20:23], s44 offen offset:0 sc1\n"
      "buffer_load_dwordx4 v[36:39], v5, s[20:23], s44 offen offset:1024 sc1\n"
      "buffer_load_dwordx4 v[40:43], v5, s[20:23], s44 offen offset:2048 sc1\n"
      "buffer_load_dwordx4 v[44:47], v5, s[20:23], s44 offen offset:3072 sc1\n"
      "buffer_load_dwordx4 v[48:51], v6, s[20:23], s44 offen offset:0 sc1\n"
      "buffer_load_dwordx4 v[52:55], v6, s[20:23], s44 offen offset:1024 sc1\n"
      "buffer_load_dwordx4 v[56:59], v7, s[20:23], s44 offen offset:0 sc1\n"
      "buffer_load_dwordx4 v[60:63], v5, s[20:23], s45 offen offset:0 sc1\n"
      "s_waitcnt vmcnt(6)\n"
      "buffer_load_dwordx4 v[64:67], v5, s[20:23], s45 offen offset:1024 sc1\n"
      "s_setprio 1\n"
      "s_waitcnt vmcnt(6)\n"
      "v_mfma_f32_16x16x32_bf16 v[16:19], v[116:119], v[32:35], 0\n"
      "v_mfma_f32_16x16x32_bf16 v[16:19], v[136:139], v[36:39], v[16:19]\n"
      "buffer_load_dwordx4 v[68:71], v5, s[20:23], s45 offen offset:2048 sc1\n"
      "v_mfma_f32_16x16x32_bf16 v[16:19], v[156:159], v[40:43], v[16:19]\n"
      "s_waitcnt vmcnt(3)\n"
      "v_mfma_f32_16x16x32_bf16 v[16:19], v[176:179], v[44:47], v[16:19]\n"
      "buffer_load_dwordx4 v[72:75], v5, s[20:23], s45 offen offset:3072 sc1\n"
      "ds_read_b32 v4, v2 offset:4276\n"
      "v_mfma_f32_16x16x32_bf16 v[16:19], v[196:199], v[48:51], v[16:19]\n"
      "v_mfma_f32_16x16x32_bf16 v[16:19], v[216:219], v[52:55], v[16:19]\n"
      "buffer_load_dwordx4 v[76:79], v6, s[20:23], s45 offen offset:0 sc1\n"
      "v_mfma_f32_16x16x32_bf16 v[16:19], v[236:239], v[56:59], v[16:19]\n"
      "v_mfma_f32_16x16x32_bf16 v[20:23], v[120:123], v[32:35], 0\n"
      "buffer_load_dwordx4 v[80:83], v6, s[20:23], s45 offen offset:1024 sc1\n"
      "v_mfma_f32_16x16x32_bf16 v[20:23], v[140:143], v[36:39], v[20:23]\n"
      "s_waitcnt lgkmcnt(0)\n"
      "v_readfirstlane_b32 s2, v4\n"
      "s_cmp_lt_u32 s2, 4\n"
      "s_cbranch_scc1 .Lw_24\n"
      ".Lw_25:\n"
      "v_mfma_f32_16x16x32_bf16 v[20:23], v[160:163], v[40:43], v[20:23]\n"
      "buffer_load_dwordx4 v[84:87], v7, s[20:23], s45 offen offset:0 sc1\n"
      "v_mfma_f32_16x16x32_bf16 v[20:23], v[180:183], v[44:47], v[20:23]\n"
      "v_mfma_f32_16x16x32_bf16 v[20:23], v[200:203], v[48:51], v[20:23]\n"
      "buffer_load_dwordx4 v[88:91], v5, s[20:23], s46 offen offset:0 sc1\n"
      "ds_write_b128 v8, v[16:19] offset:0\n"
      "v_mfma_f32_16x16x32_bf16 v[20:23], v[220:223], v[52:55], v[20:23]\n"
      "v_mfma_f32_16x16x32_bf16 v[20:23], v[240:243], v[56:59], v[20:23]\n"
      "buffer_load_dwordx4 v[92:95], v5, s[20:23], s46 offen offset:1024 sc1\n"
      "v_mfma_f32_16x16x32_bf16 v[24:27], v[124:127], v[32:35], 0\n"
      "v_mfma_f32_16x16x32_bf16 v[24:27], v[144:147], v[36:39], v[24:27]\n"
      "buffer_load_dwordx4 v[96:99], v5, s[20:23], s46 offen offset:2048 sc1\n"
      "ds_add_u32 v2, v1 offset:4192\n"
      "v_mfma_f32_16x16x32_bf16 v[24:27], v[164:167], v[40:43], v[24:27]\n"
      "v_mfma_f32_16x16x32_bf16 v[24:27], v[184:187], v[44:47], v[24:27]\n"
      "buffer_load_dwordx4 v[100:103], v5, s[20:23], s46 offen offset:3072 sc1\n"
      "v_mfma_f32_16x16x32_bf16 v[24:27], v[204:207], v[48:51], v[24:27]\n"
      "ds_write_b128 v8, v[20:23] offset:1024\n"
      "v_mfma_f32_16x16x32_bf16 v[24:27], v[224:227], v[52:55], v[24:27]\n"
      "buffer_load_dwordx4 v[104:107], v6, s[20:23], s46 offen offset:0 sc1\n"
      "v_mfma_f32_16x16x32_bf16 v[24:27], v[244:247], v[56:59], v[24:27]\n"
      "v_mfma_f32_16x16x32_bf16 v[28:31], v[128:131], v[32:35], 0\n"
      "buffer_load_dwordx4 v[108:111], v6, s[20:23], s46 offen offset:1024 sc1\n"
      "v_mfma_f32_16x16x32_bf16 v[28:31], v[148:151], v[36:39], v[28:31]\n"
      "ds_add_u32 v2, v1 offset:4192\n"
      "v_mfma_f32_16x16x32_bf16 v[28:31], v[168:171], v[40:43], v[28:31]\n"
      "buffer_load_dwordx4 v[112:115], v7, s[20:23], s46 offen offset:0 sc1\n"
      "v_mfma_f32_16x16x32_bf16 v[28:31], v[188:191], v[44:47], v[28:31]\n"
      "v_mfma_f32_16x16x32_bf16 v[28:31], v[208:211], v[48:51], v[28:31]\n"
      "ds_write_b128 v8, v[24:27] offset:2048\n"
      "v_mfma_f32_16x16x32_bf16 v[28:31], v[228:231], v[52:55], v[28:31]\n"
      "v_mfma_f32_16x16x32_bf16 v[28:31], v[248:251], v[56:59], v[28:31]\n"
      "buffer_load_dwordx2 v[10:11], v13, s[20:23], s74 offen sc1\n"
      "v_mfma_f32_16x16x32_bf16 v[16:19], v[132:135], v[32:35], 0\n"
      "buffer_load_dwordx4 v[32:35], v5, s[20:23], s47 offen offset:0 sc1\n"
      "ds_read_b32 v4, v15\n"
      "ds_read_b128 v[20:23], v9 offset:0\n"
      "v_mfma_f32_16x16x32_bf16 v[16:19], v[152:155], v[36:39], v[16:19]\n"
      "ds_add_u32 v2, v1 offset:4192\n"
      "v_mfma_f32_16x16x32_bf16 v[16:19], v[172:175], v[40:43], v[16:19]\n"
      "v_mfma_f32_16x16x32_bf16 v[16:19], v[192:195], v[44:47], v[16:19]\n"
      "s_waitcnt lgkmcnt(2)\n"
      "v_cmp_gt_u32 vcc, 1, v4\n"
      "v_mfma_f32_16x16x32_bf16 v[16:19], v[212:215], v[48:51], v[16:19]\n"
      "buffer_load_dwordx4 v[36:39], v5, s[20:23], s47 offen offset:1024 sc1\n"
      "ds_write_b128 v8, v[28:31] offset:3072\n"
      "v_mfma_f32_16x16x32_bf16 v[16:19], v[232:235], v[52:55], v[16:19]\n"
      "s_cbranch_vccnz .Lp_27\n"
      ".Lp_28:\n"
      "v_mfma_f32_16x16x32_bf16 v[16:19], v[252:255], v[56:59], v[16:19]\n"
      "s_waitcnt vmcnt(14)\n"
      "s_waitcnt lgkmcnt(2)\n"
      "v_mfma_f32_16x16x32_bf16 v[20:23], v[116:119], v[60:63], v[20:23]\n"
      "ds_read_b32 v4, v15\n"
      "ds_read_b128 v[24:27], v9 offset:1024\n"
      "v_mfma_f32_16x16x32_bf16 v[20:23], v[136:139], v[64:67], v[20:23]\n"
      "buffer_load_dwordx4 v[40:43], v5, s[20:23], s47 offen offset:2048 sc1\n"
      "ds_add_u32 v2, v1 offset:4192\n"
      "v_mfma_f32_16x16x32_bf16 v[20:23], v[156:159], v[68:71], v[20:23]\n"
      "s_waitcnt vmcnt(11)\n"
      "v_mfma_f32_16x16x32_bf16 v[20:23], v[176:179], v[72:75], v[20:23]\n"
      "s_waitcnt lgkmcnt(2)\n"
      "v_cmp_gt_u32 vcc, 2, v4\n"
      "v_mfma_f32_16x16x32_bf16 v[20:23], v[196:199], v[76:79], v[20:23]\n"
      "ds_write_b128 v8, v[16:19] offset:4096\n"
      "v_mfma_f32_16x16x32_bf16 v[20:23], v[216:219], v[80:83], v[20:23]\n"
      "buffer_load_dwordx4 v[44:47], v5, s[20:23], s47 offen offset:3072 sc1\n"
      "s_cbranch_vccnz .Lp_29\n"
      ".Lp_30:\n"
      "v_mfma_f32_16x16x32_bf16 v[20:23], v[236:239], v[84:87], v[20:23]\n"
      "s_waitcnt lgkmcnt(2)\n"
      "v_mfma_f32_16x16x32_bf16 v[24:27], v[120:123], v[60:63], v[24:27]\n"
      "ds_read_b32 v4, v15\n"
      "ds_read_b128 v[28:31], v9 offset:2048\n"
      "v_mfma_f32_16x16x32_bf16 v[24:27], v[140:143], v[64:67], v[24:27]\n"
      "ds_add_u32 v2, v1 offset:4192\n"
      "v_mfma_f32_16x16x32_bf16 v[24:27], v[160:163], v[68:71], v[24:27]\n"
      "buffer_load_dwordx4 v[48:51], v6, s[20:23], s47 offen offset:0 sc1\n"
      "v_mfma_f32_16x16x32_bf16 v[24:27], v[180:183], v[72:75], v[24:27]\n"
      "s_waitcnt lgkmcnt(2)\n"
      "v_cmp_gt_u32 vcc, 3, v4\n"
      "v_mfma_f32_16x16x32_bf16 v[24:27], v[200:203], v[76:79], v[24:27]\n"
      "ds_write_b128 v8, v[20:23] offset:20480\n"
      "v_mfma_f32_16x16x32_bf16 v[24:27], v[220:223], v[80:83], v[24:27]\n"
      "s_cbranch_vccnz .Lp_31\n"
      ".Lp_32:\n"
      "v_mfma_f32_16x16x32_bf16 v[24:27], v[240:243], v[84:87], v[24:27]\n"
      "buffer_load_dwordx4 v[52:55], v6, s[20:23], s47 offen offset:1024 sc1\n"
      "s_waitcnt lgkmcnt(2)\n"
      "v_mfma_f32_16x16x32_bf16 v[28:31], v[124:127], v[60:63], v[28:31]\n"
      "ds_read_b32 v4, v15\n"
      "ds_read_b128 v[16:19], v9 offset:3072\n"
      "v_mfma_f32_16x16x32_bf16 v[28:31], v[144:147], v[64:67], v[28:31]\n"
      "ds_add_u32 v2, v1 offset:4192\n"
      "v_mfma_f32_16x16x32_bf16 v[28:31], v[164:167], v[68:71], v[28:31]\n"
      "v_mfma_f32_16x16x32_bf16 v[28:31], v[184:187], v[72:75], v[28:31]\n"
      "buffer_load_dwordx4 v[56:59], v7, s[20:23], s47 offen offset:0 sc1\n"
      "s_waitcnt lgkmcnt(2)\n"
      "v_cmp_gt_u32 vcc, 4, v4\n"
      "v_mfma_f32_16x16x32_bf16 v[28:31], v[204:207], v[76:79], v[28:31]\n"
      "ds_write_b128 v8, v[24:27] offset:21504\n"
      "v_mfma_f32_16x16x32_bf16 v[28:31], v[224:227], v[80:83], v[28:31]\n"
      "s_cbranch_vccnz .Lp_33\n"
      ".Lp_34:\n"
      "v_mfma_f32_16x16x32_bf16 v[28:31], v[244:247], v[84:87], v[28:31]\n"
      "s_waitcnt lgkmcnt(2)\n"
      "v_mfma_f32_16x16x32_bf16 v[16:19], v[128:131], v[60:63], v[16:19]\n"
      "ds_read_b32 v4, v15\n"
      "ds_read_b128 v[20:23], v9 offset:4096\n"
      "v_mfma_f32_16x16x32_bf16 v[16:19], v[148:151], v[64:67], v[16:19]\n"
      "ds_add_u32 v2, v1 offset:4192\n"
      "v_mfma_f32_16x16x32_bf16 v[16:19], v[168:171], v[68:71], v[16:19]\n"
      "v_mfma_f32_16x16x32_bf16 v[16:19], v[188:191], v[72:75], v[16:19]\n"
      "s_waitcnt lgkmcnt(2)\n"
      "v_cmp_gt_u32 vcc, 5, v4\n"
      "v_mfma_f32_16x16x32_bf16 v[16:19], v[208:211], v[76:79], v[16:19]\n"
      "ds_write_b128 v8, v[28:31] offset:22528\n"
      "v_mfma_f32_16x16x32_bf16 v[16:19], v[228:231], v[80:83], v[16:19]\n"
      "s_cbranch_vccnz .Lp_35\n"
      ".Lp_36:\n"
      "v_mfma_f32_16x16x32_bf16 v[16:19], v[248:251], v[84:87], v[16:19]\n"
      "s_waitcnt lgkmcnt(2)\n"
      "v_mfma_f32_16x16x32_bf16 v[20:23], v[132:135], v[60:63], v[20:23]\n"
      "ds_read_b32 v4, v15\n"
      "ds_read_b128 v[24:27], v9 offset:20480\n"
      "v_mfma_f32_16x16x32_bf16 v[20:23], v[152:155], v[64:67], v[20:23]\n"
      "ds_add_u32 v2, v1 offset:4192\n"
      "v_mfma_f32_16x16x32_bf16 v[20:23], v[172:175], v[68:71], v[20:23]\n"
      "v_mfma_f32_16x16x32_bf16 v[20:23], v[192:195], v[72:75], v[20:23]\n"
      "s_waitcnt lgkmcnt(2)\n"
      "v_cmp_gt_u32 vcc, 6, v4\n"
      "v_mfma_f32_16x16x32_bf16 v[20:23], v[212:215], v[76:79], v[20:23]\n"
      "ds_write_b128 v8, v[16:19] offset:23552\n"
      "v_mfma_f32_16x16x32_bf16 v[20:23], v[232:235], v[80:83], v[20:23]\n"
      "s_cbranch_vccnz .Lp_37\n"
      ".Lp_38:\n"
      "v_mfma_f32_16x16x32_bf16 v[20:23], v[252:255], v[84:87], v[20:23]\n"
      "s_waitcnt vmcnt(12)\n"
      "s_waitcnt lgkmcnt(2)\n"
      "v_mfma_f32_16x16x32_bf16 v[24:27], v[116:119], v[88:91], v[24:27]\n"
      "ds_read_b32 v4, v15\n"
      "ds_read_b128 v[28:31], v9 offset:21504\n"
      "v_mfma_f32_16x16x32_bf16 v[24:27], v[136:139], v[92:95], v[24:27]\n"
      "ds_add_u32 v2, v1 offset:4192\n"
      "v_mfma_f32_16x16x32_bf16 v[24:27], v[156:159], v[96:99], v[24:27]\n"
      "s_waitcnt vmcnt(8)\n"
      "v_mfma_f32_16x16x32_bf16 v[24:27], v[176:179], v[100:103], v[24:27]\n"
      "s_waitcnt lgkmcnt(2)\n"
      "v_cmp_gt_u32 vcc, 7, v4\n"
      "v_mfma_f32_16x16x32_bf16 v[24:27], v[196:199], v[104:107], v[24:27]\n"
      "ds_write_b128 v8, v[20:23] offset:24576\n"
      "v_mfma_f32_16x16x32_bf16 v[24:27], v[216:219], v[108:111], v[24:27]\n"
      "s_cbranch_vccnz .Lp_39\n"
      ".Lp_40:\n"
      "v_mfma_f32_16x16x32_bf16 v[24:27], v[236:239], v[112:115], v[24:27]\n"
      "s_waitcnt lgkmcnt(2)\n"
      "v_mfma_f32_16x16x32_bf16 v[28:31], v[120:123], v[88:91], v[28:31]\n"
      "s_waitcnt vmcnt(7)\n"
      "v_cmp_ne_u64 vcc, s[58:59], v[10:11]\n"
      "s_cbranch_vccnz .Lf_41\n"
      ".Lf_42:\n"
      "buffer_load_dwordx4 v[60:63], v5, s[20:23], s48 offen offset:0 sc1\n"
      "ds_read_b32 v4, v15\n"
      "ds_read_b128 v[16:19], v9 offset:22528\n"
      "v_mfma_f32_16x16x32_bf16 v[28:31], v[140:143], v[92:95], v[28:31]\n"
      "ds_add_u32 v2, v1 offset:4192\n"
      "v_mfma_f32_16x16x32_bf16 v[28:31], v[160:163], v[96:99], v[28:31]\n"
      "buffer_load_dwordx4 v[64:67], v5, s[20:23], s48 offen offset:1024 sc1\n"
      "v_mfma_f32_16x16x32_bf16 v[28:31], v[180:183], v[100:103], v[28:31]\n"
      "s_waitcnt lgkmcnt(2)\n"
      "v_cmp_gt_u32 vcc, 8, v4\n"
      "v_mfma_f32_16x16x32_bf16 v[28:31], v[200:203], v[104:107], v[28:31]\n"
      "buffer_load_dwordx4 v[68:71], v5, s[20:23], s48 offen offset:2048 sc1\n"
      "ds_write_b128 v8, v[24:27] offset:40960\n"
      "v_mfma_f32_16x16x32_bf16 v[28:31], v[220:223], v[108:111], v[28:31]\n"
      "s_cbranch_vccnz .Lp_44\n"
      ".Lp_45:\n"
      "v_mfma_f32_16x16x32_bf16 v[28:31], v[240:243], v[112:115], v[28:31]\n"
      "buffer_load_dwordx4 v[72:75], v5, s[20:23], s48 offen offset:3072 sc1\n"
      "s_waitcnt lgkmcnt(2)\n"
      "v_mfma_f32_16x16x32_bf16 v[16:19], v[124:127], v[88:91], v[16:19]\n"
      "ds_read_b32 v4, v15\n"
      "ds_read_b128 v[20:23], v9 offset:23552\n"
      "v_mfma_f32_16x16x32_bf16 v[16:19], v[144:147], v[92:95], v[16:19]\n"
      "buffer_load_dwordx4 v[76:79], v6, s[20:23], s48 offen offset:0 sc1\n"
      "ds_add_u32 v2, v1 offset:4192\n"
      "v_mfma_f32_16x16x32_bf16 v[16:19], v[164:167], v[96:99], v[16:19]\n"
      "v_mfma_f32_16x16x32_bf16 v[16:19], v[184:187], v[100:103], v[16:19]\n"
      "buffer_load_dwordx4 v[80:83], v6, s[20:23], s48 offen offset:1024 sc1\n"
      "s_waitcnt lgkmcnt(2)\n"
      "v_cmp_gt_u32 vcc, 9, v4\n"
      "v_mfma_f32_16x16x32_bf16 v[16:19], v[204:207], v[104:107], v[16:19]\n"
      "ds_write_b128 v8, v[28:31] offset:41984\n"
      "v_mfma_f32_16x16x32_bf16 v[16:19], v[224:227], v[108:111], v[16:19]\n"
      "buffer_load_dwordx4 v[84:87], v7, s[20:23], s48 offen offset:0 sc1\n"
      "s_cbranch_vccnz .Lp_46\n"
      ".Lp_47:\n"
      "v_mfma_f32_16x16x32_bf16 v[16:19], v[244:247], v[112:115], v[16:19]\n"
      "s_waitcnt lgkmcnt(2)\n"
      "v_mfma_f32_16x16x32_bf16 v[20:23], v[128:131], v[88:91], v[20:23]\n"
      "ds_read_b32 v4, v15\n"
      "ds_read_b128 v[24:27], v9 offset:24576\n"
      "v_mfma_f32_16x16x32_bf16 v[20:23], v[148:151], v[92:95], v[20:23]\n"
      "ds_add_u32 v2, v1 offset:4192\n"
      "v_mfma_f32_16x16x32_bf16 v[20:23], v[168:171], v[96:99], v[20:23]\n"
      "v_mfma_f32_16x16x32_bf16 v[20:23], v[188:191], v[100:103], v[20:23]\n"
      "s_waitcnt lgkmcnt(2)\n"
      "v_cmp_gt_u32 vcc, 10, v4\n"
      "v_mfma_f32_16x16x32_bf16 v[20:23], v[208:211], v[104:107], v[20:23]\n"
      "ds_write_b128 v8, v[16:19] offset:43008\n"
      "v_mfma_f32_16x16x32_bf16 v[20:23], v[228:231], v[108:111], v[20:23]\n"
      "s_cbranch_vccnz .Lp_48\n"
      ".Lp_49:\n"
      "v_mfma_f32_16x16x32_bf16 v[20:23], v[248:251], v[112:115], v[20:23]\n"
      "s_waitcnt lgkmcnt(2)\n"
      "v_mfma_f32_16x16x32_bf16 v[24:27], v[132:135], v[88:91], v[24:27]\n"
      "buffer_load_dwordx4 v[88:91], v5, s[20:23], s49 offen offset:0 sc1\n"
      "ds_read_b32 v4, v15\n"
      "ds_read_b128 v[28:31], v9 offset:40960\n"
      "v_mfma_f32_16x16x32_bf16 v[24:27], v[152:155], v[92:95], v[24:27]\n"
      "ds_add_u32 v2, v1 offset:4192\n"
      "v_mfma_f32_16x16x32_bf16 v[24:27], v[172:175], v[96:99], v[24:27]\n"
      "v_mfma_f32_16x16x32_bf16 v[24:27], v[192:195], v[100:103], v[24:27]\n"
      "s_waitcnt lgkmcnt(2)\n"
      "v_cmp_gt_u32 vcc, 11, v4\n"
      "v_mfma_f32_16x16x32_bf16 v[24:27], v[212:215], v[104:107], v[24:27]\n"
      "buffer_load_dwordx4 v[92:95], v5, s[20:23], s49 offen offset:1024 sc1\n"
      "ds_write_b128 v8, v[20:23] offset:44032\n"
      "v_mfma_f32_16x16x32_bf16 v[24:27], v[232:235], v[108:111], v[24:27]\n"
      "s_cbranch_vccnz .Lp_50\n"
      ".Lp_51:\n"
      "v_mfma_f32_16x16x32_bf16 v[24:27], v[252:255], v[112:115], v[24:27]\n"
      "s_waitcnt vmcnt(13)\n"
      "s_waitcnt lgkmcnt(2)\n"
      "v_mfma_f32_16x16x32_bf16 v[28:31], v[116:119], v[32:35], v[28:31]\n"
      "ds_read_b32 v4, v15\n"
      "ds_read_b128 v[16:19], v9 offset:41984\n"
      "v_mfma_f32_16x16x32_bf16 v[28:31], v[136:139], v[36:39], v[28:31]\n"
      "buffer_load_dwordx4 v[96:99], v5, s[20:23], s49 offen offset:2048 sc1\n"
      "ds_add_u32 v2, v1 offset:4192\n"
      "v_mfma_f32_16x16x32_bf16 v[28:31], v[156:159], v[40:43], v[28:31]\n"
      "s_waitcnt vmcnt(10)\n"
      "v_mfma_f32_16x16x32_bf16 v[28:31], v[176:179], v[44:47], v[28:31]\n"
      "s_waitcnt lgkmcnt(2)\n"
      "v_cmp_gt_u32 vcc, 12, v4\n"
      "v_mfma_f32_16x16x32_bf16 v[28:31], v[196:199], v[48:51], v[28:31]\n"
      "ds_write_b128 v8, v[24:27] offset:45056\n"
      "v_mfma_f32_16x16x32_bf16 v[28:31], v[216:219], v[52:55], v[28:31]\n"
      "buffer_load_dwordx4 v[100:103], v5, s[20:23], s49 offen offset:3072 sc1\n"
      "s_cbranch_vccnz .Lp_52\n"
      ".Lp_53:\n"
      "v_mfma_f32_16x16x32_bf16 v[28:31], v[236:239], v[56:59], v[28:31]\n"
      "s_waitcnt lgkmcnt(2)\n"
      "v_mfma_f32_16x16x32_bf16 v[16:19], v[120:123], v[32:35], v[16:19]\n"
      "ds_read_b32 v4, v15\n"
      "ds_read_b128 v[20:23], v9 offset:43008\n"
      "v_mfma_f32_16x16x32_bf16 v[16:19], v[140:143], v[36:39], v[16:19]\n"
      "ds_add_u32 v2, v1 offset:4192\n"
      "v_mfma_f32_16x16x32_bf16 v[16:19], v[160:163], v[40:43], v[16:19]\n"
      "buffer_load_dwordx4 v[104:107], v6, s[20:23], s49 offen offset:0 sc1\n"
      "v_mfma_f32_16x16x32_bf16 v[16:19], v[180:183], v[44:47], v[16:19]\n"
      "s_waitcnt lgkmcnt(2)\n"
      "v_cmp_gt_u32 vcc, 13, v4\n"
      "v_mfma_f32_16x16x32_bf16 v[16:19], v[200:203], v[48:51], v[16:19]\n"
      "v_cvt_pk_bf16_f32 v10, v28, v29\n"
      "v_cvt_pk_bf16_f32 v11, v30, v31\n"
      "v_mfma_f32_16x16x32_bf16 v[16:19], v[220:223], v[52:55], v[16:19]\n"
      "s_cbranch_vccnz .Lp_54\n"
      ".Lp_55:\n"
      "v_mfma_f32_16x16x32_bf16 v[16:19], v[240:243], v[56:59], v[16:19]\n"
      "buffer_load_dwordx4 v[108:111], v6, s[20:23], s49 offen offset:1024 sc1\n"
      "s_waitcnt lgkmcnt(1)\n"
      "v_mfma_f32_16x16x32_bf16 v[20:23], v[124:127], v[32:35], v[20:23]\n"
      "ds_read_b32 v4, v15\n"
      "ds_read_b128 v[24:27], v9 offset:44032\n"
      "v_mfma_f32_16x16x32_bf16 v[20:23], v[144:147], v[36:39], v[20:23]\n"
      "v_mfma_f32_16x16x32_bf16 v[20:23], v[164:167], v[40:43], v[20:23]\n"
      "v_mfma_f32_16x16x32_bf16 v[20:23], v[184:187], v[44:47], v[20:23]\n"
      "buffer_load_dwordx4 v[112:115], v7, s[20:23], s49 offen offset:0 sc1\n"
      "s_waitcnt lgkmcnt(1)\n"
      "v_cmp_gt_u32 vcc, 14, v4\n"
      "v_mfma_f32_16x16x32_bf16 v[20:23], v[204:207], v[48:51], v[20:23]\n"
      "v_cvt_pk_bf16_f32 v12, v16, v17\n"
      "v_cvt_pk_bf16_f32 v13, v18, s75\n"
      "v_mfma_f32_16x16x32_bf16 v[20:23], v[224:227], v[52:55], v[20:23]\n"
      "s_cbranch_vccnz .Lp_56\n"
      ".Lp_57:\n"
      "v_mfma_f32_16x16x32_bf16 v[20:23], v[244:247], v[56:59], v[20:23]\n"
      "buffer_store_dwordx4 v[10:13], v5, s[20:23], s28 offen offset:0 sc1\n"
      "s_waitcnt lgkmcnt(0)\n"
      "v_mfma_f32_16x16x32_bf16 v[24:27], v[128:131], v[32:35], v[24:27]\n"
      "ds_read_b32 v4, v15\n"
      "ds_read_b128 v[28:31], v9 offset:45056\n"
      "v_mfma_f32_16x16x32_bf16 v[24:27], v[148:151], v[36:39], v[24:27]\n"
      "v_mfma_f32_16x16x32_bf16 v[24:27], v[168:171], v[40:43], v[24:27]\n"
      "v_mfma_f32_16x16x32_bf16 v[24:27], v[188:191], v[44:47], v[24:27]\n"
      "s_waitcnt lgkmcnt(1)\n"
      "v_cmp_gt_u32 vcc, 15, v4\n"
      "v_mfma_f32_16x16x32_bf16 v[24:27], v[208:211], v[48:51], v[24:27]\n"
      "v_cvt_pk_bf16_f32 v10, v19, v20\n"
      "v_cvt_pk_bf16_f32 v11, v21, v22\n"
      "v_mfma_f32_16x16x32_bf16 v[24:27], v[228:231], v[52:55], v[24:27]\n"
      "s_cbranch_vccnz .Lp_58\n"
      ".Lp_59:\n"
      "v_mfma_f32_16x16x32_bf16 v[24:27], v[248:251], v[56:59], v[24:27]\n"
      "s_waitcnt lgkmcnt(0)\n"
      "v_mfma_f32_16x16x32_bf16 v[28:31], v[132:135], v[32:35], v[28:31]\n"
      "buffer_load_dwordx4 v[32:35], v5, s[20:23], s50 offen offset:0 sc1\n"
      "ds_read_b128 v[16:19], v14 offset:0\n"
      "v_mfma_f32_16x16x32_bf16 v[28:31], v[152:155], v[36:39], v[28:31]\n"
      "v_mfma_f32_16x16x32_bf16 v[28:31], v[172:175], v[40:43], v[28:31]\n"
      "ds_read_b32 v4, v2 offset:4308\n"
      "v_mfma_f32_16x16x32_bf16 v[28:31], v[192:195], v[44:47], v[28:31]\n"
      "v_mfma_f32_16x16x32_bf16 v[28:31], v[212:215], v[48:51], v[28:31]\n"
      "buffer_load_dwordx4 v[36:39], v5, s[20:23], s50 offen offset:1024 sc1\n"
      "v_cvt_pk_bf16_f32 v12, v23, v24\n"
      "v_cvt_pk_bf16_f32 v13, v25, s75\n"
      "v_mfma_f32_16x16x32_bf16 v[28:31], v[232:235], v[52:55], v[28:31]\n"
      "v_mfma_f32_16x16x32_bf16 v[28:31], v[252:255], v[56:59], v[28:31]\n"
      "buffer_store_dwordx4 v[10:13], v5, s[20:23], s28 offen offset:1024 sc1\n"
      "s_waitcnt vmcnt(15)\n"
      "s_waitcnt lgkmcnt(1)\n"
      "v_mfma_f32_16x16x32_bf16 v[16:19], v[116:119], v[60:63], v[16:19]\n"
      "ds_read_b128 v[20:23], v14 offset:64\n"
      "v_mfma_f32_16x16x32_bf16 v[16:19], v[136:139], v[64:67], v[16:19]\n"
      "buffer_load_dwordx4 v[40:43], v5, s[20:23], s50 offen offset:2048 sc1\n"
      "v_mfma_f32_16x16x32_bf16 v[16:19], v[156:159], v[68:71], v[16:19]\n"
      "s_waitcnt vmcnt(12)\n"
      "v_mfma_f32_16x16x32_bf16 v[16:19], v[176:179], v[72:75], v[16:19]\n"
      "s_waitcnt lgkmcnt(1)\n"
      "v_readfirstlane_b32 s2, v4\n"
      "s_cmp_lt_u32 s2, 4\n"
      "s_cbranch_scc1 .Lw_60\n"
      ".Lw_61:\n"
      "v_mfma_f32_16x16x32_bf16 v[16:19], v[196:199], v[76:79], v[16:19]\n"
      "v_cvt_pk_bf16_f32 v10, v26, v27\n"
      "v_cvt_pk_bf16_f32 v11, v28, v29\n"
      "v_mfma_f32_16x16x32_bf16 v[16:19], v[216:219], v[80:83], v[16:19]\n"
      "buffer_load_dwordx4 v[44:47], v5, s[20:23], s50 offen offset:3072 sc1\n"
      "v_cvt_pk_bf16_f32 v12, v30, v31\n"
      "v_cvt_pk_bf16_f32 v13, v31, s75\n"
      "v_mfma_f32_16x16x32_bf16 v[16:19], v[236:239], v[84:87], v[16:19]\n"
      "buffer_store_dwordx4 v[10:13], v5, s[20:23], s28 offen offset:2048 sc1\n"
      "s_waitcnt lgkmcnt(0)\n"
      "v_mfma_f32_16x16x32_bf16 v[20:23], v[120:123], v[60:63], v[20:23]\n"
      "ds_read_b128 v[24:27], v14 offset:128\n"
      "v_mfma_f32_16x16x32_bf16 v[20:23], v[140:143], v[64:67], v[20:23]\n"
      "v_mfma_f32_16x16x32_bf16 v[20:23], v[160:163], v[68:71], v[20:23]\n"
      "buffer_load_dwordx4 v[48:51], v6, s[20:23], s50 offen offset:0 sc1\n"
      "v_mfma_f32_16x16x32_bf16 v[20:23], v[180:183], v[72:75], v[20:23]\n"
      "v_mfma_f32_16x16x32_bf16 v[20:23], v[200:203], v[76:79], v[20:23]\n"
      "v_add_u32 v8, 4294934528, v8\n"
      "ds_write_b128 v8, v[16:19] offset:0\n"
      "v_mfma_f32_16x16x32_bf16 v[20:23], v[220:223], v[80:83], v[20:23]\n"
      "v_mfma_f32_16x16x32_bf16 v[20:23], v[240:243], v[84:87], v[20:23]\n"
      "buffer_load_dwordx4 v[52:55], v6, s[20:23], s50 offen offset:1024 sc1\n"
      "s_waitcnt lgkmcnt(1)\n"
      "v_mfma_f32_16x16x32_bf16 v[24:27], v[124:127], v[60:63], v[24:27]\n"
      "ds_read_b128 v[28:31], v14 offset:192\n"
      "v_mfma_f32_16x16x32_bf16 v[24:27], v[144:147], v[64:67], v[24:27]\n"
      "ds_add_u32 v2, v1 offset:4192\n"
      "v_mfma_f32_16x16x32_bf16 v[24:27], v[164:167], v[68:71], v[24:27]\n"
      "v_mfma_f32_16x16x32_bf16 v[24:27], v[184:187], v[72:75], v[24:27]\n"
      "buffer_load_dwordx4 v[56:59], v7, s[20:23], s50 offen offset:0 sc1\n"
      "v_mfma_f32_16x16x32_bf16 v[24:27], v[204:207], v[76:79], v[24:27]\n"
      "ds_write_b128 v8, v[20:23] offset:1024\n"
      "v_mfma_f32_16x16x32_bf16 v[24:27], v[224:227], v[80:83], v[24:27]\n"
      "v_mfma_f32_16x16x32_bf16 v[24:27], v[244:247], v[84:87], v[24:27]\n"
      "s_waitcnt lgkmcnt(2)\n"
      "v_mfma_f32_16x16x32_bf16 v[28:31], v[128:131], v[60:63], v[28:31]\n"
      "ds_read_b128 v[16:19], v14 offset:256\n"
      "v_mfma_f32_16x16x32_bf16 v[28:31], v[148:151], v[64:67], v[28:31]\n"
      "ds_add_u32 v2, v1 offset:4192\n"
      "v_mfma_f32_16x16x32_bf16 v[28:31], v[168:171], v[68:71], v[28:31]\n"
      "v_mfma_f32_16x16x32_bf16 v[28:31], v[188:191], v[72:75], v[28:31]\n"
      "v_mfma_f32_16x16x32_bf16 v[28:31], v[208:211], v[76:79], v[28:31]\n"
      "ds_write_b128 v8, v[24:27] offset:2048\n"
      "v_mfma_f32_16x16x32_bf16 v[28:31], v[228:231], v[80:83], v[28:31]\n"
      "v_mfma_f32_16x16x32_bf16 v[28:31], v[248:251], v[84:87], v[28:31]\n"
      "s_waitcnt lgkmcnt(2)\n"
      "v_mfma_f32_16x16x32_bf16 v[16:19], v[132:135], v[60:63], v[16:19]\n"
      "buffer_load_dwordx4 v[60:63], v5, s[20:23], s51 offen offset:0 sc1\n"
      "v_add_u32 v9, 4294934528, v9\n"
      "ds_read_b32 v4, v15\n"
      "ds_read_b128 v[20:23], v9 offset:0\n"
      "v_mfma_f32_16x16x32_bf16 v[16:19], v[152:155], v[64:67], v[16:19]\n"
      "ds_add_u32 v2, v1 offset:4192\n"
      "v_mfma_f32_16x16x32_bf16 v[16:19], v[172:175], v[68:71], v[16:19]\n"
      "v_mfma_f32_16x16x32_bf16 v[16:19], v[192:195], v[72:75], v[16:19]\n"
      "s_waitcnt lgkmcnt(2)\n"
      "v_cmp_gt_u32 vcc, 16, v4\n"
      "v_mfma_f32_16x16x32_bf16 v[16:19], v[212:215], v[76:79], v[16:19]\n"
      "buffer_load_dwordx4 v[64:67], v5, s[20:23], s51 offen offset:1024 sc1\n"
      "ds_write_b128 v8, v[28:31] offset:3072\n"
      "v_mfma_f32_16x16x32_bf16 v[16:19], v[232:235], v[80:83], v[16:19]\n"
      "s_cbranch_vccnz .Lp_63\n"
      ".Lp_64:\n"
      "v_mfma_f32_16x16x32_bf16 v[16:19], v[252:255], v[84:87], v[16:19]\n"
      "s_waitcnt vmcnt(16)\n"
      "s_waitcnt lgkmcnt(2)\n"
      "v_mfma_f32_16x16x32_bf16 v[20:23], v[116:119], v[88:91], v[20:23]\n"
      "ds_read_b32 v4, v15\n"
      "ds_read_b128 v[24:27], v9 offset:1024\n"
      "v_mfma_f32_16x16x32_bf16 v[20:23], v[136:139], v[92:95], v[20:23]\n"
      "buffer_load_dwordx4 v[68:71], v5, s[20:23], s51 offen offset:2048 sc1\n"
      "ds_add_u32 v2, v1 offset:4192\n"
      "v_mfma_f32_16x16x32_bf16 v[20:23], v[156:159], v[96:99], v[20:23]\n"
      "s_waitcnt vmcnt(13)\n"
      "v_mfma_f32_16x16x32_bf16 v[20:23], v[176:179], v[100:103], v[20:23]\n"
      "s_waitcnt lgkmcnt(2)\n"
      "v_cmp_gt_u32 vcc, 17, v4\n"
      "v_mfma_f32_16x16x32_bf16 v[20:23], v[196:199], v[104:107], v[20:23]\n"
      "ds_write_b128 v8, v[16:19] offset:4096\n"
      "v_mfma_f32_16x16x32_bf16 v[20:23], v[216:219], v[108:111], v[20:23]\n"
      "buffer_load_dwordx4 v[72:75], v5, s[20:23], s51 offen offset:3072 sc1\n"
      "s_cbranch_vccnz .Lp_65\n"
      ".Lp_66:\n"
      "v_mfma_f32_16x16x32_bf16 v[20:23], v[236:239], v[112:115], v[20:23]\n"
      "s_waitcnt lgkmcnt(2)\n"
      "v_mfma_f32_16x16x32_bf16 v[24:27], v[120:123], v[88:91], v[24:27]\n"
      "ds_read_b32 v4, v15\n"
      "ds_read_b128 v[28:31], v9 offset:2048\n"
      "v_mfma_f32_16x16x32_bf16 v[24:27], v[140:143], v[92:95], v[24:27]\n"
      "ds_add_u32 v2, v1 offset:4192\n"
      "v_mfma_f32_16x16x32_bf16 v[24:27], v[160:163], v[96:99], v[24:27]\n"
      "buffer_load_dwordx4 v[76:79], v6, s[20:23], s51 offen offset:0 sc1\n"
      "v_mfma_f32_16x16x32_bf16 v[24:27], v[180:183], v[100:103], v[24:27]\n"
      "s_waitcnt lgkmcnt(2)\n"
      "v_cmp_gt_u32 vcc, 18, v4\n"
      "v_mfma_f32_16x16x32_bf16 v[24:27], v[200:203], v[104:107], v[24:27]\n"
      "v_add_u32 v8, 98304, v8\n"
      "ds_write_b128 v8, v[20:23] offset:0\n"
      "v_mfma_f32_16x16x32_bf16 v[24:27], v[220:223], v[108:111], v[24:27]\n"
      "s_cbranch_vccnz .Lp_67\n"
      ".Lp_68:\n"
      "v_mfma_f32_16x16x32_bf16 v[24:27], v[240:243], v[112:115], v[24:27]\n"
      "buffer_load_dwordx4 v[80:83], v6, s[20:23], s51 offen offset:1024 sc1\n"
      "s_waitcnt lgkmcnt(2)\n"
      "v_mfma_f32_16x16x32_bf16 v[28:31], v[124:127], v[88:91], v[28:31]\n"
      "ds_read_b32 v4, v15\n"
      "ds_read_b128 v[16:19], v9 offset:3072\n"
      "v_mfma_f32_16x16x32_bf16 v[28:31], v[144:147], v[92:95], v[28:31]\n"
      "ds_add_u32 v2, v1 offset:4192\n"
      "v_mfma_f32_16x16x32_bf16 v[28:31], v[164:167], v[96:99], v[28:31]\n"
      "v_mfma_f32_16x16x32_bf16 v[28:31], v[184:187], v[100:103], v[28:31]\n"
      "buffer_load_dwordx4 v[84:87], v7, s[20:23], s51 offen offset:0 sc1\n"
      "s_waitcnt lgkmcnt(2)\n"
      "v_cmp_gt_u32 vcc, 19, v4\n"
      "v_mfma_f32_16x16x32_bf16 v[28:31], v[204:207], v[104:107], v[28:31]\n"
      "ds_write_b128 v8, v[24:27] offset:1024\n"
      "v_mfma_f32_16x16x32_bf16 v[28:31], v[224:227], v[108:111], v[28:31]\n"
      "s_cbranch_vccnz .Lp_69\n"
      ".Lp_70:\n"
      "v_mfma_f32_16x16x32_bf16 v[28:31], v[244:247], v[112:115], v[28:31]\n"
      "s_waitcnt lgkmcnt(2)\n"
      "v_mfma_f32_16x16x32_bf16 v[16:19], v[128:131], v[88:91], v[16:19]\n"
      "ds_read_b32 v4, v15\n"
      "ds_read_b128 v[20:23], v9 offset:4096\n"
      "v_mfma_f32_16x16x32_bf16 v[16:19], v[148:151], v[92:95], v[16:19]\n"
      "ds_add_u32 v2, v1 offset:4192\n"
      "v_mfma_f32_16x16x32_bf16 v[16:19], v[168:171], v[96:99], v[16:19]\n"
      "v_mfma_f32_16x16x32_bf16 v[16:19], v[188:191], v[100:103], v[16:19]\n"
      "s_waitcnt lgkmcnt(2)\n"
      "v_cmp_gt_u32 vcc, 20, v4\n"
      "v_mfma_f32_16x16x32_bf16 v[16:19], v[208:211], v[104:107], v[16:19]\n"
      "ds_write_b128 v8, v[28:31] offset:2048\n"
      "v_mfma_f32_16x16x32_bf16 v[16:19], v[228:231], v[108:111], v[16:19]\n"
      "s_cbranch_vccnz .Lp_71\n"
      ".Lp_72:\n"
      "v_mfma_f32_16x16x32_bf16 v[16:19], v[248:251], v[112:115], v[16:19]\n"
      "s_waitcnt lgkmcnt(2)\n"
      "v_mfma_f32_16x16x32_bf16 v[20:23], v[132:135], v[88:91], v[20:23]\n"
      "v_add_u32 v9, 98304, v9\n"
      "ds_read_b32 v4, v15\n"
      "ds_read_b128 v[24:27], v9 offset:0\n"
      "v_mfma_f32_16x16x32_bf16 v[20:23], v[152:155], v[92:95], v[20:23]\n"
      "ds_add_u32 v2, v1 offset:4192\n"
      "v_mfma_f32_16x16x32_bf16 v[20:23], v[172:175], v[96:99], v[20:23]\n"
      "v_mfma_f32_16x16x32_bf16 v[20:23], v[192:195], v[100:103], v[20:23]\n"
      "s_waitcnt lgkmcnt(2)\n"
      "v_cmp_gt_u32 vcc, 21, v4\n"
      "v_mfma_f32_16x16x32_bf16 v[20:23], v[212:215], v[104:107], v[20:23]\n"
      "ds_write_b128 v8, v[16:19] offset:3072\n"
      "v_mfma_f32_16x16x32_bf16 v[20:23], v[232:235], v[108:111], v[20:23]\n"
      "s_cbranch_vccnz .Lp_73\n"
      ".Lp_74:\n"
      "v_mfma_f32_16x16x32_bf16 v[20:23], v[252:255], v[112:115], v[20:23]\n"
      "s_waitcnt vmcnt(12)\n"
      "s_waitcnt lgkmcnt(2)\n"
      "v_mfma_f32_16x16x32_bf16 v[24:27], v[116:119], v[32:35], v[24:27]\n"
      "ds_read_b32 v4, v15\n"
      "ds_read_b128 v[28:31], v9 offset:1024\n"
      "v_mfma_f32_16x16x32_bf16 v[24:27], v[136:139], v[36:39], v[24:27]\n"
      "ds_add_u32 v2, v1 offset:4192\n"
      "v_mfma_f32_16x16x32_bf16 v[24:27], v[156:159], v[40:43], v[24:27]\n"
      "s_waitcnt vmcnt(7)\n"
      "v_mfma_f32_16x16x32_bf16 v[24:27], v[176:179], v[44:47], v[24:27]\n"
      "s_waitcnt lgkmcnt(2)\n"
      "v_cmp_gt_u32 vcc, 22, v4\n"
      "v_mfma_f32_16x16x32_bf16 v[24:27], v[196:199], v[48:51], v[24:27]\n"
      "ds_write_b128 v8, v[20:23] offset:4096\n"
      "v_mfma_f32_16x16x32_bf16 v[24:27], v[216:219], v[52:55], v[24:27]\n"
      "s_cbranch_vccnz .Lp_75\n"
      ".Lp_76:\n"
      "v_mfma_f32_16x16x32_bf16 v[24:27], v[236:239], v[56:59], v[24:27]\n"
      "s_waitcnt lgkmcnt(2)\n"
      "v_mfma_f32_16x16x32_bf16 v[28:31], v[120:123], v[32:35], v[28:31]\n"
      "ds_read_b32 v4, v15\n"
      "ds_read_b128 v[16:19], v9 offset:2048\n"
      "v_mfma_f32_16x16x32_bf16 v[28:31], v[140:143], v[36:39], v[28:31]\n"
      "ds_add_u32 v2, v1 offset:4192\n"
      "v_mfma_f32_16x16x32_bf16 v[28:31], v[160:163], v[40:43], v[28:31]\n"
      "v_mfma_f32_16x16x32_bf16 v[28:31], v[180:183], v[44:47], v[28:31]\n"
      "s_waitcnt lgkmcnt(2)\n"
      "v_cmp_gt_u32 vcc, 23, v4\n"
      "v_mfma_f32_16x16x32_bf16 v[28:31], v[200:203], v[48:51], v[28:31]\n"
      "v_add_u32 v8, 4294901760, v8\n"
      "ds_write_b128 v8, v[24:27] offset:0\n"
      "v_mfma_f32_16x16x32_bf16 v[28:31], v[220:223], v[52:55], v[28:31]\n"
      "s_cbranch_vccnz .Lp_77\n"
      ".Lp_78:\n"
      "v_mfma_f32_16x16x32_bf16 v[28:31], v[240:243], v[56:59], v[28:31]\n"
      "s_waitcnt lgkmcnt(2)\n"
      "v_mfma_f32_16x16x32_bf16 v[16:19], v[124:127], v[32:35], v[16:19]\n"
      "ds_read_b32 v4, v15\n"
      "ds_read_b128 v[20:23], v9 offset:3072\n"
      "v_mfma_f32_16x16x32_bf16 v[16:19], v[144:147], v[36:39], v[16:19]\n"
      "ds_add_u32 v2, v1 offset:4192\n"
      "v_mfma_f32_16x16x32_bf16 v[16:19], v[164:167], v[40:43], v[16:19]\n"
      "v_mfma_f32_16x16x32_bf16 v[16:19], v[184:187], v[44:47], v[16:19]\n"
      "s_waitcnt lgkmcnt(2)\n"
      "v_cmp_gt_u32 vcc, 24, v4\n"
      "v_mfma_f32_16x16x32_bf16 v[16:19], v[204:207], v[48:51], v[16:19]\n"
      "ds_write_b128 v8, v[28:31] offset:1024\n"
      "v_mfma_f32_16x16x32_bf16 v[16:19], v[224:227], v[52:55], v[16:19]\n"
      "s_cbranch_vccnz .Lp_79\n"
      ".Lp_80:\n"
      "v_mfma_f32_16x16x32_bf16 v[16:19], v[244:247], v[56:59], v[16:19]\n"
      "s_waitcnt lgkmcnt(2)\n"
      "v_mfma_f32_16x16x32_bf16 v[20:23], v[128:131], v[32:35], v[20:23]\n"
      "ds_read_b32 v4, v15\n"
      "ds_read_b128 v[24:27], v9 offset:4096\n"
      "v_mfma_f32_16x16x32_bf16 v[20:23], v[148:151], v[36:39], v[20:23]\n"
      "ds_add_u32 v2, v1 offset:4192\n"
      "v_mfma_f32_16x16x32_bf16 v[20:23], v[168:171], v[40:43], v[20:23]\n"
      "v_mfma_f32_16x16x32_bf16 v[20:23], v[188:191], v[44:47], v[20:23]\n"
      "s_waitcnt lgkmcnt(2)\n"
      "v_cmp_gt_u32 vcc, 25, v4\n"
      "v_mfma_f32_16x16x32_bf16 v[20:23], v[208:211], v[48:51], v[20:23]\n"
      "ds_write_b128 v8, v[16:19] offset:2048\n"
      "v_mfma_f32_16x16x32_bf16 v[20:23], v[228:231], v[52:55], v[20:23]\n"
      "s_cbranch_vccnz .Lp_81\n"
      ".Lp_82:\n"
      "v_mfma_f32_16x16x32_bf16 v[20:23], v[248:251], v[56:59], v[20:23]\n"
      "s_waitcnt lgkmcnt(2)\n"
      "v_mfma_f32_16x16x32_bf16 v[24:27], v[132:135], v[32:35], v[24:27]\n"
      "v_add_u32 v9, 4294901760, v9\n"
      "ds_read_b32 v4, v15\n"
      "ds_read_b128 v[28:31], v9 offset:0\n"
      "v_mfma_f32_16x16x32_bf16 v[24:27], v[152:155], v[36:39], v[24:27]\n"
      "ds_add_u32 v2, v1 offset:4192\n"
      "v_mfma_f32_16x16x32_bf16 v[24:27], v[172:175], v[40:43], v[24:27]\n"
      "v_mfma_f32_16x16x32_bf16 v[24:27], v[192:195], v[44:47], v[24:27]\n"
      "s_waitcnt lgkmcnt(2)\n"
      "v_cmp_gt_u32 vcc, 26, v4\n"
      "v_mfma_f32_16x16x32_bf16 v[24:27], v[212:215], v[48:51], v[24:27]\n"
      "ds_write_b128 v8, v[20:23] offset:3072\n"
      "v_mfma_f32_16x16x32_bf16 v[24:27], v[232:235], v[52:55], v[24:27]\n"
      "s_cbranch_vccnz .Lp_83\n"
      ".Lp_84:\n"
      "v_mfma_f32_16x16x32_bf16 v[24:27], v[252:255], v[56:59], v[24:27]\n"
      "s_waitcnt vmcnt(4)\n"
      "s_waitcnt lgkmcnt(2)\n"
      "v_mfma_f32_16x16x32_bf16 v[28:31], v[116:119], v[60:63], v[28:31]\n"
      "ds_read_b32 v4, v15\n"
      "ds_read_b128 v[16:19], v9 offset:1024\n"
      "v_mfma_f32_16x16x32_bf16 v[28:31], v[136:139], v[64:67], v[28:31]\n"
      "ds_add_u32 v2, v1 offset:4192\n"
      "v_mfma_f32_16x16x32_bf16 v[28:31], v[156:159], v[68:71], v[28:31]\n"
      "s_waitcnt vmcnt(0)\n"
      "v_mfma_f32_16x16x32_bf16 v[28:31], v[176:179], v[72:75], v[28:31]\n"
      "s_waitcnt lgkmcnt(2)\n"
      "v_cmp_gt_u32 vcc, 27, v4\n"
      "v_mfma_f32_16x16x32_bf16 v[28:31], v[196:199], v[76:79], v[28:31]\n"
      "ds_write_b128 v8, v[24:27] offset:4096\n"
      "v_mfma_f32_16x16x32_bf16 v[28:31], v[216:219], v[80:83], v[28:31]\n"
      "s_cbranch_vccnz .Lp_85\n"
      ".Lp_86:\n"
      "v_mfma_f32_16x16x32_bf16 v[28:31], v[236:239], v[84:87], v[28:31]\n"
      "s_waitcnt lgkmcnt(2)\n"
      "v_mfma_f32_16x16x32_bf16 v[16:19], v[120:123], v[60:63], v[16:19]\n"
      "ds_read_b32 v4, v15\n"
      "ds_read_b128 v[20:23], v9 offset:2048\n"
      "v_mfma_f32_16x16x32_bf16 v[16:19], v[140:143], v[64:67], v[16:19]\n"
      "ds_add_u32 v2, v1 offset:4192\n"
      "v_mfma_f32_16x16x32_bf16 v[16:19], v[160:163], v[68:71], v[16:19]\n"
      "v_mfma_f32_16x16x32_bf16 v[16:19], v[180:183], v[72:75], v[16:19]\n"
      "s_waitcnt lgkmcnt(2)\n"
      "v_cmp_gt_u32 vcc, 28, v4\n"
      "v_mfma_f32_16x16x32_bf16 v[16:19], v[200:203], v[76:79], v[16:19]\n"
      "ds_write_b128 v8, v[28:31] offset:20480\n"
      "v_mfma_f32_16x16x32_bf16 v[16:19], v[220:223], v[80:83], v[16:19]\n"
      "s_cbranch_vccnz .Lp_87\n"
      ".Lp_88:\n"
      "v_mfma_f32_16x16x32_bf16 v[16:19], v[240:243], v[84:87], v[16:19]\n"
      "s_waitcnt lgkmcnt(2)\n"
      "v_mfma_f32_16x16x32_bf16 v[20:23], v[124:127], v[60:63], v[20:23]\n"
      "ds_read_b32 v4, v15\n"
      "ds_read_b128 v[24:27], v9 offset:3072\n"
      "v_mfma_f32_16x16x32_bf16 v[20:23], v[144:147], v[64:67], v[20:23]\n"
      "ds_add_u32 v2, v1 offset:4192\n"
      "v_mfma_f32_16x16x32_bf16 v[20:23], v[164:167], v[68:71], v[20:23]\n"
      "v_mfma_f32_16x16x32_bf16 v[20:23], v[184:187], v[72:75], v[20:23]\n"
      "s_waitcnt lgkmcnt(2)\n"
      "v_cmp_gt_u32 vcc, 29, v4\n"
      "v_mfma_f32_16x16x32_bf16 v[20:23], v[204:207], v[76:79], v[20:23]\n"
      "ds_write_b128 v8, v[16:19] offset:21504\n"
      "v_mfma_f32_16x16x32_bf16 v[20:23], v[224:227], v[80:83], v[20:23]\n"
      "s_cbranch_vccnz .Lp_89\n"
      ".Lp_90:\n"
      "v_mfma_f32_16x16x32_bf16 v[20:23], v[244:247], v[84:87], v[20:23]\n"
      "s_waitcnt lgkmcnt(2)\n"
      "v_mfma_f32_16x16x32_bf16 v[24:27], v[128:131], v[60:63], v[24:27]\n"
      "ds_read_b32 v4, v15\n"
      "ds_read_b128 v[28:31], v9 offset:4096\n"
      "v_mfma_f32_16x16x32_bf16 v[24:27], v[148:151], v[64:67], v[24:27]\n"
      "ds_add_u32 v2, v1 offset:4192\n"
      "v_mfma_f32_16x16x32_bf16 v[24:27], v[168:171], v[68:71], v[24:27]\n"
      "v_mfma_f32_16x16x32_bf16 v[24:27], v[188:191], v[72:75], v[24:27]\n"
      "s_waitcnt lgkmcnt(2)\n"
      "v_cmp_gt_u32 vcc, 30, v4\n"
      "v_mfma_f32_16x16x32_bf16 v[24:27], v[208:211], v[76:79], v[24:27]\n"
      "ds_write_b128 v8, v[20:23] offset:22528\n"
      "v_mfma_f32_16x16x32_bf16 v[24:27], v[228:231], v[80:83], v[24:27]\n"
      "s_cbranch_vccnz .Lp_91\n"
      ".Lp_92:\n"
      "v_mfma_f32_16x16x32_bf16 v[24:27], v[248:251], v[84:87], v[24:27]\n"
      "s_waitcnt lgkmcnt(2)\n"
      "v_mfma_f32_16x16x32_bf16 v[28:31], v[132:135], v[60:63], v[28:31]\n"
      "v_mfma_f32_16x16x32_bf16 v[28:31], v[152:155], v[64:67], v[28:31]\n"
      "ds_add_u32 v2, v1 offset:4192\n"
      "v_mfma_f32_16x16x32_bf16 v[28:31], v[172:175], v[68:71], v[28:31]\n"
      "v_mfma_f32_16x16x32_bf16 v[28:31], v[192:195], v[72:75], v[28:31]\n"
      "v_mfma_f32_16x16x32_bf16 v[28:31], v[212:215], v[76:79], v[28:31]\n"
      "s_nop 0\n"
      "ds_write_b128 v8, v[24:27] offset:23552\n"
      "v_mfma_f32_16x16x32_bf16 v[28:31], v[232:235], v[80:83], v[28:31]\n"
      "v_mfma_f32_16x16x32_bf16 v[28:31], v[252:255], v[84:87], v[28:31]\n"
      "ds_add_u32 v2, v1 offset:4192\n"
      "s_nop 6\n"
      "ds_write_b128 v8, v[28:31] offset:24576\n"
      "ds_add_u32 v2, v1 offset:4192\n"
      "s_endpgm\n"
      ".LWARM:\n"
      ".Lp_27:\n"
      "s_cbranch_vccz .Lp_29\n"
      ".Lp_93:\n"
      "ds_read_b32 v4, v15\n"
      "ds_read_b128 v[20:23], v9 offset:0\n"
      "s_sub_u32 s73, s73, 1\n"
      "s_cselect_b32 s73, 0, s73\n"
      "s_waitcnt lgkmcnt(0)\n"
      "v_cmp_gt_u32 vcc, 1, v4\n"
      "s_cselect_b64 vcc, 0, vcc\n"
      "s_cbranch_vccnz .Lp_93\n"
      "s_branch .Lp_28\n"
      ".Lp_29:\n"
      "s_cbranch_vccz .Lp_31\n"
      ".Lp_94:\n"
      "ds_read_b32 v4, v15\n"
      "ds_read_b128 v[24:27], v9 offset:1024\n"
      "s_sub_u32 s73, s73, 1\n"
      "s_cselect_b32 s73, 0, s73\n"
      "s_waitcnt lgkmcnt(0)\n"
      "v_cmp_gt_u32 vcc, 2, v4\n"
      "s_cselect_b64 vcc, 0, vcc\n"
      "s_cbranch_vccnz .Lp_94\n"
      "s_branch .Lp_30\n"
      ".Lp_31:\n"
      "s_cbranch_vccz .Lp_33\n"
      ".Lp_95:\n"
      "ds_read_b32 v4, v15\n"
      "ds_read_b128 v[28:31], v9 offset:2048\n"
      "s_sub_u32 s73, s73, 1\n"
      "s_cselect_b32 s73, 0, s73\n"
      "s_waitcnt lgkmcnt(0)\n"
      "v_cmp_gt_u32 vcc, 3, v4\n"
      "s_cselect_b64 vcc, 0, vcc\n"
      "s_cbranch_vccnz .Lp_95\n"
      "s_branch .Lp_32\n"
      ".Lp_33:\n"
      "s_cbranch_vccz .Lp_35\n"
      ".Lp_96:\n"
      "ds_read_b32 v4, v15\n"
      "ds_read_b128 v[16:19], v9 offset:3072\n"
      "s_sub_u32 s73, s73, 1\n"
      "s_cselect_b32 s73, 0, s73\n"
      "s_waitcnt lgkmcnt(0)\n"
      "v_cmp_gt_u32 vcc, 4, v4\n"
      "s_cselect_b64 vcc, 0, vcc\n"
      "s_cbranch_vccnz .Lp_96\n"
      "s_branch .Lp_34\n"
      ".Lp_35:\n"
      "s_cbranch_vccz .Lp_37\n"
      ".Lp_97:\n"
      "ds_read_b32 v4, v15\n"
      "ds_read_b128 v[20:23], v9 offset:4096\n"
      "s_sub_u32 s73, s73, 1\n"
      "s_cselect_b32 s73, 0, s73\n"
      "s_waitcnt lgkmcnt(0)\n"
      "v_cmp_gt_u32 vcc, 5, v4\n"
      "s_cselect_b64 vcc, 0, vcc\n"
      "s_cbranch_vccnz .Lp_97\n"
      "s_branch .Lp_36\n"
      ".Lp_37:\n"
      "s_cbranch_vccz .Lp_39\n"
      ".Lp_98:\n"
      "ds_read_b32 v4, v15\n"
      "ds_read_b128 v[24:27], v9 offset:20480\n"
      "s_sub_u32 s73, s73, 1\n"
      "s_cselect_b32 s73, 0, s73\n"
      "s_waitcnt lgkmcnt(0)\n"
      "v_cmp_gt_u32 vcc, 6, v4\n"
      "s_cselect_b64 vcc, 0, vcc\n"
      "s_cbranch_vccnz .Lp_98\n"
      "s_branch .Lp_38\n"
      ".Lp_39:\n"
      "s_cbranch_vccz .Lp_44\n"
      ".Lp_99:\n"
      "ds_read_b32 v4, v15\n"
      "ds_read_b128 v[28:31], v9 offset:21504\n"
      "s_sub_u32 s73, s73, 1\n"
      "s_cselect_b32 s73, 0, s73\n"
      "s_waitcnt lgkmcnt(0)\n"
      "v_cmp_gt_u32 vcc, 7, v4\n"
      "s_cselect_b64 vcc, 0, vcc\n"
      "s_cbranch_vccnz .Lp_99\n"
      "s_branch .Lp_40\n"
      ".Lp_44:\n"
      "s_cbranch_vccz .Lp_46\n"
      ".Lp_100:\n"
      "ds_read_b32 v4, v15\n"
      "ds_read_b128 v[16:19], v9 offset:22528\n"
      "s_sub_u32 s73, s73, 1\n"
      "s_cselect_b32 s73, 0, s73\n"
      "s_waitcnt lgkmcnt(0)\n"
      "v_cmp_gt_u32 vcc, 8, v4\n"
      "s_cselect_b64 vcc, 0, vcc\n"
      "s_cbranch_vccnz .Lp_100\n"
      "s_branch .Lp_45\n"
      ".Lp_46:\n"
      "s_cbranch_vccz .Lp_48\n"
      ".Lp_101:\n"
      "ds_read_b32 v4, v15\n"
      "ds_read_b128 v[20:23], v9 offset:23552\n"
      "s_sub_u32 s73, s73, 1\n"
      "s_cselect_b32 s73, 0, s73\n"
      "s_waitcnt lgkmcnt(0)\n"
      "v_cmp_gt_u32 vcc, 9, v4\n"
      "s_cselect_b64 vcc, 0, vcc\n"
      "s_cbranch_vccnz .Lp_101\n"
      "s_branch .Lp_47\n"
      ".Lp_48:\n"
      "s_cbranch_vccz .Lp_50\n"
      ".Lp_102:\n"
      "ds_read_b32 v4, v15\n"
      "ds_read_b128 v[24:27], v9 offset:24576\n"
      "s_sub_u32 s73, s73, 1\n"
      "s_cselect_b32 s73, 0, s73\n"
      "s_waitcnt lgkmcnt(0)\n"
      "v_cmp_gt_u32 vcc, 10, v4\n"
      "s_cselect_b64 vcc, 0, vcc\n"
      "s_cbranch_vccnz .Lp_102\n"
      "s_branch .Lp_49\n"
      ".Lp_50:\n"
      "s_cbranch_vccz .Lp_52\n"
      ".Lp_103:\n"
      "ds_read_b32 v4, v15\n"
      "ds_read_b128 v[28:31], v9 offset:40960\n"
      "s_sub_u32 s73, s73, 1\n"
      "s_cselect_b32 s73, 0, s73\n"
      "s_waitcnt lgkmcnt(0)\n"
      "v_cmp_gt_u32 vcc, 11, v4\n"
      "s_cselect_b64 vcc, 0, vcc\n"
      "s_cbranch_vccnz .Lp_103\n"
      "s_branch .Lp_51\n"
      ".Lp_52:\n"
      "s_cbranch_vccz .Lp_54\n"
      ".Lp_104:\n"
      "ds_read_b32 v4, v15\n"
      "ds_read_b128 v[16:19], v9 offset:41984\n"
      "s_sub_u32 s73, s73, 1\n"
      "s_cselect_b32 s73, 0, s73\n"
      "s_waitcnt lgkmcnt(0)\n"
      "v_cmp_gt_u32 vcc, 12, v4\n"
      "s_cselect_b64 vcc, 0, vcc\n"
      "s_cbranch_vccnz .Lp_104\n"
      "s_branch .Lp_53\n"
      ".Lp_54:\n"
      "s_cbranch_vccz .Lp_56\n"
      ".Lp_105:\n"
      "ds_read_b32 v4, v15\n"
      "ds_read_b128 v[20:23], v9 offset:43008\n"
      "s_sub_u32 s73, s73, 1\n"
      "s_cselect_b32 s73, 0, s73\n"
      "s_waitcnt lgkmcnt(0)\n"
      "v_cmp_gt_u32 vcc, 13, v4\n"
      "s_cselect_b64 vcc, 0, vcc\n"
      "s_cbranch_vccnz .Lp_105\n"
      "s_branch .Lp_55\n"
      ".Lp_56:\n"
      "s_cbranch_vccz .Lp_58\n"
      ".Lp_106:\n"
      "ds_read_b32 v4, v15\n"
      "ds_read_b128 v[24:27], v9 offset:44032\n"
      "s_sub_u32 s73, s73, 1\n"
      "s_cselect_b32 s73, 0, s73\n"
      "s_waitcnt lgkmcnt(0)\n"
      "v_cmp_gt_u32 vcc, 14, v4\n"
      "s_cselect_b64 vcc, 0, vcc\n"
      "s_cbranch_vccnz .Lp_106\n"
      "s_branch .Lp_57\n"
      ".Lp_58:\n"
      "s_cbranch_vccz .Lp_63\n"
      ".Lp_107:\n"
      "ds_read_b32 v4, v15\n"
      "ds_read_b128 v[28:31], v9 offset:45056\n"
      "s_sub_u32 s73, s73, 1\n"
      "s_cselect_b32 s73, 0, s73\n"
      "s_waitcnt lgkmcnt(0)\n"
      "v_cmp_gt_u32 vcc, 15, v4\n"
      "s_cselect_b64 vcc, 0, vcc\n"
      "s_cbranch_vccnz .Lp_107\n"
      "s_branch .Lp_59\n"
      ".Lp_63:\n"
      "s_cbranch_vccz .Lp_65\n"
      ".Lp_108:\n"
      "ds_read_b32 v4, v15\n"
      "ds_read_b128 v[20:23], v9 offset:0\n"
      "s_sub_u32 s73, s73, 1\n"
      "s_cselect_b32 s73, 0, s73\n"
      "s_waitcnt lgkmcnt(0)\n"
      "v_cmp_gt_u32 vcc, 16, v4\n"
      "s_cselect_b64 vcc, 0, vcc\n"
      "s_cbranch_vccnz .Lp_108\n"
      "s_branch .Lp_64\n"
      ".Lp_65:\n"
      "s_cbranch_vccz .Lp_67\n"
      ".Lp_109:\n"
      "ds_read_b32 v4, v15\n"
      "ds_read_b128 v[24:27], v9 offset:1024\n"
      "s_sub_u32 s73, s73, 1\n"
      "s_cselect_b32 s73, 0, s73\n"
      "s_waitcnt lgkmcnt(0)\n"
      "v_cmp_gt_u32 vcc, 17, v4\n"
      "s_cselect_b64 vcc, 0, vcc\n"
      "s_cbranch_vccnz .Lp_109\n"
      "s_branch .Lp_66\n"
      ".Lp_67:\n"
      "s_cbranch_vccz .Lp_69\n"
      ".Lp_110:\n"
      "ds_read_b32 v4, v15\n"
      "ds_read_b128 v[28:31], v9 offset:2048\n"
      "s_sub_u32 s73, s73, 1\n"
      "s_cselect_b32 s73, 0, s73\n"
      "s_waitcnt lgkmcnt(0)\n"
      "v_cmp_gt_u32 vcc, 18, v4\n"
      "s_cselect_b64 vcc, 0, vcc\n"
      "s_cbranch_vccnz .Lp_110\n"
      "s_branch .Lp_68\n"
      ".Lp_69:\n"
      "s_cbranch_vccz .Lp_71\n"
      ".Lp_111:\n"
      "ds_read_b32 v4, v15\n"
      "ds_read_b128 v[16:19], v9 offset:3072\n"
      "s_sub_u32 s73, s73, 1\n"
      "s_cselect_b32 s73, 0, s73\n"
      "s_waitcnt lgkmcnt(0)\n"
      "v_cmp_gt_u32 vcc, 19, v4\n"
      "s_cselect_b64 vcc, 0, vcc\n"
      "s_cbranch_vccnz .Lp_111\n"
      "s_branch .Lp_70\n"
      ".Lp_71:\n"
      "s_cbranch_vccz .Lp_73\n"
      ".Lp_112:\n"
      "ds_read_b32 v4, v15\n"
      "ds_read_b128 v[20:23], v9 offset:4096\n"
      "s_sub_u32 s73, s73, 1\n"
      "s_cselect_b32 s73, 0, s73\n"
      "s_waitcnt lgkmcnt(0)\n"
      "v_cmp_gt_u32 vcc, 20, v4\n"
      "s_cselect_b64 vcc, 0, vcc\n"
      "s_cbranch_vccnz .Lp_112\n"
      "s_branch .Lp_72\n"
      ".Lp_73:\n"
      "s_cbranch_vccz .Lp_75\n"
      ".Lp_113:\n"
      "ds_read_b32 v4, v15\n"
      "ds_read_b128 v[24:27], v9 offset:0\n"
      "s_sub_u32 s73, s73, 1\n"
      "s_cselect_b32 s73, 0, s73\n"
      "s_waitcnt lgkmcnt(0)\n"
      "v_cmp_gt_u32 vcc, 21, v4\n"
      "s_cselect_b64 vcc, 0, vcc\n"
      "s_cbranch_vccnz .Lp_113\n"
      "s_branch .Lp_74\n"
      ".Lp_75:\n"
      "s_cbranch_vccz .Lp_77\n"
      ".Lp_114:\n"
      "ds_read_b32 v4, v15\n"
      "ds_read_b128 v[28:31], v9 offset:1024\n"
      "s_sub_u32 s73, s73, 1\n"
      "s_cselect_b32 s73, 0, s73\n"
      "s_waitcnt lgkmcnt(0)\n"
      "v_cmp_gt_u32 vcc, 22, v4\n"
      "s_cselect_b64 vcc, 0, vcc\n"
      "s_cbranch_vccnz .Lp_114\n"
      "s_branch .Lp_76\n"
      ".Lp_77:\n"
      "s_cbranch_vccz .Lp_79\n"
      ".Lp_115:\n"
      "ds_read_b32 v4, v15\n"
      "ds_read_b128 v[16:19], v9 offset:2048\n"
      "s_sub_u32 s73, s73, 1\n"
      "s_cselect_b32 s73, 0, s73\n"
      "s_waitcnt lgkmcnt(0)\n"
      "v_cmp_gt_u32 vcc, 23, v4\n"
      "s_cselect_b64 vcc, 0, vcc\n"
      "s_cbranch_vccnz .Lp_115\n"
      "s_branch .Lp_78\n"
      ".Lp_79:\n"
      "s_cbranch_vccz .Lp_81\n"
      ".Lp_116:\n"
      "ds_read_b32 v4, v15\n"
      "ds_read_b128 v[20:23], v9 offset:3072\n"
      "s_sub_u32 s73, s73, 1\n"
      "s_cselect_b32 s73, 0, s73\n"
      "s_waitcnt lgkmcnt(0)\n"
      "v_cmp_gt_u32 vcc, 24, v4\n"
      "s_cselect_b64 vcc, 0, vcc\n"
      "s_cbranch_vccnz .Lp_116\n"
      "s_branch .Lp_80\n"
      ".Lp_81:\n"
      "s_cbranch_vccz .Lp_83\n"
      ".Lp_117:\n"
      "ds_read_b32 v4, v15\n"
      "ds_read_b128 v[24:27], v9 offset:4096\n"
      "s_sub_u32 s73, s73, 1\n"
      "s_cselect_b32 s73, 0, s73\n"
      "s_waitcnt lgkmcnt(0)\n"
      "v_cmp_gt_u32 vcc, 25, v4\n"
      "s_cselect_b64 vcc, 0, vcc\n"
      "s_cbranch_vccnz .Lp_117\n"
      "s_branch .Lp_82\n"
      ".Lp_83:\n"
      "s_cbranch_vccz .Lp_85\n"
      ".Lp_118:\n"
      "ds_read_b32 v4, v15\n"
      "ds_read_b128 v[28:31], v9 offset:0\n"
      "s_sub_u32 s73, s73, 1\n"
      "s_cselect_b32 s73, 0, s73\n"
      "s_waitcnt lgkmcnt(0)\n"
      "v_cmp_gt_u32 vcc, 26, v4\n"
      "s_cselect_b64 vcc, 0, vcc\n"
      "s_cbranch_vccnz .Lp_118\n"
      "s_branch .Lp_84\n"
      ".Lp_85:\n"
      "s_cbranch_vccz .Lp_87\n"
      ".Lp_119:\n"
      "ds_read_b32 v4, v15\n"
      "ds_read_b128 v[16:19], v9 offset:1024\n"
      "s_sub_u32 s73, s73, 1\n"
      "s_cselect_b32 s73, 0, s73\n"
      "s_waitcnt lgkmcnt(0)\n"
      "v_cmp_gt_u32 vcc, 27, v4\n"
      "s_cselect_b64 vcc, 0, vcc\n"
      "s_cbranch_vccnz .Lp_119\n"
      "s_branch .Lp_86\n"
      ".Lp_87:\n"
      "s_cbranch_vccz .Lp_89\n"
      ".Lp_120:\n"
      "ds_read_b32 v4, v15\n"
      "ds_read_b128 v[20:23], v9 offset:2048\n"
      "s_sub_u32 s73, s73, 1\n"
      "s_cselect_b32 s73, 0, s73\n"
      "s_waitcnt lgkmcnt(0)\n"
      "v_cmp_gt_u32 vcc, 28, v4\n"
      "s_cselect_b64 vcc, 0, vcc\n"
      "s_cbranch_vccnz .Lp_120\n"
      "s_branch .Lp_88\n"
      ".Lp_89:\n"
      "s_cbranch_vccz .Lp_91\n"
      ".Lp_121:\n"
      "ds_read_b32 v4, v15\n"
      "ds_read_b128 v[24:27], v9 offset:3072\n"
      "s_sub_u32 s73, s73, 1\n"
      "s_cselect_b32 s73, 0, s73\n"
      "s_waitcnt lgkmcnt(0)\n"
      "v_cmp_gt_u32 vcc, 29, v4\n"
      "s_cselect_b64 vcc, 0, vcc\n"
      "s_cbranch_vccnz .Lp_121\n"
      "s_branch .Lp_90\n"
      ".Lp_91:\n"
      "s_cbranch_vccz .LWARME\n"
      ".Lp_122:\n"
      "ds_read_b32 v4, v15\n"
      "ds_read_b128 v[28:31], v9 offset:4096\n"
      "s_sub_u32 s73, s73, 1\n"
      "s_cselect_b32 s73, 0, s73\n"
      "s_waitcnt lgkmcnt(0)\n"
      "v_cmp_gt_u32 vcc, 30, v4\n"
      "s_cselect_b64 vcc, 0, vcc\n"
      "s_cbranch_vccnz .Lp_122\n"
      "s_branch .Lp_92\n"
      ".LM:\n"
      "s_cmp_lt_u32 s61, 28\n"
      "s_cselect_b32 s62, s57, 14\n"
      "s_cselect_b32 s63, 0, s54\n"
      "s_add_u32 s62, s62, s63\n"
      "s_lshl4_add_u32 s62, s56, s62\n"
      "s_lshl2_add_u32 s62, s62, s53\n"
      "s_mul_i32 s63, s62, 2560\n"
      "s_lshl_b32 s69, s62, 4\n"
      "v_lshrrev_b32 v5, 5, v1\n"
      "v_and_b32 v6, 31, v1\n"
      "v_mul_u32_u24 v7, 1280, v5\n"
      "v_lshl_add_u32 v18, v6, 4, v7\n"
      "v_lshl_add_u32 v19, v6, 3, v7\n"
      "v_lshlrev_b32 v11, 2, v1\n"
      "s_mul_i32 s64, s55, 160\n"
      "s_lshl2_add_u32 s65, s57, s53\n"
      "s_lshl_b32 s65, s65, 6\n"
      "v_lshlrev_b32 v12, 1, v6\n"
      "s_and_b32 s66, s2, 7\n"
      "s_mul_i32 s66, s66, 28\n"
      "s_add_u32 s66, s66, s61\n"
      "s_mul_i32 s67, s66, 205\n"
      "s_lshr_b32 s67, s67, 11\n"
      "s_mul_i32 s68, s67, 10\n"
      "s_sub_u32 s68, s66, s68\n"
      "s_lshl2_add_u32 s66, s68, s53\n"
      "s_lshl_b32 s70, s66, 7\n"
      "v_lshrrev_b32 v13, 3, v1\n"
      "v_lshlrev_b32 v14, 4, v13\n"
      "s_mov_b32 m0, 162816\n"
      "s_waitcnt lgkmcnt(0)\n"
      "buffer_load_dwordx4 v[96:99], v18, s[4:7], s63 offen\n"
      "buffer_load_dwordx4 v[100:103], v18, s[4:7], s63 offen offset:512\n"
      "buffer_load_dwordx2 v[104:105], v19, s[4:7], s63 offen offset:1024\n"
      "buffer_load_dwordx2 v[42:43], off, s[20:23], 0 sc1\n"
      "buffer_load_dword v11, s[44:47], s64 offen lds\n"
      "buffer_load_dwordx4 v[44:47], v14, s[24:27], s70 offen\n"
      "buffer_load_ushort v9, v12, s[28:31], s65 offen\n"
      "s_barrier\n"
      "s_setprio 3\n"
      "s_lshl_b32 s62, s53, 5\n"
      "s_add_u32 s62, s62, 140160\n"
      "v_and_b32 v68, 7, v1\n"
      "v_lshrrev_b32 v69, 3, v1\n"
      "v_mul_u32_u24 v192, 1040, v68\n"
      "v_lshl_add_u32 v192, v69, 2, v192\n"
      "v_add_u32 v192, s62, v192\n"
      "v_add_u32 v193, 8720, v192\n"
      "v_mul_u32_u24 v8, 8448, v69\n"
      "v_lshl_add_u32 v8, v68, 4, v8\n"
      "s_mul_i32 s84, s66, 67584\n"
      "s_lshl_b32 s63, s67, 7\n"
      "s_add_u32 s84, s84, s63\n"
      "s_add_u32 s84, s84, 32768\n"
      "s_add_u32 s85, s84, 4224\n"
      "v_lshrrev_b32 v14, 5, v1\n"
      "v_bfe_u32 v15, v1, 2, 2\n"
      "v_lshl_add_u32 v16, v14, 3, v15\n"
      "v_lshlrev_b32 v16, 8, v16\n"
      "v_bfe_u32 v17, v1, 4, 1\n"
      "v_mul_u32_u24 v29, 57344, v17\n"
      "s_lshl_b32 s64, s53, 1\n"
      "v_or_b32 v17, s64, v17\n"
      "v_lshlrev_b32 v15, 1, v15\n"
      "v_xor_b32 v17, v17, v15\n"
      "v_lshl_add_u32 v16, v17, 5, v16\n"
      "v_and_b32 v17, 3, v1\n"
      "v_lshl_add_u32 v24, v17, 3, v16\n"
      "v_and_b32 v15, 0x2f, v1\n"
      "v_lshl_add_u32 v29, v15, 4, v29\n"
      "v_and_b32 v15, 31, v1\n"
      "v_bfe_u32 v16, v1, 1, 3\n"
      "v_xor_b32 v16, v16, v14\n"
      "v_lshlrev_b32 v16, 4, v16\n"
      "v_lshl_add_u32 v4, v15, 7, v16\n"
      "v_xor_b32 v5, 32, v4\n"
      "v_xor_b32 v6, 64, v4\n"
      "v_xor_b32 v7, 96, v4\n"
      "v_lshlrev_b32 v30, 5, v15\n"
      "v_add_u32 v30, 157696, v30\n"
      "v_lshrrev_b32 v16, 4, v1\n"
      "v_xor_b32 v16, v16, v1\n"
      "v_and_b32 v16, 1, v16\n"
      "v_cmp_eq_u32 vcc, 0, v16\n"
      "v_mov_b32 v17, 0x3f803f80\n"
      "v_mov_b64 v[10:11], 0\n"
      "v_cndmask_b32 v16, 0, v17, vcc\n"
      "v_mov_b32 v17, v16\n"
      "v_mov_b32 v18, v16\n"
      "v_mov_b32 v19, v16\n"
      "v_mov_b64 v[14:15], 0\n"
      "s_lshl2_add_u32 s64, s54, s53\n"
      "s_mul_i32 s64, s64, 7\n"
      "s_add_u32 s64, s64, s55\n"
      "s_lshl_b32 s65, s64, 10\n"
      "s_mul_i32 s78, s56, 458752\n"
      "s_add_u32 s78, s78, s65\n"
      "s_add_u32 s78, s78, 5111808\n"
      "s_lshl2_add_u32 s65, s57, s53\n"
      "s_cmp_ge_u32 s65, 54\n"
      "s_cselect_b32 s78, 1073741824, s78\n"
      "s_lshl_b32 s64, s56, 6\n"
      "s_add_u32 s64, s64, s65\n"
      "s_lshl_b32 s64, s64, 8\n"
      "s_add_u32 s79, s64, 15597568\n"
      "s_xor_b32 s74, s99, 0\n"
      "s_mul_i32 s74, s74, 114688\n"
      "s_add_u32 s74, s74, s78\n"
      "s_xor_b32 s75, s99, 1\n"
      "s_mul_i32 s75, s75, 114688\n"
      "s_add_u32 s75, s75, s78\n"
      "s_xor_b32 s76, s99, 2\n"
      "s_mul_i32 s76, s76, 114688\n"
      "s_add_u32 s76, s76, s78\n"
      "s_xor_b32 s77, s99, 3\n"
      "s_mul_i32 s77, s77, 114688\n"
      "s_add_u32 s77, s77, s78\n"
      "s_mul_i32 s63, s56, 3\n"
      "s_add_u32 s63, s63, s57\n"
      "s_mul_i32 s64, s63, 205\n"
      "s_lshr_b32 s64, s64, 11\n"
      "s_mul_i32 s64, s64, 10\n"
      "s_sub_u32 s63, s63, s64\n"
      "s_sub_u32 s64, 3, s53\n"
      "s_lshr_b32 s65, s64, 1\n"
      "s_xor_b32 s65, s65, s99\n"
      "s_and_b32 s65, s65, 1\n"
      "s_mul_i32 s65, s65, 49152\n"
      "s_and_b32 s64, s64, 1\n"
      "s_lshl_b32 s64, s64, 11\n"
      "s_add_u32 s65, s65, s64\n"
      "s_mul_i32 s64, s55, 5\n"
      "s_lshr_b32 s71, s64, 2\n"
      "s_sub_u32 s71, s71, s63\n"
      "s_cselect_b32 s72, 10, 0\n"
      "s_add_u32 s71, s71, s72\n"
      "s_lshl_b32 s71, s71, 12\n"
      "s_add_u32 s71, s71, s65\n"
      "s_and_b32 s72, s64, 3\n"
      "s_lshl_b32 s72, s72, 5\n"
      "s_or_b32 s86, s71, s72\n"
      "s_add_u32 s64, s64, 1\n"
      "s_lshr_b32 s71, s64, 2\n"
      "s_sub_u32 s71, s71, s63\n"
      "s_cselect_b32 s72, 10, 0\n"
      "s_add_u32 s71, s71, s72\n"
      "s_lshl_b32 s71, s71, 12\n"
      "s_add_u32 s71, s71, s65\n"
      "s_and_b32 s72, s64, 3\n"
      "s_lshl_b32 s72, s72, 5\n"
      "s_or_b32 s87, s71, s72\n"
      "s_add_u32 s64, s64, 1\n"
      "s_lshr_b32 s71, s64, 2\n"
      "s_sub_u32 s71, s71, s63\n"
      "s_cselect_b32 s72, 10, 0\n"
      "s_add_u32 s71, s71, s72\n"
      "s_lshl_b32 s71, s71, 12\n"
      "s_add_u32 s71, s71, s65\n"
      "s_and_b32 s72, s64, 3\n"
      "s_lshl_b32 s72, s72, 5\n"
      "s_or_b32 s88, s71, s72\n"
      "s_add_u32 s64, s64, 1\n"
      "s_lshr_b32 s71, s64, 2\n"
      "s_sub_u32 s71, s71, s63\n"
      "s_cselect_b32 s72, 10, 0\n"
      "s_add_u32 s71, s71, s72\n"
      "s_lshl_b32 s71, s71, 12\n"
      "s_add_u32 s71, s71, s65\n"
      "s_and_b32 s72, s64, 3\n"
      "s_lshl_b32 s72, s72, 5\n"
      "s_or_b32 s89, s71, s72\n"
      "s_add_u32 s64, s64, 1\n"
      "s_lshr_b32 s71, s64, 2\n"
      "s_sub_u32 s71, s71, s63\n"
      "s_cselect_b32 s72, 10, 0\n"
      "s_add_u32 s71, s71, s72\n"
      "s_lshl_b32 s71, s71, 12\n"
      "s_add_u32 s71, s71, s65\n"
      "s_and_b32 s72, s64, 3\n"
      "s_lshl_b32 s72, s72, 5\n"
      "s_or_b32 s90, s71, s72\n"
      "s_add_u32 s64, s64, 1\n"
      "v_and_b32 v50, 15, v1\n"
      "v_bfe_u32 v51, v1, 1, 3\n"
      "v_lshrrev_b32 v52, 5, v1\n"
      "v_xor_b32 v51, v51, v52\n"
      "v_lshlrev_b32 v51, 4, v51\n"
      "v_lshl_add_u32 v51, v50, 7, v51\n"
      "v_bfe_u32 v52, v1, 4, 1\n"
      "v_lshl_add_u32 v28, v52, 3, v51\n"
      "s_mov_b32 s80, s92\n"
      "s_mov_b32 s81, s92\n"
      "s_mov_b32 s82, -1\n"
      "s_mov_b32 s83, 0\n"
      "s_waitcnt vmcnt(3)\n"
      "ds_swizzle_b32 v106, v96 offset:0\n"
      "v_lshlrev_b32 v116, 16, v96\n"
      "v_and_b32 v117, s92, v96\n"
      "v_lshlrev_b32 v118, 16, v97\n"
      "v_and_b32 v119, s92, v97\n"
      "v_lshlrev_b32 v120, 16, v98\n"
      "v_and_b32 v121, s92, v98\n"
      "v_lshlrev_b32 v122, 16, v99\n"
      "v_and_b32 v123, s92, v99\n"
      "v_lshlrev_b32 v124, 16, v100\n"
      "v_and_b32 v125, s92, v100\n"
      "v_lshlrev_b32 v126, 16, v101\n"
      "v_and_b32 v127, s92, v101\n"
      "v_lshlrev_b32 v128, 16, v102\n"
      "v_and_b32 v129, s92, v102\n"
      "v_lshlrev_b32 v130, 16, v103\n"
      "v_and_b32 v131, s92, v103\n"
      "v_lshlrev_b32 v132, 16, v104\n"
      "v_and_b32 v133, s92, v104\n"
      "v_lshlrev_b32 v134, 16, v105\n"
      "v_and_b32 v135, s92, v105\n"
      "v_readfirstlane_b32 s58, v42\n"
      "v_readfirstlane_b32 s59, v43\n"
      "s_waitcnt lgkmcnt(0)\n"
      "v_lshlrev_b32 v106, 16, v106\n"
      "v_mov_b32 v107, v106\n"
      "s_add_u32 s58, s58, 1\n"
      "s_addc_u32 s59, s59, 0\n"
      "v_pk_add_f32 v[116:117], v[116:117], v[106:107] neg_lo:[0,1] neg_hi:[0,1]\n"
      "v_pk_add_f32 v[118:119], v[118:119], v[106:107] neg_lo:[0,1] neg_hi:[0,1]\n"
      "v_pk_add_f32 v[120:121], v[120:121], v[106:107] neg_lo:[0,1] neg_hi:[0,1]\n"
      "v_pk_add_f32 v[122:123], v[122:123], v[106:107] neg_lo:[0,1] neg_hi:[0,1]\n"
      "v_pk_add_f32 v[124:125], v[124:125], v[106:107] neg_lo:[0,1] neg_hi:[0,1]\n"
      "v_pk_add_f32 v[126:127], v[126:127], v[106:107] neg_lo:[0,1] neg_hi:[0,1]\n"
      "v_pk_add_f32 v[128:129], v[128:129], v[106:107] neg_lo:[0,1] neg_hi:[0,1]\n"
      "v_pk_add_f32 v[130:131], v[130:131], v[106:107] neg_lo:[0,1] neg_hi:[0,1]\n"
      "v_pk_add_f32 v[132:133], v[132:133], v[106:107] neg_lo:[0,1] neg_hi:[0,1]\n"
      "v_pk_add_f32 v[134:135], v[134:135], v[106:107] neg_lo:[0,1] neg_hi:[0,1]\n"
      "v_pk_mul_f32 v[108:109], v[116:117], v[116:117]\n"
      "v_pk_mul_f32 v[110:111], v[118:119], v[118:119]\n"
      "v_pk_fma_f32 v[108:109], v[120:121], v[120:121], v[108:109]\n"
      "v_pk_add_f32 v[116:117], v[116:117], v[120:121]\n"
      "v_pk_fma_f32 v[110:111], v[122:123], v[122:123], v[110:111]\n"
      "v_pk_add_f32 v[118:119], v[118:119], v[122:123]\n"
      "v_pk_fma_f32 v[108:109], v[124:125], v[124:125], v[108:109]\n"
      "v_pk_add_f32 v[116:117], v[116:117], v[124:125]\n"
      "v_pk_fma_f32 v[110:111], v[126:127], v[126:127], v[110:111]\n"
      "v_pk_add_f32 v[118:119], v[118:119], v[126:127]\n"
      "v_pk_fma_f32 v[108:109], v[128:129], v[128:129], v[108:109]\n"
      "v_pk_add_f32 v[116:117], v[116:117], v[128:129]\n"
      "v_pk_fma_f32 v[110:111], v[130:131], v[130:131], v[110:111]\n"
      "v_pk_add_f32 v[118:119], v[118:119], v[130:131]\n"
      "v_pk_fma_f32 v[108:109], v[132:133], v[132:133], v[108:109]\n"
      "v_pk_add_f32 v[116:117], v[116:117], v[132:133]\n"
      "v_pk_fma_f32 v[110:111], v[134:135], v[134:135], v[110:111]\n"
      "v_pk_add_f32 v[118:119], v[118:119], v[134:135]\n"
      "v_pk_add_f32 v[108:109], v[108:109], v[110:111]\n"
      "v_pk_add_f32 v[116:117], v[116:117], v[118:119]\n"
      "v_add_f32 v108, v108, v109\n"
      "v_add_f32 v109, v116, v117\n"
      "s_and_b32 s93, s58, 0x1fff\n"
      "v_add_f32_dpp v108, v108, v108 quad_perm:[1,0,3,2] row_mask:0xf bank_mask:0xf\n"
      "v_add_f32_dpp v109, v109, v109 quad_perm:[1,0,3,2] row_mask:0xf bank_mask:0xf\n"
      "s_or_b32 s93, s93, 0x4000\n"
      "v_add_f32_dpp v108, v108, v108 quad_perm:[2,3,0,1] row_mask:0xf bank_mask:0xf\n"
      "v_add_f32_dpp v109, v109, v109 quad_perm:[2,3,0,1] row_mask:0xf bank_mask:0xf\n"
      "v_mov_b32 v114, 1073741824\n"
      "v_add_f32_dpp v108, v108, v108 row_half_mirror row_mask:0xf bank_mask:0xf\n"
      "v_add_f32_dpp v109, v109, v109 row_half_mirror row_mask:0xf bank_mask:0xf\n"
      "v_writelane_b32 v114, 0, 16\n"
      "v_add_f32_dpp v108, v108, v108 row_mirror row_mask:0xf bank_mask:0xf\n"
      "v_add_f32_dpp v109, v109, v109 row_mirror row_mask:0xf bank_mask:0xf\n"
      "v_writelane_b32 v114, 8, 48\n"
      "v_add_f32_dpp v108, v108, v108 row_bcast:15 row_mask:0xa bank_mask:0xf\n"
      "v_add_f32_dpp v109, v109, v109 row_bcast:15 row_mask:0xa bank_mask:0xf\n"
      "v_mul_f32 v109, 0x3acccccd, v109\n"
      "v_mul_f32 v108, 0x3acccccd, v108\n"
      "v_fma_f32 v108, -v109, v109, v108\n"
      "v_max_f32 v108, 0, v108\n"
      "v_cvt_pk_bf16_f32 v108, 0, v108\n"
      "v_add_f32 v108, 0x3727c5ac, v108\n"
      "v_cvt_pk_bf16_f32 v108, 0, v108\n"
      "v_sqrt_f32 v108, v108\n"
      "v_add_f32 v109, v106, v109\n"
      "s_add_u32 s69, s69, 8192\n"
      "v_cvt_pk_bf16_f32 v112, v109, v108\n"
      "v_mov_b32 v113, s58\n"
      "buffer_store_dwordx2 v[112:113], v114, s[20:23], s69 offen sc1\n"
      "s_cmp_lt_u32 s61, 28\n"
      "s_cselect_b32 s62, s79, 1073741824\n"
      "s_lshl_b32 s63, s60, 7\n"
      "s_add_u32 s63, s63, 16078592\n"
      "s_cmp_eq_u32 s53, 0\n"
      "s_cselect_b32 s63, s63, 1073741824\n"
      "v_lshl_add_u32 v50, v1, 3, s62\n"
      "s_mov_b32 exec_lo, 0\n"
      "v_lshl_add_u32 v50, v1, 3, s63\n"
      "s_mov_b32 exec_lo, -1\n"
      "s_mov_b32 exec_hi, 0xffff\n"
      "s_lshl_b32 s63, s66, 5\n"
      "s_add_u32 s63, s63, s67\n"
      "s_lshl_b32 s63, s63, 7\n"
      "s_add_u32 s63, s63, 15859328\n"
      "s_cmp_lt_u32 s61, 28\n"
      "s_cselect_b32 s63, s63, 1073741824\n"
      "s_mov_b32 exec_lo, 0\n"
      "s_mov_b32 exec_hi, 0xffff0000\n"
      "v_lshl_add_u32 v50, v1, 3, s63\n"
      "s_mov_b64 exec, -1\n"
      "buffer_store_dwordx2 v[42:43], v50, s[20:23], 0 offen sc1\n"
      "s_mov_b64 exec, -1\n"
      "s_cmp_lt_u32 s61, 28\n"
      "s_cbranch_scc0 .LSPARE\n"
      "v_mov_b64 v[12:13], s[58:59]\n"
      "v_cmp_eq_u32 vcc, 0, v1\n"
      "v_lshlrev_b32 v2, 2, v1\n"
      "v_add_u32 v2, 157696, v2\n"
      "v_cndmask_b32 v3, 0, 1, vcc\n"
      "s_waitcnt vmcnt(2)\n"
      "v_lshlrev_b32 v184, 16, v44\n"
      "v_lshlrev_b32 v185, 16, v46\n"
      "v_and_b32 v186, s92, v44\n"
      "v_and_b32 v187, s92, v46\n"
      "v_lshlrev_b32 v188, 16, v45\n"
      "v_lshlrev_b32 v189, 16, v47\n"
      "v_and_b32 v190, s92, v45\n"
      "v_and_b32 v191, s92, v47\n"
      "v_lshlrev_b32 v9, 16, v9\n"
      "s_barrier\n"
      "v_mov_b32 v31, 161920\n"
      "ds_write_b64 v31, v[12:13]\n"
      "ds_read_b32 v26, v2 offset:5120\n"
      "v_lshlrev_b32 v31, 3, v1\n"
      "v_add_u32 v31, 163072, v31\n"
      "s_waitcnt lgkmcnt(0)\n"
      "v_and_b32 v27, s92, v26\n"
      "v_lshlrev_b32 v26, 16, v26\n"
      "ds_write_b64 v31, v[26:27]\n"
      "ds_read_b64_tr_b16 v[96:97], v24 offset:0\n"
      "ds_read_b64_tr_b16 v[98:99], v24 offset:1024\n"
      "ds_read_b128 v[32:35], v4 offset:16384\n"
      "ds_read_b128 v[36:39], v4 offset:20480\n"
      "ds_read_b64_tr_b16 v[100:101], v24 offset:4096\n"
      "ds_read_b64_tr_b16 v[102:103], v24 offset:5120\n"
      "ds_read_b128 v[40:43], v5 offset:16384\n"
      "ds_read_b128 v[44:47], v5 offset:20480\n"
      "ds_read_b64_tr_b16 v[104:105], v24 offset:8192\n"
      "ds_read_b64_tr_b16 v[106:107], v24 offset:9216\n"
      "ds_read_b128 v[48:51], v6 offset:16384\n"
      "ds_read_b128 v[52:55], v6 offset:20480\n"
      "ds_read_b64_tr_b16 v[108:109], v24 offset:12288\n"
      "ds_read_b64_tr_b16 v[110:111], v24 offset:13312\n"
      "ds_read_b128 v[56:59], v7 offset:16384\n"
      "ds_read_b128 v[60:63], v7 offset:20480\n"
      "s_waitcnt lgkmcnt(13)\n"
      "v_mfma_f32_32x32x16_bf16 v[80:95], v[96:99], v[32:35], 0\n"
      "s_waitcnt lgkmcnt(12)\n"
      "v_mfma_f32_32x32x16_bf16 v[64:79], v[96:99], v[36:39], 0\n"
      "v_mfma_f32_16x16x32_bf16 v[20:23], v[16:19], v[96:99], 0\n"
      "s_waitcnt lgkmcnt(10)\n"
      "v_mfma_f32_16x16x32_bf16 v[20:23], v[16:19], v[100:103], v[20:23]\n"
      "s_waitcnt lgkmcnt(9)\n"
      "v_mfma_f32_32x32x16_bf16 v[80:95], v[100:103], v[40:43], v[80:95]\n"
      "s_waitcnt lgkmcnt(0)\n"
      "v_mfma_f32_32x32x16_bf16 v[64:79], v[100:103], v[44:47], v[64:79]\n"
      "s_barrier\n"
      "ds_read2_b32 v[196:197], v192 offset0:0 offset1:32\n"
      "ds_read2_b32 v[198:199], v192 offset0:64 offset1:100\n"
      "ds_read2_b32 v[200:201], v192 offset0:132 offset1:164\n"
      "ds_read2_b32 v[202:203], v192 offset0:196 offset1:228\n"
      "ds_read_b32 v204, v192 offset:1040\n"
      "ds_read_b32 v205, v192 offset:1168\n"
      "ds_read_b32 v206, v192 offset:1296\n"
      "ds_read2_b32 v[208:209], v193 offset0:0 offset1:32\n"
      "ds_read2_b32 v[210:211], v193 offset0:64 offset1:96\n"
      "ds_read2_b32 v[212:213], v193 offset0:128 offset1:160\n"
      "ds_read2_b32 v[214:215], v193 offset0:192 offset1:224\n"
      "s_waitcnt lgkmcnt(11)\n"
      "v_mfma_f32_32x32x16_bf16 v[80:95], v[104:107], v[48:51], v[80:95]\n"
      "s_waitcnt lgkmcnt(9)\n"
      "v_lshlrev_b32 v216, 16, v196\n"
      "v_and_b32 v217, s92, v196\n"
      "v_lshlrev_b32 v218, 16, v197\n"
      "v_and_b32 v219, s92, v197\n"
      "v_mfma_f32_32x32x16_bf16 v[64:79], v[104:107], v[52:55], v[64:79]\n"
      "v_lshlrev_b32 v220, 16, v198\n"
      "v_and_b32 v221, s92, v198\n"
      "v_lshlrev_b32 v222, 16, v199\n"
      "v_and_b32 v223, s92, v199\n"
      "v_mfma_f32_32x32x16_bf16 v[80:95], v[108:111], v[56:59], v[80:95]\n"
      "s_waitcnt lgkmcnt(7)\n"
      "v_lshlrev_b32 v224, 16, v200\n"
      "v_and_b32 v225, s92, v200\n"
      "v_lshlrev_b32 v226, 16, v201\n"
      "v_and_b32 v227, s92, v201\n"
      "v_mfma_f32_32x32x16_bf16 v[64:79], v[108:111], v[60:63], v[64:79]\n"
      "v_lshlrev_b32 v228, 16, v202\n"
      "v_and_b32 v229, s92, v202\n"
      "v_lshlrev_b32 v230, 16, v203\n"
      "v_and_b32 v231, s92, v203\n"
      "v_mfma_f32_16x16x32_bf16 v[20:23], v[16:19], v[104:107], v[20:23]\n"
      "s_waitcnt lgkmcnt(4)\n"
      "v_lshlrev_b32 v232, 16, v204\n"
      "v_and_b32 v233, s92, v204\n"
      "v_lshlrev_b32 v234, 16, v205\n"
      "v_and_b32 v235, s92, v205\n"
      "v_mfma_f32_16x16x32_bf16 v[20:23], v[16:19], v[108:111], v[20:23]\n"
      "v_lshlrev_b32 v236, 16, v206\n"
      "v_and_b32 v237, s92, v206\n"
      "v_pk_mul_f32 v[240:241], v[216:217], v[190:191]\n"
      "v_pk_mul_f32 v[242:243], v[218:219], v[190:191]\n"
      "v_pk_mul_f32 v[244:245], v[220:221], v[190:191]\n"
      "v_pk_mul_f32 v[246:247], v[222:223], v[190:191]\n"
      "v_pk_mul_f32 v[248:249], v[224:225], v[190:191]\n"
      "v_pk_mul_f32 v[250:251], v[226:227], v[190:191]\n"
      "v_pk_mul_f32 v[252:253], v[228:229], v[190:191]\n"
      "v_pk_mul_f32 v[254:255], v[230:231], v[190:191]\n"
      "v_pk_fma_f32 v[240:241], v[218:219], v[188:189], v[240:241]\n"
      "v_pk_fma_f32 v[242:243], v[220:221], v[188:189], v[242:243]\n"
      "v_pk_fma_f32 v[244:245], v[222:223], v[188:189], v[244:245]\n"
      "v_pk_fma_f32 v[246:247], v[224:225], v[188:189], v[246:247]\n"
      "v_pk_fma_f32 v[248:249], v[226:227], v[188:189], v[248:249]\n"
      "v_pk_fma_f32 v[250:251], v[228:229], v[188:189], v[250:251]\n"
      "v_pk_fma_f32 v[252:253], v[230:231], v[188:189], v[252:253]\n"
      "v_pk_fma_f32 v[254:255], v[232:233], v[188:189], v[254:255]\n"
      "v_pk_fma_f32 v[240:241], v[220:221], v[186:187], v[240:241]\n"
      "v_pk_fma_f32 v[242:243], v[222:223], v[186:187], v[242:243]\n"
      "v_pk_fma_f32 v[244:245], v[224:225], v[186:187], v[244:245]\n"
      "v_pk_fma_f32 v[246:247], v[226:227], v[186:187], v[246:247]\n"
      "v_pk_fma_f32 v[248:249], v[228:229], v[186:187], v[248:249]\n"
      "v_pk_fma_f32 v[250:251], v[230:231], v[186:187], v[250:251]\n"
      "v_pk_fma_f32 v[252:253], v[232:233], v[186:187], v[252:253]\n"
      "v_pk_fma_f32 v[254:255], v[234:235], v[186:187], v[254:255]\n"
      "v_pk_fma_f32 v[240:241], v[222:223], v[184:185], v[240:241]\n"
      "v_pk_fma_f32 v[242:243], v[224:225], v[184:185], v[242:243]\n"
      "v_pk_fma_f32 v[244:245], v[226:227], v[184:185], v[244:245]\n"
      "v_pk_fma_f32 v[246:247], v[228:229], v[184:185], v[246:247]\n"
      "v_pk_fma_f32 v[248:249], v[230:231], v[184:185], v[248:249]\n"
      "v_pk_fma_f32 v[250:251], v[232:233], v[184:185], v[250:251]\n"
      "v_pk_fma_f32 v[252:253], v[234:235], v[184:185], v[252:253]\n"
      "v_pk_fma_f32 v[254:255], v[236:237], v[184:185], v[254:255]\n"
      "s_waitcnt lgkmcnt(0)\n"
      "v_lshlrev_b32 v216, 16, v208\n"
      "v_and_b32 v217, s92, v208\n"
      "v_lshlrev_b32 v218, 16, v209\n"
      "v_and_b32 v219, s92, v209\n"
      "v_lshlrev_b32 v220, 16, v210\n"
      "v_and_b32 v221, s92, v210\n"
      "v_lshlrev_b32 v222, 16, v211\n"
      "v_and_b32 v223, s92, v211\n"
      "v_lshlrev_b32 v224, 16, v212\n"
      "v_and_b32 v225, s92, v212\n"
      "v_lshlrev_b32 v226, 16, v213\n"
      "v_and_b32 v227, s92, v213\n"
      "v_lshlrev_b32 v228, 16, v214\n"
      "v_and_b32 v229, s92, v214\n"
      "v_lshlrev_b32 v230, 16, v215\n"
      "v_and_b32 v231, s92, v215\n"
      "v_pk_mul_f32 v[240:241], v[240:241], v[216:217]\n"
      "v_pk_mul_f32 v[242:243], v[242:243], v[218:219]\n"
      "v_pk_mul_f32 v[244:245], v[244:245], v[220:221]\n"
      "v_pk_mul_f32 v[246:247], v[246:247], v[222:223]\n"
      "v_pk_mul_f32 v[248:249], v[248:249], v[224:225]\n"
      "v_pk_mul_f32 v[250:251], v[250:251], v[226:227]\n"
      "v_pk_mul_f32 v[252:253], v[252:253], v[228:229]\n"
      "v_pk_mul_f32 v[254:255], v[254:255], v[230:231]\n"
      "v_cvt_pk_bf16_f32 v196, v242, v240\n"
      "v_cvt_pk_bf16_f32 v197, v246, v244\n"
      "v_cvt_pk_bf16_f32 v198, v250, v248\n"
      "v_cvt_pk_bf16_f32 v199, v254, v252\n"
      "buffer_store_dwordx4 v[196:199], v8, s[20:23], s84 offen sc1\n"
      "v_cvt_pk_bf16_f32 v200, v243, v241\n"
      "v_cvt_pk_bf16_f32 v201, v247, v245\n"
      "v_cvt_pk_bf16_f32 v202, v251, v249\n"
      "v_cvt_pk_bf16_f32 v203, v255, v253\n"
      "buffer_store_dwordx4 v[200:203], v8, s[20:23], s85 offen sc1\n"
      "s_barrier\n"
      "ds_read_b64_tr_b16 v[112:113], v24 offset:24576\n"
      "ds_read_b64_tr_b16 v[114:115], v24 offset:25600\n"
      "ds_read_b128 v[32:35], v4 offset:40960\n"
      "ds_read_b128 v[36:39], v4 offset:45056\n"
      "ds_read_b64_tr_b16 v[116:117], v24 offset:28672\n"
      "ds_read_b64_tr_b16 v[118:119], v24 offset:29696\n"
      "ds_read_b128 v[40:43], v5 offset:40960\n"
      "ds_read_b128 v[44:47], v5 offset:45056\n"
      "ds_read_b64_tr_b16 v[120:121], v24 offset:32768\n"
      "ds_read_b64_tr_b16 v[122:123], v24 offset:33792\n"
      "ds_read_b128 v[48:51], v6 offset:40960\n"
      "ds_read_b128 v[52:55], v6 offset:45056\n"
      "ds_read_b64_tr_b16 v[124:125], v24 offset:36864\n"
      "ds_read_b64_tr_b16 v[126:127], v24 offset:37888\n"
      "ds_read_b128 v[56:59], v7 offset:40960\n"
      "ds_read_b128 v[60:63], v7 offset:45056\n"
      "s_waitcnt lgkmcnt(13)\n"
      "v_mfma_f32_32x32x16_bf16 v[80:95], v[112:115], v[32:35], v[80:95]\n"
      "s_waitcnt lgkmcnt(12)\n"
      "v_mfma_f32_32x32x16_bf16 v[64:79], v[112:115], v[36:39], v[64:79]\n"
      "v_mfma_f32_16x16x32_bf16 v[20:23], v[16:19], v[112:115], v[20:23]\n"
      "s_waitcnt lgkmcnt(10)\n"
      "v_mfma_f32_16x16x32_bf16 v[20:23], v[16:19], v[116:119], v[20:23]\n"
      "s_waitcnt lgkmcnt(9)\n"
      "v_mfma_f32_32x32x16_bf16 v[80:95], v[116:119], v[40:43], v[80:95]\n"
      "s_waitcnt lgkmcnt(0)\n"
      "v_add_u32 v24, 49152, v24\n"
      "v_add_u32 v4, 49152, v4\n"
      "v_add_u32 v5, 49152, v5\n"
      "v_add_u32 v6, 49152, v6\n"
      "v_add_u32 v7, 49152, v7\n"
      "v_mfma_f32_32x32x16_bf16 v[64:79], v[116:119], v[44:47], v[64:79]\n"
      "s_barrier\n"
      "v_mfma_f32_32x32x16_bf16 v[80:95], v[120:123], v[48:51], v[80:95]\n"
      "ds_read_b64_tr_b16 v[128:129], v24 offset:0\n"
      "ds_read_b64_tr_b16 v[130:131], v24 offset:1024\n"
      "ds_read_b128 v[32:35], v4 offset:16384\n"
      "ds_read_b128 v[36:39], v4 offset:20480\n"
      "v_mfma_f32_32x32x16_bf16 v[64:79], v[120:123], v[52:55], v[64:79]\n"
      "ds_read_b64_tr_b16 v[132:133], v24 offset:4096\n"
      "ds_read_b64_tr_b16 v[134:135], v24 offset:5120\n"
      "ds_read_b128 v[40:43], v5 offset:16384\n"
      "ds_read_b128 v[44:47], v5 offset:20480\n"
      "v_mfma_f32_32x32x16_bf16 v[80:95], v[124:127], v[56:59], v[80:95]\n"
      "ds_read_b64_tr_b16 v[136:137], v24 offset:8192\n"
      "ds_read_b64_tr_b16 v[138:139], v24 offset:9216\n"
      "ds_read_b128 v[48:51], v6 offset:16384\n"
      "ds_read_b128 v[52:55], v6 offset:20480\n"
      "v_mfma_f32_32x32x16_bf16 v[64:79], v[124:127], v[60:63], v[64:79]\n"
      "ds_read_b64_tr_b16 v[140:141], v24 offset:12288\n"
      "ds_read_b64_tr_b16 v[142:143], v24 offset:13312\n"
      "ds_read_b128 v[56:59], v7 offset:16384\n"
      "ds_read_b128 v[60:63], v7 offset:20480\n"
      "v_mfma_f32_16x16x32_bf16 v[20:23], v[16:19], v[120:123], v[20:23]\n"
      "v_mfma_f32_16x16x32_bf16 v[20:23], v[16:19], v[124:127], v[20:23]\n"
      "s_waitcnt lgkmcnt(13)\n"
      "v_mfma_f32_32x32x16_bf16 v[80:95], v[128:131], v[32:35], v[80:95]\n"
      "s_waitcnt lgkmcnt(12)\n"
      "v_mfma_f32_32x32x16_bf16 v[64:79], v[128:131], v[36:39], v[64:79]\n"
      "v_mfma_f32_16x16x32_bf16 v[20:23], v[16:19], v[128:131], v[20:23]\n"
      "s_waitcnt lgkmcnt(8)\n"
      "v_mfma_f32_16x16x32_bf16 v[20:23], v[16:19], v[132:135], v[20:23]\n"
      "v_mfma_f32_32x32x16_bf16 v[80:95], v[132:135], v[40:43], v[80:95]\n"
      "s_waitcnt lgkmcnt(0)\n"
      "v_mfma_f32_32x32x16_bf16 v[64:79], v[132:135], v[44:47], v[64:79]\n"
      "s_barrier\n"
      "v_mfma_f32_32x32x16_bf16 v[80:95], v[136:139], v[48:51], v[80:95]\n"
      "ds_read_b64_tr_b16 v[144:145], v24 offset:24576\n"
      "ds_read_b64_tr_b16 v[146:147], v24 offset:25600\n"
      "ds_read_b128 v[32:35], v4 offset:40960\n"
      "ds_read_b128 v[36:39], v4 offset:45056\n"
      "v_mfma_f32_32x32x16_bf16 v[64:79], v[136:139], v[52:55], v[64:79]\n"
      "ds_read_b64_tr_b16 v[148:149], v24 offset:28672\n"
      "ds_read_b64_tr_b16 v[150:151], v24 offset:29696\n"
      "ds_read_b128 v[40:43], v5 offset:40960\n"
      "ds_read_b128 v[44:47], v5 offset:45056\n"
      "v_mfma_f32_32x32x16_bf16 v[80:95], v[140:143], v[56:59], v[80:95]\n"
      "ds_read_b64_tr_b16 v[152:153], v24 offset:32768\n"
      "ds_read_b64_tr_b16 v[154:155], v24 offset:33792\n"
      "ds_read_b128 v[48:51], v6 offset:40960\n"
      "ds_read_b128 v[52:55], v6 offset:45056\n"
      "v_mfma_f32_32x32x16_bf16 v[64:79], v[140:143], v[60:63], v[64:79]\n"
      "ds_read_b64_tr_b16 v[156:157], v24 offset:36864\n"
      "ds_read_b64_tr_b16 v[158:159], v24 offset:37888\n"
      "ds_read_b128 v[56:59], v7 offset:40960\n"
      "ds_read_b128 v[60:63], v7 offset:45056\n"
      "v_mfma_f32_16x16x32_bf16 v[20:23], v[16:19], v[136:139], v[20:23]\n"
      "v_mfma_f32_16x16x32_bf16 v[20:23], v[16:19], v[140:143], v[20:23]\n"
      "s_waitcnt lgkmcnt(13)\n"
      "v_mfma_f32_32x32x16_bf16 v[80:95], v[144:147], v[32:35], v[80:95]\n"
      "s_waitcnt lgkmcnt(12)\n"
      "v_mfma_f32_32x32x16_bf16 v[64:79], v[144:147], v[36:39], v[64:79]\n"
      "v_mfma_f32_16x16x32_bf16 v[20:23], v[16:19], v[144:147], v[20:23]\n"
      "s_waitcnt lgkmcnt(8)\n"
      "v_mfma_f32_16x16x32_bf16 v[20:23], v[16:19], v[148:151], v[20:23]\n"
      "v_mfma_f32_32x32x16_bf16 v[80:95], v[148:151], v[40:43], v[80:95]\n"
      "s_waitcnt lgkmcnt(0)\n"
      "v_add_u32 v24, 49152, v24\n"
      "v_add_u32 v4, 49152, v4\n"
      "v_add_u32 v5, 49152, v5\n"
      "v_add_u32 v6, 49152, v6\n"
      "v_add_u32 v7, 49152, v7\n"
      "v_mfma_f32_32x32x16_bf16 v[64:79], v[148:151], v[44:47], v[64:79]\n"
      "s_barrier\n"
      "v_mfma_f32_32x32x16_bf16 v[80:95], v[152:155], v[48:51], v[80:95]\n"
      "ds_read_b64_tr_b16 v[160:161], v24 offset:0\n"
      "ds_read_b64_tr_b16 v[162:163], v24 offset:1024\n"
      "ds_read_b128 v[32:35], v4 offset:16384\n"
      "ds_read_b128 v[36:39], v4 offset:20480\n"
      "v_mfma_f32_32x32x16_bf16 v[64:79], v[152:155], v[52:55], v[64:79]\n"
      "ds_read_b64_tr_b16 v[164:165], v24 offset:4096\n"
      "ds_read_b64_tr_b16 v[166:167], v24 offset:5120\n"
      "ds_read_b128 v[40:43], v5 offset:16384\n"
      "ds_read_b128 v[44:47], v5 offset:20480\n"
      "v_mfma_f32_32x32x16_bf16 v[80:95], v[156:159], v[56:59], v[80:95]\n"
      "ds_read_b64_tr_b16 v[168:169], v24 offset:8192\n"
      "ds_read_b64_tr_b16 v[170:171], v24 offset:9216\n"
      "ds_read_b128 v[48:51], v6 offset:16384\n"
      "ds_read_b128 v[52:55], v6 offset:20480\n"
      "v_mfma_f32_32x32x16_bf16 v[64:79], v[156:159], v[60:63], v[64:79]\n"
      "ds_read_b64_tr_b16 v[172:173], v24 offset:12288\n"
      "ds_read_b64_tr_b16 v[174:175], v24 offset:13312\n"
      "ds_read_b128 v[56:59], v7 offset:16384\n"
      "ds_read_b128 v[60:63], v7 offset:20480\n"
      "v_mfma_f32_16x16x32_bf16 v[20:23], v[16:19], v[152:155], v[20:23]\n"
      "v_mfma_f32_16x16x32_bf16 v[20:23], v[16:19], v[156:159], v[20:23]\n"
      "s_waitcnt lgkmcnt(13)\n"
      "v_mfma_f32_32x32x16_bf16 v[80:95], v[160:163], v[32:35], v[80:95]\n"
      "s_waitcnt lgkmcnt(12)\n"
      "v_mfma_f32_32x32x16_bf16 v[64:79], v[160:163], v[36:39], v[64:79]\n"
      "v_mfma_f32_16x16x32_bf16 v[20:23], v[16:19], v[160:163], v[20:23]\n"
      "s_waitcnt lgkmcnt(8)\n"
      "v_mfma_f32_16x16x32_bf16 v[20:23], v[16:19], v[164:167], v[20:23]\n"
      "v_mfma_f32_32x32x16_bf16 v[80:95], v[164:167], v[40:43], v[80:95]\n"
      "s_waitcnt lgkmcnt(0)\n"
      "v_add_u32 v24, 4294868992, v24\n"
      "v_add_u32 v4, 4294868992, v4\n"
      "v_add_u32 v5, 4294868992, v5\n"
      "v_add_u32 v6, 4294868992, v6\n"
      "v_add_u32 v7, 4294868992, v7\n"
      "s_waitcnt vmcnt(0)\n"
      "v_mfma_f32_32x32x16_bf16 v[64:79], v[164:167], v[44:47], v[64:79]\n"
      "s_barrier\n"
      "v_mfma_f32_32x32x16_bf16 v[80:95], v[168:171], v[48:51], v[80:95]\n"
      "ds_read_b64_tr_b16 v[176:177], v24 offset:0\n"
      "ds_read_b64_tr_b16 v[178:179], v24 offset:1024\n"
      "ds_read_b128 v[32:35], v4 offset:16384\n"
      "ds_read_b128 v[36:39], v4 offset:20480\n"
      "v_mfma_f32_32x32x16_bf16 v[64:79], v[168:171], v[52:55], v[64:79]\n"
      "ds_read_b64_tr_b16 v[180:181], v24 offset:4096\n"
      "ds_read_b64_tr_b16 v[182:183], v24 offset:5120\n"
      "ds_read_b128 v[40:43], v5 offset:16384\n"
      "ds_read_b128 v[44:47], v5 offset:20480\n"
      "v_mfma_f32_32x32x16_bf16 v[80:95], v[172:175], v[56:59], v[80:95]\n"
      "ds_read_b64_tr_b16 v[184:185], v24 offset:8192\n"
      "ds_read_b64_tr_b16 v[186:187], v24 offset:9216\n"
      "ds_read_b128 v[48:51], v6 offset:16384\n"
      "ds_read_b128 v[52:55], v6 offset:20480\n"
      "v_mfma_f32_32x32x16_bf16 v[64:79], v[172:175], v[60:63], v[64:79]\n"
      "ds_read_b64_tr_b16 v[188:189], v24 offset:12288\n"
      "ds_read_b64_tr_b16 v[190:191], v24 offset:13312\n"
      "ds_read_b128 v[56:59], v7 offset:16384\n"
      "ds_read_b128 v[60:63], v7 offset:20480\n"
      "v_mfma_f32_16x16x32_bf16 v[20:23], v[16:19], v[168:171], v[20:23]\n"
      "v_mfma_f32_16x16x32_bf16 v[20:23], v[16:19], v[172:175], v[20:23]\n"
      "s_waitcnt lgkmcnt(13)\n"
      "v_mfma_f32_32x32x16_bf16 v[80:95], v[176:179], v[32:35], v[80:95]\n"
      "s_waitcnt lgkmcnt(12)\n"
      "v_mfma_f32_32x32x16_bf16 v[64:79], v[176:179], v[36:39], v[64:79]\n"
      "v_mfma_f32_16x16x32_bf16 v[20:23], v[16:19], v[176:179], v[20:23]\n"
      "s_waitcnt lgkmcnt(8)\n"
      "v_mfma_f32_16x16x32_bf16 v[20:23], v[16:19], v[180:183], v[20:23]\n"
      "v_mfma_f32_32x32x16_bf16 v[80:95], v[180:183], v[40:43], v[80:95]\n"
      "s_waitcnt lgkmcnt(0)\n"
      "v_mfma_f32_32x32x16_bf16 v[64:79], v[180:183], v[44:47], v[64:79]\n"
      "s_barrier\n"
      "v_mfma_f32_32x32x16_bf16 v[80:95], v[184:187], v[48:51], v[80:95]\n"
      "ds_read_b64_tr_b16 v[192:193], v24 offset:24576\n"
      "ds_read_b64_tr_b16 v[194:195], v24 offset:25600\n"
      "ds_read_b128 v[32:35], v4 offset:40960\n"
      "ds_read_b128 v[36:39], v4 offset:45056\n"
      "v_mfma_f32_32x32x16_bf16 v[64:79], v[184:187], v[52:55], v[64:79]\n"
      "ds_read_b64_tr_b16 v[196:197], v24 offset:28672\n"
      "ds_read_b64_tr_b16 v[198:199], v24 offset:29696\n"
      "ds_read_b128 v[40:43], v5 offset:40960\n"
      "ds_read_b128 v[44:47], v5 offset:45056\n"
      "v_mfma_f32_32x32x16_bf16 v[80:95], v[188:191], v[56:59], v[80:95]\n"
      "ds_read_b64_tr_b16 v[200:201], v24 offset:32768\n"
      "ds_read_b64_tr_b16 v[202:203], v24 offset:33792\n"
      "ds_read_b128 v[48:51], v6 offset:40960\n"
      "ds_read_b128 v[52:55], v6 offset:45056\n"
      "v_mfma_f32_32x32x16_bf16 v[64:79], v[188:191], v[60:63], v[64:79]\n"
      "ds_read_b64_tr_b16 v[204:205], v24 offset:36864\n"
      "ds_read_b64_tr_b16 v[206:207], v24 offset:37888\n"
      "ds_read_b128 v[56:59], v7 offset:40960\n"
      "ds_read_b128 v[60:63], v7 offset:45056\n"
      "v_mfma_f32_16x16x32_bf16 v[20:23], v[16:19], v[184:187], v[20:23]\n"
      "v_mfma_f32_16x16x32_bf16 v[20:23], v[16:19], v[188:191], v[20:23]\n"
      "s_waitcnt lgkmcnt(13)\n"
      "v_mfma_f32_32x32x16_bf16 v[80:95], v[192:195], v[32:35], v[80:95]\n"
      "s_waitcnt lgkmcnt(12)\n"
      "v_mfma_f32_32x32x16_bf16 v[64:79], v[192:195], v[36:39], v[64:79]\n"
      "v_mfma_f32_16x16x32_bf16 v[20:23], v[16:19], v[192:195], v[20:23]\n"
      "s_waitcnt lgkmcnt(8)\n"
      "v_mfma_f32_16x16x32_bf16 v[20:23], v[16:19], v[196:199], v[20:23]\n"
      "v_mfma_f32_32x32x16_bf16 v[80:95], v[196:199], v[40:43], v[80:95]\n"
      "s_waitcnt lgkmcnt(0)\n"
      "v_add_u32 v24, 49152, v24\n"
      "v_add_u32 v4, 49152, v4\n"
      "v_add_u32 v5, 49152, v5\n"
      "v_add_u32 v6, 49152, v6\n"
      "v_add_u32 v7, 49152, v7\n"
      "v_mfma_f32_32x32x16_bf16 v[64:79], v[196:199], v[44:47], v[64:79]\n"
      "s_barrier\n"
      "v_mfma_f32_32x32x16_bf16 v[80:95], v[200:203], v[48:51], v[80:95]\n"
      "ds_read_b64_tr_b16 v[208:209], v24 offset:0\n"
      "ds_read_b64_tr_b16 v[210:211], v24 offset:1024\n"
      "ds_read_b128 v[32:35], v4 offset:16384\n"
      "ds_read_b128 v[36:39], v4 offset:20480\n"
      "v_mfma_f32_32x32x16_bf16 v[64:79], v[200:203], v[52:55], v[64:79]\n"
      "ds_read_b64_tr_b16 v[212:213], v24 offset:4096\n"
      "ds_read_b64_tr_b16 v[214:215], v24 offset:5120\n"
      "ds_read_b128 v[40:43], v5 offset:16384\n"
      "ds_read_b128 v[44:47], v5 offset:20480\n"
      "v_mfma_f32_32x32x16_bf16 v[80:95], v[204:207], v[56:59], v[80:95]\n"
      "ds_read_b64_tr_b16 v[216:217], v24 offset:8192\n"
      "ds_read_b64_tr_b16 v[218:219], v24 offset:9216\n"
      "ds_read_b128 v[48:51], v6 offset:16384\n"
      "ds_read_b128 v[52:55], v6 offset:20480\n"
      "v_mfma_f32_32x32x16_bf16 v[64:79], v[204:207], v[60:63], v[64:79]\n"
      "ds_read_b64_tr_b16 v[220:221], v24 offset:12288\n"
      "ds_read_b64_tr_b16 v[222:223], v24 offset:13312\n"
      "ds_read_b128 v[56:59], v7 offset:16384\n"
      "ds_read_b128 v[60:63], v7 offset:20480\n"
      "v_mfma_f32_16x16x32_bf16 v[20:23], v[16:19], v[200:203], v[20:23]\n"
      "v_mfma_f32_16x16x32_bf16 v[20:23], v[16:19], v[204:207], v[20:23]\n"
      "s_waitcnt lgkmcnt(13)\n"
      "v_mfma_f32_32x32x16_bf16 v[80:95], v[208:211], v[32:35], v[80:95]\n"
      "s_waitcnt lgkmcnt(12)\n"
      "v_mfma_f32_32x32x16_bf16 v[64:79], v[208:211], v[36:39], v[64:79]\n"
      "v_mfma_f32_16x16x32_bf16 v[20:23], v[16:19], v[208:211], v[20:23]\n"
      "s_waitcnt lgkmcnt(8)\n"
      "v_mfma_f32_16x16x32_bf16 v[20:23], v[16:19], v[212:215], v[20:23]\n"
      "v_mfma_f32_32x32x16_bf16 v[80:95], v[212:215], v[40:43], v[80:95]\n"
      "s_waitcnt lgkmcnt(0)\n"
      "v_mfma_f32_32x32x16_bf16 v[64:79], v[212:215], v[44:47], v[64:79]\n"
      "s_barrier\n"
      "v_mfma_f32_32x32x16_bf16 v[80:95], v[216:219], v[48:51], v[80:95]\n"
      "ds_read_b64_tr_b16 v[224:225], v24 offset:24576\n"
      "ds_read_b64_tr_b16 v[226:227], v24 offset:25600\n"
      "ds_read_b128 v[32:35], v4 offset:40960\n"
      "ds_read_b128 v[36:39], v4 offset:45056\n"
      "v_mfma_f32_32x32x16_bf16 v[64:79], v[216:219], v[52:55], v[64:79]\n"
      "ds_read_b64_tr_b16 v[228:229], v24 offset:28672\n"
      "ds_read_b64_tr_b16 v[230:231], v24 offset:29696\n"
      "ds_read_b128 v[40:43], v5 offset:40960\n"
      "ds_read_b128 v[44:47], v5 offset:45056\n"
      "v_mfma_f32_32x32x16_bf16 v[80:95], v[220:223], v[56:59], v[80:95]\n"
      "ds_read_b64_tr_b16 v[232:233], v24 offset:32768\n"
      "ds_read_b64_tr_b16 v[234:235], v24 offset:33792\n"
      "ds_read_b128 v[48:51], v6 offset:40960\n"
      "ds_read_b128 v[52:55], v6 offset:45056\n"
      "v_mfma_f32_32x32x16_bf16 v[64:79], v[220:223], v[60:63], v[64:79]\n"
      "ds_read_b64_tr_b16 v[236:237], v24 offset:36864\n"
      "ds_read_b64_tr_b16 v[238:239], v24 offset:37888\n"
      "ds_read_b128 v[56:59], v7 offset:40960\n"
      "ds_read_b128 v[60:63], v7 offset:45056\n"
      "v_mfma_f32_16x16x32_bf16 v[20:23], v[16:19], v[216:219], v[20:23]\n"
      "v_mfma_f32_16x16x32_bf16 v[20:23], v[16:19], v[220:223], v[20:23]\n"
      "s_waitcnt lgkmcnt(13)\n"
      "v_mfma_f32_32x32x16_bf16 v[80:95], v[224:227], v[32:35], v[80:95]\n"
      "s_waitcnt lgkmcnt(12)\n"
      "v_mfma_f32_32x32x16_bf16 v[64:79], v[224:227], v[36:39], v[64:79]\n"
      "v_mfma_f32_16x16x32_bf16 v[20:23], v[16:19], v[224:227], v[20:23]\n"
      "s_waitcnt lgkmcnt(8)\n"
      "v_mfma_f32_16x16x32_bf16 v[20:23], v[16:19], v[228:231], v[20:23]\n"
      "v_mfma_f32_32x32x16_bf16 v[80:95], v[228:231], v[40:43], v[80:95]\n"
      "s_waitcnt lgkmcnt(0)\n"
      "v_add_u32 v24, 49152, v24\n"
      "v_add_u32 v4, 49152, v4\n"
      "v_add_u32 v5, 49152, v5\n"
      "v_add_u32 v6, 49152, v6\n"
      "v_add_u32 v7, 49152, v7\n"
      "v_mfma_f32_32x32x16_bf16 v[64:79], v[228:231], v[44:47], v[64:79]\n"
      "s_barrier\n"
      "v_mfma_f32_32x32x16_bf16 v[80:95], v[232:235], v[48:51], v[80:95]\n"
      "ds_read_b64_tr_b16 v[240:241], v24 offset:0\n"
      "ds_read_b64_tr_b16 v[242:243], v24 offset:1024\n"
      "ds_read_b128 v[32:35], v4 offset:16384\n"
      "ds_read_b128 v[36:39], v4 offset:20480\n"
      "v_mfma_f32_32x32x16_bf16 v[64:79], v[232:235], v[52:55], v[64:79]\n"
      "ds_read_b64_tr_b16 v[244:245], v24 offset:4096\n"
      "ds_read_b64_tr_b16 v[246:247], v24 offset:5120\n"
      "ds_read_b128 v[40:43], v5 offset:16384\n"
      "ds_read_b128 v[44:47], v5 offset:20480\n"
      "v_mfma_f32_32x32x16_bf16 v[80:95], v[236:239], v[56:59], v[80:95]\n"
      "ds_read_b64_tr_b16 v[248:249], v24 offset:8192\n"
      "ds_read_b64_tr_b16 v[250:251], v24 offset:9216\n"
      "ds_read_b128 v[48:51], v6 offset:16384\n"
      "ds_read_b128 v[52:55], v6 offset:20480\n"
      "v_mfma_f32_32x32x16_bf16 v[64:79], v[236:239], v[60:63], v[64:79]\n"
      "ds_read_b64_tr_b16 v[252:253], v24 offset:12288\n"
      "ds_read_b64_tr_b16 v[254:255], v24 offset:13312\n"
      "ds_read_b128 v[56:59], v7 offset:16384\n"
      "ds_read_b128 v[60:63], v7 offset:20480\n"
      "v_mfma_f32_16x16x32_bf16 v[20:23], v[16:19], v[232:235], v[20:23]\n"
      "v_mfma_f32_16x16x32_bf16 v[20:23], v[16:19], v[236:239], v[20:23]\n"
      "ds_read_b32 v31, v2 offset:4144\n"
      "ds_read_b64 v[12:13], v30 offset:1040\n"
      "s_waitcnt lgkmcnt(14)\n"
      "v_mfma_f32_16x16x32_bf16 v[20:23], v[16:19], v[240:243], v[20:23]\n"
      "v_subrev_u32 v4, 98304, v4\n"
      "s_waitcnt lgkmcnt(12)\n"
      "v_mfma_f32_16x16x32_bf16 v[20:23], v[16:19], v[244:247], v[20:23]\n"
      "v_subrev_u32 v5, 98304, v5\n"
      "s_waitcnt lgkmcnt(8)\n"
      "v_mfma_f32_16x16x32_bf16 v[20:23], v[16:19], v[248:251], v[20:23]\n"
      "v_subrev_u32 v6, 98304, v6\n"
      "s_waitcnt lgkmcnt(4)\n"
      "v_mfma_f32_16x16x32_bf16 v[20:23], v[16:19], v[252:255], v[20:23]\n"
      "v_subrev_u32 v7, 98304, v7\n"
      "s_waitcnt lgkmcnt(0)\n"
      "v_mfma_f32_32x32x16_bf16 v[80:95], v[240:243], v[32:35], v[80:95]\n"
      "ds_read_b64 v[16:17], v30 offset:16\n"
      "v_readlane_b32 s64, v31, 0\n"
      "s_mov_b32 s65, 0\n"
      "s_mov_b64 vcc, s[64:65]\n"
      "s_nop 0\n"
      "v_mfma_f32_32x32x16_bf16 v[80:95], v[244:247], v[40:43], v[80:95]\n"
      "v_cndmask_b32 v24, v20, v21, s[80:81]\n"
      "v_cvt_pk_bf16_f32 v25, 0, v24\n"
      "v_sub_f32 v24, v24, v25\n"
      "v_cvt_pk_bf16_f32 v26, 0, v24\n"
      "v_sub_f32 v24, v24, v26\n"
      "v_mfma_f32_32x32x16_bf16 v[80:95], v[248:251], v[48:51], v[80:95]\n"
      "v_cvt_pk_bf16_f32 v25, v25, v26\n"
      "v_cvt_pk_bf16_f32 v26, v24, v9\n"
      "v_cndmask_b32 v8, 0, v25, s[82:83]\n"
      "v_cndmask_b32 v9, 0, v26, s[82:83]\n"
      "ds_read_b128 v[24:27], v30\n"
      "v_mfma_f32_32x32x16_bf16 v[80:95], v[252:255], v[56:59], v[80:95]\n"
      "s_waitcnt lgkmcnt(1)\n"
      "v_mfma_f32_32x32x16_bf16 v[80:95], v[8:11], v[16:19], v[80:95]\n"
      "v_mfma_f32_32x32x16_bf16 v[64:79], v[240:243], v[36:39], v[64:79]\n"
      "v_mfma_f32_32x32x16_bf16 v[64:79], v[244:247], v[44:47], v[64:79]\n"
      "v_mfma_f32_32x32x16_bf16 v[64:79], v[248:251], v[52:55], v[64:79]\n"
      "v_mfma_f32_32x32x16_bf16 v[64:79], v[252:255], v[60:63], v[64:79]\n"
      "v_mfma_f32_32x32x16_bf16 v[64:79], v[8:11], v[12:15], v[64:79]\n"
      "ds_read_b64 v[12:13], v30 offset:2064\n"
      "s_barrier\n"
      "s_mov_b64 s[84:85], 0\n"
      "s_mov_b32 s81, 2\n"
      "s_waitcnt lgkmcnt(1)\n"
      "s_cbranch_vccnz .Lg_123\n"
      ".Lg_124:\n"
      "v_mov_b32 v48, v24\n"
      "v_mov_b32 v49, v24\n"
      "v_mov_b32 v50, v25\n"
      "v_mov_b32 v51, v25\n"
      "v_mov_b32 v52, v26\n"
      "v_mov_b32 v53, v26\n"
      "ds_read_b128 v[24:27], v30 offset:1024\n"
      "ds_read_b128 v[32:35], v4\n"
      "ds_read_b128 v[36:39], v5\n"
      "ds_read_b128 v[40:43], v6\n"
      "ds_read_b128 v[44:47], v7\n"
      "v_pk_mul_f32 v[54:55], v[80:81], v[80:81]\n"
      "v_pk_mul_f32 v[56:57], v[82:83], v[82:83]\n"
      "v_pk_mul_f32 v[58:59], v[84:85], v[84:85]\n"
      "v_pk_mul_f32 v[60:61], v[86:87], v[86:87]\n"
      "v_pk_fma_f32 v[54:55], v[54:55], v[50:51], v[48:49]\n"
      "v_pk_fma_f32 v[56:57], v[56:57], v[50:51], v[48:49]\n"
      "v_pk_fma_f32 v[58:59], v[58:59], v[50:51], v[48:49]\n"
      "v_pk_fma_f32 v[60:61], v[60:61], v[50:51], v[48:49]\n"
      "v_pk_mul_f32 v[54:55], v[80:81], v[54:55]\n"
      "v_pk_mul_f32 v[56:57], v[82:83], v[56:57]\n"
      "v_pk_mul_f32 v[58:59], v[84:85], v[58:59]\n"
      "v_pk_mul_f32 v[60:61], v[86:87], v[60:61]\n"
      "v_exp_f32 v54, v54\n"
      "v_exp_f32 v55, v55\n"
      "v_exp_f32 v56, v56\n"
      "v_exp_f32 v57, v57\n"
      "v_exp_f32 v58, v58\n"
      "v_exp_f32 v59, v59\n"
      "v_exp_f32 v60, v60\n"
      "v_exp_f32 v61, v61\n"
      "v_pk_fma_f32 v[54:55], v[54:55], v[52:53], v[52:53]\n"
      "v_pk_fma_f32 v[56:57], v[56:57], v[52:53], v[52:53]\n"
      "v_pk_fma_f32 v[58:59], v[58:59], v[52:53], v[52:53]\n"
      "v_pk_fma_f32 v[60:61], v[60:61], v[52:53], v[52:53]\n"
      "v_rcp_f32 v54, v54\n"
      "v_rcp_f32 v55, v55\n"
      "v_rcp_f32 v56, v56\n"
      "v_rcp_f32 v57, v57\n"
      "v_rcp_f32 v58, v58\n"
      "v_rcp_f32 v59, v59\n"
      "v_rcp_f32 v60, v60\n"
      "v_rcp_f32 v61, v61\n"
      "v_pk_mul_f32 v[54:55], v[80:81], v[54:55]\n"
      "v_pk_mul_f32 v[56:57], v[82:83], v[56:57]\n"
      "v_pk_mul_f32 v[58:59], v[84:85], v[58:59]\n"
      "v_pk_mul_f32 v[60:61], v[86:87], v[60:61]\n"
      "v_cvt_pk_bf16_f32 v16, v54, v55\n"
      "v_cvt_pk_bf16_f32 v17, v56, v57\n"
      "v_cvt_pk_bf16_f32 v18, v58, v59\n"
      "v_cvt_pk_bf16_f32 v19, v60, v61\n"
      "buffer_store_dwordx4 v[16:19], v29, s[20:23], s74 offen offset:0 sc1\n"
      "v_pk_mul_f32 v[54:55], v[88:89], v[88:89]\n"
      "v_pk_mul_f32 v[56:57], v[90:91], v[90:91]\n"
      "v_pk_mul_f32 v[58:59], v[92:93], v[92:93]\n"
      "v_pk_mul_f32 v[60:61], v[94:95], v[94:95]\n"
      "v_pk_fma_f32 v[54:55], v[54:55], v[50:51], v[48:49]\n"
      "v_pk_fma_f32 v[56:57], v[56:57], v[50:51], v[48:49]\n"
      "v_pk_fma_f32 v[58:59], v[58:59], v[50:51], v[48:49]\n"
      "v_pk_fma_f32 v[60:61], v[60:61], v[50:51], v[48:49]\n"
      "v_pk_mul_f32 v[54:55], v[88:89], v[54:55]\n"
      "v_pk_mul_f32 v[56:57], v[90:91], v[56:57]\n"
      "v_pk_mul_f32 v[58:59], v[92:93], v[58:59]\n"
      "v_pk_mul_f32 v[60:61], v[94:95], v[60:61]\n"
      "v_exp_f32 v54, v54\n"
      "v_exp_f32 v55, v55\n"
      "v_exp_f32 v56, v56\n"
      "v_exp_f32 v57, v57\n"
      "v_exp_f32 v58, v58\n"
      "v_exp_f32 v59, v59\n"
      "v_exp_f32 v60, v60\n"
      "v_exp_f32 v61, v61\n"
      "v_pk_fma_f32 v[54:55], v[54:55], v[52:53], v[52:53]\n"
      "v_pk_fma_f32 v[56:57], v[56:57], v[52:53], v[52:53]\n"
      "v_pk_fma_f32 v[58:59], v[58:59], v[52:53], v[52:53]\n"
      "v_pk_fma_f32 v[60:61], v[60:61], v[52:53], v[52:53]\n"
      "v_rcp_f32 v54, v54\n"
      "v_rcp_f32 v55, v55\n"
      "v_rcp_f32 v56, v56\n"
      "v_rcp_f32 v57, v57\n"
      "v_rcp_f32 v58, v58\n"
      "v_rcp_f32 v59, v59\n"
      "v_rcp_f32 v60, v60\n"
      "v_rcp_f32 v61, v61\n"
      "v_pk_mul_f32 v[54:55], v[88:89], v[54:55]\n"
      "v_pk_mul_f32 v[56:57], v[90:91], v[56:57]\n"
      "v_pk_mul_f32 v[58:59], v[92:93], v[58:59]\n"
      "v_pk_mul_f32 v[60:61], v[94:95], v[60:61]\n"
      "v_cvt_pk_bf16_f32 v20, v54, v55\n"
      "v_cvt_pk_bf16_f32 v21, v56, v57\n"
      "v_cvt_pk_bf16_f32 v22, v58, v59\n"
      "v_cvt_pk_bf16_f32 v23, v60, v61\n"
      "buffer_store_dwordx4 v[20:23], v29, s[20:23], s74 offen offset:256 sc1\n"
      "ds_read_b128 v[48:51], v4 offset:4096\n"
      "ds_read_b128 v[52:55], v5 offset:4096\n"
      "ds_read_b128 v[56:59], v6 offset:4096\n"
      "ds_read_b128 v[60:63], v7 offset:4096\n"
      "s_waitcnt lgkmcnt(8)\n"
      "v_mfma_f32_32x32x16_bf16 v[80:95], v[8:11], v[12:15], 0\n"
      "s_cbranch_vccnz .Lg_125\n"
      ".Lg_126:\n"
      "ds_read_b64 v[12:13], v30 offset:3088\n"
      "s_nop 0\n"
      "s_waitcnt lgkmcnt(5)\n"
      "v_mfma_f32_32x32x16_bf16 v[80:95], v[96:99], v[32:35], v[80:95]\n"
      "ds_read_b128 v[32:35], v4 offset:8192\n"
      "v_mul_f32 v20, v64, v64\n"
      "v_fma_f32 v20, v20, v25, v24\n"
      "v_mul_f32 v20, v64, v20\n"
      "v_exp_f32 v20, v20\n"
      "v_mfma_f32_32x32x16_bf16 v[80:95], v[100:103], v[36:39], v[80:95]\n"
      "ds_read_b128 v[36:39], v5 offset:8192\n"
      "v_mul_f32 v21, v65, v65\n"
      "v_fma_f32 v20, v20, v26, v26\n"
      "v_fma_f32 v21, v21, v25, v24\n"
      "v_mfma_f32_32x32x16_bf16 v[80:95], v[104:107], v[40:43], v[80:95]\n"
      "ds_read_b128 v[40:43], v6 offset:8192\n"
      "v_rcp_f32 v20, v20\n"
      "v_mul_f32 v21, v65, v21\n"
      "v_mul_f32 v20, v64, v20\n"
      "v_exp_f32 v21, v21\n"
      "v_mfma_f32_32x32x16_bf16 v[80:95], v[108:111], v[44:47], v[80:95]\n"
      "ds_read_b128 v[44:47], v7 offset:8192\n"
      "v_mul_f32 v22, v66, v66\n"
      "v_fma_f32 v21, v21, v26, v26\n"
      "v_fma_f32 v22, v22, v25, v24\n"
      "s_waitcnt lgkmcnt(4)\n"
      "v_mfma_f32_32x32x16_bf16 v[80:95], v[112:115], v[48:51], v[80:95]\n"
      "ds_read_b128 v[48:51], v4 offset:12288\n"
      "v_rcp_f32 v21, v21\n"
      "v_mul_f32 v22, v66, v22\n"
      "v_mul_f32 v21, v65, v21\n"
      "v_cvt_pk_bf16_f32 v16, v20, v21\n"
      "v_mfma_f32_32x32x16_bf16 v[80:95], v[116:119], v[52:55], v[80:95]\n"
      "ds_read_b128 v[52:55], v5 offset:12288\n"
      "v_exp_f32 v22, v22\n"
      "v_mul_f32 v23, v67, v67\n"
      "v_fma_f32 v22, v22, v26, v26\n"
      "v_mfma_f32_32x32x16_bf16 v[80:95], v[120:123], v[56:59], v[80:95]\n"
      "ds_read_b128 v[56:59], v6 offset:12288\n"
      "v_fma_f32 v23, v23, v25, v24\n"
      "v_rcp_f32 v22, v22\n"
      "v_mul_f32 v23, v67, v23\n"
      "v_mul_f32 v22, v66, v22\n"
      "v_mfma_f32_32x32x16_bf16 v[80:95], v[124:127], v[60:63], v[80:95]\n"
      "ds_read_b128 v[60:63], v7 offset:12288\n"
      "v_exp_f32 v23, v23\n"
      "v_mul_f32 v20, v68, v68\n"
      "s_waitcnt lgkmcnt(4)\n"
      "v_mfma_f32_32x32x16_bf16 v[80:95], v[128:131], v[32:35], v[80:95]\n"
      "ds_read_b128 v[32:35], v4 offset:16384\n"
      "v_fma_f32 v23, v23, v26, v26\n"
      "v_fma_f32 v20, v20, v25, v24\n"
      "v_rcp_f32 v23, v23\n"
      "ds_read_b32 v31, v2 offset:2072\n"
      "v_mfma_f32_32x32x16_bf16 v[80:95], v[132:135], v[36:39], v[80:95]\n"
      "ds_read_b128 v[36:39], v5 offset:16384\n"
      "v_mul_f32 v20, v68, v20\n"
      "v_mul_f32 v23, v67, v23\n"
      "v_cvt_pk_bf16_f32 v17, v22, v23\n"
      "v_exp_f32 v20, v20\n"
      "v_mfma_f32_32x32x16_bf16 v[80:95], v[136:139], v[40:43], v[80:95]\n"
      "ds_read_b128 v[40:43], v6 offset:16384\n"
      "v_mul_f32 v21, v69, v69\n"
      "v_fma_f32 v20, v20, v26, v26\n"
      "v_fma_f32 v21, v21, v25, v24\n"
      "v_mfma_f32_32x32x16_bf16 v[80:95], v[140:143], v[44:47], v[80:95]\n"
      "ds_read_b128 v[44:47], v7 offset:16384\n"
      "v_rcp_f32 v20, v20\n"
      "v_mul_f32 v21, v69, v21\n"
      "v_mul_f32 v20, v68, v20\n"
      "s_waitcnt lgkmcnt(3)\n"
      "v_mfma_f32_32x32x16_bf16 v[80:95], v[144:147], v[48:51], v[80:95]\n"
      "v_readfirstlane_b32 s2, v31\n"
      "s_cmp_lt_u32 s2, 4\n"
      "s_cbranch_scc1 .Lw_127\n"
      ".Lw_128:\n"
      "ds_read_b128 v[48:51], v4 offset:20480\n"
      "v_exp_f32 v21, v21\n"
      "v_mfma_f32_32x32x16_bf16 v[80:95], v[148:151], v[52:55], v[80:95]\n"
      "ds_read_b128 v[52:55], v5 offset:20480\n"
      "v_mul_f32 v22, v70, v70\n"
      "v_fma_f32 v21, v21, v26, v26\n"
      "v_fma_f32 v22, v22, v25, v24\n"
      "v_mfma_f32_32x32x16_bf16 v[80:95], v[152:155], v[56:59], v[80:95]\n"
      "ds_read_b128 v[56:59], v6 offset:20480\n"
      "v_rcp_f32 v21, v21\n"
      "v_mul_f32 v22, v70, v22\n"
      "v_mul_f32 v21, v69, v21\n"
      "v_cvt_pk_bf16_f32 v18, v20, v21\n"
      "v_mfma_f32_32x32x16_bf16 v[80:95], v[156:159], v[60:63], v[80:95]\n"
      "ds_read_b128 v[60:63], v7 offset:20480\n"
      "v_exp_f32 v22, v22\n"
      "v_mul_f32 v23, v71, v71\n"
      "v_fma_f32 v22, v22, v26, v26\n"
      "s_waitcnt lgkmcnt(4)\n"
      "v_mfma_f32_32x32x16_bf16 v[80:95], v[160:163], v[32:35], v[80:95]\n"
      "ds_read_b128 v[32:35], v4 offset:24576\n"
      "v_fma_f32 v23, v23, v25, v24\n"
      "v_rcp_f32 v22, v22\n"
      "v_mul_f32 v23, v71, v23\n"
      "v_mfma_f32_32x32x16_bf16 v[80:95], v[164:167], v[36:39], v[80:95]\n"
      "ds_read_b128 v[36:39], v5 offset:24576\n"
      "v_mul_f32 v22, v70, v22\n"
      "v_exp_f32 v23, v23\n"
      "v_mul_f32 v20, v72, v72\n"
      "v_fma_f32 v23, v23, v26, v26\n"
      "v_mfma_f32_32x32x16_bf16 v[80:95], v[168:171], v[40:43], v[80:95]\n"
      "ds_read_b128 v[40:43], v6 offset:24576\n"
      "v_fma_f32 v20, v20, v25, v24\n"
      "v_rcp_f32 v23, v23\n"
      "v_mul_f32 v20, v72, v20\n"
      "v_mfma_f32_32x32x16_bf16 v[80:95], v[172:175], v[44:47], v[80:95]\n"
      "ds_read_b128 v[44:47], v7 offset:24576\n"
      "v_mul_f32 v23, v71, v23\n"
      "v_cvt_pk_bf16_f32 v19, v22, v23\n"
      "buffer_store_dwordx4 v[16:19], v29, s[20:23], s75 offen offset:0 sc1\n"
      "s_waitcnt lgkmcnt(4)\n"
      "v_mfma_f32_32x32x16_bf16 v[80:95], v[176:179], v[48:51], v[80:95]\n"
      "ds_read_b128 v[48:51], v4 offset:28672\n"
      "v_exp_f32 v20, v20\n"
      "v_mul_f32 v21, v73, v73\n"
      "v_mfma_f32_32x32x16_bf16 v[80:95], v[180:183], v[52:55], v[80:95]\n"
      "ds_read_b128 v[52:55], v5 offset:28672\n"
      "v_fma_f32 v20, v20, v26, v26\n"
      "v_fma_f32 v21, v21, v25, v24\n"
      "v_rcp_f32 v20, v20\n"
      "v_mul_f32 v21, v73, v21\n"
      "v_mfma_f32_32x32x16_bf16 v[80:95], v[184:187], v[56:59], v[80:95]\n"
      "ds_read_b128 v[56:59], v6 offset:28672\n"
      "v_mul_f32 v20, v72, v20\n"
      "v_exp_f32 v21, v21\n"
      "v_mul_f32 v22, v74, v74\n"
      "v_mfma_f32_32x32x16_bf16 v[80:95], v[188:191], v[60:63], v[80:95]\n"
      "ds_read_b128 v[60:63], v7 offset:28672\n"
      "v_fma_f32 v21, v21, v26, v26\n"
      "v_fma_f32 v22, v22, v25, v24\n"
      "v_rcp_f32 v21, v21\n"
      "s_waitcnt lgkmcnt(4)\n"
      "v_mfma_f32_32x32x16_bf16 v[80:95], v[192:195], v[32:35], v[80:95]\n"
      "ds_read_b128 v[32:35], v4 offset:32768\n"
      "v_mul_f32 v22, v74, v22\n"
      "v_mul_f32 v21, v73, v21\n"
      "v_cvt_pk_bf16_f32 v16, v20, v21\n"
      "v_exp_f32 v22, v22\n"
      "v_mfma_f32_32x32x16_bf16 v[80:95], v[196:199], v[36:39], v[80:95]\n"
      "ds_read_b128 v[36:39], v5 offset:32768\n"
      "v_mul_f32 v23, v75, v75\n"
      "v_fma_f32 v22, v22, v26, v26\n"
      "v_fma_f32 v23, v23, v25, v24\n"
      "v_mfma_f32_32x32x16_bf16 v[80:95], v[200:203], v[40:43], v[80:95]\n"
      "ds_read_b128 v[40:43], v6 offset:32768\n"
      "v_rcp_f32 v22, v22\n"
      "v_mul_f32 v23, v75, v23\n"
      "v_mul_f32 v22, v74, v22\n"
      "v_exp_f32 v23, v23\n"
      "v_mfma_f32_32x32x16_bf16 v[80:95], v[204:207], v[44:47], v[80:95]\n"
      "ds_read_b128 v[44:47], v7 offset:32768\n"
      "v_mul_f32 v20, v76, v76\n"
      "v_fma_f32 v23, v23, v26, v26\n"
      "v_fma_f32 v20, v20, v25, v24\n"
      "s_waitcnt lgkmcnt(4)\n"
      "v_mfma_f32_32x32x16_bf16 v[80:95], v[208:211], v[48:51], v[80:95]\n"
      "ds_read_b128 v[48:51], v4 offset:36864\n"
      "v_rcp_f32 v23, v23\n"
      "v_mul_f32 v20, v76, v20\n"
      "ds_read_b32 v31, v2 offset:3096\n"
      "v_mfma_f32_32x32x16_bf16 v[80:95], v[212:215], v[52:55], v[80:95]\n"
      "ds_read_b128 v[52:55], v5 offset:36864\n"
      "v_mul_f32 v23, v75, v23\n"
      "v_cvt_pk_bf16_f32 v17, v22, v23\n"
      "v_exp_f32 v20, v20\n"
      "v_mul_f32 v21, v77, v77\n"
      "v_mfma_f32_32x32x16_bf16 v[80:95], v[216:219], v[56:59], v[80:95]\n"
      "ds_read_b128 v[56:59], v6 offset:36864\n"
      "v_fma_f32 v20, v20, v26, v26\n"
      "v_fma_f32 v21, v21, v25, v24\n"
      "v_rcp_f32 v20, v20\n"
      "v_mfma_f32_32x32x16_bf16 v[80:95], v[220:223], v[60:63], v[80:95]\n"
      "ds_read_b128 v[60:63], v7 offset:36864\n"
      "v_mul_f32 v21, v77, v21\n"
      "v_mul_f32 v20, v76, v20\n"
      "v_exp_f32 v21, v21\n"
      "s_waitcnt lgkmcnt(3)\n"
      "v_mfma_f32_32x32x16_bf16 v[80:95], v[224:227], v[32:35], v[80:95]\n"
      "v_readfirstlane_b32 s2, v31\n"
      "s_cmp_lt_u32 s2, 4\n"
      "s_cbranch_scc1 .Lw_130\n"
      ".Lw_131:\n"
      "ds_read_b128 v[32:35], v4 offset:49152\n"
      "v_mul_f32 v22, v78, v78\n"
      "v_mfma_f32_32x32x16_bf16 v[80:95], v[228:231], v[36:39], v[80:95]\n"
      "ds_read_b128 v[36:39], v5 offset:49152\n"
      "v_fma_f32 v21, v21, v26, v26\n"
      "v_fma_f32 v22, v22, v25, v24\n"
      "v_rcp_f32 v21, v21\n"
      "v_mfma_f32_32x32x16_bf16 v[80:95], v[232:235], v[40:43], v[80:95]\n"
      "ds_read_b128 v[40:43], v6 offset:49152\n"
      "v_mul_f32 v22, v78, v22\n"
      "v_mul_f32 v21, v77, v21\n"
      "v_cvt_pk_bf16_f32 v18, v20, v21\n"
      "v_exp_f32 v22, v22\n"
      "v_mfma_f32_32x32x16_bf16 v[80:95], v[236:239], v[44:47], v[80:95]\n"
      "ds_read_b128 v[44:47], v7 offset:49152\n"
      "v_mul_f32 v23, v79, v79\n"
      "v_fma_f32 v22, v22, v26, v26\n"
      "v_fma_f32 v23, v23, v25, v24\n"
      "s_waitcnt lgkmcnt(4)\n"
      "v_mfma_f32_32x32x16_bf16 v[80:95], v[240:243], v[48:51], v[80:95]\n"
      "ds_read_b128 v[48:51], v4 offset:53248\n"
      "v_rcp_f32 v22, v22\n"
      "v_mul_f32 v23, v79, v23\n"
      "v_mul_f32 v22, v78, v22\n"
      "v_exp_f32 v23, v23\n"
      "v_mfma_f32_32x32x16_bf16 v[80:95], v[244:247], v[52:55], v[80:95]\n"
      "ds_read_b128 v[52:55], v5 offset:53248\n"
      "v_fma_f32 v23, v23, v26, v26\n"
      "v_rcp_f32 v23, v23\n"
      "s_nop 0\n"
      "v_mul_f32 v23, v79, v23\n"
      "v_mfma_f32_32x32x16_bf16 v[80:95], v[248:251], v[56:59], v[80:95]\n"
      "ds_read_b128 v[56:59], v6 offset:53248\n"
      "v_cvt_pk_bf16_f32 v19, v22, v23\n"
      "buffer_store_dwordx4 v[16:19], v29, s[20:23], s75 offen offset:256 sc1\n"
      "ds_read_b128 v[24:27], v30 offset:2048\n"
      "v_mfma_f32_32x32x16_bf16 v[64:79], v[252:255], v[60:63], v[80:95]\n"
      "ds_read_b128 v[60:63], v7 offset:53248\n"
      "s_nop 3\n"
      "v_mfma_f32_32x32x16_bf16 v[80:95], v[8:11], v[12:15], 0\n"
      "s_cbranch_vccnz .Lg_133\n"
      ".Lg_134:\n"
      "s_nop 1\n"
      "s_waitcnt lgkmcnt(1)\n"
      "v_mfma_f32_32x32x16_bf16 v[80:95], v[96:99], v[32:35], v[80:95]\n"
      "ds_read_b128 v[32:35], v4 offset:57344\n"
      "v_mul_f32 v20, v64, v64\n"
      "v_fma_f32 v20, v20, v25, v24\n"
      "v_mul_f32 v20, v64, v20\n"
      "v_exp_f32 v20, v20\n"
      "v_mfma_f32_32x32x16_bf16 v[80:95], v[100:103], v[36:39], v[80:95]\n"
      "ds_read_b128 v[36:39], v5 offset:57344\n"
      "v_mul_f32 v21, v65, v65\n"
      "v_fma_f32 v20, v20, v26, v26\n"
      "v_fma_f32 v21, v21, v25, v24\n"
      "v_mov_b64 v[12:13], s[58:59]\n"
      "v_mfma_f32_32x32x16_bf16 v[80:95], v[104:107], v[40:43], v[80:95]\n"
      "ds_read_b128 v[40:43], v6 offset:57344\n"
      "v_rcp_f32 v20, v20\n"
      "v_mul_f32 v21, v65, v21\n"
      "v_mul_f32 v20, v64, v20\n"
      "v_exp_f32 v21, v21\n"
      "v_mfma_f32_32x32x16_bf16 v[80:95], v[108:111], v[44:47], v[80:95]\n"
      "ds_read_b128 v[44:47], v7 offset:57344\n"
      "v_mul_f32 v22, v66, v66\n"
      "v_fma_f32 v21, v21, v26, v26\n"
      "v_fma_f32 v22, v22, v25, v24\n"
      "s_waitcnt lgkmcnt(4)\n"
      "v_mfma_f32_32x32x16_bf16 v[80:95], v[112:115], v[48:51], v[80:95]\n"
      "ds_read_b128 v[48:51], v4 offset:61440\n"
      "v_rcp_f32 v21, v21\n"
      "v_mul_f32 v22, v66, v22\n"
      "v_mul_f32 v21, v65, v21\n"
      "v_cvt_pk_bf16_f32 v16, v20, v21\n"
      "v_mfma_f32_32x32x16_bf16 v[80:95], v[116:119], v[52:55], v[80:95]\n"
      "ds_read_b128 v[52:55], v5 offset:61440\n"
      "v_exp_f32 v22, v22\n"
      "v_mul_f32 v23, v67, v67\n"
      "v_fma_f32 v22, v22, v26, v26\n"
      "v_add_u32 v4, 49152, v4\n"
      "v_mfma_f32_32x32x16_bf16 v[80:95], v[120:123], v[56:59], v[80:95]\n"
      "ds_read_b128 v[56:59], v6 offset:61440\n"
      "v_fma_f32 v23, v23, v25, v24\n"
      "v_rcp_f32 v22, v22\n"
      "v_mul_f32 v23, v67, v23\n"
      "v_add_u32 v5, 49152, v5\n"
      "v_mfma_f32_32x32x16_bf16 v[80:95], v[124:127], v[60:63], v[80:95]\n"
      "ds_read_b128 v[60:63], v7 offset:61440\n"
      "v_mul_f32 v22, v66, v22\n"
      "v_exp_f32 v23, v23\n"
      "v_add_u32 v6, 49152, v6\n"
      "s_waitcnt lgkmcnt(4)\n"
      "v_mfma_f32_32x32x16_bf16 v[80:95], v[128:131], v[32:35], v[80:95]\n"
      "ds_read_b128 v[32:35], v4 offset:16384\n"
      "v_mul_f32 v20, v68, v68\n"
      "v_fma_f32 v23, v23, v26, v26\n"
      "v_fma_f32 v20, v20, v25, v24\n"
      "v_add_u32 v7, 49152, v7\n"
      "v_mfma_f32_32x32x16_bf16 v[80:95], v[132:135], v[36:39], v[80:95]\n"
      "ds_read_b128 v[36:39], v5 offset:16384\n"
      "v_rcp_f32 v23, v23\n"
      "v_mul_f32 v20, v68, v20\n"
      "v_mul_f32 v23, v67, v23\n"
      "v_cvt_pk_bf16_f32 v17, v22, v23\n"
      "v_mfma_f32_32x32x16_bf16 v[80:95], v[136:139], v[40:43], v[80:95]\n"
      "ds_read_b128 v[40:43], v6 offset:16384\n"
      "v_exp_f32 v20, v20\n"
      "v_mul_f32 v21, v69, v69\n"
      "v_fma_f32 v20, v20, v26, v26\n"
      "v_fma_f32 v21, v21, v25, v24\n"
      "v_mfma_f32_32x32x16_bf16 v[80:95], v[140:143], v[44:47], v[80:95]\n"
      "ds_read_b128 v[44:47], v7 offset:16384\n"
      "v_rcp_f32 v20, v20\n"
      "v_mul_f32 v21, v69, v21\n"
      "v_mul_f32 v20, v68, v20\n"
      "s_waitcnt lgkmcnt(4)\n"
      "v_mfma_f32_32x32x16_bf16 v[80:95], v[144:147], v[48:51], v[80:95]\n"
      "ds_read_b128 v[48:51], v4 offset:20480\n"
      "v_exp_f32 v21, v21\n"
      "v_mul_f32 v22, v70, v70\n"
      "v_fma_f32 v21, v21, v26, v26\n"
      "v_fma_f32 v22, v22, v25, v24\n"
      "v_mfma_f32_32x32x16_bf16 v[80:95], v[148:151], v[52:55], v[80:95]\n"
      "ds_read_b128 v[52:55], v5 offset:20480\n"
      "s_getreg_b32 s2, hwreg(HW_REG_IB_STS, 0, 4)\n"
      "s_add_u32 s2, s2, s84\n"
      "s_cmp_le_u32 s2, 0\n"
      "s_cbranch_scc1 .Lh_135\n"
      ".Lh_136:\n"
      "v_mfma_f32_32x32x16_bf16 v[80:95], v[152:155], v[56:59], v[80:95]\n"
      "ds_read_b128 v[56:59], v6 offset:20480\n"
      "v_rcp_f32 v21, v21\n"
      "v_mul_f32 v22, v70, v22\n"
      "v_mul_f32 v21, v69, v21\n"
      "v_cvt_pk_bf16_f32 v18, v20, v21\n"
      "v_mfma_f32_32x32x16_bf16 v[80:95], v[156:159], v[60:63], v[80:95]\n"
      "ds_read_b128 v[60:63], v7 offset:20480\n"
      "v_exp_f32 v22, v22\n"
      "v_mul_f32 v23, v71, v71\n"
      "v_fma_f32 v22, v22, v26, v26\n"
      "s_waitcnt lgkmcnt(4)\n"
      "v_mfma_f32_32x32x16_bf16 v[80:95], v[160:163], v[32:35], v[80:95]\n"
      "ds_read_b128 v[32:35], v4 offset:24576\n"
      "v_fma_f32 v23, v23, v25, v24\n"
      "v_rcp_f32 v22, v22\n"
      "v_mul_f32 v23, v71, v23\n"
      "v_mul_f32 v22, v70, v22\n"
      "v_mfma_f32_32x32x16_bf16 v[80:95], v[164:167], v[36:39], v[80:95]\n"
      "ds_read_b128 v[36:39], v5 offset:24576\n"
      "v_exp_f32 v23, v23\n"
      "v_mul_f32 v20, v72, v72\n"
      "v_fma_f32 v23, v23, v26, v26\n"
      "v_fma_f32 v20, v20, v25, v24\n"
      "v_mfma_f32_32x32x16_bf16 v[80:95], v[168:171], v[40:43], v[80:95]\n"
      "ds_read_b128 v[40:43], v6 offset:24576\n"
      "v_rcp_f32 v23, v23\n"
      "v_mul_f32 v20, v72, v20\n"
      "v_mul_f32 v23, v71, v23\n"
      "v_cvt_pk_bf16_f32 v19, v22, v23\n"
      "v_mfma_f32_32x32x16_bf16 v[80:95], v[172:175], v[44:47], v[80:95]\n"
      "ds_read_b128 v[44:47], v7 offset:24576\n"
      "buffer_store_dwordx4 v[16:19], v29, s[20:23], s76 offen offset:0 sc1\n"
      "s_waitcnt lgkmcnt(4)\n"
      "v_mfma_f32_32x32x16_bf16 v[80:95], v[176:179], v[48:51], v[80:95]\n"
      "ds_read_b128 v[48:51], v4 offset:28672\n"
      "v_exp_f32 v20, v20\n"
      "v_mul_f32 v21, v73, v73\n"
      "v_fma_f32 v20, v20, v26, v26\n"
      "v_fma_f32 v21, v21, v25, v24\n"
      "v_mfma_f32_32x32x16_bf16 v[80:95], v[180:183], v[52:55], v[80:95]\n"
      "ds_read_b128 v[52:55], v5 offset:28672\n"
      "s_getreg_b32 s2, hwreg(HW_REG_IB_STS, 0, 4)\n"
      "s_add_u32 s2, s2, s84\n"
      "s_cmp_le_u32 s2, 1\n"
      "s_cbranch_scc1 .Lh_137\n"
      ".Lh_138:\n"
      "v_mfma_f32_32x32x16_bf16 v[80:95], v[184:187], v[56:59], v[80:95]\n"
      "ds_read_b128 v[56:59], v6 offset:28672\n"
      "v_rcp_f32 v20, v20\n"
      "v_mul_f32 v21, v73, v21\n"
      "v_mul_f32 v20, v72, v20\n"
      "v_exp_f32 v21, v21\n"
      "v_mfma_f32_32x32x16_bf16 v[80:95], v[188:191], v[60:63], v[80:95]\n"
      "ds_read_b128 v[60:63], v7 offset:28672\n"
      "v_mul_f32 v22, v74, v74\n"
      "v_fma_f32 v21, v21, v26, v26\n"
      "v_fma_f32 v22, v22, v25, v24\n"
      "s_waitcnt lgkmcnt(4)\n"
      "v_mfma_f32_32x32x16_bf16 v[80:95], v[192:195], v[32:35], v[80:95]\n"
      "ds_read_b128 v[32:35], v4 offset:32768\n"
      "v_rcp_f32 v21, v21\n"
      "v_mul_f32 v22, v74, v22\n"
      "v_mul_f32 v21, v73, v21\n"
      "v_cvt_pk_bf16_f32 v16, v20, v21\n"
      "v_mfma_f32_32x32x16_bf16 v[80:95], v[196:199], v[36:39], v[80:95]\n"
      "ds_read_b128 v[36:39], v5 offset:32768\n"
      "v_exp_f32 v22, v22\n"
      "v_mul_f32 v23, v75, v75\n"
      "v_fma_f32 v22, v22, v26, v26\n"
      "v_fma_f32 v23, v23, v25, v24\n"
      "v_mfma_f32_32x32x16_bf16 v[80:95], v[200:203], v[40:43], v[80:95]\n"
      "ds_read_b128 v[40:43], v6 offset:32768\n"
      "v_rcp_f32 v22, v22\n"
      "v_mul_f32 v23, v75, v23\n"
      "v_mul_f32 v22, v74, v22\n"
      "v_mfma_f32_32x32x16_bf16 v[80:95], v[204:207], v[44:47], v[80:95]\n"
      "ds_read_b128 v[44:47], v7 offset:32768\n"
      "v_exp_f32 v23, v23\n"
      "v_mul_f32 v20, v76, v76\n"
      "v_fma_f32 v23, v23, v26, v26\n"
      "s_waitcnt lgkmcnt(4)\n"
      "v_mfma_f32_32x32x16_bf16 v[80:95], v[208:211], v[48:51], v[80:95]\n"
      "ds_read_b128 v[48:51], v4 offset:36864\n"
      "v_fma_f32 v20, v20, v25, v24\n"
      "v_rcp_f32 v23, v23\n"
      "v_mul_f32 v20, v76, v20\n"
      "v_mul_f32 v23, v75, v23\n"
      "v_mfma_f32_32x32x16_bf16 v[80:95], v[212:215], v[52:55], v[80:95]\n"
      "ds_read_b128 v[52:55], v5 offset:36864\n"
      "s_getreg_b32 s2, hwreg(HW_REG_IB_STS, 0, 4)\n"
      "s_add_u32 s2, s2, s84\n"
      "s_cmp_le_u32 s2, 1\n"
      "s_cbranch_scc1 .Lh_139\n"
      ".Lh_140:\n"
      "v_mfma_f32_32x32x16_bf16 v[80:95], v[216:219], v[56:59], v[80:95]\n"
      "ds_read_b128 v[56:59], v6 offset:36864\n"
      "v_cvt_pk_bf16_f32 v17, v22, v23\n"
      "v_exp_f32 v20, v20\n"
      "v_mul_f32 v21, v77, v77\n"
      "v_fma_f32 v20, v20, v26, v26\n"
      "v_mfma_f32_32x32x16_bf16 v[80:95], v[220:223], v[60:63], v[80:95]\n"
      "ds_read_b128 v[60:63], v7 offset:36864\n"
      "v_fma_f32 v21, v21, v25, v24\n"
      "v_rcp_f32 v20, v20\n"
      "v_mul_f32 v21, v77, v21\n"
      "s_waitcnt lgkmcnt(4)\n"
      "v_mfma_f32_32x32x16_bf16 v[80:95], v[224:227], v[32:35], v[80:95]\n"
      "v_mul_f32 v20, v76, v20\n"
      "v_exp_f32 v21, v21\n"
      "v_mul_f32 v22, v78, v78\n"
      "v_fma_f32 v21, v21, v26, v26\n"
      "v_fma_f32 v22, v22, v25, v24\n"
      "v_mfma_f32_32x32x16_bf16 v[80:95], v[228:231], v[36:39], v[80:95]\n"
      "v_rcp_f32 v21, v21\n"
      "v_mul_f32 v22, v78, v22\n"
      "v_mul_f32 v21, v77, v21\n"
      "v_xor_b32 v31, s86, v28\n"
      "ds_read_b64 v[218:219], v31\n"
      "v_mfma_f32_32x32x16_bf16 v[80:95], v[232:235], v[40:43], v[80:95]\n"
      "v_cvt_pk_bf16_f32 v18, v20, v21\n"
      "v_exp_f32 v22, v22\n"
      "v_mul_f32 v23, v79, v79\n"
      "v_xor_b32 v31, s87, v28\n"
      "ds_read_b64 v[220:221], v31\n"
      "v_mfma_f32_32x32x16_bf16 v[80:95], v[236:239], v[44:47], v[80:95]\n"
      "v_fma_f32 v22, v22, v26, v26\n"
      "v_fma_f32 v23, v23, v25, v24\n"
      "v_xor_b32 v31, s88, v28\n"
      "ds_read_b64 v[222:223], v31\n"
      "s_waitcnt lgkmcnt(3)\n"
      "v_mfma_f32_32x32x16_bf16 v[80:95], v[240:243], v[48:51], v[80:95]\n"
      "v_rcp_f32 v22, v22\n"
      "v_mul_f32 v23, v79, v23\n"
      "v_mul_f32 v22, v78, v22\n"
      "v_xor_b32 v31, s89, v28\n"
      "ds_read_b64 v[224:225], v31\n"
      "v_mfma_f32_32x32x16_bf16 v[80:95], v[244:247], v[52:55], v[80:95]\n"
      "v_exp_f32 v23, v23\n"
      "s_nop 0\n"
      "v_fma_f32 v23, v23, v26, v26\n"
      "v_rcp_f32 v23, v23\n"
      "v_xor_b32 v31, s90, v28\n"
      "ds_read_b64 v[226:227], v31\n"
      "v_mfma_f32_32x32x16_bf16 v[80:95], v[248:251], v[56:59], v[80:95]\n"
      "v_mul_f32 v23, v79, v23\n"
      "v_cvt_pk_bf16_f32 v19, v22, v23\n"
      "buffer_store_dwordx4 v[16:19], v29, s[20:23], s76 offen offset:256 sc1\n"
      "ds_read_b128 v[24:27], v30 offset:3072\n"
      "v_mfma_f32_32x32x16_bf16 v[64:79], v[252:255], v[60:63], v[80:95]\n"
      "ds_add_u32 v2, v3 offset:4276\n"
      "s_getreg_b32 s2, hwreg(HW_REG_IB_STS, 0, 4)\n"
      "s_add_u32 s2, s2, s84\n"
      "s_cmp_le_u32 s2, 2\n"
      "s_cbranch_scc1 .Lh_141\n"
      ".Lh_142:\n"
      "s_waitcnt lgkmcnt(0)\n"
      "s_cbranch_vccnz .Lg_143\n"
      ".Lg_144:\n"
      "v_mov_b32 v32, v24\n"
      "v_mov_b32 v33, v24\n"
      "v_mov_b32 v34, v25\n"
      "v_mov_b32 v35, v25\n"
      "v_mov_b32 v36, v26\n"
      "v_mov_b32 v37, v26\n"
      "v_pk_mul_f32 v[38:39], v[64:65], v[64:65]\n"
      "v_pk_mul_f32 v[40:41], v[66:67], v[66:67]\n"
      "v_pk_mul_f32 v[42:43], v[68:69], v[68:69]\n"
      "v_pk_mul_f32 v[44:45], v[70:71], v[70:71]\n"
      "v_pk_fma_f32 v[38:39], v[38:39], v[34:35], v[32:33]\n"
      "v_pk_fma_f32 v[40:41], v[40:41], v[34:35], v[32:33]\n"
      "v_pk_fma_f32 v[42:43], v[42:43], v[34:35], v[32:33]\n"
      "v_pk_fma_f32 v[44:45], v[44:45], v[34:35], v[32:33]\n"
      "v_pk_mul_f32 v[38:39], v[64:65], v[38:39]\n"
      "v_pk_mul_f32 v[40:41], v[66:67], v[40:41]\n"
      "v_pk_mul_f32 v[42:43], v[68:69], v[42:43]\n"
      "v_pk_mul_f32 v[44:45], v[70:71], v[44:45]\n"
      "v_exp_f32 v38, v38\n"
      "v_exp_f32 v39, v39\n"
      "v_exp_f32 v40, v40\n"
      "v_exp_f32 v41, v41\n"
      "v_exp_f32 v42, v42\n"
      "v_exp_f32 v43, v43\n"
      "v_exp_f32 v44, v44\n"
      "v_exp_f32 v45, v45\n"
      "v_pk_fma_f32 v[38:39], v[38:39], v[36:37], v[36:37]\n"
      "v_pk_fma_f32 v[40:41], v[40:41], v[36:37], v[36:37]\n"
      "v_pk_fma_f32 v[42:43], v[42:43], v[36:37], v[36:37]\n"
      "v_pk_fma_f32 v[44:45], v[44:45], v[36:37], v[36:37]\n"
      "v_rcp_f32 v38, v38\n"
      "v_rcp_f32 v39, v39\n"
      "v_rcp_f32 v40, v40\n"
      "v_rcp_f32 v41, v41\n"
      "v_rcp_f32 v42, v42\n"
      "v_rcp_f32 v43, v43\n"
      "v_rcp_f32 v44, v44\n"
      "v_rcp_f32 v45, v45\n"
      "v_pk_mul_f32 v[38:39], v[64:65], v[38:39]\n"
      "v_pk_mul_f32 v[40:41], v[66:67], v[40:41]\n"
      "v_pk_mul_f32 v[42:43], v[68:69], v[42:43]\n"
      "v_pk_mul_f32 v[44:45], v[70:71], v[44:45]\n"
      "v_cvt_pk_bf16_f32 v16, v38, v39\n"
      "v_cvt_pk_bf16_f32 v17, v40, v41\n"
      "v_cvt_pk_bf16_f32 v18, v42, v43\n"
      "v_cvt_pk_bf16_f32 v19, v44, v45\n"
      "buffer_store_dwordx4 v[16:19], v29, s[20:23], s77 offen offset:0 sc1\n"
      "v_pk_mul_f32 v[38:39], v[72:73], v[72:73]\n"
      "v_pk_mul_f32 v[40:41], v[74:75], v[74:75]\n"
      "v_pk_mul_f32 v[42:43], v[76:77], v[76:77]\n"
      "v_pk_mul_f32 v[44:45], v[78:79], v[78:79]\n"
      "v_pk_fma_f32 v[38:39], v[38:39], v[34:35], v[32:33]\n"
      "v_pk_fma_f32 v[40:41], v[40:41], v[34:35], v[32:33]\n"
      "v_pk_fma_f32 v[42:43], v[42:43], v[34:35], v[32:33]\n"
      "v_pk_fma_f32 v[44:45], v[44:45], v[34:35], v[32:33]\n"
      "v_pk_mul_f32 v[38:39], v[72:73], v[38:39]\n"
      "v_pk_mul_f32 v[40:41], v[74:75], v[40:41]\n"
      "v_pk_mul_f32 v[42:43], v[76:77], v[42:43]\n"
      "v_pk_mul_f32 v[44:45], v[78:79], v[44:45]\n"
      "v_exp_f32 v38, v38\n"
      "v_exp_f32 v39, v39\n"
      "v_exp_f32 v40, v40\n"
      "v_exp_f32 v41, v41\n"
      "v_exp_f32 v42, v42\n"
      "v_exp_f32 v43, v43\n"
      "v_exp_f32 v44, v44\n"
      "v_exp_f32 v45, v45\n"
      "v_pk_fma_f32 v[38:39], v[38:39], v[36:37], v[36:37]\n"
      "v_pk_fma_f32 v[40:41], v[40:41], v[36:37], v[36:37]\n"
      "v_pk_fma_f32 v[42:43], v[42:43], v[36:37], v[36:37]\n"
      "v_pk_fma_f32 v[44:45], v[44:45], v[36:37], v[36:37]\n"
      "ds_read_b32 v31, v2 offset:4304\n"
      "v_rcp_f32 v38, v38\n"
      "v_rcp_f32 v39, v39\n"
      "v_rcp_f32 v40, v40\n"
      "v_rcp_f32 v41, v41\n"
      "v_rcp_f32 v42, v42\n"
      "v_rcp_f32 v43, v43\n"
      "v_rcp_f32 v44, v44\n"
      "v_rcp_f32 v45, v45\n"
      "v_pk_mul_f32 v[38:39], v[72:73], v[38:39]\n"
      "v_pk_mul_f32 v[40:41], v[74:75], v[40:41]\n"
      "v_pk_mul_f32 v[42:43], v[76:77], v[42:43]\n"
      "v_pk_mul_f32 v[44:45], v[78:79], v[44:45]\n"
      "v_cvt_pk_bf16_f32 v20, v38, v39\n"
      "v_cvt_pk_bf16_f32 v21, v40, v41\n"
      "v_cvt_pk_bf16_f32 v22, v42, v43\n"
      "v_cvt_pk_bf16_f32 v23, v44, v45\n"
      "buffer_store_dwordx4 v[20:23], v29, s[20:23], s77 offen offset:256 sc1\n"
      "s_getreg_b32 s2, hwreg(HW_REG_IB_STS, 0, 4)\n"
      "s_add_u32 s2, s2, s84\n"
      "s_cmp_le_u32 s2, 4\n"
      "s_cbranch_scc1 .Lh_145\n"
      ".Lh_146:\n"
      ".LCONV:\n"
      "v_and_b32 v10, 1, v1\n"
      "v_xor_b32 v10, 1, v10\n"
      "v_mul_u32_u24 v10, 4160, v10\n"
      "v_and_b32 v11, 14, v1\n"
      "v_lshl_add_u32 v10, v11, 1, v10\n"
      "v_and_b32 v11, 48, v1\n"
      "v_sub_u32 v10, v10, v11\n"
      "v_and_b32 v21, 15, v1\n"
      "v_lshl_add_u32 v11, v21, 5, v11\n"
      "s_lshr_b32 s64, s53, 1\n"
      "s_and_b32 s65, s53, 1\n"
      "s_lshl_b32 s66, s65, 8\n"
      "s_mul_i32 s67, s81, 26112\n"
      "s_cmp_eq_u32 s81, 2\n"
      "s_cselect_b32 s67, 122880, s67\n"
      "s_mul_i32 s62, s64, 8320\n"
      "s_add_u32 s62, s62, s66\n"
      "s_add_u32 s62, s62, s67\n"
      "s_add_u32 s62, s62, 84\n"
      "v_add_u32 v212, s62, v10\n"
      "s_mul_i32 s62, s64, 4608\n"
      "s_sub_u32 s63, s62, s66\n"
      "s_add_u32 s63, s63, s67\n"
      "s_add_u32 s63, s63, 16928\n"
      "v_add_u32 v217, s63, v11\n"
      "s_lshl_b32 s62, s81, 5\n"
      "v_add_u32 v30, s62, v2\n"
      "s_waitcnt lgkmcnt(0)\n"
      "v_readfirstlane_b32 s2, v31\n"
      "s_cmp_lt_u32 s2, 4\n"
      "s_cbranch_scc1 .Lw_147\n"
      ".Lw_148:\n"
      "v_add_u32 v213, 1024, v212\n"
      "v_add_u32 v214, 2048, v212\n"
      "v_add_u32 v215, 3072, v212\n"
      "ds_read2_b32 v[144:145], v212 offset0:3 offset1:2\n"
      "ds_read2_b32 v[146:147], v212 offset0:1 offset1:0\n"
      "ds_read_b128 v[188:191], v217 offset:1728\n"
      "ds_read_b128 v[184:187], v217 offset:1216\n"
      "ds_read_b128 v[180:183], v217 offset:704\n"
      "ds_read_b128 v[176:179], v217 offset:192\n"
      "ds_read2_b32 v[148:149], v212 offset0:131 offset1:130\n"
      "ds_read2_b32 v[150:151], v212 offset0:129 offset1:128\n"
      "ds_read2_b32 v[152:153], v213 offset0:3 offset1:2\n"
      "ds_read2_b32 v[154:155], v213 offset0:1 offset1:0\n"
      "s_waitcnt lgkmcnt(7)\n"
      "v_mfma_f32_16x16x32_bf16 v[108:111], v[144:147], v[188:191], 0\n"
      "ds_read2_b32 v[156:157], v213 offset0:131 offset1:130\n"
      "s_waitcnt lgkmcnt(7)\n"
      "v_mfma_f32_16x16x32_bf16 v[104:107], v[144:147], v[184:187], 0\n"
      "ds_read2_b32 v[158:159], v213 offset0:129 offset1:128\n"
      "s_waitcnt lgkmcnt(7)\n"
      "v_mfma_f32_16x16x32_bf16 v[100:103], v[144:147], v[180:183], 0\n"
      "ds_read2_b32 v[160:161], v214 offset0:3 offset1:2\n"
      "v_lshlrev_b32 v9, 4, v1\n"
      "s_waitcnt lgkmcnt(7)\n"
      "v_mfma_f32_16x16x32_bf16 v[96:99], v[144:147], v[176:179], 0\n"
      "ds_read2_b32 v[162:163], v214 offset0:1 offset1:0\n"
      "s_and_b32 s63, s81, 1\n"
      "s_waitcnt lgkmcnt(6)\n"
      "v_mfma_f32_16x16x32_bf16 v[112:115], v[148:151], v[188:191], 0\n"
      "ds_read2_b32 v[164:165], v214 offset0:131 offset1:130\n"
      "s_mul_i32 s63, s63, 45056\n"
      "v_mfma_f32_16x16x32_bf16 v[108:111], v[148:151], v[184:187], v[108:111]\n"
      "ds_read2_b32 v[166:167], v214 offset0:129 offset1:128\n"
      "s_sub_u32 s63, 98304, s63\n"
      "v_mfma_f32_16x16x32_bf16 v[104:107], v[148:151], v[180:183], v[104:107]\n"
      "ds_read2_b32 v[168:169], v215 offset0:3 offset1:2\n"
      "s_lshl_b32 s62, s53, 12\n"
      "v_mfma_f32_16x16x32_bf16 v[100:103], v[148:151], v[176:179], v[100:103]\n"
      "ds_read2_b32 v[170:171], v215 offset0:1 offset1:0\n"
      "s_add_u32 s67, s62, s63\n"
      "s_waitcnt lgkmcnt(7)\n"
      "v_mfma_f32_16x16x32_bf16 v[116:119], v[152:155], v[188:191], 0\n"
      "ds_read2_b32 v[172:173], v215 offset0:131 offset1:130\n"
      "v_add_u32 v14, s67, v9\n"
      "v_mfma_f32_16x16x32_bf16 v[112:115], v[152:155], v[184:187], v[112:115]\n"
      "ds_read2_b32 v[174:175], v215 offset0:129 offset1:128\n"
      "s_xor_b32 s62, s62, 4096\n"
      "v_mfma_f32_16x16x32_bf16 v[108:111], v[152:155], v[180:183], v[108:111]\n"
      "ds_read2_b32 v[144:145], v212 offset0:19 offset1:18\n"
      "s_add_u32 s67, s62, s63\n"
      "v_mfma_f32_16x16x32_bf16 v[104:107], v[152:155], v[176:179], v[104:107]\n"
      "ds_read2_b32 v[146:147], v212 offset0:17 offset1:16\n"
      "v_add_u32 v15, s67, v9\n"
      "s_waitcnt lgkmcnt(7)\n"
      "v_mfma_f32_16x16x32_bf16 v[120:123], v[156:159], v[188:191], 0\n"
      "ds_read_b128 v[204:207], v217 offset:1664\n"
      "s_lshl_b32 s62, s53, 2\n"
      "v_mfma_f32_16x16x32_bf16 v[116:119], v[156:159], v[184:187], v[116:119]\n"
      "ds_read_b128 v[200:203], v217 offset:1152\n"
      "v_add_u32 v19, s62, v30\n"
      "v_mfma_f32_16x16x32_bf16 v[112:115], v[156:159], v[180:183], v[112:115]\n"
      "ds_read_b128 v[196:199], v217 offset:640\n"
      "s_xor_b32 s62, s62, 4\n"
      "v_mfma_f32_16x16x32_bf16 v[108:111], v[156:159], v[176:179], v[108:111]\n"
      "ds_read_b128 v[192:195], v217 offset:128\n"
      "v_add_u32 v20, s62, v30\n"
      "v_mfma_f32_16x16x32_bf16 v[124:127], v[160:163], v[188:191], 0\n"
      "ds_read2_b32 v[148:149], v212 offset0:147 offset1:146\n"
      "v_and_b32 v16, 15, v1\n"
      "v_mfma_f32_16x16x32_bf16 v[120:123], v[160:163], v[184:187], v[120:123]\n"
      "ds_read2_b32 v[150:151], v212 offset0:145 offset1:144\n"
      "v_lshrrev_b32 v17, 4, v1\n"
      "v_mfma_f32_16x16x32_bf16 v[116:119], v[160:163], v[180:183], v[116:119]\n"
      "ds_read2_b32 v[152:153], v213 offset0:19 offset1:18\n"
      "v_lshrrev_b32 v21, 5, v1\n"
      "v_mfma_f32_16x16x32_bf16 v[112:115], v[160:163], v[176:179], v[112:115]\n"
      "ds_read2_b32 v[154:155], v213 offset0:17 offset1:16\n"
      "v_lshlrev_b32 v21, 4, v21\n"
      "s_waitcnt lgkmcnt(7)\n"
      "v_mfma_f32_16x16x32_bf16 v[124:127], v[164:167], v[184:187], v[124:127]\n"
      "s_getreg_b32 s2, hwreg(HW_REG_IB_STS, 0, 4)\n"
      "s_add_u32 s2, s2, s84\n"
      "s_cmp_le_u32 s2, 4\n"
      "s_cbranch_scc1 .Lh_150\n"
      ".Lh_151:\n"
      "v_mfma_f32_16x16x32_bf16 v[120:123], v[164:167], v[180:183], v[120:123]\n"
      "ds_read2_b32 v[156:157], v213 offset0:147 offset1:146\n"
      "v_lshl_add_u32 v21, v16, 5, v21\n"
      "v_mfma_f32_16x16x32_bf16 v[116:119], v[164:167], v[176:179], v[116:119]\n"
      "ds_read2_b32 v[158:159], v213 offset0:145 offset1:144\n"
      "v_and_b32 v22, 1, v17\n"
      "v_mfma_f32_16x16x32_bf16 v[124:127], v[168:171], v[180:183], v[124:127]\n"
      "ds_read2_b32 v[160:161], v214 offset0:19 offset1:18\n"
      "v_lshl_add_u32 v5, v22, 9, v21\n"
      "v_mfma_f32_16x16x32_bf16 v[120:123], v[168:171], v[176:179], v[120:123]\n"
      "ds_read2_b32 v[162:163], v214 offset0:17 offset1:16\n"
      "s_lshl1_add_u32 s63, s81, s98\n"
      "v_mfma_f32_16x16x32_bf16 v[124:127], v[172:175], v[176:179], v[124:127]\n"
      "ds_read2_b32 v[164:165], v214 offset0:147 offset1:146\n"
      "s_add_u32 s63, s63, s64\n"
      "v_mfma_f32_16x16x32_bf16 v[108:111], v[144:147], v[204:207], v[108:111]\n"
      "ds_read2_b32 v[166:167], v214 offset0:145 offset1:144\n"
      "s_mul_i32 s63, s63, 4224\n"
      "s_waitcnt lgkmcnt(6)\n"
      "v_mfma_f32_16x16x32_bf16 v[104:107], v[144:147], v[200:203], v[104:107]\n"
      "ds_read2_b32 v[168:169], v215 offset0:19 offset1:18\n"
      "s_lshl_b32 s67, s65, 11\n"
      "v_mfma_f32_16x16x32_bf16 v[100:103], v[144:147], v[196:199], v[100:103]\n"
      "ds_read2_b32 v[170:171], v215 offset0:17 offset1:16\n"
      "s_add_u32 s63, s63, s67\n"
      "v_mfma_f32_16x16x32_bf16 v[96:99], v[144:147], v[192:195], v[96:99]\n"
      "ds_read2_b32 v[172:173], v215 offset0:147 offset1:146\n"
      "s_add_u32 s80, s63, 15872\n"
      "v_mfma_f32_16x16x32_bf16 v[112:115], v[148:151], v[204:207], v[112:115]\n"
      "s_getreg_b32 s2, hwreg(HW_REG_IB_STS, 0, 4)\n"
      "s_add_u32 s2, s2, s85\n"
      "s_cmp_le_u32 s2, 0\n"
      "s_cbranch_scc1 .Lh_152\n"
      ".Lh_153:\n"
      "v_mfma_f32_16x16x32_bf16 v[108:111], v[148:151], v[200:203], v[108:111]\n"
      "ds_read2_b32 v[174:175], v215 offset0:145 offset1:144\n"
      "s_sub_u32 s82, 3, s53\n"
      "v_mfma_f32_16x16x32_bf16 v[104:107], v[148:151], v[196:199], v[104:107]\n"
      "ds_read2_b32 v[144:145], v212 offset0:35 offset1:34\n"
      "s_lshr_b32 s63, s82, 1\n"
      "v_mfma_f32_16x16x32_bf16 v[100:103], v[148:151], v[192:195], v[100:103]\n"
      "ds_read2_b32 v[146:147], v212 offset0:33 offset1:32\n"
      "s_lshl1_add_u32 s63, s54, s63\n"
      "v_mfma_f32_16x16x32_bf16 v[116:119], v[152:155], v[204:207], v[116:119]\n"
      "ds_read_b128 v[188:191], v217 offset:1600\n"
      "s_xor_b32 s63, s63, s99\n"
      "v_mfma_f32_16x16x32_bf16 v[112:115], v[152:155], v[200:203], v[112:115]\n"
      "ds_read_b128 v[184:187], v217 offset:1088\n"
      "s_lshl_b32 s63, s63, 10\n"
      "v_mfma_f32_16x16x32_bf16 v[108:111], v[152:155], v[196:199], v[108:111]\n"
      "ds_read_b128 v[180:183], v217 offset:576\n"
      "s_and_b32 s67, s82, 1\n"
      "v_mfma_f32_16x16x32_bf16 v[104:107], v[152:155], v[192:195], v[104:107]\n"
      "ds_read_b128 v[176:179], v217 offset:64\n"
      "s_lshl_b32 s67, s67, 9\n"
      "s_waitcnt lgkmcnt(7)\n"
      "v_mfma_f32_16x16x32_bf16 v[120:123], v[156:159], v[204:207], v[120:123]\n"
      "ds_read2_b32 v[148:149], v212 offset0:163 offset1:162\n"
      "s_add_u32 s63, s63, s67\n"
      "v_mfma_f32_16x16x32_bf16 v[116:119], v[156:159], v[200:203], v[116:119]\n"
      "ds_read2_b32 v[150:151], v212 offset0:161 offset1:160\n"
      "s_add_u32 s63, s63, 157716\n"
      "v_mfma_f32_16x16x32_bf16 v[112:115], v[156:159], v[196:199], v[112:115]\n"
      "ds_read2_b32 v[152:153], v213 offset0:35 offset1:34\n"
      "v_lshl_add_u32 v21, v16, 5, s63\n"
      "v_mfma_f32_16x16x32_bf16 v[108:111], v[156:159], v[192:195], v[108:111]\n"
      "ds_read2_b32 v[154:155], v213 offset0:33 offset1:32\n"
      "ds_read_b32 v23, v21\n"
      "v_mfma_f32_16x16x32_bf16 v[124:127], v[160:163], v[204:207], v[124:127]\n"
      "s_cmp_eq_u32 s84, 0\n"
      "s_cbranch_scc1 .Lh_154\n"
      ".Lh_155:\n"
      "v_mfma_f32_16x16x32_bf16 v[120:123], v[160:163], v[200:203], v[120:123]\n"
      "ds_read2_b32 v[156:157], v213 offset0:163 offset1:162\n"
      "s_lshl1_add_u32 s62, s56, s54\n"
      "v_mfma_f32_16x16x32_bf16 v[116:119], v[160:163], v[196:199], v[116:119]\n"
      "ds_read2_b32 v[158:159], v213 offset0:161 offset1:160\n"
      "s_lshl2_add_u32 s63, s62, s82\n"
      "v_mfma_f32_16x16x32_bf16 v[112:115], v[160:163], v[192:195], v[112:115]\n"
      "ds_read2_b32 v[160:161], v214 offset0:35 offset1:34\n"
      "s_mul_i32 s70, s63, 20480\n"
      "v_mfma_f32_16x16x32_bf16 v[124:127], v[164:167], v[200:203], v[124:127]\n"
      "s_getreg_b32 s2, hwreg(HW_REG_IB_STS, 0, 4)\n"
      "s_add_u32 s2, s2, s85\n"
      "s_cmp_le_u32 s2, 0\n"
      "s_cbranch_scc1 .Lh_156\n"
      ".Lh_157:\n"
      "v_mfma_f32_16x16x32_bf16 v[120:123], v[164:167], v[196:199], v[120:123]\n"
      "ds_read2_b32 v[162:163], v214 offset0:33 offset1:32\n"
      "s_mul_i32 s63, s55, 160\n"
      "v_mfma_f32_16x16x32_bf16 v[116:119], v[164:167], v[192:195], v[116:119]\n"
      "ds_read2_b32 v[164:165], v214 offset0:163 offset1:162\n"
      "s_add_u32 s70, s70, s63\n"
      "v_mfma_f32_16x16x32_bf16 v[124:127], v[168:171], v[196:199], v[124:127]\n"
      "ds_read2_b32 v[166:167], v214 offset0:161 offset1:160\n"
      "v_mul_u32_u24 v4, 1280, v16\n"
      "v_mfma_f32_16x16x32_bf16 v[120:123], v[168:171], v[192:195], v[120:123]\n"
      "v_lshl_add_u32 v4, v17, 3, v4\n"
      "s_mul_i32 s63, s53, 5120\n"
      "s_waitcnt lgkmcnt(4)\n"
      "v_mfma_f32_16x16x32_bf16 v[124:127], v[172:175], v[192:195], v[124:127]\n"
      "ds_read2_b32 v[168:169], v215 offset0:35 offset1:34\n"
      "s_lshl2_add_u32 s72, s60, s53\n"
      "v_mfma_f32_16x16x32_bf16 v[108:111], v[144:147], v[188:191], v[108:111]\n"
      "ds_read2_b32 v[170:171], v215 offset0:33 offset1:32\n"
      "s_mul_i32 s72, s72, 3072\n"
      "v_mfma_f32_16x16x32_bf16 v[104:107], v[144:147], v[184:187], v[104:107]\n"
      "ds_read2_b32 v[172:173], v215 offset0:163 offset1:162\n"
      "s_add_u32 s72, s72, 12451840\n"
      "v_mfma_f32_16x16x32_bf16 v[100:103], v[144:147], v[180:183], v[100:103]\n"
      "ds_read2_b32 v[174:175], v215 offset0:161 offset1:160\n"
      "s_add_u32 s63, s63, 53248\n"
      "v_mfma_f32_16x16x32_bf16 v[96:99], v[144:147], v[176:179], v[96:99]\n"
      "ds_read2_b32 v[144:145], v212 offset0:51 offset1:50\n"
      "v_add_u32 v8, s63, v9\n"
      "v_mfma_f32_16x16x32_bf16 v[112:115], v[148:151], v[188:191], v[112:115]\n"
      "ds_read2_b32 v[146:147], v212 offset0:49 offset1:48\n"
      "s_lshl_b32 s74, s93, 16\n"
      "v_mfma_f32_16x16x32_bf16 v[108:111], v[148:151], v[184:187], v[108:111]\n"
      "ds_read_b128 v[204:207], v217 offset:1536\n"
      "s_lshl2_add_u32 s63, s55, s53\n"
      "v_mfma_f32_16x16x32_bf16 v[104:107], v[148:151], v[180:183], v[104:107]\n"
      "ds_read_b128 v[200:203], v217 offset:1024\n"
      "s_mul_i32 s73, s63, 84480\n"
      "v_mfma_f32_16x16x32_bf16 v[100:103], v[148:151], v[176:179], v[100:103]\n"
      "ds_read_b128 v[196:199], v217 offset:512\n"
      "s_lshl_b32 s62, s62, 7\n"
      "v_mfma_f32_16x16x32_bf16 v[116:119], v[152:155], v[188:191], v[116:119]\n"
      "ds_read_b128 v[192:195], v217 offset:0\n"
      "s_add_u32 s73, s73, s62\n"
      "v_mfma_f32_16x16x32_bf16 v[112:115], v[152:155], v[184:187], v[112:115]\n"
      "ds_read2_b32 v[148:149], v212 offset0:179 offset1:178\n"
      "s_add_u32 s73, s73, 32768\n"
      "v_mfma_f32_16x16x32_bf16 v[108:111], v[152:155], v[180:183], v[108:111]\n"
      "ds_read2_b32 v[150:151], v212 offset0:177 offset1:176\n"
      "s_mul_i32 s63, s63, 10\n"
      "v_mfma_f32_16x16x32_bf16 v[104:107], v[152:155], v[176:179], v[104:107]\n"
      "ds_read2_b32 v[152:153], v213 offset0:51 offset1:50\n"
      "v_min_u32 v21, 9, v1\n"
      "v_mfma_f32_16x16x32_bf16 v[120:123], v[156:159], v[188:191], v[120:123]\n"
      "ds_read2_b32 v[154:155], v213 offset0:49 offset1:48\n"
      "v_add_u32 v21, s63, v21\n"
      "v_mfma_f32_16x16x32_bf16 v[116:119], v[156:159], v[184:187], v[116:119]\n"
      "v_add_u32 v22, 448, v21\n"
      "v_mul_u32_u24 v22, 21846, v22\n"
      "v_mfma_f32_16x16x32_bf16 v[112:115], v[156:159], v[180:183], v[112:115]\n"
      "s_getreg_b32 s2, hwreg(HW_REG_IB_STS, 0, 4)\n"
      "s_add_u32 s2, s2, s85\n"
      "s_cmp_le_u32 s2, 0\n"
      "s_cbranch_scc1 .Lh_158\n"
      ".Lh_159:\n"
      "v_mfma_f32_16x16x32_bf16 v[108:111], v[156:159], v[176:179], v[108:111]\n"
      "ds_read2_b32 v[156:157], v213 offset0:179 offset1:178\n"
      "v_lshrrev_b32 v22, 16, v22\n"
      "s_waitcnt lgkmcnt(4)\n"
      "v_mfma_f32_16x16x32_bf16 v[124:127], v[160:163], v[188:191], v[124:127]\n"
      "ds_read2_b32 v[158:159], v213 offset0:177 offset1:176\n"
      "v_min_u32 v21, v21, v22\n"
      "v_mfma_f32_16x16x32_bf16 v[120:123], v[160:163], v[184:187], v[120:123]\n"
      "s_mov_b32 s63, 16078848\n"
      "v_lshl_add_u32 v25, v21, 7, s63\n"
      "v_mfma_f32_16x16x32_bf16 v[116:119], v[160:163], v[180:183], v[116:119]\n"
      "v_and_b32 v21, 7, v1\n"
      "v_lshrrev_b32 v22, 3, v1\n"
      "v_mfma_f32_16x16x32_bf16 v[112:115], v[160:163], v[176:179], v[112:115]\n"
      "ds_read2_b32 v[160:161], v214 offset0:51 offset1:50\n"
      "v_mul_u32_u24 v6, 4224, v22\n"
      "v_mfma_f32_16x16x32_bf16 v[124:127], v[164:167], v[184:187], v[124:127]\n"
      "ds_read2_b32 v[162:163], v214 offset0:49 offset1:48\n"
      "v_lshl_add_u32 v6, v21, 4, v6\n"
      "v_mfma_f32_16x16x32_bf16 v[120:123], v[164:167], v[180:183], v[120:123]\n"
      "s_lshl_b32 s62, s82, 5\n"
      "s_add_u32 s62, s62, 139520\n"
      "v_mfma_f32_16x16x32_bf16 v[116:119], v[164:167], v[176:179], v[116:119]\n"
      "ds_read2_b32 v[164:165], v214 offset0:179 offset1:178\n"
      "v_lshrrev_b32 v21, 2, v1\n"
      "v_mfma_f32_16x16x32_bf16 v[124:127], v[168:171], v[180:183], v[124:127]\n"
      "ds_read2_b32 v[166:167], v214 offset0:177 offset1:176\n"
      "v_lshl_add_u32 v21, v21, 7, s62\n"
      "v_mfma_f32_16x16x32_bf16 v[120:123], v[168:171], v[176:179], v[120:123]\n"
      "v_and_b32 v22, 3, v1\n"
      "v_lshl_add_u32 v7, v22, 3, v21\n"
      "v_mfma_f32_16x16x32_bf16 v[124:127], v[172:175], v[176:179], v[124:127]\n"
      "ds_read2_b32 v[168:169], v215 offset0:51 offset1:50\n"
      "s_mov_b32 s76, 0xbfb8aa3b\n"
      "v_mfma_f32_16x16x32_bf16 v[108:111], v[144:147], v[204:207], v[108:111]\n"
      "ds_read2_b32 v[170:171], v215 offset0:49 offset1:48\n"
      "s_mov_b32 s77, 0xbf317218\n"
      "v_mfma_f32_16x16x32_bf16 v[104:107], v[144:147], v[200:203], v[104:107]\n"
      "ds_read2_b32 v[172:173], v215 offset0:179 offset1:178\n"
      "v_and_b32 v21, s92, v23\n"
      "v_mfma_f32_16x16x32_bf16 v[100:103], v[144:147], v[196:199], v[100:103]\n"
      "ds_read2_b32 v[174:175], v215 offset0:177 offset1:176\n"
      "v_mfma_f32_16x16x32_bf16 v[96:99], v[144:147], v[192:195], v[96:99]\n"
      "ds_read2_b32 v[144:145], v212 offset0:3 offset1:2\n"
      "s_waitcnt lgkmcnt(6)\n"
      "v_mfma_f32_16x16x32_bf16 v[112:115], v[148:151], v[204:207], v[112:115]\n"
      "ds_read2_b32 v[146:147], v212 offset0:1 offset1:0\n"
      "v_mfma_f32_16x16x32_bf16 v[108:111], v[148:151], v[200:203], v[108:111]\n"
      "v_rcp_f32 v21, v21\n"
      "v_mfma_f32_16x16x32_bf16 v[104:107], v[148:151], v[196:199], v[104:107]\n"
      "s_getreg_b32 s2, hwreg(HW_REG_IB_STS, 0, 4)\n"
      "s_add_u32 s2, s2, s85\n"
      "s_cmp_le_u32 s2, 0\n"
      "s_cbranch_scc1 .Lh_160\n"
      ".Lh_161:\n"
      "v_mfma_f32_16x16x32_bf16 v[100:103], v[148:151], v[192:195], v[100:103]\n"
      "ds_read2_b32 v[148:149], v212 offset0:131 offset1:130\n"
      "v_lshlrev_b32 v22, 16, v23\n"
      "v_mfma_f32_16x16x32_bf16 v[116:119], v[152:155], v[204:207], v[116:119]\n"
      "ds_read_b32 v31, v2 offset:4280\n"
      "v_mul_f32 v21, s77, v21\n"
      "v_mfma_f32_16x16x32_bf16 v[112:115], v[152:155], v[200:203], v[112:115]\n"
      "ds_read2_b32 v[150:151], v212 offset0:129 offset1:128\n"
      "v_mul_f32 v22, v22, v21\n"
      "v_mfma_f32_16x16x32_bf16 v[108:111], v[152:155], v[196:199], v[108:111]\n"
      "v_lshlrev_b32 v32, 16, v218\n"
      "v_and_b32 v33, s92, v218\n"
      "v_mfma_f32_16x16x32_bf16 v[104:107], v[152:155], v[192:195], v[104:107]\n"
      "ds_read2_b32 v[152:153], v213 offset0:3 offset1:2\n"
      "v_lshlrev_b32 v34, 16, v219\n"
      "v_mfma_f32_16x16x32_bf16 v[120:123], v[156:159], v[204:207], v[120:123]\n"
      "ds_read2_b32 v[154:155], v213 offset0:1 offset1:0\n"
      "v_and_b32 v35, s92, v219\n"
      "v_mfma_f32_16x16x32_bf16 v[116:119], v[156:159], v[200:203], v[116:119]\n"
      "v_lshlrev_b32 v36, 16, v220\n"
      "v_and_b32 v37, s92, v220\n"
      "v_mfma_f32_16x16x32_bf16 v[112:115], v[156:159], v[196:199], v[112:115]\n"
      "s_waitcnt lgkmcnt(3)\n"
      "v_readfirstlane_b32 s2, v31\n"
      "s_cmp_lt_u32 s2, 4\n"
      "s_cbranch_scc1 .Lw_162\n"
      ".Lw_163:\n"
      "v_mfma_f32_16x16x32_bf16 v[108:111], v[156:159], v[192:195], v[108:111]\n"
      "ds_read2_b32 v[156:157], v213 offset0:131 offset1:130\n"
      "v_lshlrev_b32 v38, 16, v221\n"
      "v_mfma_f32_16x16x32_bf16 v[124:127], v[160:163], v[204:207], v[124:127]\n"
      "ds_read_b128 v[204:207], v217 offset:3776\n"
      "v_and_b32 v39, s92, v221\n"
      "v_mfma_f32_16x16x32_bf16 v[120:123], v[160:163], v[200:203], v[120:123]\n"
      "ds_read2_b32 v[158:159], v213 offset0:129 offset1:128\n"
      "v_lshlrev_b32 v40, 16, v222\n"
      "v_mfma_f32_16x16x32_bf16 v[116:119], v[160:163], v[196:199], v[116:119]\n"
      "ds_read_b128 v[188:191], v217 offset:3712\n"
      "v_and_b32 v41, s92, v222\n"
      "v_mfma_f32_16x16x32_bf16 v[112:115], v[160:163], v[192:195], v[112:115]\n"
      "ds_read2_b32 v[160:161], v212 offset0:19 offset1:18\n"
      "v_lshlrev_b32 v42, 16, v223\n"
      "v_mfma_f32_16x16x32_bf16 v[124:127], v[164:167], v[200:203], v[124:127]\n"
      "ds_read_b128 v[200:203], v217 offset:3264\n"
      "v_and_b32 v43, s92, v223\n"
      "v_mfma_f32_16x16x32_bf16 v[120:123], v[164:167], v[196:199], v[120:123]\n"
      "ds_read2_b32 v[162:163], v212 offset0:17 offset1:16\n"
      "v_lshlrev_b32 v44, 16, v224\n"
      "v_mfma_f32_16x16x32_bf16 v[116:119], v[164:167], v[192:195], v[116:119]\n"
      "ds_read_b128 v[184:187], v217 offset:3200\n"
      "v_and_b32 v45, s92, v224\n"
      "v_mfma_f32_16x16x32_bf16 v[124:127], v[168:171], v[196:199], v[124:127]\n"
      "ds_read_b128 v[196:199], v217 offset:2752\n"
      "v_lshlrev_b32 v46, 16, v225\n"
      "v_mfma_f32_16x16x32_bf16 v[120:123], v[168:171], v[192:195], v[120:123]\n"
      "s_cmp_eq_u32 s85, 0\n"
      "s_cbranch_scc1 .Lh_165\n"
      ".Lh_166:\n"
      "v_mfma_f32_16x16x32_bf16 v[124:127], v[172:175], v[192:195], v[124:127]\n"
      "ds_read_b128 v[192:195], v217 offset:2240\n"
      "v_and_b32 v47, s92, v225\n"
      "s_waitcnt lgkmcnt(6)\n"
      "v_mfma_f32_16x16x32_bf16 v[124:127], v[144:147], v[204:207], v[124:127]\n"
      "ds_read_b128 v[180:183], v217 offset:2688\n"
      "v_lshlrev_b32 v48, 16, v226\n"
      "s_waitcnt lgkmcnt(5)\n"
      "v_mfma_f32_16x16x32_bf16 v[120:123], v[144:147], v[200:203], v[120:123]\n"
      "ds_read_b128 v[176:179], v217 offset:2176\n"
      "v_and_b32 v49, s92, v226\n"
      "s_waitcnt lgkmcnt(3)\n"
      "v_mfma_f32_16x16x32_bf16 v[116:119], v[144:147], v[196:199], v[116:119]\n"
      "ds_read2_b32 v[164:165], v212 offset0:147 offset1:146\n"
      "v_lshlrev_b32 v50, 16, v227\n"
      "s_waitcnt lgkmcnt(3)\n"
      "v_mfma_f32_16x16x32_bf16 v[112:115], v[144:147], v[192:195], v[112:115]\n"
      "ds_read2_b32 v[166:167], v212 offset0:145 offset1:144\n"
      "v_and_b32 v51, s92, v227\n"
      "v_mfma_f32_16x16x32_bf16 v[124:127], v[148:151], v[200:203], v[124:127]\n"
      "ds_read2_b32 v[168:169], v213 offset0:19 offset1:18\n"
      "v_fma_f32 v52, v32, v21, v22\n"
      "v_mfma_f32_16x16x32_bf16 v[120:123], v[148:151], v[196:199], v[120:123]\n"
      "ds_read2_b32 v[170:171], v213 offset0:17 offset1:16\n"
      "v_fma_f32 v53, v33, v21, v22\n"
      "v_mfma_f32_16x16x32_bf16 v[116:119], v[148:151], v[192:195], v[116:119]\n"
      "ds_read2_b32 v[172:173], v213 offset0:147 offset1:146\n"
      "v_fma_f32 v54, v34, v21, v22\n"
      "v_mfma_f32_16x16x32_bf16 v[124:127], v[152:155], v[196:199], v[124:127]\n"
      "ds_read2_b32 v[174:175], v213 offset0:145 offset1:144\n"
      "v_fma_f32 v55, v35, v21, v22\n"
      "v_mfma_f32_16x16x32_bf16 v[120:123], v[152:155], v[192:195], v[120:123]\n"
      "ds_read_b32 v31, v2 offset:4276\n"
      "v_fma_f32 v56, v36, v21, v22\n"
      "v_mfma_f32_16x16x32_bf16 v[124:127], v[156:159], v[192:195], v[124:127]\n"
      "ds_read2_b32 v[144:145], v212 offset0:35 offset1:34\n"
      "ds_read2_b32 v[146:147], v212 offset0:33 offset1:32\n"
      "v_mfma_f32_16x16x32_bf16 v[124:127], v[160:163], v[188:191], v[124:127]\n"
      "ds_read_b128 v[204:207], v217 offset:3648\n"
      "ds_read_b128 v[200:203], v217 offset:3136\n"
      "v_mfma_f32_16x16x32_bf16 v[120:123], v[160:163], v[184:187], v[120:123]\n"
      "ds_read_b128 v[196:199], v217 offset:2624\n"
      "v_fma_f32 v57, v37, v21, v22\n"
      "s_waitcnt lgkmcnt(9)\n"
      "v_mfma_f32_16x16x32_bf16 v[116:119], v[160:163], v[180:183], v[116:119]\n"
      "ds_read_b128 v[192:195], v217 offset:2112\n"
      "v_fma_f32 v58, v38, v21, v22\n"
      "v_mfma_f32_16x16x32_bf16 v[112:115], v[160:163], v[176:179], v[112:115]\n"
      "ds_read2_b32 v[148:149], v212 offset0:163 offset1:162\n"
      "ds_read2_b32 v[150:151], v212 offset0:161 offset1:160\n"
      "v_mfma_f32_16x16x32_bf16 v[124:127], v[164:167], v[184:187], v[124:127]\n"
      "ds_read2_b32 v[152:153], v213 offset0:35 offset1:34\n"
      "v_fma_f32 v59, v39, v21, v22\n"
      "v_mfma_f32_16x16x32_bf16 v[120:123], v[164:167], v[180:183], v[120:123]\n"
      "ds_read2_b32 v[154:155], v213 offset0:33 offset1:32\n"
      "v_fma_f32 v60, v40, v21, v22\n"
      "v_mfma_f32_16x16x32_bf16 v[116:119], v[164:167], v[176:179], v[116:119]\n"
      "s_waitcnt lgkmcnt(8)\n"
      "v_readfirstlane_b32 s2, v31\n"
      "s_cmp_lt_u32 s2, 4\n"
      "s_cbranch_scc1 .Lw_167\n"
      ".Lw_168:\n"
      "v_mfma_f32_16x16x32_bf16 v[124:127], v[168:171], v[180:183], v[124:127]\n"
      "ds_read2_b32 v[156:157], v213 offset0:163 offset1:162\n"
      "v_fma_f32 v61, v41, v21, v22\n"
      "v_mfma_f32_16x16x32_bf16 v[120:123], v[168:171], v[176:179], v[120:123]\n"
      "ds_read2_b32 v[158:159], v213 offset0:161 offset1:160\n"
      "v_fma_f32 v62, v42, v21, v22\n"
      "v_mfma_f32_16x16x32_bf16 v[124:127], v[172:175], v[176:179], v[124:127]\n"
      "ds_read2_b32 v[160:161], v212 offset0:51 offset1:50\n"
      "ds_read2_b32 v[162:163], v212 offset0:49 offset1:48\n"
      "s_waitcnt lgkmcnt(7)\n"
      "v_mfma_f32_16x16x32_bf16 v[124:127], v[144:147], v[204:207], v[124:127]\n"
      "ds_read_b128 v[188:191], v217 offset:3584\n"
      "ds_read_b128 v[184:187], v217 offset:3072\n"
      "v_mfma_f32_16x16x32_bf16 v[120:123], v[144:147], v[200:203], v[120:123]\n"
      "ds_read_b128 v[180:183], v217 offset:2560\n"
      "v_fma_f32 v63, v43, v21, v22\n"
      "v_mfma_f32_16x16x32_bf16 v[116:119], v[144:147], v[196:199], v[116:119]\n"
      "ds_read_b128 v[176:179], v217 offset:2048\n"
      "v_fma_f32 v64, v44, v21, v22\n"
      "v_mfma_f32_16x16x32_bf16 v[112:115], v[144:147], v[192:195], v[112:115]\n"
      "ds_read2_b32 v[164:165], v212 offset0:179 offset1:178\n"
      "ds_read2_b32 v[166:167], v212 offset0:177 offset1:176\n"
      "s_waitcnt lgkmcnt(10)\n"
      "v_mfma_f32_16x16x32_bf16 v[124:127], v[148:151], v[200:203], v[124:127]\n"
      "ds_read2_b32 v[168:169], v213 offset0:51 offset1:50\n"
      "v_fma_f32 v65, v45, v21, v22\n"
      "v_mfma_f32_16x16x32_bf16 v[120:123], v[148:151], v[196:199], v[120:123]\n"
      "ds_read2_b32 v[170:171], v213 offset0:49 offset1:48\n"
      "v_fma_f32 v66, v46, v21, v22\n"
      "v_mfma_f32_16x16x32_bf16 v[116:119], v[148:151], v[192:195], v[116:119]\n"
      "ds_read2_b32 v[172:173], v213 offset0:179 offset1:178\n"
      "v_fma_f32 v67, v47, v21, v22\n"
      "v_mfma_f32_16x16x32_bf16 v[124:127], v[152:155], v[196:199], v[124:127]\n"
      "ds_read2_b32 v[174:175], v213 offset0:177 offset1:176\n"
      "v_fma_f32 v68, v48, v21, v22\n"
      "v_mfma_f32_16x16x32_bf16 v[120:123], v[152:155], v[192:195], v[120:123]\n"
      "v_fma_f32 v69, v49, v21, v22\n"
      "v_fma_f32 v70, v50, v21, v22\n"
      "s_waitcnt lgkmcnt(7)\n"
      "v_mfma_f32_16x16x32_bf16 v[124:127], v[156:159], v[192:195], v[124:127]\n"
      "v_fma_f32 v71, v51, v21, v22\n"
      "v_mfma_f32_16x16x32_bf16 v[124:127], v[160:163], v[188:191], v[124:127]\n"
      "v_mfma_f32_16x16x32_bf16 v[120:123], v[160:163], v[184:187], v[120:123]\n"
      "v_mfma_f32_16x16x32_bf16 v[116:119], v[160:163], v[180:183], v[116:119]\n"
      "s_waitcnt lgkmcnt(2)\n"
      "v_mfma_f32_16x16x32_bf16 v[112:115], v[160:163], v[176:179], v[112:115]\n"
      "v_mfma_f32_16x16x32_bf16 v[124:127], v[164:167], v[184:187], v[124:127]\n"
      "v_mfma_f32_16x16x32_bf16 v[120:123], v[164:167], v[180:183], v[120:123]\n"
      "v_mfma_f32_16x16x32_bf16 v[116:119], v[164:167], v[176:179], v[116:119]\n"
      "v_mfma_f32_16x16x32_bf16 v[124:127], v[168:171], v[180:183], v[124:127]\n"
      "v_mfma_f32_16x16x32_bf16 v[120:123], v[168:171], v[176:179], v[120:123]\n"
      "s_waitcnt lgkmcnt(0)\n"
      "v_mfma_f32_16x16x32_bf16 v[124:127], v[172:175], v[176:179], v[124:127]\n"
      "s_setprio 3\n"
      "s_cmp_eq_u32 s65, 0\n"
      "s_cbranch_scc1 .Lx_170\n"
      "ds_write_b128 v14, v[96:99] offset:0\n"
      "ds_write_b128 v14, v[100:103] offset:1024\n"
      "ds_write_b128 v14, v[104:107] offset:2048\n"
      "ds_write_b128 v14, v[108:111] offset:3072\n"
      "ds_add_u32 v19, v3 offset:4248\n"
      "v_mov_b64 v[96:97], v[112:113]\n"
      "v_mov_b64 v[98:99], v[114:115]\n"
      "v_mov_b64 v[100:101], v[116:117]\n"
      "v_mov_b64 v[102:103], v[118:119]\n"
      "v_mov_b64 v[104:105], v[120:121]\n"
      "v_mov_b64 v[106:107], v[122:123]\n"
      "v_mov_b64 v[108:109], v[124:125]\n"
      "v_mov_b64 v[110:111], v[126:127]\n"
      "s_branch .Lx_171\n"
      ".Lx_170:\n"
      "ds_write_b128 v14, v[112:115] offset:0\n"
      "ds_write_b128 v14, v[116:119] offset:1024\n"
      "s_nop 0\n"
      "ds_write_b128 v14, v[120:123] offset:2048\n"
      "s_nop 0\n"
      "ds_write_b128 v14, v[124:127] offset:3072\n"
      "ds_add_u32 v19, v3 offset:4248\n"
      ".Lx_171:\n"
      "s_mov_b32 s3, 0x80000\n"
      ".Lp_172:\n"
      "ds_read_b32 v31, v20 offset:4248\n"
      "s_sub_u32 s3, s3, 1\n"
      "s_waitcnt lgkmcnt(0)\n"
      "v_readlane_b32 s2, v31, 0\n"
      "s_cselect_b32 s2, -1, s2\n"
      "s_cmp_lt_u32 s2, 1\n"
      "s_cbranch_scc1 .Lp_172\n"
      "ds_read_b128 v[72:75], v15 offset:0\n"
      "ds_read_b128 v[76:79], v15 offset:1024\n"
      "ds_read_b128 v[80:83], v15 offset:2048\n"
      "ds_read_b128 v[84:87], v15 offset:3072\n"
      "ds_add_u32 v30, v3 offset:4244\n"
      "s_waitcnt lgkmcnt(3)\n"
      "v_add_f32 v96, v96, v72\n"
      "v_add_f32 v97, v97, v73\n"
      "v_add_f32 v98, v98, v74\n"
      "v_add_f32 v99, v99, v75\n"
      "v_add_f32 v100, v100, v76\n"
      "v_add_f32 v101, v101, v77\n"
      "v_add_f32 v102, v102, v78\n"
      "v_add_f32 v103, v103, v79\n"
      "v_cvt_pk_bf16_f32 v88, v96, v97\n"
      "v_cvt_pk_bf16_f32 v90, v100, v101\n"
      "v_cvt_pk_bf16_f32 v89, v98, v99\n"
      "v_cvt_pk_bf16_f32 v91, v102, v103\n"
      "v_permlane16_swap_b32 v88, v90\n"
      "s_nop 0\n"
      "v_permlane16_swap_b32 v89, v91\n"
      "buffer_store_dwordx4 v[88:91], v5, s[20:23], s80 offen offset:0 sc1\n"
      "s_waitcnt lgkmcnt(1)\n"
      "v_add_f32 v104, v104, v80\n"
      "v_add_f32 v105, v105, v81\n"
      "v_add_f32 v106, v106, v82\n"
      "v_add_f32 v107, v107, v83\n"
      "v_add_f32 v108, v108, v84\n"
      "v_add_f32 v109, v109, v85\n"
      "v_add_f32 v110, v110, v86\n"
      "v_add_f32 v111, v111, v87\n"
      "v_cvt_pk_bf16_f32 v92, v104, v105\n"
      "v_cvt_pk_bf16_f32 v94, v108, v109\n"
      "v_cvt_pk_bf16_f32 v93, v106, v107\n"
      "v_cvt_pk_bf16_f32 v95, v110, v111\n"
      "v_permlane16_swap_b32 v92, v94\n"
      "s_nop 0\n"
      "v_permlane16_swap_b32 v93, v95\n"
      "buffer_store_dwordx4 v[92:95], v5, s[20:23], s80 offen offset:1024 sc1\n"
      "s_lshl_b32 s62, s60, 7\n"
      "s_add_u32 s62, s62, 16078848\n"
      "s_waitcnt vmcnt(0)\n"
      "ds_add_rtn_u32 v31, v2, v3 offset:4172\n"
      "s_waitcnt lgkmcnt(0)\n"
      "v_readfirstlane_b32 s2, v31\n"
      "s_cmp_eq_u32 s2, 3\n"
      "s_cselect_b32 s62, s62, 1073741824\n"
      "s_mov_b64 exec, 255\n"
      "buffer_store_dwordx4 v[12:15], v9, s[20:23], s62 offen sc1\n"
      "s_mov_b64 exec, -1\n"
      "ds_read_b32 v31, v2 offset:4308\n"
      "s_waitcnt lgkmcnt(0)\n"
      "v_readfirstlane_b32 s2, v31\n"
      "s_cmp_lt_u32 s2, 4\n"
      "s_cbranch_scc1 .Lw_173\n"
      ".Lw_174:\n"
      "v_and_b32 v24, 1, v17\n"
      "v_mul_u32_u24 v24, 24, v24\n"
      "v_add_u32 v24, v24, v4\n"
      "s_mul_i32 s62, s53, 2560\n"
      "s_add_u32 s63, s73, 32768\n"
      "s_add_u32 s64, s73, 65536\n"
      "s_add_u32 m0, s62, 139520\n"
      "s_mov_b32 s3, 0x80000\n"
      ".Lp_176:\n"
      "s_sleep 1\n"
      "ds_read_b32 v31, v2 offset:4192\n"
      "s_sub_u32 s3, s3, 1\n"
      "s_waitcnt lgkmcnt(0)\n"
      "v_readlane_b32 s2, v31, s53\n"
      "s_cselect_b32 s2, -1, s2\n"
      "s_cmp_lt_u32 s2, 16\n"
      "s_cbranch_scc1 .Lp_176\n"
      "buffer_load_dwordx2 v[26:27], v25, s[20:23], 0 offen sc1\n"
      "s_waitcnt vmcnt(0)\n"
      "v_cmp_ne_u64 vcc, s[58:59], v[26:27]\n"
      "s_cbranch_vccnz .Lf_177\n"
      ".Lf_178:\n"
      "buffer_load_dwordx4 v6, s[20:23], s73 offen sc1 lds\n"
      "buffer_load_dwordx4 v6, s[20:23], s63 offen offset:1024 sc1 lds\n"
      "s_mov_b32 exec_hi, 0\n"
      "buffer_load_dwordx4 v6, s[20:23], s64 offen offset:2048 sc1 lds\n"
      "s_mov_b32 exec_hi, -1\n"
      "s_mov_b32 s3, 0x80000\n"
      ".Lp_180:\n"
      "s_sleep 1\n"
      "ds_read_b32 v31, v2 offset:4192\n"
      "s_sub_u32 s3, s3, 1\n"
      "s_waitcnt lgkmcnt(0)\n"
      "v_readlane_b32 s2, v31, s53\n"
      "s_cselect_b32 s2, -1, s2\n"
      "s_cmp_lt_u32 s2, 17\n"
      "s_cbranch_scc1 .Lp_180\n"
      "buffer_load_dwordx4 v[164:167], v9, s[20:23], s72 offen offset:0 sc1\n"
      "buffer_load_dwordx4 v[168:171], v9, s[20:23], s72 offen offset:1024 sc1\n"
      "buffer_load_dwordx4 v[172:175], v9, s[20:23], s72 offen offset:2048 sc1\n"
      "s_waitcnt vmcnt(3)\n"
      "ds_add_u32 v2, v3 offset:4140\n"
      "s_mov_b32 s3, 0x80000\n"
      ".Lp_181:\n"
      "ds_read_b32 v31, v2 offset:4140\n"
      "s_sub_u32 s3, s3, 1\n"
      "s_waitcnt lgkmcnt(0)\n"
      "v_readlane_b32 s2, v31, 0\n"
      "s_cselect_b32 s2, -1, s2\n"
      "s_cmp_lt_u32 s2, 4\n"
      "s_cbranch_scc1 .Lp_181\n"
      "ds_read_b64_tr_b16 v[218:219], v7 offset:0\n"
      "ds_read_b64_tr_b16 v[220:221], v7 offset:2048\n"
      "ds_read_b64_tr_b16 v[222:223], v7 offset:4096\n"
      "ds_read_b64_tr_b16 v[224:225], v7 offset:6144\n"
      "ds_read_b64_tr_b16 v[226:227], v7 offset:8192\n"
      "s_waitcnt lgkmcnt(3)\n"
      "v_lshlrev_b32 v21, 16, v218\n"
      "v_and_b32 v22, s92, v218\n"
      "v_mul_f32 v52, v52, v21\n"
      "v_mul_f32 v53, v53, v22\n"
      "v_lshlrev_b32 v21, 16, v219\n"
      "v_and_b32 v22, s92, v219\n"
      "v_mul_f32 v54, v54, v21\n"
      "v_mul_f32 v55, v55, v22\n"
      "v_lshlrev_b32 v21, 16, v220\n"
      "v_and_b32 v22, s92, v220\n"
      "v_mul_f32 v56, v56, v21\n"
      "v_mul_f32 v57, v57, v22\n"
      "v_lshlrev_b32 v21, 16, v221\n"
      "v_and_b32 v22, s92, v221\n"
      "v_mul_f32 v58, v58, v21\n"
      "v_mul_f32 v59, v59, v22\n"
      "s_waitcnt lgkmcnt(0)\n"
      "v_lshlrev_b32 v21, 16, v222\n"
      "v_and_b32 v22, s92, v222\n"
      "v_mul_f32 v60, v60, v21\n"
      "v_mul_f32 v61, v61, v22\n"
      "v_lshlrev_b32 v21, 16, v223\n"
      "v_and_b32 v22, s92, v223\n"
      "v_mul_f32 v62, v62, v21\n"
      "v_mul_f32 v63, v63, v22\n"
      "v_lshlrev_b32 v21, 16, v224\n"
      "v_and_b32 v22, s92, v224\n"
      "v_mul_f32 v64, v64, v21\n"
      "v_mul_f32 v65, v65, v22\n"
      "v_lshlrev_b32 v21, 16, v225\n"
      "v_and_b32 v22, s92, v225\n"
      "v_mul_f32 v66, v66, v21\n"
      "v_mul_f32 v67, v67, v22\n"
      "v_lshlrev_b32 v21, 16, v226\n"
      "v_and_b32 v22, s92, v226\n"
      "v_mul_f32 v68, v68, v21\n"
      "v_mul_f32 v69, v69, v22\n"
      "v_lshlrev_b32 v21, 16, v227\n"
      "v_and_b32 v22, s92, v227\n"
      "v_mul_f32 v70, v70, v21\n"
      "v_mul_f32 v71, v71, v22\n"
      "s_waitcnt vmcnt(0)\n"
      "s_waitcnt vmcnt(63)\n"
      "v_xor_b32 v21, s74, v167\n"
      "v_bitop3_b32 v21, v21, v171, s74 bitop3:0xf6\n"
      "v_bitop3_b32 v21, v21, v175, s74 bitop3:0xf6\n"
      "v_cmp_lt_u32 vcc, 0xffff, v21\n"
      "s_cbranch_vccnz .Ly_182\n"
      ".Ly_183:\n"
      "v_lshlrev_b32 v144, 16, v164\n"
      "v_and_b32 v145, s92, v164\n"
      "v_lshlrev_b32 v146, 16, v165\n"
      "v_and_b32 v147, s92, v165\n"
      "v_lshlrev_b32 v148, 16, v166\n"
      "v_and_b32 v149, s92, v166\n"
      "v_lshlrev_b32 v150, 16, v167\n"
      "v_lshlrev_b32 v151, 16, v168\n"
      "v_and_b32 v152, s92, v168\n"
      "v_lshlrev_b32 v153, 16, v169\n"
      "v_and_b32 v154, s92, v169\n"
      "v_lshlrev_b32 v155, 16, v170\n"
      "v_and_b32 v156, s92, v170\n"
      "v_lshlrev_b32 v157, 16, v171\n"
      "v_lshlrev_b32 v158, 16, v172\n"
      "v_and_b32 v159, s92, v172\n"
      "v_lshlrev_b32 v160, 16, v173\n"
      "v_and_b32 v161, s92, v173\n"
      "v_lshlrev_b32 v162, 16, v174\n"
      "v_and_b32 v163, s92, v174\n"
      "s_mov_b32 s77, s76\n"
      "v_pk_mul_f32 v[144:145], v[144:145], s[76:77]\n"
      "v_pk_mul_f32 v[146:147], v[146:147], s[76:77]\n"
      "v_pk_mul_f32 v[148:149], v[148:149], s[76:77]\n"
      "v_pk_mul_f32 v[150:151], v[150:151], s[76:77]\n"
      "v_pk_mul_f32 v[152:153], v[152:153], s[76:77]\n"
      "v_pk_mul_f32 v[154:155], v[154:155], s[76:77]\n"
      "v_pk_mul_f32 v[156:157], v[156:157], s[76:77]\n"
      "v_pk_mul_f32 v[158:159], v[158:159], s[76:77]\n"
      "v_pk_mul_f32 v[160:161], v[160:161], s[76:77]\n"
      "v_pk_mul_f32 v[162:163], v[162:163], s[76:77]\n"
      "v_mov_b32 v22, 1.0\n"
      "v_mov_b32 v23, 1.0\n"
      "s_mov_b32 s3, 0x80000\n"
      ".Lp_185:\n"
      "ds_read_b32 v31, v2 offset:4192\n"
      "s_sub_u32 s3, s3, 1\n"
      "s_waitcnt lgkmcnt(0)\n"
      "v_readlane_b32 s2, v31, s53\n"
      "s_cselect_b32 s2, -1, s2\n"
      "s_cmp_lt_u32 s2, 32\n"
      "s_cbranch_scc1 .Lp_185\n"
      "ds_read_b128 v[176:179], v8 offset:0\n"
      "ds_read_b128 v[180:183], v8 offset:1024\n"
      "s_waitcnt lgkmcnt(1)\n"
      "v_pk_fma_f32 v[176:177], v[176:177], s[76:77], v[144:145]\n"
      "v_pk_fma_f32 v[178:179], v[178:179], s[76:77], v[146:147]\n"
      "s_waitcnt lgkmcnt(0)\n"
      "v_pk_fma_f32 v[180:181], v[180:181], s[76:77], v[148:149]\n"
      "v_pk_fma_f32 v[182:183], v[182:183], s[76:77], v[150:151]\n"
      "v_exp_f32 v196, v176\n"
      "v_exp_f32 v197, v177\n"
      "v_exp_f32 v198, v178\n"
      "v_exp_f32 v199, v179\n"
      "v_exp_f32 v200, v180\n"
      "v_exp_f32 v201, v181\n"
      "v_exp_f32 v202, v182\n"
      "v_exp_f32 v203, v183\n"
      "v_pk_mul_f32 v[52:53], v[52:53], v[176:177]\n"
      "v_pk_mul_f32 v[54:55], v[54:55], v[178:179]\n"
      "v_pk_mul_f32 v[56:57], v[56:57], v[180:181]\n"
      "v_pk_mul_f32 v[58:59], v[58:59], v[182:183]\n"
      "v_pk_add_f32 v[196:197], v[196:197], v[22:23]\n"
      "v_pk_add_f32 v[198:199], v[198:199], v[22:23]\n"
      "v_pk_add_f32 v[200:201], v[200:201], v[22:23]\n"
      "v_pk_add_f32 v[202:203], v[202:203], v[22:23]\n"
      "v_rcp_f32 v196, v196\n"
      "v_rcp_f32 v197, v197\n"
      "v_rcp_f32 v198, v198\n"
      "v_rcp_f32 v199, v199\n"
      "v_rcp_f32 v200, v200\n"
      "v_rcp_f32 v201, v201\n"
      "v_rcp_f32 v202, v202\n"
      "v_rcp_f32 v203, v203\n"
      "v_pk_fma_f32 v[52:53], v[52:53], v[196:197], v[32:33]\n"
      "v_pk_fma_f32 v[54:55], v[54:55], v[198:199], v[34:35]\n"
      "v_pk_fma_f32 v[56:57], v[56:57], v[200:201], v[36:37]\n"
      "v_pk_fma_f32 v[58:59], v[58:59], v[202:203], v[38:39]\n"
      "v_cvt_pk_bf16_f32 v218, v52, v53\n"
      "v_cvt_pk_bf16_f32 v219, v54, v55\n"
      "v_cvt_pk_bf16_f32 v220, v56, v57\n"
      "v_cvt_pk_bf16_f32 v221, v58, v59\n"
      "s_nop 0\n"
      "v_permlane16_swap_b32 v218, v220\n"
      "v_permlane16_swap_b32 v219, v221\n"
      "buffer_store_dwordx4 v[218:221], v24, s[48:51], s70 offen offset:0\n"
      "s_mov_b32 s3, 0x80000\n"
      ".Lp_186:\n"
      "ds_read_b32 v31, v2 offset:4192\n"
      "s_sub_u32 s3, s3, 1\n"
      "s_waitcnt lgkmcnt(0)\n"
      "v_readlane_b32 s2, v31, s53\n"
      "s_cselect_b32 s2, -1, s2\n"
      "s_cmp_lt_u32 s2, 34\n"
      "s_cbranch_scc1 .Lp_186\n"
      "ds_read_b128 v[184:187], v8 offset:2048\n"
      "ds_read_b128 v[188:191], v8 offset:3072\n"
      "s_waitcnt lgkmcnt(1)\n"
      "v_pk_fma_f32 v[184:185], v[184:185], s[76:77], v[152:153]\n"
      "v_pk_fma_f32 v[186:187], v[186:187], s[76:77], v[154:155]\n"
      "s_waitcnt lgkmcnt(0)\n"
      "v_pk_fma_f32 v[188:189], v[188:189], s[76:77], v[156:157]\n"
      "v_pk_fma_f32 v[190:191], v[190:191], s[76:77], v[158:159]\n"
      "v_exp_f32 v204, v184\n"
      "v_exp_f32 v205, v185\n"
      "v_exp_f32 v206, v186\n"
      "v_exp_f32 v207, v187\n"
      "v_exp_f32 v208, v188\n"
      "v_exp_f32 v209, v189\n"
      "v_exp_f32 v210, v190\n"
      "v_exp_f32 v211, v191\n"
      "v_pk_mul_f32 v[60:61], v[60:61], v[184:185]\n"
      "v_pk_mul_f32 v[62:63], v[62:63], v[186:187]\n"
      "v_pk_mul_f32 v[64:65], v[64:65], v[188:189]\n"
      "v_pk_mul_f32 v[66:67], v[66:67], v[190:191]\n"
      "v_pk_add_f32 v[204:205], v[204:205], v[22:23]\n"
      "v_pk_add_f32 v[206:207], v[206:207], v[22:23]\n"
      "v_pk_add_f32 v[208:209], v[208:209], v[22:23]\n"
      "v_pk_add_f32 v[210:211], v[210:211], v[22:23]\n"
      "v_rcp_f32 v204, v204\n"
      "v_rcp_f32 v205, v205\n"
      "v_rcp_f32 v206, v206\n"
      "v_rcp_f32 v207, v207\n"
      "v_rcp_f32 v208, v208\n"
      "v_rcp_f32 v209, v209\n"
      "v_rcp_f32 v210, v210\n"
      "v_rcp_f32 v211, v211\n"
      "v_pk_fma_f32 v[60:61], v[60:61], v[204:205], v[40:41]\n"
      "v_pk_fma_f32 v[62:63], v[62:63], v[206:207], v[42:43]\n"
      "v_pk_fma_f32 v[64:65], v[64:65], v[208:209], v[44:45]\n"
      "v_pk_fma_f32 v[66:67], v[66:67], v[210:211], v[46:47]\n"
      "v_cvt_pk_bf16_f32 v222, v60, v61\n"
      "v_cvt_pk_bf16_f32 v223, v62, v63\n"
      "v_cvt_pk_bf16_f32 v224, v64, v65\n"
      "v_cvt_pk_bf16_f32 v225, v66, v67\n"
      "s_nop 0\n"
      "v_permlane16_swap_b32 v222, v224\n"
      "v_permlane16_swap_b32 v223, v225\n"
      "buffer_store_dwordx4 v[222:225], v24, s[48:51], s70 offen offset:64\n"
      "s_mov_b32 s3, 0x80000\n"
      ".Lp_187:\n"
      "ds_read_b32 v31, v2 offset:4192\n"
      "s_sub_u32 s3, s3, 1\n"
      "s_waitcnt lgkmcnt(0)\n"
      "v_readlane_b32 s2, v31, s53\n"
      "s_cselect_b32 s2, -1, s2\n"
      "s_cmp_lt_u32 s2, 35\n"
      "s_cbranch_scc1 .Lp_187\n"
      "ds_read_b128 v[192:195], v8 offset:4096\n"
      "s_waitcnt lgkmcnt(0)\n"
      "v_pk_fma_f32 v[192:193], v[192:193], s[76:77], v[160:161]\n"
      "v_pk_fma_f32 v[194:195], v[194:195], s[76:77], v[162:163]\n"
      "v_exp_f32 v212, v192\n"
      "v_exp_f32 v213, v193\n"
      "v_exp_f32 v214, v194\n"
      "v_exp_f32 v215, v195\n"
      "v_pk_mul_f32 v[68:69], v[68:69], v[192:193]\n"
      "v_pk_mul_f32 v[70:71], v[70:71], v[194:195]\n"
      "v_pk_add_f32 v[212:213], v[212:213], v[22:23]\n"
      "v_pk_add_f32 v[214:215], v[214:215], v[22:23]\n"
      "v_rcp_f32 v212, v212\n"
      "v_rcp_f32 v213, v213\n"
      "v_rcp_f32 v214, v214\n"
      "v_rcp_f32 v215, v215\n"
      "v_pk_fma_f32 v[68:69], v[68:69], v[212:213], v[48:49]\n"
      "v_pk_fma_f32 v[70:71], v[70:71], v[214:215], v[50:51]\n"
      "v_cvt_pk_bf16_f32 v226, v68, v69\n"
      "v_cvt_pk_bf16_f32 v227, v70, v71\n"
      "buffer_store_dwordx2 v[226:227], v4, s[48:51], s70 offen offset:128\n"
      "s_endpgm\n"
      ".LSPARE:\n"
      "s_and_b32 s70, s60, 7\n"
      "s_lshl2_add_u32 s70, s70, s61\n"
      "s_sub_u32 s70, s70, 28\n"
      "s_cmp_lt_u32 s52, 4\n"
      "s_cbranch_scc1 .LSPM\n"
      "s_lshl_b32 s71, s53, 5\n"
      "s_add_u32 s71, s71, s70\n"
      "s_add_u32 s71, s71, 224\n"
      "v_lshrrev_b32 v20, 4, v1\n"
      "v_mul_u32_u24 v20, 1280, v20\n"
      "v_and_b32 v21, 15, v1\n"
      "v_lshl_add_u32 v20, v21, 4, v20\n"
      "v_add_u32 v21, 4080, v20\n"
      "v_add_u32 v22, 8160, v20\n"
      "v_add_u32 v23, 12240, v20\n"
      "s_mul_i32 s72, s71, 205\n"
      "s_lshr_b32 s72, s72, 10\n"
      "s_mul_i32 s73, s72, 5\n"
      "s_sub_u32 s73, s71, s73\n"
      "s_mul_i32 s74, s53, 8320\n"
      "s_add_u32 s74, s74, 53248\n"
      "s_mul_i32 s62, s72, 40960\n"
      "s_lshl_b32 s63, s73, 8\n"
      "s_add_u32 s62, s62, s63\n"
      "s_mov_b32 m0, s74\n"
      "s_add_u32 s63, s62, 20480\n"
      "s_lshl_b32 s94, s53, 12\n"
      "s_lshl_b32 s95, s53, 10\n"
      "s_cmp_lt_u32 s71, 270\n"
      "s_waitcnt lgkmcnt(0)\n"
      "s_barrier\n"
      "buffer_load_dwordx2 v[46:47], off, s[20:23], 0 sc1\n"
      "s_cbranch_scc0 .Ls_188\n"
      "buffer_load_dwordx4 v20, s[40:43], s62 offen offset:0 nt lds\n"
      "buffer_load_dwordx4 v21, s[40:43], s62 offen offset:1040 nt lds\n"
      "buffer_load_dwordx4 v22, s[40:43], s62 offen offset:2080 nt lds\n"
      "buffer_load_dwordx4 v23, s[40:43], s62 offen offset:3120 nt lds\n"
      "s_add_u32 m0, s74, 4160\n"
      "s_nop 0\n"
      "buffer_load_dwordx4 v20, s[40:43], s63 offen offset:0 nt lds\n"
      "buffer_load_dwordx4 v21, s[40:43], s63 offen offset:1040 nt lds\n"
      "buffer_load_dwordx4 v22, s[40:43], s63 offen offset:2080 nt lds\n"
      "buffer_load_dwordx4 v23, s[40:43], s63 offen offset:3120 nt lds\n"
      ".Ls_188:\n"
      "v_mov_b64 v[36:37], 0\n"
      "v_mov_b64 v[38:39], 0\n"
      "s_lshl_b32 s63, s53, 10\n"
      "s_add_u32 s63, s63, 157696\n"
      "v_lshl_add_u32 v40, v1, 4, s63\n"
      "ds_write_b128 v40, v[36:39]\n"
      "v_lshlrev_b32 v40, 2, v1\n"
      "v_add_u32 v40, 161792, v40\n"
      "ds_write_b32 v40, v36\n"
      "v_lshlrev_b32 v5, 4, v1\n"
      "v_add_u32 v6, 4096, v5\n"
      "s_lshl_b32 s24, s98, 12\n"
      "s_add_u32 s24, s24, s95\n"
      "s_add_u32 s25, s24, 4096\n"
      "v_mov_b32 v32, s25\n"
      "v_writelane_b32 v32, s24, 0\n"
      "s_cmp_eq_u32 s53, 0\n"
      "s_cselect_b32 s63, 1073741824, -4\n"
      "v_add_u32 v32, s63, v32\n"
      "v_lshrrev_b32 v33, 11, v32\n"
      "v_xor_b32 v33, 524288, v33\n"
      "s_sub_u32 s91, s98, 4\n"
      "s_lshr_b32 s91, s91, 4\n"
      "s_lshr_b32 s63, s98, 4\n"
      "s_sub_u32 s63, s63, s91\n"
      "s_lshl_b32 s63, s63, 9\n"
      "v_and_b32 v72, 8, v1\n"
      "v_mul_u32_u24 v72, s63, v72\n"
      "v_and_b32 v73, 7, v1\n"
      "v_lshl_add_u32 v30, v73, 7, v72\n"
      "s_lshl_b32 s91, s91, 12\n"
      "s_add_u32 s91, s91, s95\n"
      "s_add_u32 s91, s91, 15859712\n"
      "v_add_u32 v30, s91, v30\n"
      "s_and_b32 s63, s53, 1\n"
      "s_mul_i32 s63, s63, 4608\n"
      "s_add_u32 s63, s63, 139520\n"
      "v_lshl_add_u32 v34, v1, 3, s63\n"
      "v_lshrrev_b32 v72, 1, v1\n"
      "v_min_u32 v72, 6, v72\n"
      "s_mul_i32 s62, s54, 28\n"
      "s_add_u32 s62, s62, s53\n"
      "v_lshl_add_u32 v72, v72, 2, s62\n"
      "v_min_u32 v72, 53, v72\n"
      "s_lshl_b32 s63, s56, 14\n"
      "s_add_u32 s63, s63, 15597568\n"
      "v_lshl_add_u32 v13, v72, 8, s63\n"
      "s_mul_i32 s63, s55, 80\n"
      "s_add_u32 s91, s63, 79\n"
      "s_lshr_b32 s63, s63, 7\n"
      "s_lshr_b32 s91, s91, 7\n"
      "s_sub_u32 s91, s91, s63\n"
      "v_and_b32 v73, s91, v1\n"
      "v_lshl_add_u32 v72, v72, 3, v73\n"
      "s_lshl_b32 s63, s63, 7\n"
      "s_add_u32 s63, s63, 16023552\n"
      "v_lshl_add_u32 v31, v72, 7, s63\n"
      "v_lshrrev_b32 v72, 3, v1\n"
      "v_and_b32 v72, 3, v72\n"
      "v_mul_u32_u24 v73, 4160, v72\n"
      "v_add_u32 v73, 122880, v73\n"
      "v_and_b32 v72, 7, v1\n"
      "v_lshl_add_u32 v35, v72, 3, v73\n"
      "s_add_u32 s62, s95, 122944\n"
      "v_add_u32 v40, s62, v5\n"
      "v_and_b32 v72, 31, v1\n"
      "v_lshlrev_b32 v42, 3, v72\n"
      "s_add_u32 s62, s95, 157696\n"
      "v_lshl_add_u32 v43, v72, 5, s62\n"
      "s_xor_b32 s31, s99, s53\n"
      "s_lshl2_add_u32 s31, s56, s31\n"
      "s_lshl_b32 s31, s31, 8\n"
      "s_add_u32 s31, s31, 8192\n"
      "s_lshl2_add_u32 s62, s54, s53\n"
      "s_cmp_ge_u32 s62, 6\n"
      "s_cselect_b32 s63, 1073741824, 6144\n"
      "v_add_u32 v7, s63, v5\n"
      "s_mul_i32 s62, s62, 7168\n"
      "s_mul_i32 s63, s55, 286720\n"
      "s_add_u32 s63, s63, s62\n"
      "s_add_u32 s27, s63, 2818048\n"
      "s_mul_i32 s63, s56, 458752\n"
      "s_add_u32 s63, s63, s62\n"
      "s_add_u32 s63, s63, 5111808\n"
      "s_mul_i32 s62, s54, 229376\n"
      "s_add_u32 s91, s63, s62\n"
      "s_sub_u32 s62, 229376, s62\n"
      "s_add_u32 s93, s63, s62\n"
      "s_sub_u32 s62, 4, s53\n"
      "s_and_b32 s62, s62, 3\n"
      "s_mul_i32 s62, s62, 57344\n"
      "s_add_u32 s44, s93, s62\n"
      "s_add_u32 s48, s91, s62\n"
      "s_sub_u32 s62, 5, s53\n"
      "s_and_b32 s62, s62, 3\n"
      "s_mul_i32 s62, s62, 57344\n"
      "s_add_u32 s45, s93, s62\n"
      "s_add_u32 s49, s91, s62\n"
      "s_sub_u32 s62, 6, s53\n"
      "s_and_b32 s62, s62, 3\n"
      "s_mul_i32 s62, s62, 57344\n"
      "s_add_u32 s46, s93, s62\n"
      "s_add_u32 s50, s91, s62\n"
      "s_sub_u32 s62, 7, s53\n"
      "s_and_b32 s62, s62, 3\n"
      "s_mul_i32 s62, s62, 57344\n"
      "s_add_u32 s47, s93, s62\n"
      "s_add_u32 s51, s91, s62\n"
      "s_add_u32 s91, s53, 3\n"
      "s_and_b32 s91, s91, 3\n"
      "s_lshl2_add_u32 s62, s91, 161888\n"
      "v_mov_b32 v15, s62\n"
      "s_mul_i32 s62, s53, 5120\n"
      "s_add_u32 s63, s62, 32768\n"
      "v_add_u32 v8, s63, v5\n"
      "s_mul_i32 s63, s91, 5120\n"
      "s_add_u32 s63, s63, 32768\n"
      "v_add_u32 v9, s63, v5\n"
      "s_xor_b32 s63, s60, 1\n"
      "s_lshl2_add_u32 s63, s63, s53\n"
      "s_mul_i32 s28, s63, 3072\n"
      "s_add_u32 s28, s28, 12451840\n"
      "v_cmp_eq_u32 vcc, s53, v1\n"
      "v_and_b32 v14, 48, v1\n"
      "v_add_u32 v14, 163072, v14\n"
      "v_cndmask_b32 v55, 0, 1, vcc\n"
      "s_sub_u32 s62, s24, 16384\n"
      "buffer_load_dwordx4 v[140:143], v5, s[36:39], s62 offen nt\n"
      "s_sub_u32 s62, s25, 16384\n"
      "buffer_load_dwordx4 v[144:147], v5, s[36:39], s62 offen nt\n"
      "v_subrev_u32 v72, 16384, v32\n"
      "v_subrev_u32 v73, 8, v33\n"
      "buffer_load_dword v132, v72, s[36:39], 0 offen nt\n"
      "buffer_load_ushort v133, v73, s[32:35], 0 offen\n"
      "s_sub_u32 s62, s24, 8192\n"
      "buffer_load_dwordx4 v[148:151], v5, s[36:39], s62 offen nt\n"
      "s_sub_u32 s62, s25, 8192\n"
      "buffer_load_dwordx4 v[152:155], v5, s[36:39], s62 offen nt\n"
      "v_subrev_u32 v72, 8192, v32\n"
      "v_subrev_u32 v73, 4, v33\n"
      "buffer_load_dword v134, v72, s[36:39], 0 offen nt\n"
      "buffer_load_ushort v135, v73, s[32:35], 0 offen\n"
      "v_lshrrev_b32 v20, 4, v1\n"
      "v_and_b32 v21, 1, v20\n"
      "v_lshrrev_b32 v20, 1, v20\n"
      "v_lshl_add_u32 v20, v21, 2, v20\n"
      "v_mul_u32_u24 v20, 1040, v20\n"
      "v_bfe_u32 v21, v1, 2, 2\n"
      "v_lshl_add_u32 v20, v21, 8, v20\n"
      "v_and_b32 v21, 3, v1\n"
      "v_lshl_add_u32 v20, v21, 3, v20\n"
      "s_lshl3_add_u32 s62, s72, s73\n"
      "s_lshl_b32 s62, s62, 7\n"
      "s_add_u32 s62, s62, 16023552\n"
      "v_mov_b32 v23, 1073741824\n"
      "s_mov_b32 exec_lo, 0xffff\n"
      "s_mov_b32 exec_hi, 0x0\n"
      "v_lshl_add_u32 v23, v1, 3, s62\n"
      "s_mov_b64 exec, -1\n"
      "v_add_u32 v21, s74, v20\n"
      "s_lshr_b32 s63, s72, 2\n"
      "s_and_b32 s75, s72, 3\n"
      "s_mul_i32 s75, s75, 7\n"
      "s_add_u32 s75, s75, s63\n"
      "s_cmp_ge_u32 s63, 7\n"
      "s_cselect_b32 s63, 21, 0\n"
      "s_add_u32 s75, s75, s63\n"
      "s_lshl_b32 s75, s75, 10\n"
      "s_mul_i32 s63, s73, 458752\n"
      "s_add_u32 s75, s75, s63\n"
      "s_add_u32 s75, s75, 2818048\n"
      "v_subrev_u32 v136, 52736, v40\n"
      "v_subrev_u32 v137, 52736, v34\n"
      "v_subrev_u32 v138, 52736, v35\n"
      "s_waitcnt vmcnt(8) lgkmcnt(0)\n"
      "v_cmp_eq_u32 vcc, 0, v1\n"
      "v_lshlrev_b32 v2, 2, v1\n"
      "v_add_u32 v2, 157696, v2\n"
      "v_cndmask_b32 v3, 0, 1, vcc\n"
      "v_add_co_u32 v46, vcc, 1, v46\n"
      "s_nop 1\n"
      "v_addc_co_u32 v47, vcc, 0, v47, vcc\n"
      "s_lshr_b32 s62, s53, 1\n"
      "s_lshl_b32 s62, s62, 5\n"
      "v_add_u32 v139, s62, v2\n"
      "v_readfirstlane_b32 s58, v46\n"
      "v_readfirstlane_b32 s59, v47\n"
      "v_mov_b32 v1, v55\n"
      "s_barrier\n"
      "s_cmp_lt_u32 s71, 270\n"
      "s_cbranch_scc0 .Ls_189\n"
      "ds_read_b64_tr_b16 v[160:161], v21 offset:0\n"
      "ds_read_b64_tr_b16 v[162:163], v21 offset:2080\n"
      "ds_read_b64_tr_b16 v[164:165], v21 offset:32\n"
      "ds_read_b64_tr_b16 v[166:167], v21 offset:2112\n"
      "ds_read_b64_tr_b16 v[168:169], v21 offset:64\n"
      "ds_read_b64_tr_b16 v[170:171], v21 offset:2144\n"
      "ds_read_b64_tr_b16 v[172:173], v21 offset:96\n"
      "ds_read_b64_tr_b16 v[174:175], v21 offset:2176\n"
      "s_waitcnt lgkmcnt(6)\n"
      "ds_read_b64_tr_b16 v[176:177], v21 offset:128\n"
      "ds_read_b64_tr_b16 v[178:179], v21 offset:2208\n"
      "ds_read_b64_tr_b16 v[180:181], v21 offset:160\n"
      "ds_read_b64_tr_b16 v[182:183], v21 offset:2240\n"
      "ds_read_b64_tr_b16 v[184:185], v21 offset:192\n"
      "ds_read_b64_tr_b16 v[186:187], v21 offset:2272\n"
      "ds_read_b64_tr_b16 v[188:189], v21 offset:224\n"
      "ds_read_b64_tr_b16 v[190:191], v21 offset:2304\n"
      "s_waitcnt lgkmcnt(14)\n"
      "buffer_store_dwordx4 v[160:163], v5, s[20:23], s75 offen sc1\n"
      "s_add_u32 s75, s75, 57344\n"
      "s_waitcnt lgkmcnt(12)\n"
      "buffer_store_dwordx4 v[164:167], v5, s[20:23], s75 offen sc1\n"
      "s_add_u32 s75, s75, 57344\n"
      "s_waitcnt lgkmcnt(10)\n"
      "buffer_store_dwordx4 v[168:171], v5, s[20:23], s75 offen sc1\n"
      "s_add_u32 s75, s75, 57344\n"
      "s_waitcnt lgkmcnt(8)\n"
      "buffer_store_dwordx4 v[172:175], v5, s[20:23], s75 offen sc1\n"
      "s_add_u32 s75, s75, 57344\n"
      "s_waitcnt lgkmcnt(6)\n"
      "buffer_store_dwordx4 v[176:179], v5, s[20:23], s75 offen sc1\n"
      "s_add_u32 s75, s75, 57344\n"
      "s_waitcnt lgkmcnt(4)\n"
      "buffer_store_dwordx4 v[180:183], v5, s[20:23], s75 offen sc1\n"
      "s_add_u32 s75, s75, 57344\n"
      "s_waitcnt lgkmcnt(2)\n"
      "buffer_store_dwordx4 v[184:187], v5, s[20:23], s75 offen sc1\n"
      "s_add_u32 s75, s75, 57344\n"
      "s_waitcnt lgkmcnt(0)\n"
      "buffer_store_dwordx4 v[188:191], v5, s[20:23], s75 offen sc1\n"
      "s_waitcnt vmcnt(8)\n"
      "s_branch .Ls_190\n"
      ".Ls_189:\n"
      "v_mov_b32 v23, 1073741824\n"
      "s_waitcnt vmcnt(0)\n"
      ".Ls_190:\n"
      "ds_write_b64 v137, v[36:37] offset:0\n"
      "ds_write_b64 v138, v[36:37] offset:0\n"
      "s_cmp_eq_u32 s53, 0\n"
      "s_cbranch_scc0 .Ld_191\n"
      "v_readlane_b32 s62, v133, 0\n"
      "v_readlane_b32 s63, v133, 1\n"
      "s_mov_b64 exec, 1\n"
      "v_lshlrev_b32 v72, 16, v140\n"
      "s_lshl_b32 s62, s62, 16\n"
      "v_and_b32 v73, s92, v140\n"
      "v_add_f32 v72, s62, v72\n"
      "v_cvt_pk_bf16_f32 v140, v72, v73\n"
      "v_lshlrev_b32 v72, 16, v144\n"
      "s_lshl_b32 s63, s63, 16\n"
      "v_and_b32 v73, s92, v144\n"
      "v_add_f32 v72, s63, v72\n"
      "v_cvt_pk_bf16_f32 v144, v72, v73\n"
      "s_mov_b64 exec, -1\n"
      ".Ld_191:\n"
      "v_readlane_b32 s62, v132, 1\n"
      "ds_write_b128 v136, v[144:147] offset:8320\n"
      "v_alignbyte_b32 v103, v147, v146, 2\n"
      "v_writelane_b32 v72, s62, 0\n"
      "v_alignbyte_b32 v101, v145, v144, 2\n"
      "v_alignbyte_b32 v102, v146, v145, 2\n"
      "v_mov_b32_dpp v72, v147 wave_shr:1 row_mask:0xf bank_mask:0xf\n"
      "v_alignbyte_b32 v100, v144, v72, 2\n"
      "ds_write_b128 v136, v[100:103] offset:12480\n"
      "ds_write_b128 v136, v[140:143] offset:0\n"
      "v_alignbyte_b32 v99, v143, v142, 2\n"
      "v_alignbyte_b32 v97, v141, v140, 2\n"
      "v_alignbyte_b32 v98, v142, v141, 2\n"
      "v_mov_b32_dpp v132, v143 wave_shr:1 row_mask:0xf bank_mask:0xf\n"
      "v_alignbyte_b32 v96, v140, v132, 2\n"
      "ds_write_b128 v136, v[96:99] offset:4160\n"
      "ds_write_b64 v137, v[36:37] offset:26112\n"
      "ds_write_b64 v138, v[36:37] offset:26112\n"
      "s_cmp_eq_u32 s53, 0\n"
      "s_cbranch_scc0 .Ld_192\n"
      "v_readlane_b32 s62, v135, 0\n"
      "v_readlane_b32 s63, v135, 1\n"
      "s_mov_b64 exec, 1\n"
      "v_lshlrev_b32 v72, 16, v148\n"
      "s_lshl_b32 s62, s62, 16\n"
      "v_and_b32 v73, s92, v148\n"
      "v_add_f32 v72, s62, v72\n"
      "v_cvt_pk_bf16_f32 v148, v72, v73\n"
      "v_lshlrev_b32 v72, 16, v152\n"
      "s_lshl_b32 s63, s63, 16\n"
      "v_and_b32 v73, s92, v152\n"
      "v_add_f32 v72, s63, v72\n"
      "v_cvt_pk_bf16_f32 v152, v72, v73\n"
      "s_mov_b64 exec, -1\n"
      ".Ld_192:\n"
      "v_readlane_b32 s62, v134, 1\n"
      "ds_write_b128 v136, v[152:155] offset:34432\n"
      "v_alignbyte_b32 v103, v155, v154, 2\n"
      "v_writelane_b32 v72, s62, 0\n"
      "v_alignbyte_b32 v101, v153, v152, 2\n"
      "v_alignbyte_b32 v102, v154, v153, 2\n"
      "v_mov_b32_dpp v72, v155 wave_shr:1 row_mask:0xf bank_mask:0xf\n"
      "v_alignbyte_b32 v100, v152, v72, 2\n"
      "ds_write_b128 v136, v[100:103] offset:38592\n"
      "ds_write_b128 v136, v[148:151] offset:26112\n"
      "v_alignbyte_b32 v99, v151, v150, 2\n"
      "v_alignbyte_b32 v97, v149, v148, 2\n"
      "v_alignbyte_b32 v98, v150, v149, 2\n"
      "v_mov_b32_dpp v134, v151 wave_shr:1 row_mask:0xf bank_mask:0xf\n"
      "v_alignbyte_b32 v96, v148, v134, 2\n"
      "ds_write_b128 v136, v[96:99] offset:30272\n"
      "s_waitcnt vmcnt(0)\n"
      "buffer_store_dwordx2 v[46:47], v23, s[20:23], 0 offen sc1\n"
      "s_mov_b32 s3, 0x80000\n"
      ".Lp_193:\n"
      "s_sleep 2\n"
      "ds_read_b32 v4, v2 offset:4104\n"
      "s_sub_u32 s3, s3, 1\n"
      "s_waitcnt lgkmcnt(0)\n"
      "v_readfirstlane_b32 s2, v4\n"
      "s_cselect_b32 s2, -1, s2\n"
      "s_cmp_lt_u32 s2, 4\n"
      "s_cbranch_scc1 .Lp_193\n"
      "buffer_load_dwordx2 v[76:77], v42, s[20:23], s31 offen sc1\n"
      "buffer_load_dwordx4 v[56:59], v5, s[36:39], s24 offen nt\n"
      "buffer_load_dwordx4 v[60:63], v5, s[36:39], s25 offen nt\n"
      "buffer_load_dword v52, v32, s[36:39], 0 offen nt\n"
      "buffer_load_ushort v53, v33, s[32:35], 0 offen\n"
      "s_waitcnt vmcnt(4)\n"
      "v_cmp_ne_u32 vcc, s58, v77\n"
      "s_cbranch_vccnz .Lt_194\n"
      ".Lt_195:\n"
      "v_and_b32 v82, s92, v76\n"
      "v_rcp_f32 v77, v82\n"
      "v_xor_b32 v85, 0x8000, v76\n"
      "s_mov_b32 s62, 0x01000100\n"
      "v_mul_f32 v80, 0xc0135761, v77\n"
      "v_mul_f32 v81, v77, v77\n"
      "v_cmp_nle_f32 vcc, 0x3089705f, v77\n"
      "v_mul_f32 v81, 0x3d372713, v81\n"
      "v_perm_b32 v84, v85, v85, s62\n"
      "v_mul_f32 v81, v81, v80\n"
      "v_mov_b32 v83, 1.0\n"
      "ds_write_b64 v43, v[84:85] offset:16\n"
      "s_cbranch_vccnz .Lb_197\n"
      ".Lb_198:\n"
      "ds_write_b128 v43, v[80:83]\n"
      "ds_add_u32 v2, v3 offset:4240\n"
      "s_cmp_lt_u32 s53, 2\n"
      "s_cbranch_scc1 .Ls_199\n"
      "s_mov_b32 s3, 0x80000\n"
      ".Lp_200:\n"
      "s_sleep 2\n"
      "ds_read_b32 v4, v2 offset:4108\n"
      "s_sub_u32 s3, s3, 1\n"
      "s_waitcnt lgkmcnt(0)\n"
      "v_readfirstlane_b32 s2, v4\n"
      "s_cselect_b32 s2, -1, s2\n"
      "s_cmp_lt_u32 s2, 4\n"
      "s_cbranch_scc1 .Lp_200\n"
      "s_sleep 24\n"
      ".Ls_199:\n"
      "buffer_load_dwordx2 v[48:49], v30, s[20:23], 0 offen sc1\n"
      "s_waitcnt vmcnt(0)\n"
      "v_cmp_ne_u64 vcc, s[58:59], v[48:49]\n"
      "s_cbranch_vccnz .Lf_201\n"
      ".Lf_202:\n"
      "s_mul_i32 s62, s98, 4224\n"
      "s_add_u32 s62, s62, s95\n"
      "s_add_u32 m0, s95, 87296\n"
      "s_add_u32 s63, s62, 15872\n"
      "buffer_load_dwordx4 v5, s[20:23], s63 offen sc1 lds\n"
      "s_add_u32 m0, s95, 91904\n"
      "s_add_u32 s63, s62, 20096\n"
      "buffer_load_dwordx4 v5, s[20:23], s63 offen sc1 lds\n"
      "s_mul_i32 s62, s98, 4224\n"
      "s_add_u32 s62, s62, s95\n"
      "s_add_u32 m0, s95, 113408\n"
      "s_add_u32 s63, s62, 24320\n"
      "buffer_load_dwordx4 v5, s[20:23], s63 offen sc1 lds\n"
      "s_add_u32 m0, s95, 118016\n"
      "s_add_u32 s63, s62, 28544\n"
      "buffer_load_dwordx4 v5, s[20:23], s63 offen sc1 lds\n"
      "buffer_load_dwordx2 v[50:51], v31, s[20:23], 0 offen sc1\n"
      "s_waitcnt vmcnt(1)\n"
      "ds_add_u32 v139, v3 offset:4240\n"
      "s_waitcnt vmcnt(0)\n"
      "s_branch .LS3\n"
      ".LSPM:\n"
      "v_lshrrev_b32 v24, 3, v1\n"
      "v_mul_u32_u24 v25, 1280, v24\n"
      "v_and_b32 v26, 7, v1\n"
      "v_lshl_add_u32 v25, v26, 4, v25\n"
      "v_cmp_gt_u32 vcc, 40, v1\n"
      "v_mov_b32 v26, 1073741824\n"
      "v_lshlrev_b32 v24, 4, v24\n"
      "v_cndmask_b32 v26, v25, v26, vcc\n"
      "v_add_u32 v27, 9200, v25\n"
      "s_mul_i32 s62, s70, 3\n"
      "s_add_u32 s62, s62, 224\n"
      "s_mul_i32 s64, s53, 2080\n"
      "s_mul_i32 s65, s53, 20480\n"
      "s_mul_i32 s80, s62, 205\n"
      "s_lshr_b32 s80, s80, 11\n"
      "s_mul_i32 s81, s80, 10\n"
      "s_sub_u32 s81, s62, s81\n"
      "s_mul_i32 s66, s80, 81920\n"
      "s_lshl_b32 s63, s81, 7\n"
      "s_add_u32 s66, s66, s63\n"
      "s_lshl2_add_u32 s81, s81, s53\n"
      "s_lshl_b32 s63, s81, 7\n"
      "buffer_load_dwordx4 v[168:171], v24, s[24:27], s63 offen\n"
      "s_mov_b32 m0, 0\n"
      "s_sub_u32 s63, s66, 10240\n"
      "buffer_load_dwordx4 v26, s[12:15], s63 offen nt lds\n"
      "s_add_u32 s63, s66, s65\n"
      "s_add_u32 m0, s64, 1040\n"
      "s_nop 0\n"
      "buffer_load_dwordx4 v25, s[12:15], s63 offen nt lds\n"
      "buffer_load_dwordx4 v27, s[12:15], s63 offen offset:1040 nt lds\n"
      "s_add_u32 m0, s64, 9360\n"
      "s_nop 0\n"
      "buffer_load_dwordx4 v25, s[16:19], s63 offen nt lds\n"
      "buffer_load_dwordx4 v27, s[16:19], s63 offen offset:1040 nt lds\n"
      "s_add_u32 s62, s62, 1\n"
      "v_cmp_eq_u32 vcc, 0, v1\n"
      "v_lshlrev_b32 v2, 2, v1\n"
      "v_add_u32 v2, 157696, v2\n"
      "v_cndmask_b32 v3, 0, 1, vcc\n"
      "s_barrier\n"
      "s_mul_i32 s82, s62, 205\n"
      "s_lshr_b32 s82, s82, 11\n"
      "s_mul_i32 s83, s82, 10\n"
      "s_sub_u32 s83, s62, s83\n"
      "s_mul_i32 s66, s82, 81920\n"
      "s_lshl_b32 s63, s83, 7\n"
      "s_add_u32 s66, s66, s63\n"
      "s_lshl2_add_u32 s83, s83, s53\n"
      "s_lshl_b32 s63, s83, 7\n"
      "buffer_load_dwordx4 v[172:175], v24, s[24:27], s63 offen\n"
      "s_mov_b32 m0, 17680\n"
      "s_sub_u32 s63, s66, 10240\n"
      "buffer_load_dwordx4 v26, s[12:15], s63 offen nt lds\n"
      "s_add_u32 s63, s66, s65\n"
      "s_add_u32 m0, s64, 18720\n"
      "s_nop 0\n"
      "buffer_load_dwordx4 v25, s[12:15], s63 offen nt lds\n"
      "buffer_load_dwordx4 v27, s[12:15], s63 offen offset:1040 nt lds\n"
      "s_add_u32 m0, s64, 27040\n"
      "s_nop 0\n"
      "buffer_load_dwordx4 v25, s[16:19], s63 offen nt lds\n"
      "buffer_load_dwordx4 v27, s[16:19], s63 offen offset:1040 nt lds\n"
      "s_add_u32 s62, s62, 1\n"
      "s_mul_i32 s84, s62, 205\n"
      "s_lshr_b32 s84, s84, 11\n"
      "s_mul_i32 s85, s84, 10\n"
      "s_sub_u32 s85, s62, s85\n"
      "s_mul_i32 s66, s84, 81920\n"
      "s_lshl_b32 s63, s85, 7\n"
      "s_add_u32 s66, s66, s63\n"
      "s_lshl2_add_u32 s85, s85, s53\n"
      "s_lshl_b32 s63, s85, 7\n"
      "buffer_load_dwordx4 v[176:179], v24, s[24:27], s63 offen\n"
      "s_mov_b32 m0, 35360\n"
      "s_sub_u32 s63, s66, 10240\n"
      "buffer_load_dwordx4 v26, s[12:15], s63 offen nt lds\n"
      "s_add_u32 s63, s66, s65\n"
      "s_add_u32 m0, s64, 36400\n"
      "s_nop 0\n"
      "buffer_load_dwordx4 v25, s[12:15], s63 offen nt lds\n"
      "buffer_load_dwordx4 v27, s[12:15], s63 offen offset:1040 nt lds\n"
      "s_add_u32 m0, s64, 44720\n"
      "s_nop 0\n"
      "buffer_load_dwordx4 v25, s[16:19], s63 offen nt lds\n"
      "buffer_load_dwordx4 v27, s[16:19], s63 offen offset:1040 nt lds\n"
      "s_add_u32 s62, s62, 1\n"
      "v_mov_b64 v[12:13], s[58:59]\n"
      "s_lshl_b32 s62, s53, 5\n"
      "s_add_u32 s62, s62, 640\n"
      "v_and_b32 v68, 7, v1\n"
      "v_lshrrev_b32 v69, 3, v1\n"
      "v_mul_u32_u24 v192, 1040, v68\n"
      "v_lshl_add_u32 v192, v69, 2, v192\n"
      "v_add_u32 v192, s62, v192\n"
      "v_add_u32 v193, 8720, v192\n"
      "v_mul_u32_u24 v8, 8448, v69\n"
      "v_lshl_add_u32 v8, v68, 4, v8\n"
      "v_mov_b32 v23, 1073741824\n"
      "s_lshl3_add_u32 s65, s60, 4096\n"
      "s_cmp_eq_u32 s52, 0\n"
      "s_cselect_b32 s65, s65, 1073741824\n"
      "v_writelane_b32 v23, s65, 48\n"
      "s_waitcnt vmcnt(12)\n"
      "ds_add_u32 v2, v3 offset:4096\n"
      "v_lshlrev_b32 v184, 16, v168\n"
      "v_lshlrev_b32 v185, 16, v170\n"
      "v_and_b32 v186, s92, v168\n"
      "v_and_b32 v187, s92, v170\n"
      "v_lshlrev_b32 v188, 16, v169\n"
      "v_lshlrev_b32 v189, 16, v171\n"
      "v_and_b32 v190, s92, v169\n"
      "v_and_b32 v191, s92, v171\n"
      "s_mul_i32 s62, s81, 67584\n"
      "s_lshl_b32 s63, s80, 7\n"
      "s_add_u32 s62, s62, s63\n"
      "s_add_u32 s62, s62, 32768\n"
      "s_add_u32 s63, s62, 4224\n"
      "s_lshl_b32 s64, s81, 5\n"
      "s_add_u32 s64, s64, s80\n"
      "s_lshl_b32 s64, s64, 7\n"
      "s_add_u32 s64, s64, 15859712\n"
      "s_mov_b32 exec_lo, 0xffff\n"
      "s_mov_b32 exec_hi, 0x0\n"
      "v_lshl_add_u32 v23, v1, 3, s64\n"
      "s_mov_b64 exec, -1\n"
      "s_mov_b32 s3, 0x80000\n"
      ".Lp_204:\n"
      "ds_read_b32 v31, v2 offset:4096\n"
      "s_sub_u32 s3, s3, 1\n"
      "s_waitcnt lgkmcnt(0)\n"
      "v_readlane_b32 s2, v31, 0\n"
      "s_cselect_b32 s2, -1, s2\n"
      "s_cmp_lt_u32 s2, 4\n"
      "s_cbranch_scc1 .Lp_204\n"
      "ds_read2_b32 v[196:197], v192 offset0:0 offset1:32\n"
      "ds_read2_b32 v[198:199], v192 offset0:64 offset1:100\n"
      "ds_read2_b32 v[200:201], v192 offset0:132 offset1:164\n"
      "ds_read2_b32 v[202:203], v192 offset0:196 offset1:228\n"
      "ds_read_b32 v204, v192 offset:1040\n"
      "ds_read_b32 v205, v192 offset:1168\n"
      "ds_read_b32 v206, v192 offset:1296\n"
      "ds_read2_b32 v[208:209], v193 offset0:0 offset1:32\n"
      "ds_read2_b32 v[210:211], v193 offset0:64 offset1:96\n"
      "ds_read2_b32 v[212:213], v193 offset0:128 offset1:160\n"
      "ds_read2_b32 v[214:215], v193 offset0:192 offset1:224\n"
      "s_waitcnt lgkmcnt(9)\n"
      "v_lshlrev_b32 v216, 16, v196\n"
      "v_and_b32 v217, s92, v196\n"
      "v_lshlrev_b32 v218, 16, v197\n"
      "v_and_b32 v219, s92, v197\n"
      "v_lshlrev_b32 v220, 16, v198\n"
      "v_and_b32 v221, s92, v198\n"
      "v_lshlrev_b32 v222, 16, v199\n"
      "v_and_b32 v223, s92, v199\n"
      "s_waitcnt lgkmcnt(7)\n"
      "v_lshlrev_b32 v224, 16, v200\n"
      "v_and_b32 v225, s92, v200\n"
      "v_lshlrev_b32 v226, 16, v201\n"
      "v_and_b32 v227, s92, v201\n"
      "v_lshlrev_b32 v228, 16, v202\n"
      "v_and_b32 v229, s92, v202\n"
      "v_lshlrev_b32 v230, 16, v203\n"
      "v_and_b32 v231, s92, v203\n"
      "s_waitcnt lgkmcnt(4)\n"
      "v_lshlrev_b32 v232, 16, v204\n"
      "v_and_b32 v233, s92, v204\n"
      "v_lshlrev_b32 v234, 16, v205\n"
      "v_and_b32 v235, s92, v205\n"
      "v_lshlrev_b32 v236, 16, v206\n"
      "v_and_b32 v237, s92, v206\n"
      "v_pk_mul_f32 v[240:241], v[216:217], v[190:191]\n"
      "v_pk_mul_f32 v[242:243], v[218:219], v[190:191]\n"
      "v_pk_mul_f32 v[244:245], v[220:221], v[190:191]\n"
      "v_pk_mul_f32 v[246:247], v[222:223], v[190:191]\n"
      "v_pk_mul_f32 v[248:249], v[224:225], v[190:191]\n"
      "v_pk_mul_f32 v[250:251], v[226:227], v[190:191]\n"
      "v_pk_mul_f32 v[252:253], v[228:229], v[190:191]\n"
      "v_pk_mul_f32 v[254:255], v[230:231], v[190:191]\n"
      "v_pk_fma_f32 v[240:241], v[218:219], v[188:189], v[240:241]\n"
      "v_pk_fma_f32 v[242:243], v[220:221], v[188:189], v[242:243]\n"
      "v_pk_fma_f32 v[244:245], v[222:223], v[188:189], v[244:245]\n"
      "v_pk_fma_f32 v[246:247], v[224:225], v[188:189], v[246:247]\n"
      "v_pk_fma_f32 v[248:249], v[226:227], v[188:189], v[248:249]\n"
      "v_pk_fma_f32 v[250:251], v[228:229], v[188:189], v[250:251]\n"
      "v_pk_fma_f32 v[252:253], v[230:231], v[188:189], v[252:253]\n"
      "v_pk_fma_f32 v[254:255], v[232:233], v[188:189], v[254:255]\n"
      "v_pk_fma_f32 v[240:241], v[220:221], v[186:187], v[240:241]\n"
      "v_pk_fma_f32 v[242:243], v[222:223], v[186:187], v[242:243]\n"
      "v_pk_fma_f32 v[244:245], v[224:225], v[186:187], v[244:245]\n"
      "v_pk_fma_f32 v[246:247], v[226:227], v[186:187], v[246:247]\n"
      "v_pk_fma_f32 v[248:249], v[228:229], v[186:187], v[248:249]\n"
      "v_pk_fma_f32 v[250:251], v[230:231], v[186:187], v[250:251]\n"
      "v_pk_fma_f32 v[252:253], v[232:233], v[186:187], v[252:253]\n"
      "v_pk_fma_f32 v[254:255], v[234:235], v[186:187], v[254:255]\n"
      "v_pk_fma_f32 v[240:241], v[222:223], v[184:185], v[240:241]\n"
      "v_pk_fma_f32 v[242:243], v[224:225], v[184:185], v[242:243]\n"
      "v_pk_fma_f32 v[244:245], v[226:227], v[184:185], v[244:245]\n"
      "v_pk_fma_f32 v[246:247], v[228:229], v[184:185], v[246:247]\n"
      "v_pk_fma_f32 v[248:249], v[230:231], v[184:185], v[248:249]\n"
      "v_pk_fma_f32 v[250:251], v[232:233], v[184:185], v[250:251]\n"
      "v_pk_fma_f32 v[252:253], v[234:235], v[184:185], v[252:253]\n"
      "v_pk_fma_f32 v[254:255], v[236:237], v[184:185], v[254:255]\n"
      "s_waitcnt lgkmcnt(0)\n"
      "v_lshlrev_b32 v216, 16, v208\n"
      "v_and_b32 v217, s92, v208\n"
      "v_lshlrev_b32 v218, 16, v209\n"
      "v_and_b32 v219, s92, v209\n"
      "v_lshlrev_b32 v220, 16, v210\n"
      "v_and_b32 v221, s92, v210\n"
      "v_lshlrev_b32 v222, 16, v211\n"
      "v_and_b32 v223, s92, v211\n"
      "v_lshlrev_b32 v224, 16, v212\n"
      "v_and_b32 v225, s92, v212\n"
      "v_lshlrev_b32 v226, 16, v213\n"
      "v_and_b32 v227, s92, v213\n"
      "v_lshlrev_b32 v228, 16, v214\n"
      "v_and_b32 v229, s92, v214\n"
      "v_lshlrev_b32 v230, 16, v215\n"
      "v_and_b32 v231, s92, v215\n"
      "v_pk_mul_f32 v[240:241], v[240:241], v[216:217]\n"
      "v_pk_mul_f32 v[242:243], v[242:243], v[218:219]\n"
      "v_pk_mul_f32 v[244:245], v[244:245], v[220:221]\n"
      "v_pk_mul_f32 v[246:247], v[246:247], v[222:223]\n"
      "v_pk_mul_f32 v[248:249], v[248:249], v[224:225]\n"
      "v_pk_mul_f32 v[250:251], v[250:251], v[226:227]\n"
      "v_pk_mul_f32 v[252:253], v[252:253], v[228:229]\n"
      "v_pk_mul_f32 v[254:255], v[254:255], v[230:231]\n"
      "v_cvt_pk_bf16_f32 v196, v242, v240\n"
      "v_cvt_pk_bf16_f32 v197, v246, v244\n"
      "v_cvt_pk_bf16_f32 v198, v250, v248\n"
      "v_cvt_pk_bf16_f32 v199, v254, v252\n"
      "buffer_store_dwordx4 v[196:199], v8, s[20:23], s62 offen sc1\n"
      "v_cvt_pk_bf16_f32 v200, v243, v241\n"
      "v_cvt_pk_bf16_f32 v201, v247, v245\n"
      "v_cvt_pk_bf16_f32 v202, v251, v249\n"
      "v_cvt_pk_bf16_f32 v203, v255, v253\n"
      "buffer_store_dwordx4 v[200:203], v8, s[20:23], s63 offen sc1\n"
      "v_add_u32 v192, 17680, v192\n"
      "v_add_u32 v193, 17680, v193\n"
      "s_waitcnt vmcnt(8)\n"
      "ds_add_u32 v2, v3 offset:4100\n"
      "v_lshlrev_b32 v184, 16, v172\n"
      "v_lshlrev_b32 v185, 16, v174\n"
      "v_and_b32 v186, s92, v172\n"
      "v_and_b32 v187, s92, v174\n"
      "v_lshlrev_b32 v188, 16, v173\n"
      "v_lshlrev_b32 v189, 16, v175\n"
      "v_and_b32 v190, s92, v173\n"
      "v_and_b32 v191, s92, v175\n"
      "s_mul_i32 s62, s83, 67584\n"
      "s_lshl_b32 s63, s82, 7\n"
      "s_add_u32 s62, s62, s63\n"
      "s_add_u32 s62, s62, 32768\n"
      "s_add_u32 s63, s62, 4224\n"
      "s_lshl_b32 s64, s83, 5\n"
      "s_add_u32 s64, s64, s82\n"
      "s_lshl_b32 s64, s64, 7\n"
      "s_add_u32 s64, s64, 15859712\n"
      "s_mov_b32 exec_lo, 0xffff0000\n"
      "s_mov_b32 exec_hi, 0x0\n"
      "s_sub_u32 s64, s64, 128\n"
      "v_lshl_add_u32 v23, v1, 3, s64\n"
      "s_mov_b64 exec, -1\n"
      "s_mov_b32 s3, 0x80000\n"
      ".Lp_205:\n"
      "ds_read_b32 v31, v2 offset:4100\n"
      "s_sub_u32 s3, s3, 1\n"
      "s_waitcnt lgkmcnt(0)\n"
      "v_readlane_b32 s2, v31, 0\n"
      "s_cselect_b32 s2, -1, s2\n"
      "s_cmp_lt_u32 s2, 4\n"
      "s_cbranch_scc1 .Lp_205\n"
      "ds_read2_b32 v[196:197], v192 offset0:0 offset1:32\n"
      "ds_read2_b32 v[198:199], v192 offset0:64 offset1:100\n"
      "ds_read2_b32 v[200:201], v192 offset0:132 offset1:164\n"
      "ds_read2_b32 v[202:203], v192 offset0:196 offset1:228\n"
      "ds_read_b32 v204, v192 offset:1040\n"
      "ds_read_b32 v205, v192 offset:1168\n"
      "ds_read_b32 v206, v192 offset:1296\n"
      "ds_read2_b32 v[208:209], v193 offset0:0 offset1:32\n"
      "ds_read2_b32 v[210:211], v193 offset0:64 offset1:96\n"
      "ds_read2_b32 v[212:213], v193 offset0:128 offset1:160\n"
      "ds_read2_b32 v[214:215], v193 offset0:192 offset1:224\n"
      "s_waitcnt lgkmcnt(9)\n"
      "v_lshlrev_b32 v216, 16, v196\n"
      "v_and_b32 v217, s92, v196\n"
      "v_lshlrev_b32 v218, 16, v197\n"
      "v_and_b32 v219, s92, v197\n"
      "v_lshlrev_b32 v220, 16, v198\n"
      "v_and_b32 v221, s92, v198\n"
      "v_lshlrev_b32 v222, 16, v199\n"
      "v_and_b32 v223, s92, v199\n"
      "s_waitcnt lgkmcnt(7)\n"
      "v_lshlrev_b32 v224, 16, v200\n"
      "v_and_b32 v225, s92, v200\n"
      "v_lshlrev_b32 v226, 16, v201\n"
      "v_and_b32 v227, s92, v201\n"
      "v_lshlrev_b32 v228, 16, v202\n"
      "v_and_b32 v229, s92, v202\n"
      "v_lshlrev_b32 v230, 16, v203\n"
      "v_and_b32 v231, s92, v203\n"
      "s_waitcnt lgkmcnt(4)\n"
      "v_lshlrev_b32 v232, 16, v204\n"
      "v_and_b32 v233, s92, v204\n"
      "v_lshlrev_b32 v234, 16, v205\n"
      "v_and_b32 v235, s92, v205\n"
      "v_lshlrev_b32 v236, 16, v206\n"
      "v_and_b32 v237, s92, v206\n"
      "v_pk_mul_f32 v[240:241], v[216:217], v[190:191]\n"
      "v_pk_mul_f32 v[242:243], v[218:219], v[190:191]\n"
      "v_pk_mul_f32 v[244:245], v[220:221], v[190:191]\n"
      "v_pk_mul_f32 v[246:247], v[222:223], v[190:191]\n"
      "v_pk_mul_f32 v[248:249], v[224:225], v[190:191]\n"
      "v_pk_mul_f32 v[250:251], v[226:227], v[190:191]\n"
      "v_pk_mul_f32 v[252:253], v[228:229], v[190:191]\n"
      "v_pk_mul_f32 v[254:255], v[230:231], v[190:191]\n"
      "v_pk_fma_f32 v[240:241], v[218:219], v[188:189], v[240:241]\n"
      "v_pk_fma_f32 v[242:243], v[220:221], v[188:189], v[242:243]\n"
      "v_pk_fma_f32 v[244:245], v[222:223], v[188:189], v[244:245]\n"
      "v_pk_fma_f32 v[246:247], v[224:225], v[188:189], v[246:247]\n"
      "v_pk_fma_f32 v[248:249], v[226:227], v[188:189], v[248:249]\n"
      "v_pk_fma_f32 v[250:251], v[228:229], v[188:189], v[250:251]\n"
      "v_pk_fma_f32 v[252:253], v[230:231], v[188:189], v[252:253]\n"
      "v_pk_fma_f32 v[254:255], v[232:233], v[188:189], v[254:255]\n"
      "v_pk_fma_f32 v[240:241], v[220:221], v[186:187], v[240:241]\n"
      "v_pk_fma_f32 v[242:243], v[222:223], v[186:187], v[242:243]\n"
      "v_pk_fma_f32 v[244:245], v[224:225], v[186:187], v[244:245]\n"
      "v_pk_fma_f32 v[246:247], v[226:227], v[186:187], v[246:247]\n"
      "v_pk_fma_f32 v[248:249], v[228:229], v[186:187], v[248:249]\n"
      "v_pk_fma_f32 v[250:251], v[230:231], v[186:187], v[250:251]\n"
      "v_pk_fma_f32 v[252:253], v[232:233], v[186:187], v[252:253]\n"
      "v_pk_fma_f32 v[254:255], v[234:235], v[186:187], v[254:255]\n"
      "v_pk_fma_f32 v[240:241], v[222:223], v[184:185], v[240:241]\n"
      "v_pk_fma_f32 v[242:243], v[224:225], v[184:185], v[242:243]\n"
      "v_pk_fma_f32 v[244:245], v[226:227], v[184:185], v[244:245]\n"
      "v_pk_fma_f32 v[246:247], v[228:229], v[184:185], v[246:247]\n"
      "v_pk_fma_f32 v[248:249], v[230:231], v[184:185], v[248:249]\n"
      "v_pk_fma_f32 v[250:251], v[232:233], v[184:185], v[250:251]\n"
      "v_pk_fma_f32 v[252:253], v[234:235], v[184:185], v[252:253]\n"
      "v_pk_fma_f32 v[254:255], v[236:237], v[184:185], v[254:255]\n"
      "s_waitcnt lgkmcnt(0)\n"
      "v_lshlrev_b32 v216, 16, v208\n"
      "v_and_b32 v217, s92, v208\n"
      "v_lshlrev_b32 v218, 16, v209\n"
      "v_and_b32 v219, s92, v209\n"
      "v_lshlrev_b32 v220, 16, v210\n"
      "v_and_b32 v221, s92, v210\n"
      "v_lshlrev_b32 v222, 16, v211\n"
      "v_and_b32 v223, s92, v211\n"
      "v_lshlrev_b32 v224, 16, v212\n"
      "v_and_b32 v225, s92, v212\n"
      "v_lshlrev_b32 v226, 16, v213\n"
      "v_and_b32 v227, s92, v213\n"
      "v_lshlrev_b32 v228, 16, v214\n"
      "v_and_b32 v229, s92, v214\n"
      "v_lshlrev_b32 v230, 16, v215\n"
      "v_and_b32 v231, s92, v215\n"
      "v_pk_mul_f32 v[240:241], v[240:241], v[216:217]\n"
      "v_pk_mul_f32 v[242:243], v[242:243], v[218:219]\n"
      "v_pk_mul_f32 v[244:245], v[244:245], v[220:221]\n"
      "v_pk_mul_f32 v[246:247], v[246:247], v[222:223]\n"
      "v_pk_mul_f32 v[248:249], v[248:249], v[224:225]\n"
      "v_pk_mul_f32 v[250:251], v[250:251], v[226:227]\n"
      "v_pk_mul_f32 v[252:253], v[252:253], v[228:229]\n"
      "v_pk_mul_f32 v[254:255], v[254:255], v[230:231]\n"
      "v_cvt_pk_bf16_f32 v196, v242, v240\n"
      "v_cvt_pk_bf16_f32 v197, v246, v244\n"
      "v_cvt_pk_bf16_f32 v198, v250, v248\n"
      "v_cvt_pk_bf16_f32 v199, v254, v252\n"
      "buffer_store_dwordx4 v[196:199], v8, s[20:23], s62 offen sc1\n"
      "v_cvt_pk_bf16_f32 v200, v243, v241\n"
      "v_cvt_pk_bf16_f32 v201, v247, v245\n"
      "v_cvt_pk_bf16_f32 v202, v251, v249\n"
      "v_cvt_pk_bf16_f32 v203, v255, v253\n"
      "buffer_store_dwordx4 v[200:203], v8, s[20:23], s63 offen sc1\n"
      "v_add_u32 v192, 17680, v192\n"
      "v_add_u32 v193, 17680, v193\n"
      "s_waitcnt vmcnt(4)\n"
      "ds_add_u32 v2, v3 offset:4104\n"
      "v_lshlrev_b32 v184, 16, v176\n"
      "v_lshlrev_b32 v185, 16, v178\n"
      "v_and_b32 v186, s92, v176\n"
      "v_and_b32 v187, s92, v178\n"
      "v_lshlrev_b32 v188, 16, v177\n"
      "v_lshlrev_b32 v189, 16, v179\n"
      "v_and_b32 v190, s92, v177\n"
      "v_and_b32 v191, s92, v179\n"
      "s_mul_i32 s62, s85, 67584\n"
      "s_lshl_b32 s63, s84, 7\n"
      "s_add_u32 s62, s62, s63\n"
      "s_add_u32 s62, s62, 32768\n"
      "s_add_u32 s63, s62, 4224\n"
      "s_lshl_b32 s64, s85, 5\n"
      "s_add_u32 s64, s64, s84\n"
      "s_lshl_b32 s64, s64, 7\n"
      "s_add_u32 s64, s64, 15859712\n"
      "s_mov_b32 exec_lo, 0x0\n"
      "s_mov_b32 exec_hi, 0xffff\n"
      "s_sub_u32 s64, s64, 256\n"
      "v_lshl_add_u32 v23, v1, 3, s64\n"
      "s_mov_b64 exec, -1\n"
      "s_mov_b32 s3, 0x80000\n"
      ".Lp_206:\n"
      "ds_read_b32 v31, v2 offset:4104\n"
      "s_sub_u32 s3, s3, 1\n"
      "s_waitcnt lgkmcnt(0)\n"
      "v_readlane_b32 s2, v31, 0\n"
      "s_cselect_b32 s2, -1, s2\n"
      "s_cmp_lt_u32 s2, 4\n"
      "s_cbranch_scc1 .Lp_206\n"
      "ds_read2_b32 v[196:197], v192 offset0:0 offset1:32\n"
      "ds_read2_b32 v[198:199], v192 offset0:64 offset1:100\n"
      "ds_read2_b32 v[200:201], v192 offset0:132 offset1:164\n"
      "ds_read2_b32 v[202:203], v192 offset0:196 offset1:228\n"
      "ds_read_b32 v204, v192 offset:1040\n"
      "ds_read_b32 v205, v192 offset:1168\n"
      "ds_read_b32 v206, v192 offset:1296\n"
      "ds_read2_b32 v[208:209], v193 offset0:0 offset1:32\n"
      "ds_read2_b32 v[210:211], v193 offset0:64 offset1:96\n"
      "ds_read2_b32 v[212:213], v193 offset0:128 offset1:160\n"
      "ds_read2_b32 v[214:215], v193 offset0:192 offset1:224\n"
      "s_waitcnt lgkmcnt(9)\n"
      "v_lshlrev_b32 v216, 16, v196\n"
      "v_and_b32 v217, s92, v196\n"
      "v_lshlrev_b32 v218, 16, v197\n"
      "v_and_b32 v219, s92, v197\n"
      "v_lshlrev_b32 v220, 16, v198\n"
      "v_and_b32 v221, s92, v198\n"
      "v_lshlrev_b32 v222, 16, v199\n"
      "v_and_b32 v223, s92, v199\n"
      "s_waitcnt lgkmcnt(7)\n"
      "v_lshlrev_b32 v224, 16, v200\n"
      "v_and_b32 v225, s92, v200\n"
      "v_lshlrev_b32 v226, 16, v201\n"
      "v_and_b32 v227, s92, v201\n"
      "v_lshlrev_b32 v228, 16, v202\n"
      "v_and_b32 v229, s92, v202\n"
      "v_lshlrev_b32 v230, 16, v203\n"
      "v_and_b32 v231, s92, v203\n"
      "s_waitcnt lgkmcnt(4)\n"
      "v_lshlrev_b32 v232, 16, v204\n"
      "v_and_b32 v233, s92, v204\n"
      "v_lshlrev_b32 v234, 16, v205\n"
      "v_and_b32 v235, s92, v205\n"
      "v_lshlrev_b32 v236, 16, v206\n"
      "v_and_b32 v237, s92, v206\n"
      "v_pk_mul_f32 v[240:241], v[216:217], v[190:191]\n"
      "v_pk_mul_f32 v[242:243], v[218:219], v[190:191]\n"
      "v_pk_mul_f32 v[244:245], v[220:221], v[190:191]\n"
      "v_pk_mul_f32 v[246:247], v[222:223], v[190:191]\n"
      "v_pk_mul_f32 v[248:249], v[224:225], v[190:191]\n"
      "v_pk_mul_f32 v[250:251], v[226:227], v[190:191]\n"
      "v_pk_mul_f32 v[252:253], v[228:229], v[190:191]\n"
      "v_pk_mul_f32 v[254:255], v[230:231], v[190:191]\n"
      "v_pk_fma_f32 v[240:241], v[218:219], v[188:189], v[240:241]\n"
      "v_pk_fma_f32 v[242:243], v[220:221], v[188:189], v[242:243]\n"
      "v_pk_fma_f32 v[244:245], v[222:223], v[188:189], v[244:245]\n"
      "v_pk_fma_f32 v[246:247], v[224:225], v[188:189], v[246:247]\n"
      "v_pk_fma_f32 v[248:249], v[226:227], v[188:189], v[248:249]\n"
      "v_pk_fma_f32 v[250:251], v[228:229], v[188:189], v[250:251]\n"
      "v_pk_fma_f32 v[252:253], v[230:231], v[188:189], v[252:253]\n"
      "v_pk_fma_f32 v[254:255], v[232:233], v[188:189], v[254:255]\n"
      "v_pk_fma_f32 v[240:241], v[220:221], v[186:187], v[240:241]\n"
      "v_pk_fma_f32 v[242:243], v[222:223], v[186:187], v[242:243]\n"
      "v_pk_fma_f32 v[244:245], v[224:225], v[186:187], v[244:245]\n"
      "v_pk_fma_f32 v[246:247], v[226:227], v[186:187], v[246:247]\n"
      "v_pk_fma_f32 v[248:249], v[228:229], v[186:187], v[248:249]\n"
      "v_pk_fma_f32 v[250:251], v[230:231], v[186:187], v[250:251]\n"
      "v_pk_fma_f32 v[252:253], v[232:233], v[186:187], v[252:253]\n"
      "v_pk_fma_f32 v[254:255], v[234:235], v[186:187], v[254:255]\n"
      "v_pk_fma_f32 v[240:241], v[222:223], v[184:185], v[240:241]\n"
      "v_pk_fma_f32 v[242:243], v[224:225], v[184:185], v[242:243]\n"
      "v_pk_fma_f32 v[244:245], v[226:227], v[184:185], v[244:245]\n"
      "v_pk_fma_f32 v[246:247], v[228:229], v[184:185], v[246:247]\n"
      "v_pk_fma_f32 v[248:249], v[230:231], v[184:185], v[248:249]\n"
      "v_pk_fma_f32 v[250:251], v[232:233], v[184:185], v[250:251]\n"
      "v_pk_fma_f32 v[252:253], v[234:235], v[184:185], v[252:253]\n"
      "v_pk_fma_f32 v[254:255], v[236:237], v[184:185], v[254:255]\n"
      "s_waitcnt lgkmcnt(0)\n"
      "v_lshlrev_b32 v216, 16, v208\n"
      "v_and_b32 v217, s92, v208\n"
      "v_lshlrev_b32 v218, 16, v209\n"
      "v_and_b32 v219, s92, v209\n"
      "v_lshlrev_b32 v220, 16, v210\n"
      "v_and_b32 v221, s92, v210\n"
      "v_lshlrev_b32 v222, 16, v211\n"
      "v_and_b32 v223, s92, v211\n"
      "v_lshlrev_b32 v224, 16, v212\n"
      "v_and_b32 v225, s92, v212\n"
      "v_lshlrev_b32 v226, 16, v213\n"
      "v_and_b32 v227, s92, v213\n"
      "v_lshlrev_b32 v228, 16, v214\n"
      "v_and_b32 v229, s92, v214\n"
      "v_lshlrev_b32 v230, 16, v215\n"
      "v_and_b32 v231, s92, v215\n"
      "v_pk_mul_f32 v[240:241], v[240:241], v[216:217]\n"
      "v_pk_mul_f32 v[242:243], v[242:243], v[218:219]\n"
      "v_pk_mul_f32 v[244:245], v[244:245], v[220:221]\n"
      "v_pk_mul_f32 v[246:247], v[246:247], v[222:223]\n"
      "v_pk_mul_f32 v[248:249], v[248:249], v[224:225]\n"
      "v_pk_mul_f32 v[250:251], v[250:251], v[226:227]\n"
      "v_pk_mul_f32 v[252:253], v[252:253], v[228:229]\n"
      "v_pk_mul_f32 v[254:255], v[254:255], v[230:231]\n"
      "v_cvt_pk_bf16_f32 v196, v242, v240\n"
      "v_cvt_pk_bf16_f32 v197, v246, v244\n"
      "v_cvt_pk_bf16_f32 v198, v250, v248\n"
      "v_cvt_pk_bf16_f32 v199, v254, v252\n"
      "buffer_store_dwordx4 v[196:199], v8, s[20:23], s62 offen sc1\n"
      "v_cvt_pk_bf16_f32 v200, v243, v241\n"
      "v_cvt_pk_bf16_f32 v201, v247, v245\n"
      "v_cvt_pk_bf16_f32 v202, v251, v249\n"
      "v_cvt_pk_bf16_f32 v203, v255, v253\n"
      "buffer_store_dwordx4 v[200:203], v8, s[20:23], s63 offen sc1\n"
      "s_waitcnt vmcnt(0)\n"
      "buffer_store_dwordx2 v[12:13], v23, s[20:23], 0 offen sc1\n"
      "ds_add_u32 v2, v3 offset:4108\n"
      "ds_read_b32 v26, v2 offset:5120\n"
      "v_lshlrev_b32 v31, 3, v1\n"
      "v_add_u32 v31, 163072, v31\n"
      "s_waitcnt lgkmcnt(0)\n"
      "v_and_b32 v27, s92, v26\n"
      "v_lshlrev_b32 v26, 16, v26\n"
      "ds_write_b64 v31, v[26:27]\n"
      "ds_add_u32 v2, v3 offset:4168\n"
      "s_mov_b32 s81, 2\n"
      "s_lshl3_add_u32 s64, s60, s52\n"
      "s_cmp_eq_u32 s64, 2040\n"
      "s_cbranch_scc0 .Le_207\n"
      "v_lshlrev_b32 v25, 3, v1\n"
      "s_mov_b32 s64, 4096\n"
      "buffer_load_dwordx2 v[26:27], v25, s[20:23], s64 offen sc1\n"
      "s_waitcnt vmcnt(0)\n"
      "v_cmp_ne_u64 vcc, s[58:59], v[26:27]\n"
      "s_cbranch_vccnz .Lf_208\n"
      ".Lf_209:\n"
      "s_mov_b32 s64, 4608\n"
      "buffer_load_dwordx2 v[26:27], v25, s[20:23], s64 offen sc1\n"
      "s_waitcnt vmcnt(0)\n"
      "v_cmp_ne_u64 vcc, s[58:59], v[26:27]\n"
      "s_cbranch_vccnz .Lf_211\n"
      ".Lf_212:\n"
      "s_mov_b32 s64, 5120\n"
      "buffer_load_dwordx2 v[26:27], v25, s[20:23], s64 offen sc1\n"
      "s_waitcnt vmcnt(0)\n"
      "v_cmp_ne_u64 vcc, s[58:59], v[26:27]\n"
      "s_cbranch_vccnz .Lf_214\n"
      ".Lf_215:\n"
      "s_mov_b32 s64, 5632\n"
      "buffer_load_dwordx2 v[26:27], v25, s[20:23], s64 offen sc1\n"
      "s_waitcnt vmcnt(0)\n"
      "v_cmp_ne_u64 vcc, s[58:59], v[26:27]\n"
      "s_cbranch_vccnz .Lf_217\n"
      ".Lf_218:\n"
      "s_mov_b64 exec, 1\n"
      "buffer_store_dwordx2 v[12:13], off, s[20:23], 0 sc1\n"
      "s_mov_b64 exec, -1\n"
      ".Le_207:\n"
      "s_sub_u32 s66, 3, s53\n"
      "s_lshl1_add_u32 s62, s56, s54\n"
      "s_lshl2_add_u32 s63, s62, s66\n"
      "s_mul_i32 s62, s63, 20480\n"
      "s_mul_i32 s63, s55, 160\n"
      "s_add_u32 s62, s62, s63\n"
      "v_and_b32 v21, 15, v1\n"
      "v_mul_u32_u24 v21, 1280, v21\n"
      "v_lshrrev_b32 v22, 4, v1\n"
      "v_lshl_add_u32 v21, v22, 3, v21\n"
      "buffer_load_dwordx2 v[218:219], v21, s[4:7], s62 offen offset:0\n"
      "buffer_load_dwordx2 v[220:221], v21, s[4:7], s62 offen offset:32\n"
      "buffer_load_dwordx2 v[222:223], v21, s[4:7], s62 offen offset:64\n"
      "buffer_load_dwordx2 v[224:225], v21, s[4:7], s62 offen offset:96\n"
      "buffer_load_dwordx2 v[226:227], v21, s[4:7], s62 offen offset:128\n"
      "s_mov_b32 s79, 1073741824\n"
      "s_mov_b32 s77, 1073741824\n"
      "s_mov_b32 s84, 64\n"
      "s_mov_b32 s85, 64\n"
      "s_setprio 3\n"
      "v_and_b32 v10, 1, v1\n"
      "v_xor_b32 v10, 1, v10\n"
      "v_mul_u32_u24 v10, 4160, v10\n"
      "v_and_b32 v11, 14, v1\n"
      "v_lshl_add_u32 v10, v11, 1, v10\n"
      "v_and_b32 v11, 48, v1\n"
      "v_sub_u32 v10, v10, v11\n"
      "v_and_b32 v21, 15, v1\n"
      "v_lshl_add_u32 v11, v21, 5, v11\n"
      "s_lshr_b32 s64, s53, 1\n"
      "s_and_b32 s65, s53, 1\n"
      "s_mul_i32 s67, s64, 26112\n"
      "s_add_u32 s67, s67, 70144\n"
      "s_mul_i32 s62, s65, 8320\n"
      "s_add_u32 s62, s62, s67\n"
      "s_add_u32 s62, s62, 84\n"
      "v_add_u32 v212, s62, v10\n"
      "s_mul_i32 s62, s65, 4608\n"
      "s_add_u32 s62, s62, s67\n"
      "s_add_u32 s62, s62, 16672\n"
      "v_add_u32 v217, s62, v11\n"
      "v_lshrrev_b32 v22, 5, v1\n"
      "v_lshlrev_b32 v22, 4, v22\n"
      "v_lshl_add_u32 v22, v21, 5, v22\n"
      "v_bfe_u32 v21, v1, 4, 1\n"
      "v_lshl_add_u32 v5, v21, 9, v22\n"
      "s_add_u32 s63, s98, s53\n"
      "s_mul_i32 s63, s63, 4224\n"
      "s_add_u32 s63, s63, 15872\n"
      "s_mov_b32 s3, 0x80000\n"
      ".Lp_220:\n"
      "s_sleep 1\n"
      "ds_read_b32 v31, v2 offset:4240\n"
      "s_sub_u32 s3, s3, 1\n"
      "s_waitcnt lgkmcnt(0)\n"
      "v_readlane_b32 s2, v31, 0\n"
      "s_cselect_b32 s2, -1, s2\n"
      "s_cmp_lt_u32 s2, 6\n"
      "s_cbranch_scc1 .Lp_220\n"
      "v_add_u32 v213, 1024, v212\n"
      "v_add_u32 v214, 2048, v212\n"
      "v_add_u32 v215, 3072, v212\n"
      "ds_read2_b32 v[144:145], v212 offset0:3 offset1:2\n"
      "ds_read2_b32 v[146:147], v212 offset0:1 offset1:0\n"
      "ds_read_b128 v[188:191], v217 offset:1984\n"
      "ds_read_b128 v[184:187], v217 offset:1472\n"
      "ds_read_b128 v[180:183], v217 offset:960\n"
      "ds_read_b128 v[176:179], v217 offset:448\n"
      "ds_read2_b32 v[148:149], v212 offset0:131 offset1:130\n"
      "ds_read2_b32 v[150:151], v212 offset0:129 offset1:128\n"
      "ds_read2_b32 v[152:153], v213 offset0:3 offset1:2\n"
      "ds_read2_b32 v[154:155], v213 offset0:1 offset1:0\n"
      "s_waitcnt lgkmcnt(7)\n"
      "v_mfma_f32_16x16x32_bf16 v[108:111], v[144:147], v[188:191], 0\n"
      "ds_read2_b32 v[156:157], v213 offset0:131 offset1:130\n"
      "s_waitcnt lgkmcnt(7)\n"
      "v_mfma_f32_16x16x32_bf16 v[104:107], v[144:147], v[184:187], 0\n"
      "ds_read2_b32 v[158:159], v213 offset0:129 offset1:128\n"
      "s_waitcnt lgkmcnt(7)\n"
      "v_mfma_f32_16x16x32_bf16 v[100:103], v[144:147], v[180:183], 0\n"
      "ds_read2_b32 v[160:161], v214 offset0:3 offset1:2\n"
      "s_waitcnt lgkmcnt(7)\n"
      "v_mfma_f32_16x16x32_bf16 v[96:99], v[144:147], v[176:179], 0\n"
      "ds_read2_b32 v[162:163], v214 offset0:1 offset1:0\n"
      "s_waitcnt lgkmcnt(6)\n"
      "v_mfma_f32_16x16x32_bf16 v[112:115], v[148:151], v[188:191], 0\n"
      "ds_read2_b32 v[164:165], v214 offset0:131 offset1:130\n"
      "v_mfma_f32_16x16x32_bf16 v[108:111], v[148:151], v[184:187], v[108:111]\n"
      "ds_read2_b32 v[166:167], v214 offset0:129 offset1:128\n"
      "v_mfma_f32_16x16x32_bf16 v[104:107], v[148:151], v[180:183], v[104:107]\n"
      "ds_read2_b32 v[168:169], v215 offset0:3 offset1:2\n"
      "v_mfma_f32_16x16x32_bf16 v[100:103], v[148:151], v[176:179], v[100:103]\n"
      "ds_read2_b32 v[170:171], v215 offset0:1 offset1:0\n"
      "s_waitcnt lgkmcnt(7)\n"
      "v_mfma_f32_16x16x32_bf16 v[116:119], v[152:155], v[188:191], 0\n"
      "ds_read2_b32 v[172:173], v215 offset0:131 offset1:130\n"
      "v_mfma_f32_16x16x32_bf16 v[112:115], v[152:155], v[184:187], v[112:115]\n"
      "ds_read2_b32 v[174:175], v215 offset0:129 offset1:128\n"
      "v_mfma_f32_16x16x32_bf16 v[108:111], v[152:155], v[180:183], v[108:111]\n"
      "ds_read2_b32 v[144:145], v212 offset0:19 offset1:18\n"
      "v_mfma_f32_16x16x32_bf16 v[104:107], v[152:155], v[176:179], v[104:107]\n"
      "ds_read2_b32 v[146:147], v212 offset0:17 offset1:16\n"
      "s_waitcnt lgkmcnt(7)\n"
      "v_mfma_f32_16x16x32_bf16 v[120:123], v[156:159], v[188:191], 0\n"
      "ds_read_b128 v[204:207], v217 offset:1920\n"
      "v_mfma_f32_16x16x32_bf16 v[116:119], v[156:159], v[184:187], v[116:119]\n"
      "ds_read_b128 v[200:203], v217 offset:1408\n"
      "v_mfma_f32_16x16x32_bf16 v[112:115], v[156:159], v[180:183], v[112:115]\n"
      "ds_read_b128 v[196:199], v217 offset:896\n"
      "v_mfma_f32_16x16x32_bf16 v[108:111], v[156:159], v[176:179], v[108:111]\n"
      "ds_read_b128 v[192:195], v217 offset:384\n"
      "v_mfma_f32_16x16x32_bf16 v[124:127], v[160:163], v[188:191], 0\n"
      "ds_read2_b32 v[148:149], v212 offset0:147 offset1:146\n"
      "v_mfma_f32_16x16x32_bf16 v[120:123], v[160:163], v[184:187], v[120:123]\n"
      "ds_read2_b32 v[150:151], v212 offset0:145 offset1:144\n"
      "v_mfma_f32_16x16x32_bf16 v[116:119], v[160:163], v[180:183], v[116:119]\n"
      "ds_read2_b32 v[152:153], v213 offset0:19 offset1:18\n"
      "v_mfma_f32_16x16x32_bf16 v[112:115], v[160:163], v[176:179], v[112:115]\n"
      "ds_read2_b32 v[154:155], v213 offset0:17 offset1:16\n"
      "s_waitcnt lgkmcnt(7)\n"
      "v_mfma_f32_16x16x32_bf16 v[124:127], v[164:167], v[184:187], v[124:127]\n"
      "ds_read2_b32 v[156:157], v213 offset0:147 offset1:146\n"
      "v_mfma_f32_16x16x32_bf16 v[120:123], v[164:167], v[180:183], v[120:123]\n"
      "ds_read2_b32 v[158:159], v213 offset0:145 offset1:144\n"
      "v_mfma_f32_16x16x32_bf16 v[116:119], v[164:167], v[176:179], v[116:119]\n"
      "ds_read2_b32 v[160:161], v214 offset0:19 offset1:18\n"
      "v_mfma_f32_16x16x32_bf16 v[124:127], v[168:171], v[180:183], v[124:127]\n"
      "ds_read2_b32 v[162:163], v214 offset0:17 offset1:16\n"
      "v_mfma_f32_16x16x32_bf16 v[120:123], v[168:171], v[176:179], v[120:123]\n"
      "ds_read2_b32 v[164:165], v214 offset0:147 offset1:146\n"
      "v_mfma_f32_16x16x32_bf16 v[124:127], v[172:175], v[176:179], v[124:127]\n"
      "ds_read2_b32 v[166:167], v214 offset0:145 offset1:144\n"
      "v_mfma_f32_16x16x32_bf16 v[108:111], v[144:147], v[204:207], v[108:111]\n"
      "ds_read2_b32 v[168:169], v215 offset0:19 offset1:18\n"
      "s_waitcnt lgkmcnt(7)\n"
      "v_mfma_f32_16x16x32_bf16 v[104:107], v[144:147], v[200:203], v[104:107]\n"
      "ds_read2_b32 v[170:171], v215 offset0:17 offset1:16\n"
      "v_mfma_f32_16x16x32_bf16 v[100:103], v[144:147], v[196:199], v[100:103]\n"
      "ds_read2_b32 v[172:173], v215 offset0:147 offset1:146\n"
      "v_mfma_f32_16x16x32_bf16 v[96:99], v[144:147], v[192:195], v[96:99]\n"
      "ds_read2_b32 v[174:175], v215 offset0:145 offset1:144\n"
      "v_mfma_f32_16x16x32_bf16 v[112:115], v[148:151], v[204:207], v[112:115]\n"
      "ds_read2_b32 v[144:145], v212 offset0:35 offset1:34\n"
      "v_mfma_f32_16x16x32_bf16 v[108:111], v[148:151], v[200:203], v[108:111]\n"
      "ds_read2_b32 v[146:147], v212 offset0:33 offset1:32\n"
      "v_mfma_f32_16x16x32_bf16 v[104:107], v[148:151], v[196:199], v[104:107]\n"
      "ds_read_b128 v[188:191], v217 offset:1856\n"
      "v_mfma_f32_16x16x32_bf16 v[100:103], v[148:151], v[192:195], v[100:103]\n"
      "ds_read_b128 v[184:187], v217 offset:1344\n"
      "v_mfma_f32_16x16x32_bf16 v[116:119], v[152:155], v[204:207], v[116:119]\n"
      "ds_read_b128 v[180:183], v217 offset:832\n"
      "v_mfma_f32_16x16x32_bf16 v[112:115], v[152:155], v[200:203], v[112:115]\n"
      "ds_read_b128 v[176:179], v217 offset:320\n"
      "v_mfma_f32_16x16x32_bf16 v[108:111], v[152:155], v[196:199], v[108:111]\n"
      "ds_read2_b32 v[148:149], v212 offset0:163 offset1:162\n"
      "v_mfma_f32_16x16x32_bf16 v[104:107], v[152:155], v[192:195], v[104:107]\n"
      "ds_read2_b32 v[150:151], v212 offset0:161 offset1:160\n"
      "s_waitcnt lgkmcnt(7)\n"
      "v_mfma_f32_16x16x32_bf16 v[120:123], v[156:159], v[204:207], v[120:123]\n"
      "ds_read2_b32 v[152:153], v213 offset0:35 offset1:34\n"
      "v_mfma_f32_16x16x32_bf16 v[116:119], v[156:159], v[200:203], v[116:119]\n"
      "ds_read2_b32 v[154:155], v213 offset0:33 offset1:32\n"
      "v_mfma_f32_16x16x32_bf16 v[112:115], v[156:159], v[196:199], v[112:115]\n"
      "v_mfma_f32_16x16x32_bf16 v[108:111], v[156:159], v[192:195], v[108:111]\n"
      "ds_read2_b32 v[156:157], v213 offset0:163 offset1:162\n"
      "v_mfma_f32_16x16x32_bf16 v[124:127], v[160:163], v[204:207], v[124:127]\n"
      "ds_read2_b32 v[158:159], v213 offset0:161 offset1:160\n"
      "v_mfma_f32_16x16x32_bf16 v[120:123], v[160:163], v[200:203], v[120:123]\n"
      "v_mfma_f32_16x16x32_bf16 v[116:119], v[160:163], v[196:199], v[116:119]\n"
      "v_mfma_f32_16x16x32_bf16 v[112:115], v[160:163], v[192:195], v[112:115]\n"
      "ds_read2_b32 v[160:161], v214 offset0:35 offset1:34\n"
      "v_mfma_f32_16x16x32_bf16 v[124:127], v[164:167], v[200:203], v[124:127]\n"
      "ds_read2_b32 v[162:163], v214 offset0:33 offset1:32\n"
      "v_mfma_f32_16x16x32_bf16 v[120:123], v[164:167], v[196:199], v[120:123]\n"
      "v_mfma_f32_16x16x32_bf16 v[116:119], v[164:167], v[192:195], v[116:119]\n"
      "ds_read2_b32 v[164:165], v214 offset0:163 offset1:162\n"
      "v_mfma_f32_16x16x32_bf16 v[124:127], v[168:171], v[196:199], v[124:127]\n"
      "ds_read2_b32 v[166:167], v214 offset0:161 offset1:160\n"
      "v_mfma_f32_16x16x32_bf16 v[120:123], v[168:171], v[192:195], v[120:123]\n"
      "v_mfma_f32_16x16x32_bf16 v[124:127], v[172:175], v[192:195], v[124:127]\n"
      "ds_read2_b32 v[168:169], v215 offset0:35 offset1:34\n"
      "s_waitcnt lgkmcnt(5)\n"
      "v_mfma_f32_16x16x32_bf16 v[108:111], v[144:147], v[188:191], v[108:111]\n"
      "ds_read2_b32 v[170:171], v215 offset0:33 offset1:32\n"
      "v_mfma_f32_16x16x32_bf16 v[104:107], v[144:147], v[184:187], v[104:107]\n"
      "ds_read2_b32 v[172:173], v215 offset0:163 offset1:162\n"
      "v_mfma_f32_16x16x32_bf16 v[100:103], v[144:147], v[180:183], v[100:103]\n"
      "ds_read2_b32 v[174:175], v215 offset0:161 offset1:160\n"
      "v_mfma_f32_16x16x32_bf16 v[96:99], v[144:147], v[176:179], v[96:99]\n"
      "ds_read2_b32 v[144:145], v212 offset0:51 offset1:50\n"
      "v_mfma_f32_16x16x32_bf16 v[112:115], v[148:151], v[188:191], v[112:115]\n"
      "ds_read2_b32 v[146:147], v212 offset0:49 offset1:48\n"
      "v_mfma_f32_16x16x32_bf16 v[108:111], v[148:151], v[184:187], v[108:111]\n"
      "ds_read_b128 v[204:207], v217 offset:1792\n"
      "v_mfma_f32_16x16x32_bf16 v[104:107], v[148:151], v[180:183], v[104:107]\n"
      "ds_read_b128 v[200:203], v217 offset:1280\n"
      "v_mfma_f32_16x16x32_bf16 v[100:103], v[148:151], v[176:179], v[100:103]\n"
      "ds_read_b128 v[196:199], v217 offset:768\n"
      "v_mfma_f32_16x16x32_bf16 v[116:119], v[152:155], v[188:191], v[116:119]\n"
      "ds_read_b128 v[192:195], v217 offset:256\n"
      "v_mfma_f32_16x16x32_bf16 v[112:115], v[152:155], v[184:187], v[112:115]\n"
      "ds_read2_b32 v[148:149], v212 offset0:179 offset1:178\n"
      "v_mfma_f32_16x16x32_bf16 v[108:111], v[152:155], v[180:183], v[108:111]\n"
      "ds_read2_b32 v[150:151], v212 offset0:177 offset1:176\n"
      "v_mfma_f32_16x16x32_bf16 v[104:107], v[152:155], v[176:179], v[104:107]\n"
      "ds_read2_b32 v[152:153], v213 offset0:51 offset1:50\n"
      "v_mfma_f32_16x16x32_bf16 v[120:123], v[156:159], v[188:191], v[120:123]\n"
      "ds_read2_b32 v[154:155], v213 offset0:49 offset1:48\n"
      "v_mfma_f32_16x16x32_bf16 v[116:119], v[156:159], v[184:187], v[116:119]\n"
      "v_mfma_f32_16x16x32_bf16 v[112:115], v[156:159], v[180:183], v[112:115]\n"
      "v_mfma_f32_16x16x32_bf16 v[108:111], v[156:159], v[176:179], v[108:111]\n"
      "ds_read2_b32 v[156:157], v213 offset0:179 offset1:178\n"
      "s_waitcnt lgkmcnt(5)\n"
      "v_mfma_f32_16x16x32_bf16 v[124:127], v[160:163], v[188:191], v[124:127]\n"
      "ds_read2_b32 v[158:159], v213 offset0:177 offset1:176\n"
      "v_mfma_f32_16x16x32_bf16 v[120:123], v[160:163], v[184:187], v[120:123]\n"
      "v_mfma_f32_16x16x32_bf16 v[116:119], v[160:163], v[180:183], v[116:119]\n"
      "v_mfma_f32_16x16x32_bf16 v[112:115], v[160:163], v[176:179], v[112:115]\n"
      "ds_read2_b32 v[160:161], v214 offset0:51 offset1:50\n"
      "v_mfma_f32_16x16x32_bf16 v[124:127], v[164:167], v[184:187], v[124:127]\n"
      "ds_read2_b32 v[162:163], v214 offset0:49 offset1:48\n"
      "v_mfma_f32_16x16x32_bf16 v[120:123], v[164:167], v[180:183], v[120:123]\n"
      "v_mfma_f32_16x16x32_bf16 v[116:119], v[164:167], v[176:179], v[116:119]\n"
      "ds_read2_b32 v[164:165], v214 offset0:179 offset1:178\n"
      "v_mfma_f32_16x16x32_bf16 v[124:127], v[168:171], v[180:183], v[124:127]\n"
      "ds_read2_b32 v[166:167], v214 offset0:177 offset1:176\n"
      "v_mfma_f32_16x16x32_bf16 v[120:123], v[168:171], v[176:179], v[120:123]\n"
      "v_mfma_f32_16x16x32_bf16 v[124:127], v[172:175], v[176:179], v[124:127]\n"
      "ds_read2_b32 v[168:169], v215 offset0:51 offset1:50\n"
      "v_mfma_f32_16x16x32_bf16 v[108:111], v[144:147], v[204:207], v[108:111]\n"
      "ds_read2_b32 v[170:171], v215 offset0:49 offset1:48\n"
      "v_mfma_f32_16x16x32_bf16 v[104:107], v[144:147], v[200:203], v[104:107]\n"
      "ds_read2_b32 v[172:173], v215 offset0:179 offset1:178\n"
      "v_mfma_f32_16x16x32_bf16 v[100:103], v[144:147], v[196:199], v[100:103]\n"
      "ds_read2_b32 v[174:175], v215 offset0:177 offset1:176\n"
      "v_mfma_f32_16x16x32_bf16 v[96:99], v[144:147], v[192:195], v[96:99]\n"
      "ds_read2_b32 v[144:145], v212 offset0:67 offset1:66\n"
      "s_waitcnt lgkmcnt(6)\n"
      "v_mfma_f32_16x16x32_bf16 v[112:115], v[148:151], v[204:207], v[112:115]\n"
      "ds_read2_b32 v[146:147], v212 offset0:65 offset1:64\n"
      "v_mfma_f32_16x16x32_bf16 v[108:111], v[148:151], v[200:203], v[108:111]\n"
      "ds_read_b128 v[188:191], v217 offset:1728\n"
      "v_mfma_f32_16x16x32_bf16 v[104:107], v[148:151], v[196:199], v[104:107]\n"
      "ds_read_b128 v[184:187], v217 offset:1216\n"
      "v_mfma_f32_16x16x32_bf16 v[100:103], v[148:151], v[192:195], v[100:103]\n"
      "ds_read_b128 v[180:183], v217 offset:704\n"
      "v_mfma_f32_16x16x32_bf16 v[116:119], v[152:155], v[204:207], v[116:119]\n"
      "ds_read_b128 v[176:179], v217 offset:192\n"
      "v_mfma_f32_16x16x32_bf16 v[112:115], v[152:155], v[200:203], v[112:115]\n"
      "ds_read2_b32 v[148:149], v212 offset0:195 offset1:194\n"
      "v_mfma_f32_16x16x32_bf16 v[108:111], v[152:155], v[196:199], v[108:111]\n"
      "ds_read2_b32 v[150:151], v212 offset0:193 offset1:192\n"
      "v_mfma_f32_16x16x32_bf16 v[104:107], v[152:155], v[192:195], v[104:107]\n"
      "ds_read2_b32 v[152:153], v213 offset0:67 offset1:66\n"
      "v_mfma_f32_16x16x32_bf16 v[120:123], v[156:159], v[204:207], v[120:123]\n"
      "ds_read2_b32 v[154:155], v213 offset0:65 offset1:64\n"
      "v_mfma_f32_16x16x32_bf16 v[116:119], v[156:159], v[200:203], v[116:119]\n"
      "v_mfma_f32_16x16x32_bf16 v[112:115], v[156:159], v[196:199], v[112:115]\n"
      "v_mfma_f32_16x16x32_bf16 v[108:111], v[156:159], v[192:195], v[108:111]\n"
      "ds_read2_b32 v[156:157], v213 offset0:195 offset1:194\n"
      "v_mfma_f32_16x16x32_bf16 v[124:127], v[160:163], v[204:207], v[124:127]\n"
      "ds_read2_b32 v[158:159], v213 offset0:193 offset1:192\n"
      "v_mfma_f32_16x16x32_bf16 v[120:123], v[160:163], v[200:203], v[120:123]\n"
      "v_mfma_f32_16x16x32_bf16 v[116:119], v[160:163], v[196:199], v[116:119]\n"
      "v_mfma_f32_16x16x32_bf16 v[112:115], v[160:163], v[192:195], v[112:115]\n"
      "ds_read2_b32 v[160:161], v214 offset0:67 offset1:66\n"
      "s_waitcnt lgkmcnt(3)\n"
      "v_mfma_f32_16x16x32_bf16 v[124:127], v[164:167], v[200:203], v[124:127]\n"
      "ds_read2_b32 v[162:163], v214 offset0:65 offset1:64\n"
      "v_mfma_f32_16x16x32_bf16 v[120:123], v[164:167], v[196:199], v[120:123]\n"
      "v_mfma_f32_16x16x32_bf16 v[116:119], v[164:167], v[192:195], v[116:119]\n"
      "ds_read2_b32 v[164:165], v214 offset0:195 offset1:194\n"
      "v_mfma_f32_16x16x32_bf16 v[124:127], v[168:171], v[196:199], v[124:127]\n"
      "ds_read2_b32 v[166:167], v214 offset0:193 offset1:192\n"
      "v_mfma_f32_16x16x32_bf16 v[120:123], v[168:171], v[192:195], v[120:123]\n"
      "v_mfma_f32_16x16x32_bf16 v[124:127], v[172:175], v[192:195], v[124:127]\n"
      "ds_read2_b32 v[168:169], v215 offset0:67 offset1:66\n"
      "v_mfma_f32_16x16x32_bf16 v[108:111], v[144:147], v[188:191], v[108:111]\n"
      "ds_read2_b32 v[170:171], v215 offset0:65 offset1:64\n"
      "v_mfma_f32_16x16x32_bf16 v[104:107], v[144:147], v[184:187], v[104:107]\n"
      "ds_read2_b32 v[172:173], v215 offset0:195 offset1:194\n"
      "v_mfma_f32_16x16x32_bf16 v[100:103], v[144:147], v[180:183], v[100:103]\n"
      "ds_read2_b32 v[174:175], v215 offset0:193 offset1:192\n"
      "v_mfma_f32_16x16x32_bf16 v[96:99], v[144:147], v[176:179], v[96:99]\n"
      "ds_read2_b32 v[144:145], v212 offset0:83 offset1:82\n"
      "v_mfma_f32_16x16x32_bf16 v[112:115], v[148:151], v[188:191], v[112:115]\n"
      "ds_read2_b32 v[146:147], v212 offset0:81 offset1:80\n"
      "v_mfma_f32_16x16x32_bf16 v[108:111], v[148:151], v[184:187], v[108:111]\n"
      "ds_read_b128 v[204:207], v217 offset:1664\n"
      "v_mfma_f32_16x16x32_bf16 v[104:107], v[148:151], v[180:183], v[104:107]\n"
      "ds_read_b128 v[200:203], v217 offset:1152\n"
      "v_mfma_f32_16x16x32_bf16 v[100:103], v[148:151], v[176:179], v[100:103]\n"
      "ds_read_b128 v[196:199], v217 offset:640\n"
      "v_mfma_f32_16x16x32_bf16 v[116:119], v[152:155], v[188:191], v[116:119]\n"
      "ds_read_b128 v[192:195], v217 offset:128\n"
      "v_mfma_f32_16x16x32_bf16 v[112:115], v[152:155], v[184:187], v[112:115]\n"
      "ds_read2_b32 v[148:149], v212 offset0:211 offset1:210\n"
      "v_mfma_f32_16x16x32_bf16 v[108:111], v[152:155], v[180:183], v[108:111]\n"
      "ds_read2_b32 v[150:151], v212 offset0:209 offset1:208\n"
      "v_mfma_f32_16x16x32_bf16 v[104:107], v[152:155], v[176:179], v[104:107]\n"
      "ds_read2_b32 v[152:153], v213 offset0:83 offset1:82\n"
      "s_waitcnt lgkmcnt(7)\n"
      "v_mfma_f32_16x16x32_bf16 v[120:123], v[156:159], v[188:191], v[120:123]\n"
      "ds_read2_b32 v[154:155], v213 offset0:81 offset1:80\n"
      "v_mfma_f32_16x16x32_bf16 v[116:119], v[156:159], v[184:187], v[116:119]\n"
      "v_mfma_f32_16x16x32_bf16 v[112:115], v[156:159], v[180:183], v[112:115]\n"
      "v_mfma_f32_16x16x32_bf16 v[108:111], v[156:159], v[176:179], v[108:111]\n"
      "ds_read2_b32 v[156:157], v213 offset0:211 offset1:210\n"
      "v_mfma_f32_16x16x32_bf16 v[124:127], v[160:163], v[188:191], v[124:127]\n"
      "ds_read2_b32 v[158:159], v213 offset0:209 offset1:208\n"
      "v_mfma_f32_16x16x32_bf16 v[120:123], v[160:163], v[184:187], v[120:123]\n"
      "v_mfma_f32_16x16x32_bf16 v[116:119], v[160:163], v[180:183], v[116:119]\n"
      "v_mfma_f32_16x16x32_bf16 v[112:115], v[160:163], v[176:179], v[112:115]\n"
      "ds_read2_b32 v[160:161], v214 offset0:83 offset1:82\n"
      "v_mfma_f32_16x16x32_bf16 v[124:127], v[164:167], v[184:187], v[124:127]\n"
      "ds_read2_b32 v[162:163], v214 offset0:81 offset1:80\n"
      "v_mfma_f32_16x16x32_bf16 v[120:123], v[164:167], v[180:183], v[120:123]\n"
      "v_mfma_f32_16x16x32_bf16 v[116:119], v[164:167], v[176:179], v[116:119]\n"
      "ds_read2_b32 v[164:165], v214 offset0:211 offset1:210\n"
      "v_mfma_f32_16x16x32_bf16 v[124:127], v[168:171], v[180:183], v[124:127]\n"
      "ds_read2_b32 v[166:167], v214 offset0:209 offset1:208\n"
      "v_mfma_f32_16x16x32_bf16 v[120:123], v[168:171], v[176:179], v[120:123]\n"
      "v_mfma_f32_16x16x32_bf16 v[124:127], v[172:175], v[176:179], v[124:127]\n"
      "ds_read2_b32 v[168:169], v215 offset0:83 offset1:82\n"
      "s_waitcnt lgkmcnt(5)\n"
      "v_mfma_f32_16x16x32_bf16 v[108:111], v[144:147], v[204:207], v[108:111]\n"
      "ds_read2_b32 v[170:171], v215 offset0:81 offset1:80\n"
      "v_mfma_f32_16x16x32_bf16 v[104:107], v[144:147], v[200:203], v[104:107]\n"
      "ds_read2_b32 v[172:173], v215 offset0:211 offset1:210\n"
      "v_mfma_f32_16x16x32_bf16 v[100:103], v[144:147], v[196:199], v[100:103]\n"
      "ds_read2_b32 v[174:175], v215 offset0:209 offset1:208\n"
      "v_mfma_f32_16x16x32_bf16 v[96:99], v[144:147], v[192:195], v[96:99]\n"
      "ds_read2_b32 v[144:145], v212 offset0:99 offset1:98\n"
      "v_mfma_f32_16x16x32_bf16 v[112:115], v[148:151], v[204:207], v[112:115]\n"
      "ds_read2_b32 v[146:147], v212 offset0:97 offset1:96\n"
      "v_mfma_f32_16x16x32_bf16 v[108:111], v[148:151], v[200:203], v[108:111]\n"
      "ds_read_b128 v[188:191], v217 offset:1600\n"
      "v_mfma_f32_16x16x32_bf16 v[104:107], v[148:151], v[196:199], v[104:107]\n"
      "ds_read_b128 v[184:187], v217 offset:1088\n"
      "v_mfma_f32_16x16x32_bf16 v[100:103], v[148:151], v[192:195], v[100:103]\n"
      "ds_read_b128 v[180:183], v217 offset:576\n"
      "v_mfma_f32_16x16x32_bf16 v[116:119], v[152:155], v[204:207], v[116:119]\n"
      "ds_read_b128 v[176:179], v217 offset:64\n"
      "v_mfma_f32_16x16x32_bf16 v[112:115], v[152:155], v[200:203], v[112:115]\n"
      "ds_read2_b32 v[148:149], v212 offset0:227 offset1:226\n"
      "v_mfma_f32_16x16x32_bf16 v[108:111], v[152:155], v[196:199], v[108:111]\n"
      "ds_read2_b32 v[150:151], v212 offset0:225 offset1:224\n"
      "v_mfma_f32_16x16x32_bf16 v[104:107], v[152:155], v[192:195], v[104:107]\n"
      "ds_read2_b32 v[152:153], v213 offset0:99 offset1:98\n"
      "v_mfma_f32_16x16x32_bf16 v[120:123], v[156:159], v[204:207], v[120:123]\n"
      "ds_read2_b32 v[154:155], v213 offset0:97 offset1:96\n"
      "v_mfma_f32_16x16x32_bf16 v[116:119], v[156:159], v[200:203], v[116:119]\n"
      "v_mfma_f32_16x16x32_bf16 v[112:115], v[156:159], v[196:199], v[112:115]\n"
      "v_mfma_f32_16x16x32_bf16 v[108:111], v[156:159], v[192:195], v[108:111]\n"
      "ds_read2_b32 v[156:157], v213 offset0:227 offset1:226\n"
      "s_waitcnt lgkmcnt(5)\n"
      "v_mfma_f32_16x16x32_bf16 v[124:127], v[160:163], v[204:207], v[124:127]\n"
      "ds_read2_b32 v[158:159], v213 offset0:225 offset1:224\n"
      "v_mfma_f32_16x16x32_bf16 v[120:123], v[160:163], v[200:203], v[120:123]\n"
      "v_mfma_f32_16x16x32_bf16 v[116:119], v[160:163], v[196:199], v[116:119]\n"
      "v_mfma_f32_16x16x32_bf16 v[112:115], v[160:163], v[192:195], v[112:115]\n"
      "ds_read2_b32 v[160:161], v214 offset0:99 offset1:98\n"
      "v_mfma_f32_16x16x32_bf16 v[124:127], v[164:167], v[200:203], v[124:127]\n"
      "ds_read2_b32 v[162:163], v214 offset0:97 offset1:96\n"
      "v_mfma_f32_16x16x32_bf16 v[120:123], v[164:167], v[196:199], v[120:123]\n"
      "v_mfma_f32_16x16x32_bf16 v[116:119], v[164:167], v[192:195], v[116:119]\n"
      "ds_read2_b32 v[164:165], v214 offset0:227 offset1:226\n"
      "v_mfma_f32_16x16x32_bf16 v[124:127], v[168:171], v[196:199], v[124:127]\n"
      "ds_read2_b32 v[166:167], v214 offset0:225 offset1:224\n"
      "v_mfma_f32_16x16x32_bf16 v[120:123], v[168:171], v[192:195], v[120:123]\n"
      "v_mfma_f32_16x16x32_bf16 v[124:127], v[172:175], v[192:195], v[124:127]\n"
      "ds_read2_b32 v[168:169], v215 offset0:99 offset1:98\n"
      "v_mfma_f32_16x16x32_bf16 v[108:111], v[144:147], v[188:191], v[108:111]\n"
      "ds_read2_b32 v[170:171], v215 offset0:97 offset1:96\n"
      "v_mfma_f32_16x16x32_bf16 v[104:107], v[144:147], v[184:187], v[104:107]\n"
      "ds_read2_b32 v[172:173], v215 offset0:227 offset1:226\n"
      "v_mfma_f32_16x16x32_bf16 v[100:103], v[144:147], v[180:183], v[100:103]\n"
      "ds_read2_b32 v[174:175], v215 offset0:225 offset1:224\n"
      "v_mfma_f32_16x16x32_bf16 v[96:99], v[144:147], v[176:179], v[96:99]\n"
      "ds_read2_b32 v[144:145], v212 offset0:115 offset1:114\n"
      "s_waitcnt lgkmcnt(6)\n"
      "v_mfma_f32_16x16x32_bf16 v[112:115], v[148:151], v[188:191], v[112:115]\n"
      "ds_read2_b32 v[146:147], v212 offset0:113 offset1:112\n"
      "v_mfma_f32_16x16x32_bf16 v[108:111], v[148:151], v[184:187], v[108:111]\n"
      "ds_read_b128 v[204:207], v217 offset:1536\n"
      "v_mfma_f32_16x16x32_bf16 v[104:107], v[148:151], v[180:183], v[104:107]\n"
      "ds_read_b128 v[200:203], v217 offset:1024\n"
      "v_mfma_f32_16x16x32_bf16 v[100:103], v[148:151], v[176:179], v[100:103]\n"
      "ds_read_b128 v[196:199], v217 offset:512\n"
      "v_mfma_f32_16x16x32_bf16 v[116:119], v[152:155], v[188:191], v[116:119]\n"
      "ds_read_b128 v[192:195], v217 offset:0\n"
      "v_mfma_f32_16x16x32_bf16 v[112:115], v[152:155], v[184:187], v[112:115]\n"
      "ds_read2_b32 v[148:149], v212 offset0:243 offset1:242\n"
      "v_mfma_f32_16x16x32_bf16 v[108:111], v[152:155], v[180:183], v[108:111]\n"
      "ds_read2_b32 v[150:151], v212 offset0:241 offset1:240\n"
      "v_mfma_f32_16x16x32_bf16 v[104:107], v[152:155], v[176:179], v[104:107]\n"
      "ds_read2_b32 v[152:153], v213 offset0:115 offset1:114\n"
      "v_mfma_f32_16x16x32_bf16 v[120:123], v[156:159], v[188:191], v[120:123]\n"
      "ds_read2_b32 v[154:155], v213 offset0:113 offset1:112\n"
      "v_mfma_f32_16x16x32_bf16 v[116:119], v[156:159], v[184:187], v[116:119]\n"
      "v_mfma_f32_16x16x32_bf16 v[112:115], v[156:159], v[180:183], v[112:115]\n"
      "v_mfma_f32_16x16x32_bf16 v[108:111], v[156:159], v[176:179], v[108:111]\n"
      "ds_read2_b32 v[156:157], v213 offset0:243 offset1:242\n"
      "v_mfma_f32_16x16x32_bf16 v[124:127], v[160:163], v[188:191], v[124:127]\n"
      "ds_read2_b32 v[158:159], v213 offset0:241 offset1:240\n"
      "v_mfma_f32_16x16x32_bf16 v[120:123], v[160:163], v[184:187], v[120:123]\n"
      "v_mfma_f32_16x16x32_bf16 v[116:119], v[160:163], v[180:183], v[116:119]\n"
      "v_mfma_f32_16x16x32_bf16 v[112:115], v[160:163], v[176:179], v[112:115]\n"
      "ds_read2_b32 v[160:161], v214 offset0:115 offset1:114\n"
      "s_waitcnt lgkmcnt(3)\n"
      "v_mfma_f32_16x16x32_bf16 v[124:127], v[164:167], v[184:187], v[124:127]\n"
      "ds_read2_b32 v[162:163], v214 offset0:113 offset1:112\n"
      "v_mfma_f32_16x16x32_bf16 v[120:123], v[164:167], v[180:183], v[120:123]\n"
      "v_mfma_f32_16x16x32_bf16 v[116:119], v[164:167], v[176:179], v[116:119]\n"
      "ds_read2_b32 v[164:165], v214 offset0:243 offset1:242\n"
      "v_mfma_f32_16x16x32_bf16 v[124:127], v[168:171], v[180:183], v[124:127]\n"
      "ds_read2_b32 v[166:167], v214 offset0:241 offset1:240\n"
      "v_mfma_f32_16x16x32_bf16 v[120:123], v[168:171], v[176:179], v[120:123]\n"
      "v_mfma_f32_16x16x32_bf16 v[124:127], v[172:175], v[176:179], v[124:127]\n"
      "ds_read2_b32 v[168:169], v215 offset0:115 offset1:114\n"
      "v_mfma_f32_16x16x32_bf16 v[108:111], v[144:147], v[204:207], v[108:111]\n"
      "ds_read2_b32 v[170:171], v215 offset0:113 offset1:112\n"
      "v_mfma_f32_16x16x32_bf16 v[104:107], v[144:147], v[200:203], v[104:107]\n"
      "ds_read2_b32 v[172:173], v215 offset0:243 offset1:242\n"
      "v_mfma_f32_16x16x32_bf16 v[100:103], v[144:147], v[196:199], v[100:103]\n"
      "ds_read2_b32 v[174:175], v215 offset0:241 offset1:240\n"
      "v_mfma_f32_16x16x32_bf16 v[96:99], v[144:147], v[192:195], v[96:99]\n"
      "ds_read2_b32 v[144:145], v212 offset0:3 offset1:2\n"
      "v_mfma_f32_16x16x32_bf16 v[112:115], v[148:151], v[204:207], v[112:115]\n"
      "ds_read2_b32 v[146:147], v212 offset0:1 offset1:0\n"
      "v_mfma_f32_16x16x32_bf16 v[108:111], v[148:151], v[200:203], v[108:111]\n"
      "v_mfma_f32_16x16x32_bf16 v[104:107], v[148:151], v[196:199], v[104:107]\n"
      "v_mfma_f32_16x16x32_bf16 v[100:103], v[148:151], v[192:195], v[100:103]\n"
      "ds_read2_b32 v[148:149], v212 offset0:131 offset1:130\n"
      "v_mfma_f32_16x16x32_bf16 v[116:119], v[152:155], v[204:207], v[116:119]\n"
      "ds_read_b32 v31, v2 offset:4272\n"
      "v_mfma_f32_16x16x32_bf16 v[112:115], v[152:155], v[200:203], v[112:115]\n"
      "ds_read2_b32 v[150:151], v212 offset0:129 offset1:128\n"
      "v_mfma_f32_16x16x32_bf16 v[108:111], v[152:155], v[196:199], v[108:111]\n"
      "v_mfma_f32_16x16x32_bf16 v[104:107], v[152:155], v[192:195], v[104:107]\n"
      "ds_read2_b32 v[152:153], v213 offset0:3 offset1:2\n"
      "s_waitcnt lgkmcnt(4)\n"
      "v_mfma_f32_16x16x32_bf16 v[120:123], v[156:159], v[204:207], v[120:123]\n"
      "ds_read2_b32 v[154:155], v213 offset0:1 offset1:0\n"
      "v_mfma_f32_16x16x32_bf16 v[116:119], v[156:159], v[200:203], v[116:119]\n"
      "v_mfma_f32_16x16x32_bf16 v[112:115], v[156:159], v[196:199], v[112:115]\n"
      "s_waitcnt lgkmcnt(3)\n"
      "v_readfirstlane_b32 s2, v31\n"
      "s_cmp_lt_u32 s2, 2\n"
      "s_cbranch_scc1 .Lw_221\n"
      ".Lw_222:\n"
      "v_mfma_f32_16x16x32_bf16 v[108:111], v[156:159], v[192:195], v[108:111]\n"
      "ds_read2_b32 v[156:157], v213 offset0:131 offset1:130\n"
      "v_mfma_f32_16x16x32_bf16 v[124:127], v[160:163], v[204:207], v[124:127]\n"
      "ds_read_b128 v[204:207], v217 offset:4032\n"
      "v_mfma_f32_16x16x32_bf16 v[120:123], v[160:163], v[200:203], v[120:123]\n"
      "ds_read2_b32 v[158:159], v213 offset0:129 offset1:128\n"
      "v_mfma_f32_16x16x32_bf16 v[116:119], v[160:163], v[196:199], v[116:119]\n"
      "ds_read_b128 v[188:191], v217 offset:3968\n"
      "v_mfma_f32_16x16x32_bf16 v[112:115], v[160:163], v[192:195], v[112:115]\n"
      "ds_read2_b32 v[160:161], v212 offset0:19 offset1:18\n"
      "v_mfma_f32_16x16x32_bf16 v[124:127], v[164:167], v[200:203], v[124:127]\n"
      "ds_read_b128 v[200:203], v217 offset:3520\n"
      "v_mfma_f32_16x16x32_bf16 v[120:123], v[164:167], v[196:199], v[120:123]\n"
      "ds_read2_b32 v[162:163], v212 offset0:17 offset1:16\n"
      "v_mfma_f32_16x16x32_bf16 v[116:119], v[164:167], v[192:195], v[116:119]\n"
      "ds_read_b128 v[184:187], v217 offset:3456\n"
      "v_mfma_f32_16x16x32_bf16 v[124:127], v[168:171], v[196:199], v[124:127]\n"
      "ds_read_b128 v[196:199], v217 offset:3008\n"
      "v_mfma_f32_16x16x32_bf16 v[120:123], v[168:171], v[192:195], v[120:123]\n"
      "ds_read_b128 v[180:183], v217 offset:2944\n"
      "v_mfma_f32_16x16x32_bf16 v[124:127], v[172:175], v[192:195], v[124:127]\n"
      "ds_read_b128 v[192:195], v217 offset:2496\n"
      "s_waitcnt lgkmcnt(7)\n"
      "v_mfma_f32_16x16x32_bf16 v[124:127], v[144:147], v[204:207], v[124:127]\n"
      "ds_read_b128 v[176:179], v217 offset:2432\n"
      "s_waitcnt lgkmcnt(6)\n"
      "v_mfma_f32_16x16x32_bf16 v[120:123], v[144:147], v[200:203], v[120:123]\n"
      "ds_read2_b32 v[164:165], v212 offset0:147 offset1:146\n"
      "s_waitcnt lgkmcnt(4)\n"
      "v_mfma_f32_16x16x32_bf16 v[116:119], v[144:147], v[196:199], v[116:119]\n"
      "ds_read2_b32 v[166:167], v212 offset0:145 offset1:144\n"
      "s_waitcnt lgkmcnt(3)\n"
      "v_mfma_f32_16x16x32_bf16 v[112:115], v[144:147], v[192:195], v[112:115]\n"
      "ds_read2_b32 v[168:169], v213 offset0:19 offset1:18\n"
      "v_mfma_f32_16x16x32_bf16 v[124:127], v[148:151], v[200:203], v[124:127]\n"
      "ds_read2_b32 v[170:171], v213 offset0:17 offset1:16\n"
      "v_mfma_f32_16x16x32_bf16 v[120:123], v[148:151], v[196:199], v[120:123]\n"
      "ds_read2_b32 v[172:173], v213 offset0:147 offset1:146\n"
      "v_mfma_f32_16x16x32_bf16 v[116:119], v[148:151], v[192:195], v[116:119]\n"
      "ds_read2_b32 v[174:175], v213 offset0:145 offset1:144\n"
      "v_mfma_f32_16x16x32_bf16 v[124:127], v[152:155], v[196:199], v[124:127]\n"
      "ds_read2_b32 v[144:145], v212 offset0:35 offset1:34\n"
      "v_mfma_f32_16x16x32_bf16 v[120:123], v[152:155], v[192:195], v[120:123]\n"
      "ds_read2_b32 v[146:147], v212 offset0:33 offset1:32\n"
      "v_mfma_f32_16x16x32_bf16 v[124:127], v[156:159], v[192:195], v[124:127]\n"
      "ds_read_b128 v[204:207], v217 offset:3904\n"
      "v_mfma_f32_16x16x32_bf16 v[124:127], v[160:163], v[188:191], v[124:127]\n"
      "ds_read_b128 v[200:203], v217 offset:3392\n"
      "v_mfma_f32_16x16x32_bf16 v[120:123], v[160:163], v[184:187], v[120:123]\n"
      "ds_read_b128 v[196:199], v217 offset:2880\n"
      "v_mfma_f32_16x16x32_bf16 v[116:119], v[160:163], v[180:183], v[116:119]\n"
      "ds_read_b128 v[192:195], v217 offset:2368\n"
      "s_waitcnt lgkmcnt(7)\n"
      "v_mfma_f32_16x16x32_bf16 v[112:115], v[160:163], v[176:179], v[112:115]\n"
      "ds_read2_b32 v[148:149], v212 offset0:163 offset1:162\n"
      "ds_read2_b32 v[150:151], v212 offset0:161 offset1:160\n"
      "v_mfma_f32_16x16x32_bf16 v[124:127], v[164:167], v[184:187], v[124:127]\n"
      "ds_read2_b32 v[152:153], v213 offset0:35 offset1:34\n"
      "v_mfma_f32_16x16x32_bf16 v[120:123], v[164:167], v[180:183], v[120:123]\n"
      "ds_read2_b32 v[154:155], v213 offset0:33 offset1:32\n"
      "v_mfma_f32_16x16x32_bf16 v[116:119], v[164:167], v[176:179], v[116:119]\n"
      "ds_read2_b32 v[156:157], v213 offset0:163 offset1:162\n"
      "v_mfma_f32_16x16x32_bf16 v[124:127], v[168:171], v[180:183], v[124:127]\n"
      "ds_read2_b32 v[158:159], v213 offset0:161 offset1:160\n"
      "v_mfma_f32_16x16x32_bf16 v[120:123], v[168:171], v[176:179], v[120:123]\n"
      "ds_read2_b32 v[160:161], v212 offset0:51 offset1:50\n"
      "s_waitcnt lgkmcnt(8)\n"
      "v_mfma_f32_16x16x32_bf16 v[124:127], v[172:175], v[176:179], v[124:127]\n"
      "ds_read2_b32 v[162:163], v212 offset0:49 offset1:48\n"
      "ds_read_b128 v[188:191], v217 offset:3840\n"
      "v_mfma_f32_16x16x32_bf16 v[124:127], v[144:147], v[204:207], v[124:127]\n"
      "ds_read_b128 v[184:187], v217 offset:3328\n"
      "v_mfma_f32_16x16x32_bf16 v[120:123], v[144:147], v[200:203], v[120:123]\n"
      "ds_read_b128 v[180:183], v217 offset:2816\n"
      "v_mfma_f32_16x16x32_bf16 v[116:119], v[144:147], v[196:199], v[116:119]\n"
      "ds_read_b128 v[176:179], v217 offset:2304\n"
      "s_waitcnt lgkmcnt(8)\n"
      "v_mfma_f32_16x16x32_bf16 v[112:115], v[144:147], v[192:195], v[112:115]\n"
      "ds_read2_b32 v[164:165], v212 offset0:179 offset1:178\n"
      "ds_read2_b32 v[166:167], v212 offset0:177 offset1:176\n"
      "v_mfma_f32_16x16x32_bf16 v[124:127], v[148:151], v[200:203], v[124:127]\n"
      "ds_read2_b32 v[168:169], v213 offset0:51 offset1:50\n"
      "v_mfma_f32_16x16x32_bf16 v[120:123], v[148:151], v[196:199], v[120:123]\n"
      "ds_read2_b32 v[170:171], v213 offset0:49 offset1:48\n"
      "v_mfma_f32_16x16x32_bf16 v[116:119], v[148:151], v[192:195], v[116:119]\n"
      "ds_read2_b32 v[172:173], v213 offset0:179 offset1:178\n"
      "v_mfma_f32_16x16x32_bf16 v[124:127], v[152:155], v[196:199], v[124:127]\n"
      "ds_read2_b32 v[174:175], v213 offset0:177 offset1:176\n"
      "v_mfma_f32_16x16x32_bf16 v[120:123], v[152:155], v[192:195], v[120:123]\n"
      "ds_read2_b32 v[144:145], v212 offset0:67 offset1:66\n"
      "s_waitcnt lgkmcnt(8)\n"
      "v_mfma_f32_16x16x32_bf16 v[124:127], v[156:159], v[192:195], v[124:127]\n"
      "ds_read2_b32 v[146:147], v212 offset0:65 offset1:64\n"
      "ds_read_b128 v[204:207], v217 offset:3776\n"
      "v_mfma_f32_16x16x32_bf16 v[124:127], v[160:163], v[188:191], v[124:127]\n"
      "ds_read_b128 v[200:203], v217 offset:3264\n"
      "v_mfma_f32_16x16x32_bf16 v[120:123], v[160:163], v[184:187], v[120:123]\n"
      "ds_read_b128 v[196:199], v217 offset:2752\n"
      "v_mfma_f32_16x16x32_bf16 v[116:119], v[160:163], v[180:183], v[116:119]\n"
      "ds_read_b128 v[192:195], v217 offset:2240\n"
      "s_waitcnt lgkmcnt(8)\n"
      "v_mfma_f32_16x16x32_bf16 v[112:115], v[160:163], v[176:179], v[112:115]\n"
      "ds_read2_b32 v[148:149], v212 offset0:195 offset1:194\n"
      "ds_read2_b32 v[150:151], v212 offset0:193 offset1:192\n"
      "v_mfma_f32_16x16x32_bf16 v[124:127], v[164:167], v[184:187], v[124:127]\n"
      "ds_read2_b32 v[152:153], v213 offset0:67 offset1:66\n"
      "v_mfma_f32_16x16x32_bf16 v[120:123], v[164:167], v[180:183], v[120:123]\n"
      "ds_read2_b32 v[154:155], v213 offset0:65 offset1:64\n"
      "v_mfma_f32_16x16x32_bf16 v[116:119], v[164:167], v[176:179], v[116:119]\n"
      "ds_read2_b32 v[156:157], v213 offset0:195 offset1:194\n"
      "v_mfma_f32_16x16x32_bf16 v[124:127], v[168:171], v[180:183], v[124:127]\n"
      "ds_read2_b32 v[158:159], v213 offset0:193 offset1:192\n"
      "v_mfma_f32_16x16x32_bf16 v[120:123], v[168:171], v[176:179], v[120:123]\n"
      "ds_read2_b32 v[160:161], v212 offset0:83 offset1:82\n"
      "s_waitcnt lgkmcnt(8)\n"
      "v_mfma_f32_16x16x32_bf16 v[124:127], v[172:175], v[176:179], v[124:127]\n"
      "ds_read2_b32 v[162:163], v212 offset0:81 offset1:80\n"
      "ds_read_b128 v[188:191], v217 offset:3712\n"
      "v_mfma_f32_16x16x32_bf16 v[124:127], v[144:147], v[204:207], v[124:127]\n"
      "ds_read_b128 v[184:187], v217 offset:3200\n"
      "v_mfma_f32_16x16x32_bf16 v[120:123], v[144:147], v[200:203], v[120:123]\n"
      "ds_read_b128 v[180:183], v217 offset:2688\n"
      "v_mfma_f32_16x16x32_bf16 v[116:119], v[144:147], v[196:199], v[116:119]\n"
      "ds_read_b128 v[176:179], v217 offset:2176\n"
      "s_waitcnt lgkmcnt(8)\n"
      "v_mfma_f32_16x16x32_bf16 v[112:115], v[144:147], v[192:195], v[112:115]\n"
      "ds_read2_b32 v[164:165], v212 offset0:211 offset1:210\n"
      "ds_read2_b32 v[166:167], v212 offset0:209 offset1:208\n"
      "v_mfma_f32_16x16x32_bf16 v[124:127], v[148:151], v[200:203], v[124:127]\n"
      "ds_read2_b32 v[168:169], v213 offset0:83 offset1:82\n"
      "v_mfma_f32_16x16x32_bf16 v[120:123], v[148:151], v[196:199], v[120:123]\n"
      "ds_read2_b32 v[170:171], v213 offset0:81 offset1:80\n"
      "v_mfma_f32_16x16x32_bf16 v[116:119], v[148:151], v[192:195], v[116:119]\n"
      "ds_read2_b32 v[172:173], v213 offset0:211 offset1:210\n"
      "v_mfma_f32_16x16x32_bf16 v[124:127], v[152:155], v[196:199], v[124:127]\n"
      "ds_read2_b32 v[174:175], v213 offset0:209 offset1:208\n"
      "v_mfma_f32_16x16x32_bf16 v[120:123], v[152:155], v[192:195], v[120:123]\n"
      "ds_read2_b32 v[144:145], v212 offset0:99 offset1:98\n"
      "s_waitcnt lgkmcnt(8)\n"
      "v_mfma_f32_16x16x32_bf16 v[124:127], v[156:159], v[192:195], v[124:127]\n"
      "ds_read2_b32 v[146:147], v212 offset0:97 offset1:96\n"
      "ds_read_b128 v[204:207], v217 offset:3648\n"
      "v_mfma_f32_16x16x32_bf16 v[124:127], v[160:163], v[188:191], v[124:127]\n"
      "ds_read_b128 v[200:203], v217 offset:3136\n"
      "v_mfma_f32_16x16x32_bf16 v[120:123], v[160:163], v[184:187], v[120:123]\n"
      "ds_read_b128 v[196:199], v217 offset:2624\n"
      "v_mfma_f32_16x16x32_bf16 v[116:119], v[160:163], v[180:183], v[116:119]\n"
      "ds_read_b128 v[192:195], v217 offset:2112\n"
      "s_waitcnt lgkmcnt(8)\n"
      "v_mfma_f32_16x16x32_bf16 v[112:115], v[160:163], v[176:179], v[112:115]\n"
      "ds_read2_b32 v[148:149], v212 offset0:227 offset1:226\n"
      "ds_read2_b32 v[150:151], v212 offset0:225 offset1:224\n"
      "v_mfma_f32_16x16x32_bf16 v[124:127], v[164:167], v[184:187], v[124:127]\n"
      "ds_read2_b32 v[152:153], v213 offset0:99 offset1:98\n"
      "v_mfma_f32_16x16x32_bf16 v[120:123], v[164:167], v[180:183], v[120:123]\n"
      "ds_read2_b32 v[154:155], v213 offset0:97 offset1:96\n"
      "v_mfma_f32_16x16x32_bf16 v[116:119], v[164:167], v[176:179], v[116:119]\n"
      "ds_read2_b32 v[156:157], v213 offset0:227 offset1:226\n"
      "v_mfma_f32_16x16x32_bf16 v[124:127], v[168:171], v[180:183], v[124:127]\n"
      "ds_read2_b32 v[158:159], v213 offset0:225 offset1:224\n"
      "v_mfma_f32_16x16x32_bf16 v[120:123], v[168:171], v[176:179], v[120:123]\n"
      "ds_read2_b32 v[160:161], v212 offset0:115 offset1:114\n"
      "s_waitcnt lgkmcnt(8)\n"
      "v_mfma_f32_16x16x32_bf16 v[124:127], v[172:175], v[176:179], v[124:127]\n"
      "ds_read2_b32 v[162:163], v212 offset0:113 offset1:112\n"
      "ds_read_b128 v[188:191], v217 offset:3584\n"
      "v_mfma_f32_16x16x32_bf16 v[124:127], v[144:147], v[204:207], v[124:127]\n"
      "ds_read_b128 v[184:187], v217 offset:3072\n"
      "v_mfma_f32_16x16x32_bf16 v[120:123], v[144:147], v[200:203], v[120:123]\n"
      "ds_read_b128 v[180:183], v217 offset:2560\n"
      "v_mfma_f32_16x16x32_bf16 v[116:119], v[144:147], v[196:199], v[116:119]\n"
      "ds_read_b128 v[176:179], v217 offset:2048\n"
      "s_waitcnt lgkmcnt(8)\n"
      "v_mfma_f32_16x16x32_bf16 v[112:115], v[144:147], v[192:195], v[112:115]\n"
      "ds_read2_b32 v[164:165], v212 offset0:243 offset1:242\n"
      "ds_read2_b32 v[166:167], v212 offset0:241 offset1:240\n"
      "v_mfma_f32_16x16x32_bf16 v[124:127], v[148:151], v[200:203], v[124:127]\n"
      "ds_read2_b32 v[168:169], v213 offset0:115 offset1:114\n"
      "v_mfma_f32_16x16x32_bf16 v[120:123], v[148:151], v[196:199], v[120:123]\n"
      "ds_read2_b32 v[170:171], v213 offset0:113 offset1:112\n"
      "v_mfma_f32_16x16x32_bf16 v[116:119], v[148:151], v[192:195], v[116:119]\n"
      "ds_read2_b32 v[172:173], v213 offset0:243 offset1:242\n"
      "v_mfma_f32_16x16x32_bf16 v[124:127], v[152:155], v[196:199], v[124:127]\n"
      "ds_read2_b32 v[174:175], v213 offset0:241 offset1:240\n"
      "v_mfma_f32_16x16x32_bf16 v[120:123], v[152:155], v[192:195], v[120:123]\n"
      "s_waitcnt lgkmcnt(7)\n"
      "v_mfma_f32_16x16x32_bf16 v[124:127], v[156:159], v[192:195], v[124:127]\n"
      "v_mfma_f32_16x16x32_bf16 v[124:127], v[160:163], v[188:191], v[124:127]\n"
      "v_mfma_f32_16x16x32_bf16 v[120:123], v[160:163], v[184:187], v[120:123]\n"
      "v_mfma_f32_16x16x32_bf16 v[116:119], v[160:163], v[180:183], v[116:119]\n"
      "s_waitcnt lgkmcnt(2)\n"
      "v_mfma_f32_16x16x32_bf16 v[112:115], v[160:163], v[176:179], v[112:115]\n"
      "v_mfma_f32_16x16x32_bf16 v[124:127], v[164:167], v[184:187], v[124:127]\n"
      "v_mfma_f32_16x16x32_bf16 v[120:123], v[164:167], v[180:183], v[120:123]\n"
      "v_mfma_f32_16x16x32_bf16 v[116:119], v[164:167], v[176:179], v[116:119]\n"
      "v_mfma_f32_16x16x32_bf16 v[124:127], v[168:171], v[180:183], v[124:127]\n"
      "v_mfma_f32_16x16x32_bf16 v[120:123], v[168:171], v[176:179], v[120:123]\n"
      "s_waitcnt lgkmcnt(0)\n"
      "v_mfma_f32_16x16x32_bf16 v[124:127], v[172:175], v[176:179], v[124:127]\n"
      "ds_add_u32 v2, v3 offset:4276\n"
      "s_waitcnt vmcnt(0)\n"
      "ds_read_b32 v31, v2 offset:4304\n"
      "v_cvt_pk_bf16_f32 v72, v96, v97\n"
      "v_cvt_pk_bf16_f32 v74, v100, v101\n"
      "v_cvt_pk_bf16_f32 v73, v98, v99\n"
      "v_cvt_pk_bf16_f32 v75, v102, v103\n"
      "v_permlane16_swap_b32 v72, v74\n"
      "s_nop 0\n"
      "v_permlane16_swap_b32 v73, v75\n"
      "buffer_store_dwordx4 v[72:75], v5, s[20:23], s63 offen offset:0 sc1\n"
      "v_cvt_pk_bf16_f32 v76, v104, v105\n"
      "v_cvt_pk_bf16_f32 v78, v108, v109\n"
      "v_cvt_pk_bf16_f32 v77, v106, v107\n"
      "v_cvt_pk_bf16_f32 v79, v110, v111\n"
      "v_permlane16_swap_b32 v76, v78\n"
      "s_nop 0\n"
      "v_permlane16_swap_b32 v77, v79\n"
      "buffer_store_dwordx4 v[76:79], v5, s[20:23], s63 offen offset:1024 sc1\n"
      "v_cvt_pk_bf16_f32 v80, v112, v113\n"
      "v_cvt_pk_bf16_f32 v82, v116, v117\n"
      "v_cvt_pk_bf16_f32 v81, v114, v115\n"
      "v_cvt_pk_bf16_f32 v83, v118, v119\n"
      "v_permlane16_swap_b32 v80, v82\n"
      "s_nop 0\n"
      "v_permlane16_swap_b32 v81, v83\n"
      "buffer_store_dwordx4 v[80:83], v5, s[20:23], s63 offen offset:2048 sc1\n"
      "v_cvt_pk_bf16_f32 v84, v120, v121\n"
      "v_cvt_pk_bf16_f32 v86, v124, v125\n"
      "v_cvt_pk_bf16_f32 v85, v122, v123\n"
      "v_cvt_pk_bf16_f32 v87, v126, v127\n"
      "v_permlane16_swap_b32 v84, v86\n"
      "s_nop 0\n"
      "v_permlane16_swap_b32 v85, v87\n"
      "buffer_store_dwordx4 v[84:87], v5, s[20:23], s63 offen offset:3072 sc1\n"
      "s_branch .LCONV\n"
      ".Lt_1:\n"
      "s_mov_b32 s3, 0x10000\n"
      ".Lt_3:\n"
      "s_sleep 4\n"
      "buffer_load_dwordx2 v[76:77], v42, s[20:23], s31 offen sc1\n"
      "s_waitcnt vmcnt(0)\n"
      "v_cmp_ne_u32 vcc, s58, v77\n"
      "s_sub_u32 s3, s3, 1\n"
      "s_cselect_b64 vcc, 0, vcc\n"
      "s_cbranch_vccnz .Lt_3\n"
      "s_branch .Lt_2\n"
      ".Lb_4:\n"
      "s_mov_b64 exec, vcc\n"
      "v_mov_b32 v80, 0xc0135761\n"
      "v_mov_b32 v81, 0xbdd2d3e8\n"
      "v_mov_b32 v82, 1.0\n"
      "v_mov_b32 v83, v77\n"
      "s_mov_b64 exec, -1\n"
      "ds_add_u32 v2, v3 offset:4144\n"
      "s_branch .Lb_5\n"
      ".Lw_6:\n"
      "s_mov_b32 s3, 0x80000\n"
      ".Lw_8:\n"
      "ds_read_b32 v4, v2 offset:4152\n"
      "s_waitcnt lgkmcnt(0)\n"
      "v_readfirstlane_b32 s2, v4\n"
      "s_sub_u32 s3, s3, 1\n"
      "s_cselect_b32 s2, -1, s2\n"
      "s_cmp_lt_u32 s2, 4\n"
      "s_cbranch_scc1 .Lw_8\n"
      "s_branch .Lw_7\n"
      ".Lf_9:\n"
      "s_mov_b32 s3, 0x10000\n"
      ".Lf_11:\n"
      "s_sleep 4\n"
      "buffer_load_dwordx2 v[50:51], v31, s[20:23], 0 offen sc1\n"
      "s_waitcnt vmcnt(0)\n"
      "v_cmp_ne_u64 vcc, s[58:59], v[50:51]\n"
      "s_sub_u32 s3, s3, 1\n"
      "s_cselect_b64 vcc, 0, vcc\n"
      "s_cbranch_vccnz .Lf_11\n"
      "s_branch .Lf_10\n"
      ".Lf_13:\n"
      "s_mov_b32 s3, 0x10000\n"
      ".Lf_15:\n"
      "s_sleep 4\n"
      "buffer_load_dwordx2 v[48:49], v30, s[20:23], 0 offen sc1\n"
      "s_waitcnt vmcnt(0)\n"
      "v_cmp_ne_u64 vcc, s[58:59], v[48:49]\n"
      "s_sub_u32 s3, s3, 1\n"
      "s_cselect_b64 vcc, 0, vcc\n"
      "s_cbranch_vccnz .Lf_15\n"
      "s_branch .Lf_14\n"
      ".Lf_18:\n"
      "s_mov_b32 s3, 0x10000\n"
      ".Lf_20:\n"
      "s_sleep 4\n"
      "buffer_load_dwordx2 v[48:49], v30, s[20:23], 0 offen sc1\n"
      "s_waitcnt vmcnt(0)\n"
      "v_cmp_ne_u64 vcc, s[58:59], v[48:49]\n"
      "s_sub_u32 s3, s3, 1\n"
      "s_cselect_b64 vcc, 0, vcc\n"
      "s_cbranch_vccnz .Lf_20\n"
      "s_branch .Lf_19\n"
      ".Lf_21:\n"
      "s_mov_b32 s3, 0x10000\n"
      ".Lf_23:\n"
      "s_sleep 4\n"
      "buffer_load_dwordx2 v[10:11], v13, s[20:23], 0 offen sc1\n"
      "s_waitcnt vmcnt(0)\n"
      "v_cmp_ne_u64 vcc, s[58:59], v[10:11]\n"
      "s_sub_u32 s3, s3, 1\n"
      "s_cselect_b64 vcc, 0, vcc\n"
      "s_cbranch_vccnz .Lf_23\n"
      "s_branch .Lf_22\n"
      ".Lw_24:\n"
      "s_mov_b32 s3, 0x80000\n"
      ".Lw_26:\n"
      "ds_read_b32 v4, v2 offset:4276\n"
      "s_waitcnt lgkmcnt(0)\n"
      "v_readfirstlane_b32 s2, v4\n"
      "s_sub_u32 s3, s3, 1\n"
      "s_cselect_b32 s2, -1, s2\n"
      "s_cmp_lt_u32 s2, 4\n"
      "s_cbranch_scc1 .Lw_26\n"
      "s_branch .Lw_25\n"
      ".Lf_41:\n"
      "s_mov_b32 s3, 0x10000\n"
      ".Lf_43:\n"
      "s_sleep 4\n"
      "buffer_load_dwordx2 v[10:11], v13, s[20:23], s74 offen sc1\n"
      "s_waitcnt vmcnt(0)\n"
      "v_cmp_ne_u64 vcc, s[58:59], v[10:11]\n"
      "s_sub_u32 s3, s3, 1\n"
      "s_cselect_b64 vcc, 0, vcc\n"
      "s_cbranch_vccnz .Lf_43\n"
      "s_branch .Lf_42\n"
      ".Lw_60:\n"
      "s_mov_b32 s3, 0x80000\n"
      ".Lw_62:\n"
      "ds_read_b32 v4, v2 offset:4308\n"
      "s_waitcnt lgkmcnt(0)\n"
      "v_readfirstlane_b32 s2, v4\n"
      "s_sub_u32 s3, s3, 1\n"
      "s_cselect_b32 s2, -1, s2\n"
      "s_cmp_lt_u32 s2, 4\n"
      "s_cbranch_scc1 .Lw_62\n"
      "s_branch .Lw_61\n"
      ".Lg_123:\n"
      "s_waitcnt lgkmcnt(0)\n"
      "s_nop 11\n"
      "v_mul_f32 v80, v80, v27\n"
      "v_mul_f32 v81, v81, v27\n"
      "v_mul_f32 v82, v82, v27\n"
      "v_mul_f32 v83, v83, v27\n"
      "v_mul_f32 v84, v84, v27\n"
      "v_mul_f32 v85, v85, v27\n"
      "v_mul_f32 v86, v86, v27\n"
      "v_mul_f32 v87, v87, v27\n"
      "v_mul_f32 v88, v88, v27\n"
      "v_mul_f32 v89, v89, v27\n"
      "v_mul_f32 v90, v90, v27\n"
      "v_mul_f32 v91, v91, v27\n"
      "v_mul_f32 v92, v92, v27\n"
      "v_mul_f32 v93, v93, v27\n"
      "v_mul_f32 v94, v94, v27\n"
      "v_mul_f32 v95, v95, v27\n"
      "s_branch .Lg_124\n"
      ".Lg_125:\n"
      "s_waitcnt lgkmcnt(0)\n"
      "s_nop 11\n"
      "v_mul_f32 v64, v64, v27\n"
      "v_mul_f32 v65, v65, v27\n"
      "v_mul_f32 v66, v66, v27\n"
      "v_mul_f32 v67, v67, v27\n"
      "v_mul_f32 v68, v68, v27\n"
      "v_mul_f32 v69, v69, v27\n"
      "v_mul_f32 v70, v70, v27\n"
      "v_mul_f32 v71, v71, v27\n"
      "v_mul_f32 v72, v72, v27\n"
      "v_mul_f32 v73, v73, v27\n"
      "v_mul_f32 v74, v74, v27\n"
      "v_mul_f32 v75, v75, v27\n"
      "v_mul_f32 v76, v76, v27\n"
      "v_mul_f32 v77, v77, v27\n"
      "v_mul_f32 v78, v78, v27\n"
      "v_mul_f32 v79, v79, v27\n"
      "s_branch .Lg_126\n"
      ".Lw_127:\n"
      "s_mov_b32 s3, 0x80000\n"
      ".Lw_129:\n"
      "ds_read_b32 v31, v2 offset:2072\n"
      "s_waitcnt lgkmcnt(0)\n"
      "v_readfirstlane_b32 s2, v31\n"
      "s_sub_u32 s3, s3, 1\n"
      "s_cselect_b32 s2, -1, s2\n"
      "s_cmp_lt_u32 s2, 4\n"
      "s_cbranch_scc1 .Lw_129\n"
      "s_branch .Lw_128\n"
      ".Lw_130:\n"
      "s_mov_b32 s3, 0x80000\n"
      ".Lw_132:\n"
      "ds_read_b32 v31, v2 offset:3096\n"
      "s_waitcnt lgkmcnt(0)\n"
      "v_readfirstlane_b32 s2, v31\n"
      "s_sub_u32 s3, s3, 1\n"
      "s_cselect_b32 s2, -1, s2\n"
      "s_cmp_lt_u32 s2, 4\n"
      "s_cbranch_scc1 .Lw_132\n"
      "s_branch .Lw_131\n"
      ".Lg_133:\n"
      "s_waitcnt lgkmcnt(0)\n"
      "s_nop 11\n"
      "v_mul_f32 v64, v64, v27\n"
      "v_mul_f32 v65, v65, v27\n"
      "v_mul_f32 v66, v66, v27\n"
      "v_mul_f32 v67, v67, v27\n"
      "v_mul_f32 v68, v68, v27\n"
      "v_mul_f32 v69, v69, v27\n"
      "v_mul_f32 v70, v70, v27\n"
      "v_mul_f32 v71, v71, v27\n"
      "v_mul_f32 v72, v72, v27\n"
      "v_mul_f32 v73, v73, v27\n"
      "v_mul_f32 v74, v74, v27\n"
      "v_mul_f32 v75, v75, v27\n"
      "v_mul_f32 v76, v76, v27\n"
      "v_mul_f32 v77, v77, v27\n"
      "v_mul_f32 v78, v78, v27\n"
      "v_mul_f32 v79, v79, v27\n"
      "s_branch .Lg_134\n"
      ".Lh_135:\n"
      "s_waitcnt vmcnt(0)\n"
      "s_mov_b64 exec, 255\n"
      "buffer_store_dwordx4 v[12:15], v29, s[20:23], s79 offen offset:0 sc1\n"
      "s_mov_b64 exec, -1\n"
      "s_mov_b32 s84, 64\n"
      "ds_add_u32 v2, v3 offset:4168\n"
      "s_branch .Lh_136\n"
      ".Lh_137:\n"
      "s_waitcnt vmcnt(1)\n"
      "s_mov_b64 exec, 255\n"
      "buffer_store_dwordx4 v[12:15], v29, s[20:23], s79 offen offset:0 sc1\n"
      "s_mov_b64 exec, -1\n"
      "s_mov_b32 s84, 64\n"
      "ds_add_u32 v2, v3 offset:4168\n"
      "s_branch .Lh_138\n"
      ".Lh_139:\n"
      "s_waitcnt vmcnt(1)\n"
      "s_mov_b64 exec, 255\n"
      "buffer_store_dwordx4 v[12:15], v29, s[20:23], s79 offen offset:0 sc1\n"
      "s_mov_b64 exec, -1\n"
      "s_mov_b32 s84, 64\n"
      "ds_add_u32 v2, v3 offset:4168\n"
      "s_branch .Lh_140\n"
      ".Lh_141:\n"
      "s_waitcnt vmcnt(2)\n"
      "s_mov_b64 exec, 255\n"
      "buffer_store_dwordx4 v[12:15], v29, s[20:23], s79 offen offset:0 sc1\n"
      "s_mov_b64 exec, -1\n"
      "s_mov_b32 s84, 64\n"
      "ds_add_u32 v2, v3 offset:4168\n"
      "s_branch .Lh_142\n"
      ".Lg_143:\n"
      "s_waitcnt lgkmcnt(0)\n"
      "s_nop 11\n"
      "v_mul_f32 v64, v64, v27\n"
      "v_mul_f32 v65, v65, v27\n"
      "v_mul_f32 v66, v66, v27\n"
      "v_mul_f32 v67, v67, v27\n"
      "v_mul_f32 v68, v68, v27\n"
      "v_mul_f32 v69, v69, v27\n"
      "v_mul_f32 v70, v70, v27\n"
      "v_mul_f32 v71, v71, v27\n"
      "v_mul_f32 v72, v72, v27\n"
      "v_mul_f32 v73, v73, v27\n"
      "v_mul_f32 v74, v74, v27\n"
      "v_mul_f32 v75, v75, v27\n"
      "v_mul_f32 v76, v76, v27\n"
      "v_mul_f32 v77, v77, v27\n"
      "v_mul_f32 v78, v78, v27\n"
      "v_mul_f32 v79, v79, v27\n"
      "s_branch .Lg_144\n"
      ".Lh_145:\n"
      "s_waitcnt vmcnt(4)\n"
      "s_mov_b64 exec, 255\n"
      "buffer_store_dwordx4 v[12:15], v29, s[20:23], s79 offen offset:0 sc1\n"
      "s_mov_b64 exec, -1\n"
      "s_mov_b32 s84, 64\n"
      "ds_add_u32 v2, v3 offset:4168\n"
      "s_branch .Lh_146\n"
      ".Lw_147:\n"
      "s_mov_b32 s3, 0x80000\n"
      ".Lw_149:\n"
      "ds_read_b32 v31, v30 offset:4240\n"
      "s_waitcnt lgkmcnt(0)\n"
      "v_readfirstlane_b32 s2, v31\n"
      "s_sub_u32 s3, s3, 1\n"
      "s_cselect_b32 s2, -1, s2\n"
      "s_cmp_lt_u32 s2, 4\n"
      "s_cbranch_scc1 .Lw_149\n"
      "s_branch .Lw_148\n"
      ".Lh_150:\n"
      "s_waitcnt vmcnt(4)\n"
      "s_mov_b64 exec, 255\n"
      "buffer_store_dwordx4 v[12:15], v29, s[20:23], s79 offen offset:0 sc1\n"
      "s_mov_b64 exec, -1\n"
      "s_mov_b32 s84, 64\n"
      "ds_add_u32 v2, v3 offset:4168\n"
      "s_branch .Lh_151\n"
      ".Lh_152:\n"
      "s_waitcnt vmcnt(0)\n"
      "s_mov_b64 exec, 255\n"
      "buffer_store_dwordx4 v[12:15], v29, s[20:23], s79 offen offset:128 sc1\n"
      "s_mov_b64 exec, -1\n"
      "s_mov_b32 s85, 64\n"
      "s_branch .Lh_153\n"
      ".Lh_154:\n"
      "s_waitcnt vmcnt(4)\n"
      "s_mov_b64 exec, 255\n"
      "buffer_store_dwordx4 v[12:15], v29, s[20:23], s79 offen offset:0 sc1\n"
      "s_mov_b64 exec, -1\n"
      "s_mov_b32 s84, 64\n"
      "ds_add_u32 v2, v3 offset:4168\n"
      "s_branch .Lh_155\n"
      ".Lh_156:\n"
      "s_waitcnt vmcnt(0)\n"
      "s_mov_b64 exec, 255\n"
      "buffer_store_dwordx4 v[12:15], v29, s[20:23], s79 offen offset:128 sc1\n"
      "s_mov_b64 exec, -1\n"
      "s_mov_b32 s85, 64\n"
      "s_branch .Lh_157\n"
      ".Lh_158:\n"
      "s_waitcnt vmcnt(0)\n"
      "s_mov_b64 exec, 255\n"
      "buffer_store_dwordx4 v[12:15], v29, s[20:23], s79 offen offset:128 sc1\n"
      "s_mov_b64 exec, -1\n"
      "s_mov_b32 s85, 64\n"
      "s_branch .Lh_159\n"
      ".Lh_160:\n"
      "s_waitcnt vmcnt(0)\n"
      "s_mov_b64 exec, 255\n"
      "buffer_store_dwordx4 v[12:15], v29, s[20:23], s79 offen offset:128 sc1\n"
      "s_mov_b64 exec, -1\n"
      "s_mov_b32 s85, 64\n"
      "s_branch .Lh_161\n"
      ".Lw_162:\n"
      "s_mov_b32 s3, 0x80000\n"
      ".Lw_164:\n"
      "ds_read_b32 v31, v2 offset:4280\n"
      "s_waitcnt lgkmcnt(0)\n"
      "v_readfirstlane_b32 s2, v31\n"
      "s_sub_u32 s3, s3, 1\n"
      "s_cselect_b32 s2, -1, s2\n"
      "s_cmp_lt_u32 s2, 4\n"
      "s_cbranch_scc1 .Lw_164\n"
      "s_branch .Lw_163\n"
      ".Lh_165:\n"
      "s_waitcnt vmcnt(0)\n"
      "s_mov_b64 exec, 255\n"
      "buffer_store_dwordx4 v[12:15], v29, s[20:23], s79 offen offset:128 sc1\n"
      "s_mov_b64 exec, -1\n"
      "s_mov_b32 s85, 64\n"
      "s_branch .Lh_166\n"
      ".Lw_167:\n"
      "s_mov_b32 s3, 0x80000\n"
      ".Lw_169:\n"
      "ds_read_b32 v31, v2 offset:4276\n"
      "s_waitcnt lgkmcnt(0)\n"
      "v_readfirstlane_b32 s2, v31\n"
      "s_sub_u32 s3, s3, 1\n"
      "s_cselect_b32 s2, -1, s2\n"
      "s_cmp_lt_u32 s2, 4\n"
      "s_cbranch_scc1 .Lw_169\n"
      "s_branch .Lw_168\n"
      ".Lw_173:\n"
      "s_mov_b32 s3, 0x80000\n"
      ".Lw_175:\n"
      "ds_read_b32 v31, v2 offset:4308\n"
      "s_waitcnt lgkmcnt(0)\n"
      "v_readfirstlane_b32 s2, v31\n"
      "s_sub_u32 s3, s3, 1\n"
      "s_cselect_b32 s2, -1, s2\n"
      "s_cmp_lt_u32 s2, 4\n"
      "s_cbranch_scc1 .Lw_175\n"
      "s_branch .Lw_174\n"
      ".Lf_177:\n"
      "s_mov_b32 s3, 0x10000\n"
      ".Lf_179:\n"
      "s_sleep 4\n"
      "buffer_load_dwordx2 v[26:27], v25, s[20:23], 0 offen sc1\n"
      "s_waitcnt vmcnt(0)\n"
      "v_cmp_ne_u64 vcc, s[58:59], v[26:27]\n"
      "s_sub_u32 s3, s3, 1\n"
      "s_cselect_b64 vcc, 0, vcc\n"
      "s_cbranch_vccnz .Lf_179\n"
      "s_branch .Lf_178\n"
      ".Ly_182:\n"
      "s_mov_b32 s3, 0x10000\n"
      ".Ly_184:\n"
      "s_sleep 4\n"
      "buffer_load_dwordx4 v[164:167], v9, s[20:23], s72 offen offset:0 sc1\n"
      "buffer_load_dwordx4 v[168:171], v9, s[20:23], s72 offen offset:1024 sc1\n"
      "buffer_load_dwordx4 v[172:175], v9, s[20:23], s72 offen offset:2048 sc1\n"
      "s_waitcnt vmcnt(0)\n"
      "v_xor_b32 v21, s74, v167\n"
      "v_bitop3_b32 v21, v21, v171, s74 bitop3:0xf6\n"
      "v_bitop3_b32 v21, v21, v175, s74 bitop3:0xf6\n"
      "v_cmp_lt_u32 vcc, 0xffff, v21\n"
      "s_sub_u32 s3, s3, 1\n"
      "s_cselect_b64 vcc, 0, vcc\n"
      "s_cbranch_vccnz .Ly_184\n"
      "s_branch .Ly_183\n"
      ".Lt_194:\n"
      "s_mov_b32 s3, 0x10000\n"
      ".Lt_196:\n"
      "s_sleep 4\n"
      "buffer_load_dwordx2 v[76:77], v42, s[20:23], s31 offen sc1\n"
      "s_waitcnt vmcnt(0)\n"
      "v_cmp_ne_u32 vcc, s58, v77\n"
      "s_sub_u32 s3, s3, 1\n"
      "s_cselect_b64 vcc, 0, vcc\n"
      "s_cbranch_vccnz .Lt_196\n"
      "s_branch .Lt_195\n"
      ".Lb_197:\n"
      "s_mov_b64 exec, vcc\n"
      "v_mov_b32 v80, 0xc0135761\n"
      "v_mov_b32 v81, 0xbdd2d3e8\n"
      "v_mov_b32 v82, 1.0\n"
      "v_mov_b32 v83, v77\n"
      "s_mov_b64 exec, -1\n"
      "ds_add_u32 v2, v3 offset:4144\n"
      "s_branch .Lb_198\n"
      ".Lf_201:\n"
      "s_mov_b32 s3, 0x10000\n"
      ".Lf_203:\n"
      "s_sleep 4\n"
      "buffer_load_dwordx2 v[48:49], v30, s[20:23], 0 offen sc1\n"
      "s_waitcnt vmcnt(0)\n"
      "v_cmp_ne_u64 vcc, s[58:59], v[48:49]\n"
      "s_sub_u32 s3, s3, 1\n"
      "s_cselect_b64 vcc, 0, vcc\n"
      "s_cbranch_vccnz .Lf_203\n"
      "s_branch .Lf_202\n"
      ".Lf_208:\n"
      "s_mov_b32 s3, 0x10000\n"
      ".Lf_210:\n"
      "s_sleep 4\n"
      "buffer_load_dwordx2 v[26:27], v25, s[20:23], s64 offen sc1\n"
      "s_waitcnt vmcnt(0)\n"
      "v_cmp_ne_u64 vcc, s[58:59], v[26:27]\n"
      "s_sub_u32 s3, s3, 1\n"
      "s_cselect_b64 vcc, 0, vcc\n"
      "s_cbranch_vccnz .Lf_210\n"
      "s_branch .Lf_209\n"
      ".Lf_211:\n"
      "s_mov_b32 s3, 0x10000\n"
      ".Lf_213:\n"
      "s_sleep 4\n"
      "buffer_load_dwordx2 v[26:27], v25, s[20:23], s64 offen sc1\n"
      "s_waitcnt vmcnt(0)\n"
      "v_cmp_ne_u64 vcc, s[58:59], v[26:27]\n"
      "s_sub_u32 s3, s3, 1\n"
      "s_cselect_b64 vcc, 0, vcc\n"
      "s_cbranch_vccnz .Lf_213\n"
      "s_branch .Lf_212\n"
      ".Lf_214:\n"
      "s_mov_b32 s3, 0x10000\n"
      ".Lf_216:\n"
      "s_sleep 4\n"
      "buffer_load_dwordx2 v[26:27], v25, s[20:23], s64 offen sc1\n"
      "s_waitcnt vmcnt(0)\n"
      "v_cmp_ne_u64 vcc, s[58:59], v[26:27]\n"
      "s_sub_u32 s3, s3, 1\n"
      "s_cselect_b64 vcc, 0, vcc\n"
      "s_cbranch_vccnz .Lf_216\n"
      "s_branch .Lf_215\n"
      ".Lf_217:\n"
      "s_mov_b32 s3, 0x10000\n"
      ".Lf_219:\n"
      "s_sleep 4\n"
      "buffer_load_dwordx2 v[26:27], v25, s[20:23], s64 offen sc1\n"
      "s_waitcnt vmcnt(0)\n"
      "v_cmp_ne_u64 vcc, s[58:59], v[26:27]\n"
      "s_sub_u32 s3, s3, 1\n"
      "s_cselect_b64 vcc, 0, vcc\n"
      "s_cbranch_vccnz .Lf_219\n"
      "s_branch .Lf_218\n"
      ".Lw_221:\n"
      "s_mov_b32 s3, 0x80000\n"
      ".Lw_223:\n"
      "ds_read_b32 v31, v2 offset:4272\n"
      "s_waitcnt lgkmcnt(0)\n"
      "v_readfirstlane_b32 s2, v31\n"
      "s_sub_u32 s3, s3, 1\n"
      "s_cselect_b32 s2, -1, s2\n"
      "s_cmp_lt_u32 s2, 2\n"
      "s_cbranch_scc1 .Lw_223\n"
      "s_branch .Lw_222\n"
      ".Lfz_end:\n"
      ".fill 320, 4, 0xBF800000\n"
      : "+{v0}"(tid) : "{s[0:1]}"(__builtin_amdgcn_kernarg_segment_ptr()), "{s2}"(blockIdx.x) : "v1", "v2", "v3", "v4", "v5", "v6", "v7", "v8", "v9", "v10", "v11", "v12", "v13", "v14", "v15", "v16", "v17", "v18", "v19", "v20", "v21", "v22", "v23", "v24", "v25", "v26", "v27", "v28", "v29", "v30", "v31", "v32", "v33", "v34", "v35", "v36", "v37", "v38", "v39", "v40", "v41", "v42", "v43", "v44", "v45", "v46", "v47", "v48", "v49", "v50", "v51", "v52", "v53", "v54", "v55", "v56", "v57", "v58", "v59", "v60", "v61", "v62", "v63", "v64", "v65", "v66", "v67", "v68", "v69", "v70", "v71", "v72", "v73", "v74", "v75", "v76", "v77", "v78", "v79", "v80", "v81", "v82", "v83", "v84", "v85", "v86", "v87", "v88", "v89", "v90", "v91", "v92", "v93", "v94", "v95", "v96", "v97", "v98", "v99", "v100", "v101", "v102", "v103", "v104", "v105", "v106", "v107", "v108", "v109", "v110", "v111", "v112", "v113", "v114", "v115", "v116", "v117", "v118", "v119", "v120", "v121", "v122", "v123", "v124", "v125", "v126", "v127", "v128", "v129", "v130", "v131", "v132", "v133", "v134", "v135", "v136", "v137", "v138", "v139", "v140", "v141", "v142", "v143", "v144", "v145", "v146", "v147", "v148", "v149", "v150", "v151", "v152", "v153", "v154", "v155", "v156", "v157", "v158", "v159", "v160", "v161", "v162", "v163", "v164", "v165", "v166", "v167", "v168", "v169", "v170", "v171", "v172", "v173", "v174", "v175", "v176", "v177", "v178", "v179", "v180", "v181", "v182", "v183", "v184", "v185", "v186", "v187", "v188", "v189", "v190", "v191", "v192", "v193", "v194", "v195", "v196", "v197", "v198", "v199", "v200", "v201", "v202", "v203", "v204", "v205", "v206", "v207", "v208", "v209", "v210", "v211", "v212", "v213", "v214", "v215", "v216", "v217", "v218", "v219", "v220", "v221", "v222", "v223", "v224", "v225", "v226", "v227", "v228", "v229", "v230", "v231", "v232", "v233", "v234", "v235", "v236", "v237", "v238", "v239", "v240", "v241", "v242", "v243", "v244", "v245", "v246", "v247", "v248", "v249", "v250", "v251", "v252", "v253", "v254", "v255", "s3", "s4", "s5", "s6", "s7", "s8", "s9", "s10", "s11", "s12", "s13", "s14", "s15", "s16", "s17", "s18", "s19", "s20", "s21", "s22", "s23", "s24", "s25", "s26", "s27", "s28", "s29", "s30", "s31", "s33", "s34", "s35", "s36", "s37", "s38", "s39", "s40", "s41", "s42", "s43", "s44", "s45", "s46", "s47", "s48", "s49", "s50", "s51", "s52", "s53", "s54", "s55", "s56", "s57", "s58", "s59", "s60", "s61", "s62", "s63", "s64", "s65", "s66", "s67", "s68", "s69", "s70", "s71", "s72", "s73", "s74", "s75", "s76", "s77", "s78", "s79", "s80", "s81", "s82", "s83", "s84", "s85", "s86", "s87", "s88", "s89", "s90", "s91", "s92", "s93", "s94", "s95", "s96", "s97", "s98", "s99", "vcc", "scc", "memory");
#endif
}
}

extern "C" void kernel_launch(void* const* d_in, const int* in_sizes, int n_in, void* d_out, int out_size, void* d_ws,
                              size_t ws_size, hipStream_t stream) {
  (void)hipFuncSetAttribute((const void*)fused_kernel, hipFuncAttributeMaxDynamicSharedMemorySize, 163840);
  fused_kernel<<<256, 512, 163840, stream>>>(d_in[0], 2621440u, 0x20000u, d_in[7], 2211840u, 0x20000u, d_in[1], 2621440u, 0x20000u, d_in[2], 2621440u, 0x20000u, d_ws, 16111616u, 0x20000u, d_in[3], 5120u, 0x20000u, d_in[8], 3456u, 0x20000u, d_in[5], 1280u, 0x20000u, d_in[4], 2621440u, 0x20000u, d_in[9], 2211840u, 0x20000u, d_in[10], 1280u, 0x20000u, d_out, 2621440u, 0x20000u);
}
```
